```python
import torch

def _softmax(x, axis=-1):
    return torch.softmax(x, dim=axis)
def _gelu(x):
    return torch.nn.functional.gelu(x, approximate="tanh")
def _silu(x):
    return torch.nn.functional.silu(x)
def _sigmoid(x):
    return torch.sigmoid(x)
def _layernorm(x, axis=-1, eps=1e-5):
    m = x.mean(dim=axis, keepdim=True)
    v = x.var(dim=axis, keepdim=True, unbiased=False)
    return (x - m) / torch.sqrt(v + eps)
def _scatter_add(tgt, idx, src, axis=0):
    return tgt.clone().index_add_(axis, idx, src)
def _segment_sum(src, seg, n):
    out = torch.zeros((n,) + src.shape[1:], dtype=src.dtype)
    return out.index_add_(0, seg, src)
def _pad(x, pw):
    flat = tuple(v for pair in reversed(pw) for v in pair)
    return torch.nn.functional.pad(x, flat)

kernel_name = 'parallel_sum__ssm_conv_s5__geglu_ffn__prompt'

def setup_inputs():

    _w = (lambda a: (lambda t: t.to(torch.bfloat16) if t.is_floating_point() else t)(torch.as_tensor(a)))
    def _A_setup():
        B, S, D = 1, 2048, 576
        P = D
        Lambda_re = -0.5 * (lambda _x: torch.exp(torch.as_tensor(_x)))((lambda *s: torch.randn(*s, dtype=torch.bfloat16) * 0.8)(P))
        Lambda_im = (lambda *s: torch.randn(*s, dtype=torch.bfloat16) * 0.8)(P) * 1.0
        B_re = (lambda *s: torch.randn(*s, dtype=torch.bfloat16) * 0.8)(P, D) * (D ** -0.5)
        B_im = (lambda *s: torch.randn(*s, dtype=torch.bfloat16) * 0.8)(P, D) * (D ** -0.5)
        C_re = (lambda *s: torch.randn(*s, dtype=torch.bfloat16) * 0.8)(D, P) * (P ** -0.5)
        C_im = (lambda *s: torch.randn(*s, dtype=torch.bfloat16) * 0.8)(D, P) * (P ** -0.5)
        log_dt = (lambda *s: torch.randn(*s, dtype=torch.bfloat16) * 0.8)(P) * 0.3 - 2.0
        Dskip = (lambda *s: torch.randn(*s, dtype=torch.bfloat16) * 0.8)(D)
        return {
            "x": _w((lambda *s: torch.randn(*s, dtype=torch.bfloat16) * 0.05)(B, S, D)),
            "Lambda_re": _w(Lambda_re),
            "Lambda_im": _w(Lambda_im),
            "B_re": _w(B_re),
            "B_im": _w(B_im),
            "C_re": _w(C_re),
            "C_im": _w(C_im),
            "log_dt": _w(log_dt),
            "Dskip": _w(Dskip),
        }
    _A_in = _A_setup()

    def _B_setup():
        B, S, D, F = 1, 2048, 576, 4608
        return {
            "x": _w((lambda *s: torch.randn(*s, dtype=torch.bfloat16) * 0.05)(B, S, D)),
            "w_gate": _w((lambda *s: torch.randn(*s, dtype=torch.bfloat16) * 0.05)(D, F)),
            "w_up": _w((lambda *s: torch.randn(*s, dtype=torch.bfloat16) * 0.05)(D, F)),
            "w_down": _w((lambda *s: torch.randn(*s, dtype=torch.bfloat16) * 0.05)(F, D)),
        }
    _B_in = _B_setup()

    return {
        "A_x": _A_in["x"],
        "A_Lambda_re": _A_in["Lambda_re"],
        "A_Lambda_im": _A_in["Lambda_im"],
        "A_B_re": _A_in["B_re"],
        "A_B_im": _A_in["B_im"],
        "A_C_re": _A_in["C_re"],
        "A_C_im": _A_in["C_im"],
        "A_log_dt": _A_in["log_dt"],
        "A_Dskip": _A_in["Dskip"],
        "B_x": _B_in["x"],
        "B_w_gate": _B_in["w_gate"],
        "B_w_up": _B_in["w_up"],
        "B_w_down": _B_in["w_down"],
    }

def reference(A_x, A_Lambda_re, A_Lambda_im, A_B_re, A_B_im, A_C_re, A_C_im, A_log_dt, A_Dskip, B_x, B_w_gate, B_w_up, B_w_down):
    def _A_ref(x, Lambda_re, Lambda_im, B_re, B_im, C_re, C_im, log_dt, Dskip):
        Bsz, L, H = x.shape
        Lam = Lambda_re + 1j * Lambda_im
        Bmat = B_re + 1j * B_im
        Cmat = C_re + 1j * C_im
        dt = torch.exp(log_dt)
        Lam_bar = torch.exp(dt * Lam)
        B_bar = ((Lam_bar - 1.0) / Lam)[:, None] * Bmat
        Bu = torch.einsum("bld,pd->blp", x + 0j, B_bar)
        h = torch.zeros_like(Bu[:, 0, :])
        hs = []
        for t in range(L):
            h = Lam_bar[None, :] * h + Bu[:, t, :]
            hs.append(h)
        hseq = torch.stack(hs, 1)
        y = 2.0 * torch.einsum("blp,dp->bld", hseq, Cmat).real + x * Dskip[None, None, :]
        return y

    def _B_ref(x, w_gate, w_up, w_down):
        return (_gelu(x @ w_gate) * (x @ w_up)) @ w_down

    x = A_x
    xn = _layernorm(x, axis=-1)
    return x + _A_ref(xn, A_Lambda_re, A_Lambda_im, A_B_re, A_B_im, A_C_re, A_C_im, A_log_dt, A_Dskip) + _B_ref(xn, B_w_gate, B_w_up, B_w_down)
```

```cpp
#include <hip/hip_runtime.h>
#include <hip/hip_bf16.h>
#define MIMIC 0
typedef __hip_bfloat16 bf16;
constexpr int L=2048, D=576, P=576, F=4608;
__device__ __forceinline__ float b2f(bf16 x){return __bfloat162float(x);}
__device__ __forceinline__ float rb(float x){
  return x;
}
__global__ void k_ln(const bf16* x, float* xn){
  int row=blockIdx.x; int t=threadIdx.x;
  float s=0; for(int i=t;i<D;i+=64) s+=b2f(x[row*D+i]);
  for(int o=32;o;o>>=1) s+=__shfl_xor(s,o);
  float mean=s/D;
  float v=0; for(int i=t;i<D;i+=64){float d=b2f(x[row*D+i])-mean; v+=d*d;}
  for(int o=32;o;o>>=1) v+=__shfl_xor(v,o);
  v/=D;
  float m=rb(mean); float vv=rb(v);
  float sd=rb(sqrtf(rb(vv+1e-5f)));
  for(int i=t;i<D;i+=64){ float d=rb(b2f(x[row*D+i])-m); xn[row*D+i]=rb(d/sd);}
}
__device__ __forceinline__ float gelu(float x){ float u=0.7978845608028654f*(x+0.044715f*x*x*x); return 0.5f*x*(1.f+tanhf(u)); }
__global__ void k_gu(const float* xn, const bf16* wg, const bf16* wu, float* act){
  int f=blockIdx.x*256+threadIdx.x; int r0=blockIdx.y*8;
  __shared__ float xs[8][D];
  for(int i=threadIdx.x;i<8*D;i+=256) xs[i/D][i%D]=xn[r0*D+i];
  __syncthreads();
  float g[8]={},u[8]={};
  for(int k=0;k<D;k++){ float a=b2f(wg[k*F+f]), b=b2f(wu[k*F+f]);
    for(int r=0;r<8;r++){g[r]+=xs[r][k]*a; u[r]+=xs[r][k]*b;} }
  for(int r=0;r<8;r++) act[(size_t)(r0+r)*F+f]=rb(rb(gelu(rb(g[r])))*rb(u[r]));
}
__global__ void k_bu(const float* xn, const bf16* bre, const bf16* bim, float* bu){
  int p=blockIdx.x*64+threadIdx.x; int r0=blockIdx.y*8;
  __shared__ float xs[8][D];
  for(int i=threadIdx.x;i<8*D;i+=64) xs[i/D][i%D]=xn[r0*D+i];
  __syncthreads();
  float g[8]={},u[8]={};
  for(int k=0;k<D;k++){ float a=b2f(bre[p*D+k]), b=b2f(bim[p*D+k]);
    for(int r=0;r<8;r++){g[r]+=xs[r][k]*a; u[r]+=xs[r][k]*b;} }
  for(int r=0;r<8;r++){ bu[((size_t)(r0+r)*P+p)*2]=g[r]; bu[((size_t)(r0+r)*P+p)*2+1]=u[r]; }
}
__global__ void k_scan(const bf16* lre,const bf16* lim,const bf16* ldt, float* bu){
  int p=blockIdx.x*64+threadIdx.x;
  float dt=rb(expf(b2f(ldt[p]))); float a=b2f(lre[p]), b=b2f(lim[p]);
  float e=expf(dt*a); float lr=e*cosf(dt*b), li=e*sinf(dt*b);
  float nr=lr-1.f, ni=li; float den=a*a+b*b; float cr=(nr*a+ni*b)/den, ci=(ni*a-nr*b)/den;
  float hr=0,hi=0;
  for(int t=0;t<L;t++){ float ur=bu[((size_t)t*P+p)*2], ui=bu[((size_t)t*P+p)*2+1];
    float br=cr*ur-ci*ui, bi=cr*ui+ci*ur;
    float nhr=lr*hr-li*hi+br, nhi=lr*hi+li*hr+bi; hr=nhr; hi=nhi;
    bu[((size_t)t*P+p)*2]=hr; bu[((size_t)t*P+p)*2+1]=hi; }
}
__global__ void k_out(const bf16* x,const float* xn,const float* act,const float* h,const bf16* wd,const bf16* cre,const bf16* cim,const bf16* dsk,float* out){
  int d=blockIdx.x*64+threadIdx.x; int r0=blockIdx.y*4;
  float acc[4]={}, y[4]={};
  for(int k=0;k<F;k++){ float w=b2f(wd[k*D+d]); for(int r=0;r<4;r++) acc[r]+=act[(size_t)(r0+r)*F+k]*w; }
  for(int p=0;p<P;p++){ float a=b2f(cre[d*P+p]), b=b2f(cim[d*P+p]); for(int r=0;r<4;r++) y[r]+=h[((size_t)(r0+r)*P+p)*2]*a-h[((size_t)(r0+r)*P+p)*2+1]*b; }
  for(int r=0;r<4;r++){ int i=(r0+r)*D+d; float ya=2.f*y[r]+rb(xn[i]*b2f(dsk[d])); out[i]=(b2f(x[i])+ya)+rb(acc[r]); }
}
extern "C" void kernel_launch(void* const* d_in, const int* in_sizes, int n_in, void* d_out, int out_size, void* d_ws, size_t ws_size, hipStream_t stream){
  const bf16* x=(const bf16*)d_in[0];
  float* xn=(float*)d_ws; float* act=xn+L*D; float* bu=act+(size_t)L*F;
  k_ln<<<L,64,0,stream>>>(x,xn);
  k_gu<<<dim3(F/256,L/8),256,0,stream>>>(xn,(const bf16*)d_in[10],(const bf16*)d_in[11],act);
  k_bu<<<dim3(P/64,L/8),64,0,stream>>>(xn,(const bf16*)d_in[3],(const bf16*)d_in[4],bu);
  k_scan<<<P/64,64,0,stream>>>((const bf16*)d_in[1],(const bf16*)d_in[2],(const bf16*)d_in[7],bu);
  k_out<<<dim3(D/64,L/4),64,0,stream>>>(x,xn,act,bu,(const bf16*)d_in[12],(const bf16*)d_in[5],(const bf16*)d_in[6],(const bf16*)d_in[8],(float*)d_out);
}
```

```cpp
#include <hip/hip_runtime.h>
#include <hip/hip_bf16.h>
#include <stdint.h>
#include <type_traits>

typedef unsigned short u16;
typedef unsigned int u32;
typedef unsigned long long u64;
typedef __attribute__((ext_vector_type(8))) __bf16 bf16x8;
typedef __attribute__((ext_vector_type(2))) __bf16 bf16x2;
typedef __attribute__((ext_vector_type(4))) short s16x4;
typedef __attribute__((ext_vector_type(8))) short s16x8;
typedef __attribute__((ext_vector_type(4))) float f32x4;
typedef __attribute__((ext_vector_type(2))) float f32x2;
typedef __attribute__((ext_vector_type(4))) u32 u32x4;
typedef __attribute__((ext_vector_type(2))) u32 u32x2;
#define LDSP __attribute__((address_space(3)))
#define GLBP __attribute__((address_space(1)))
#define DI __device__ __forceinline__

constexpr int SEQ = 2048, DM = 576, NP = 576, FF = 4608;
constexpr size_t WS_PART = 0;
constexpr size_t WS_HLOC = WS_PART + (size_t)8 * 32 * 9 * 8 * 1024;
constexpr size_t WS_ETILE = WS_HLOC + (size_t)32 * 36 * 4 * 1024;
constexpr size_t WS_SCH = WS_ETILE + (size_t)128 * 576 * 8;
constexpr size_t WS_POW = WS_SCH + (size_t)32 * 576 * 8;
constexpr size_t WS_LAM = WS_POW + (size_t)36 * 2048;
constexpr size_t WS_CP = WS_LAM + (size_t)2 * 576 * 8;
constexpr size_t WS_STAT = WS_CP + (size_t)8 * 9 * 24576;
constexpr size_t WS_STAMP = WS_STAT + (size_t)2048 * 8;

#define STAMP(k, i) do {} while (0)

DI float bf_lo(u32 v) { return __uint_as_float(v << 16); }
DI float bf_hi(u32 v) { return __uint_as_float(v & 0xffff0000u); }
DI float bf2f(u16 v) { return __uint_as_float((u32)v << 16); }
DI u32 pack2(float a, float b) { bf16x2 r = {(__bf16)a, (__bf16)b}; return __builtin_bit_cast(u32, r); }
DI float rbf(float a) { return (float)(__bf16)a; }
DI f32x4 mfma16(s16x8 a, s16x8 b, f32x4 c) {
  return __builtin_amdgcn_mfma_f32_16x16x32_bf16(__builtin_bit_cast(bf16x8, a), __builtin_bit_cast(bf16x8, b), c, 0, 0, 0);
}
DI int pt_start(int j) { return (9 * j + 1) >> 1; }
DI int pt_count(int j) { return 5 - (j & 1); }

DI void ln_rows_to_lds(const u32x4 (&xv)[9], char* img, int m, int q, float& mean_out, float& sd_out) {
  float s = 0.f;
#pragma unroll
  for (int i = 0; i < 9; ++i)
#pragma unroll
    for (int e = 0; e < 4; ++e) s += bf_lo(xv[i][e]) + bf_hi(xv[i][e]);
  s += __shfl_xor(s, 1); s += __shfl_xor(s, 2); s += __shfl_xor(s, 4);
  const float mean = s * (1.f / DM);
  float v = 0.f;
#pragma unroll
  for (int i = 0; i < 9; ++i)
#pragma unroll
    for (int e = 0; e < 4; ++e) { float a = bf_lo(xv[i][e]) - mean, b = bf_hi(xv[i][e]) - mean; v += a * a + b * b; }
  v += __shfl_xor(v, 1); v += __shfl_xor(v, 2); v += __shfl_xor(v, 4);
  const float mb = rbf(mean);
  const float sd = rbf(sqrtf(rbf(rbf(v * (1.f / DM)) + 1e-5f)));
  const float rs = 1.f / sd;
  mean_out = mb; sd_out = rs;
  const int cs = (q & 3) | ((q & 4) ? 12 : 0);
  char* dst = img + (((m >> 4) * 18 + (q >> 2)) << 10) + 256 * (q & 3) + 16 * ((m & 15) ^ cs);
#pragma unroll
  for (int i = 0; i < 9; ++i) {
    u32x4 o;
#pragma unroll
    for (int e = 0; e < 4; ++e) o[e] = pack2(rbf(bf_lo(xv[i][e]) - mb) * rs, rbf(bf_hi(xv[i][e]) - mb) * rs);
    *(u32x4*)(dst + i * 2048) = o;
  }
}
DI int xn_rd_off(int lane, int odd) {
  const int g = lane >> 4, mm = lane & 15;
  return 256 * g + 16 * (mm ^ (g | (odd ? 12 : 0)));
}

struct cf { float r, i; };
DI cf cmul(cf a, cf b) { return {a.r * b.r - a.i * b.i, a.r * b.i + a.i * b.r}; }
DI cf cfma(cf a, cf b, cf c) { return {fmaf(a.r, b.r, fmaf(-a.i, b.i, c.r)), fmaf(a.r, b.i, fmaf(a.i, b.r, c.i))}; }
template <int N> DI float dpp_shr(float v) {
  return __int_as_float(__builtin_amdgcn_update_dpp(0, __float_as_int(v), 0x110 + N, 0xf, 0xf, true));
}

constexpr int K0_BU_WGS = 288, K0_CP_WGS = 90;
__global__ __launch_bounds__(256) void k0_ssm_prep(const u16* __restrict__ x, const u16* __restrict__ lre, const u16* __restrict__ lim,
                                                   const u16* __restrict__ bre, const u16* __restrict__ bim, const u16* __restrict__ cre,
                                                   const u16* __restrict__ cim, const u16* __restrict__ ldt, char* __restrict__ ws) {
  __shared__ __attribute__((aligned(16))) char smem[73728];
  const int tid = threadIdx.x, lane = tid & 63, w = __builtin_amdgcn_readfirstlane(tid >> 6);
  const int g = lane >> 4, mm = lane & 15;
  STAMP(0, 0);
  if (blockIdx.x >= K0_BU_WGS) {
    const int base = ((blockIdx.x - K0_BU_WGS) * 4 + w) * 4;
#pragma unroll
    for (int i = 0; i < 4; ++i) {
      const int fr = base + i;
      const int ct = fr & 3, t = fr >> 2, ks = t % 5, t2 = t / 5, pass = t2 % 9, j = t2 / 9;
      const int d = 64 * pass + 16 * ct + mm;
      u32x4 o = {0, 0, 0, 0};
      if (ks < pt_count(j)) {
        const int p = 16 * (pt_start(j) + ks) + 4 * g;
        const u32x2 a = *(const u32x2*)(cre + d * NP + p), b = *(const u32x2*)(cim + d * NP + p);
        o = u32x4{a[0], a[1], b[0], b[1]};
      }
      *(u32x4*)(ws + WS_CP + (size_t)(j * 9 + pass) * 24576 + ks * 4096 + ct * 1024 + lane * 16) = o;
    }
    STAMP(0, 1);
    return;
  }
  const int r = blockIdx.x / 9, pq = blockIdx.x % 9, pt = 4 * pq + w;
  u32x4 xv[2][9];
  const int q = lane & 7;
#pragma unroll
  for (int h = 0; h < 2; ++h) {
    const int m = 16 * w + 8 * h + (lane >> 3);
#pragma unroll
    for (int i = 0; i < 9; ++i) xv[h][i] = *(const u32x4*)(x + (size_t)(64 * r + m) * DM + (q + 8 * i) * 8);
  }
  s16x8 fre[18], fim[18];
#pragma unroll
  for (int s = 0; s < 18; ++s) {
    fre[s] = *(const s16x8*)(bre + (16 * pt + mm) * DM + 32 * s + 8 * g);
    fim[s] = *(const s16x8*)(bim + (16 * pt + mm) * DM + 32 * s + 8 * g);
  }
  cf lam[4], coef[4];
#pragma unroll
  for (int i = 0; i < 4; ++i) {
    const int p = 16 * pt + 4 * g + i;
    const float dt = rbf(expf(bf2f(ldt[p]))), a = bf2f(lre[p]), b = bf2f(lim[p]);
    const float e = expf(dt * a);
    float sn, cs; sincosf(dt * b, &sn, &cs);
    lam[i] = {e * cs, e * sn};
    const float nr = lam[i].r - 1.f, ni = lam[i].i, den = 1.f / (a * a + b * b);
    coef[i] = {(nr * a + ni * b) * den, (ni * a - nr * b) * den};
  }
#pragma unroll
  for (int h = 0; h < 2; ++h) {
    const int m = 16 * w + 8 * h + (lane >> 3);
    float mean, rs;
    ln_rows_to_lds(xv[h], smem, m, q, mean, rs);
    if (pq == 0 && q == 0) *(f32x2*)(ws + WS_STAT + (size_t)(64 * r + m) * 8) = f32x2{mean, rs};
  }
  __syncthreads();
  f32x4 ar[4], ai[4];
#pragma unroll
  for (int t = 0; t < 4; ++t) { ar[t] = f32x4{0, 0, 0, 0}; ai[t] = f32x4{0, 0, 0, 0}; }
  const int o0 = xn_rd_off(lane, 0), o1 = xn_rd_off(lane, 1);
#pragma unroll
  for (int s = 0; s < 18; ++s)
#pragma unroll
    for (int t = 0; t < 4; ++t) {
      const s16x8 xf = *(const s16x8*)(smem + ((t * 18 + s) << 10) + ((s & 1) ? o1 : o0));
      ar[t] = mfma16(fre[s], xf, ar[t]);
      ai[t] = mfma16(fim[s], xf, ai[t]);
    }
  cf l2[4], l4[4], l8[4], l16[4];
#pragma unroll
  for (int i = 0; i < 4; ++i) { l2[i] = cmul(lam[i], lam[i]); l4[i] = cmul(l2[i], l2[i]); l8[i] = cmul(l4[i], l4[i]); l16[i] = cmul(l8[i], l8[i]); }
  cf S[4];
#pragma unroll
  for (int t = 0; t < 4; ++t) {
    cf h[4];
#pragma unroll
    for (int i = 0; i < 4; ++i) {
      cf u = cmul(coef[i], cf{ar[t][i], ai[t][i]});
      u = cfma(lam[i], cf{dpp_shr<1>(u.r), dpp_shr<1>(u.i)}, u);
      u = cfma(l2[i], cf{dpp_shr<2>(u.r), dpp_shr<2>(u.i)}, u);
      u = cfma(l4[i], cf{dpp_shr<4>(u.r), dpp_shr<4>(u.i)}, u);
      u = cfma(l8[i], cf{dpp_shr<8>(u.r), dpp_shr<8>(u.i)}, u);
      h[i] = u;
      S[i] = (t == 0) ? u : cfma(l16[i], S[i], u);
    }
    u32x4 o = {pack2(2.f * h[0].r, 2.f * h[1].r), pack2(2.f * h[2].r, 2.f * h[3].r), pack2(-2.f * h[0].i, -2.f * h[1].i), pack2(-2.f * h[2].i, -2.f * h[3].i)};
    *(u32x4*)(ws + WS_HLOC + ((size_t)((r * 36 + pt) * 4 + t) << 10) + lane * 16) = o;
    if (mm == 15) {
      f32x4* e = (f32x4*)(ws + WS_ETILE + ((size_t)(4 * r + t) * NP + 16 * pt + 4 * g) * 8);
      e[0] = f32x4{h[0].r, h[0].i, h[1].r, h[1].i};
      e[1] = f32x4{h[2].r, h[2].i, h[3].r, h[3].i};
    }
  }
  if (mm == 15) {
    f32x4* e = (f32x4*)(ws + WS_SCH + ((size_t)r * NP + 16 * pt + 4 * g) * 8);
    e[0] = f32x4{S[0].r, S[0].i, S[1].r, S[1].i};
    e[1] = f32x4{S[2].r, S[2].i, S[3].r, S[3].i};
  }
  if (r == 0) {
    cf pw[4];
    const int n = mm + 1;
#pragma unroll
    for (int i = 0; i < 4; ++i) {
      cf a = {1.f, 0.f};
      if (n & 1) a = lam[i];
      if (n & 2) a = cmul(a, l2[i]);
      if (n & 4) a = cmul(a, l4[i]);
      if (n & 8) a = cmul(a, l8[i]);
      if (n & 16) a = l16[i];
      pw[i] = a;
    }
    f32x4* e = (f32x4*)(ws + WS_POW + (size_t)pt * 2048 + lane * 32);
    e[0] = f32x4{pw[0].r, pw[0].i, pw[1].r, pw[1].i};
    e[1] = f32x4{pw[2].r, pw[2].i, pw[3].r, pw[3].i};
    if (mm == 0) {
#pragma unroll
      for (int i = 0; i < 4; ++i) {
        const cf l32 = cmul(l16[i], l16[i]), l64 = cmul(l32, l32);
        const int p = 16 * pt + 4 * g + i;
        *(f32x2*)(ws + WS_LAM + p * 8) = f32x2{l16[i].r, l16[i].i};
        *(f32x2*)(ws + WS_LAM + (NP + p) * 8) = f32x2{l64.r, l64.i};
      }
    }
  }
  STAMP(0, 1);
}

constexpr int L_ACT = 0, L_H = 73728, L_RING = 94208, L_MISC = 159744, L_TOTAL = 163840;
DI void glds16(const char* sbase, u32 voff, u32 lds_addr) {
  asm volatile("s_mov_b32 m0, %2\n\ts_nop 0\n\tglobal_load_lds_dwordx4 %0, %1" :: "v"(voff), "s"(sbase), "s"(lds_addr));
}
DI s16x8 tr_frag(const char* p) {
  const s16x4 a = __builtin_amdgcn_ds_read_tr16_b64_v4i16((LDSP s16x4*)p);
  const s16x4 b = __builtin_amdgcn_ds_read_tr16_b64_v4i16((LDSP s16x4*)(p + 512));
  return __builtin_shufflevector(a, b, 0, 1, 2, 3, 4, 5, 6, 7);
}
DI float geglu(float x, float u) {
  const float a = fmaf(x * x, -0.10294324f, -2.3022082f);
  const float e = __builtin_amdgcn_exp2f(x * a);
  return x * __builtin_amdgcn_rcpf(1.f + e) * u;
}
#define WAITV(n) asm volatile("s_waitcnt vmcnt(" #n ")" ::: "memory")
#define WAITL0() asm volatile("s_waitcnt lgkmcnt(0)" ::: "memory")
#define BAR() __builtin_amdgcn_s_barrier()
#define LBAR() do { WAITL0(); __builtin_amdgcn_s_barrier(); } while (0)
#define SB() __builtin_amdgcn_sched_barrier(0)
#define MF(x) x

__global__ __launch_bounds__(512, 2) void k1_main(const u16* __restrict__ x, const u16* __restrict__ wg, const u16* __restrict__ wu,
                                                  const u16* __restrict__ wd, char* __restrict__ ws) {
  __shared__ __attribute__((aligned(16))) char smem[L_TOTAL];
  const int tid = threadIdx.x, lane = tid & 63, w = __builtin_amdgcn_readfirstlane(tid >> 6);
  const int j = blockIdx.x & 7, r = blockIdx.x >> 3;
  const int g = lane >> 4, mm = lane & 15, rh = w >> 2, ct = w & 3;
  STAMP(1, 0);
  const int lr = lane >> 3, lc = lane & 7;
  const int swz_ld = (((lr >> 1) & 1) | ((w & 1) << 1)) << 1;
  const u32 voff1 = (u32)(lr * (FF * 2) + ((lc ^ swz_ld) << 4));
  const u32 voff2 = (u32)(lr * (DM * 2) + ((lc ^ swz_ld) << 4));
  const u32 voff3 = (u32)(lane << 4);
  const char* base1 = (const char*)(rh ? wu : wg) + (size_t)(8 * ct) * (FF * 2) + j * 1152;
  const char* base2 = (const char*)wd + (size_t)(576 * j + 8 * w) * (DM * 2);
  const char* base3 = ws + WS_CP + (size_t)j * 9 * 24576 + w * 1024;
  const u32 ring_w = (u32)(size_t)(LDSP char*)(smem + L_RING) + w * 1024;
  auto ld1 = [&](int pass, int s, int u) { glds16(base1 + (size_t)s * (32 * FF * 2) + pass * 128, voff1, ring_w + ((u & 7) << 13)); };
  auto ld2 = [&](int pass, int s, int u) {
    if (s < 9) glds16(base2 + (size_t)s * (64 * DM * 2) + pass * 128, voff2, ring_w + ((u & 7) << 13));
    else if (s < 11 || w < 4) glds16(base3 + pass * 24576 + (s - 9) * 8192, voff3, ring_w + ((u & 7) << 13));
  };
  u32x4 xv[9];
  {
    const int m = 8 * w + lr;
#pragma unroll
    for (int i = 0; i < 9; ++i) xv[i] = *(const u32x4*)(x + (size_t)(64 * r + m) * DM + (lc + 8 * i) * 8);
  }
#pragma unroll
  for (int u = 0; u < 6; ++u) ld1(0, u, u);
  const int pts = pt_start(j), ptc = pt_count(j);
  if (tid < 80) {
    cf T = {0.f, 0.f};
    f32x2* dst = (f32x2*)(smem + L_MISC) + tid;
    if (tid < 16 * ptc) {
      const int p = 16 * pts + tid;
      const f32x2 a = *(const f32x2*)(ws + WS_LAM + p * 8), b = *(const f32x2*)(ws + WS_LAM + (NP + p) * 8);
      const cf l16 = {a[0], a[1]}, l64 = {b[0], b[1]};
      for (int c = 0; c < r; ++c) { const f32x2 s = *(const f32x2*)(ws + WS_SCH + ((size_t)c * NP + p) * 8); T = cfma(l64, T, cf{s[0], s[1]}); }
#pragma unroll
      for (int t = 0; t < 4; ++t) {
        dst[t * 80] = f32x2{T.r, T.i};
        const f32x2 e = *(const f32x2*)(ws + WS_ETILE + ((size_t)(4 * r + t) * NP + p) * 8);
        T = cfma(l16, T, cf{e[0], e[1]});
      }
    } else {
#pragma unroll
      for (int t = 0; t < 4; ++t) dst[t * 80] = f32x2{0.f, 0.f};
    }
  }
  { float mean, rs; ln_rows_to_lds(xv, smem + L_ACT, 8 * w + lr, lc, mean, rs); }
  __syncthreads();
  for (int fr = w; fr < 20; fr += 8) {
    const int mt = fr & 3, ks = fr >> 2;
    u32x4 o = {0, 0, 0, 0};
    if (ks < ptc) {
      const int pt = pts + ks;
      const u32x4 hl = *(const u32x4*)(ws + WS_HLOC + ((size_t)((r * 36 + pt) * 4 + mt) << 10) + lane * 16);
      const f32x4 p0 = *(const f32x4*)(ws + WS_POW + (size_t)pt * 2048 + lane * 32), p1 = *(const f32x4*)(ws + WS_POW + (size_t)pt * 2048 + lane * 32 + 16);
      const f32x4 t0 = *(const f32x4*)(smem + L_MISC + (mt * 80 + ks * 16 + 4 * g) * 8), t1 = *(const f32x4*)(smem + L_MISC + (mt * 80 + ks * 16 + 4 * g) * 8 + 16);
      const cf c0 = cmul(cf{p0[0], p0[1]}, cf{t0[0], t0[1]}), c1 = cmul(cf{p0[2], p0[3]}, cf{t0[2], t0[3]});
      const cf c2 = cmul(cf{p1[0], p1[1]}, cf{t1[0], t1[1]}), c3 = cmul(cf{p1[2], p1[3]}, cf{t1[2], t1[3]});
      o[0] = pack2(fmaf(2.f, c0.r, bf_lo(hl[0])), fmaf(2.f, c1.r, bf_hi(hl[0])));
      o[1] = pack2(fmaf(2.f, c2.r, bf_lo(hl[1])), fmaf(2.f, c3.r, bf_hi(hl[1])));
      o[2] = pack2(fmaf(-2.f, c0.i, bf_lo(hl[2])), fmaf(-2.f, c1.i, bf_hi(hl[2])));
      o[3] = pack2(fmaf(-2.f, c2.i, bf_lo(hl[3])), fmaf(-2.f, c3.i, bf_hi(hl[3])));
    }
    *(u32x4*)(smem + L_H + ((mt * 5 + ks) << 10) + lane * 16) = o;
  }
  s16x8 af[2][23];
  {
    const int o0 = xn_rd_off(lane, 0), o1 = xn_rd_off(lane, 1);
#pragma unroll
    for (int t = 0; t < 2; ++t)
#pragma unroll
      for (int s = 0; s < 18; ++s) af[t][s] = *(const s16x8*)(smem + L_ACT + (((2 * rh + t) * 18 + s) << 10) + ((s & 1) ? o1 : o0));
  }
  const int rq = (lane & 15) >> 2, rp = lane & 3;
  const int swz_rd = (((rq >> 1) & 1) | ((g & 1) << 1)) << 1;
  const char* rd_tr = smem + L_RING + 1024 * g + 128 * rq + 16 * ((2 * ct + (rp >> 1)) ^ swz_rd) + 8 * (rp & 1);
  const char* rd_lin = smem + L_RING + 1024 * ct + 16 * lane;
  char* act_wr = smem + L_ACT + ((2 * rh * 18 + (ct >> 1)) << 10) + 256 * (2 * (ct & 1) + (g >> 1)) + 16 * mm + 8 * (g & 1);
  WAITV(0); WAITL0(); BAR();
  STAMP(1, 1);

  f32x4 pg[2], pu[2];
  pg[0] = pg[1] = pu[0] = pu[1] = f32x4{0, 0, 0, 0};
  s16x8 fa, fb;
  fa = tr_frag(rd_tr); fb = tr_frag(rd_tr + 4096);
  int ub = 0;
  float ev[4];
  auto epi_elem = [&](int e, char* dst) {
    const int t = e >> 2, i = e & 3;
    ev[i] = geglu(pg[t][i], pu[t][i]);
    if (i == 3) *(u32x2*)(dst + t * 18 * 1024) = u32x2{pack2(ev[0], ev[1]), pack2(ev[2], ev[3])};
  };
  auto pass1 = [&](int pass, auto first, auto last) {
    f32x4 ag[2], au[2];
    ag[0] = ag[1] = au[0] = au[1] = f32x4{0, 0, 0, 0};
    char* dst = act_wr + (pass - 1) * 2048;
#pragma unroll
    for (int i = 0; i < 9; ++i) {
      const int u = ub + 2 * i;
      if (!(decltype(first)::value && i == 0)) { WAITV(3); LBAR(); }
      SB();
      const char* p1 = rd_tr + (((u + 1) & 7) << 13);
      const char* p2 = rd_tr + (((u + 2) & 7) << 13);
      MF(ag[0] = mfma16(fa, af[0][2 * i], ag[0]));
      SB();
      const s16x8 ga = tr_frag(p1), gb = tr_frag(p1 + 4096);
      SB();
      MF(ag[1] = mfma16(fa, af[1][2 * i], ag[1]));
      MF(au[0] = mfma16(fb, af[0][2 * i], au[0])); MF(au[1] = mfma16(fb, af[1][2 * i], au[1]));
      SB();
      fa = tr_frag(p2); fb = tr_frag(p2 + 4096);
      SB();
      if (i < 6) { ld1(pass, 2 * i + 6, u + 6); ld1(pass, 2 * i + 7, u + 7); }
      else if (!decltype(last)::value) { ld1(pass + 1, 2 * i - 12, u + 6); ld1(pass + 1, 2 * i - 11, u + 7); }
      else { ld2(0, 2 * i - 12, u + 6); ld2(0, 2 * i - 11, u + 7); }
      SB();

      if (!decltype(first)::value && i < 8) epi_elem(i, dst);
      MF(ag[0] = mfma16(ga, af[0][2 * i + 1], ag[0])); MF(ag[1] = mfma16(ga, af[1][2 * i + 1], ag[1]));
      MF(au[0] = mfma16(gb, af[0][2 * i + 1], au[0])); MF(au[1] = mfma16(gb, af[1][2 * i + 1], au[1]));
      SB();
    }
    pg[0] = ag[0]; pg[1] = ag[1]; pu[0] = au[0]; pu[1] = au[1];
    ub += 18;
  };
  using T_ = std::integral_constant<bool, true>; using F_ = std::integral_constant<bool, false>;
  pass1(0, T_{}, F_{});
  for (int pass = 1; pass < 8; ++pass) pass1(pass, F_{}, F_{});
  pass1(8, F_{}, T_{});
  {
    char* dst = act_wr + 8 * 2048;
#pragma unroll
    for (int e = 0; e < 8; ++e) epi_elem(e, dst);
  }
  WAITL0(); BAR();
  STAMP(1, 2);
#pragma unroll
  for (int t = 0; t < 2; ++t) {
#pragma unroll
    for (int s = 0; s < 18; ++s) af[t][s] = *(const s16x8*)(smem + L_ACT + (((2 * rh + t) * 18 + s) << 10) + lane * 16);
#pragma unroll
    for (int s = 0; s < 5; ++s) af[t][18 + s] = *(const s16x8*)(smem + L_H + (((2 * rh + t) * 5 + s) << 10) + lane * 16);
  }
  char* part = ws + WS_PART + ((size_t)((j * 32 + r) * 9) * 8 + w) * 1024 + lane * 16;
  auto rd2 = [&](int s, int u, s16x8& a, s16x8& b) {
    if (s < 9) { const char* p = rd_tr + ((u & 7) << 13); a = tr_frag(p); b = tr_frag(p + 4096); }
    else { const char* p = rd_lin + ((u & 7) << 13); a = *(const s16x8*)p; if (s < 11) b = *(const s16x8*)(p + 4096); }
  };
  auto pass2 = [&](int pass, auto last) {
    f32x4 y[2];
    y[0] = y[1] = f32x4{0, 0, 0, 0};
#pragma unroll
    for (int i = 0; i < 6; ++i) {
      const int u = ub + 2 * i;
      if (decltype(last)::value && i >= 3) { WAITV(0); }
      else { WAITV(2); }
      LBAR();
      SB();
      s16x8 ga, gb;
      MF(y[0] = mfma16(fa, af[0][4 * i], y[0]));
      SB();
      rd2(2 * i + 1, u + 1, ga, gb);
      SB();
      MF(y[1] = mfma16(fa, af[1][4 * i], y[1]));
      MF(y[0] = mfma16(fb, af[0][4 * i + 1], y[0])); MF(y[1] = mfma16(fb, af[1][4 * i + 1], y[1]));
      SB();
      if (!(decltype(last)::value && i == 5)) rd2((2 * i + 2) % 12, u + 2, fa, fb);
      SB();
      if (i < 3) { ld2(pass, 2 * i + 6, u + 6); ld2(pass, 2 * i + 7, u + 7); }
      else if (!decltype(last)::value) { ld2(pass + 1, 2 * i - 6, u + 6); ld2(pass + 1, 2 * i - 5, u + 7); }
      SB();
      MF(y[0] = mfma16(ga, af[0][4 * i + 2], y[0])); MF(y[1] = mfma16(ga, af[1][4 * i + 2], y[1]));
      if (i < 5) { MF(y[0] = mfma16(gb, af[0][4 * i + 3], y[0])); MF(y[1] = mfma16(gb, af[1][4 * i + 3], y[1])); }
      SB();
    }
    *(u32x4*)(part + pass * 8192) = u32x4{pack2(y[0][0], y[0][1]), pack2(y[0][2], y[0][3]), pack2(y[1][0], y[1][1]), pack2(y[1][2], y[1][3])};
    ub += 12;
  };
  for (int pass = 0; pass < 8; ++pass) pass2(pass, F_{});
  pass2(8, T_{});
  STAMP(1, 3);
}

__global__ __launch_bounds__(256) void k2_reduce(const u16* __restrict__ x, const u16* __restrict__ dsk, char* __restrict__ ws, float* __restrict__ out) {
  STAMP(2, 0);
  const int t = blockIdx.x * 256 + threadIdx.x;
  const int lane = t & 63, w = (t >> 6) & 7, rp = t >> 9, pass = rp % 9, r = rp / 9;
  u32x4 v[8];
#pragma unroll
  for (int j = 0; j < 8; ++j) v[j] = *(const u32x4*)(ws + WS_PART + (size_t)j * (32 * 9 * 8 * 1024) + (size_t)t * 16);
  const int d = 64 * pass + 16 * (w & 3) + 4 * (lane >> 4);
  const int m0 = 64 * r + 32 * (w >> 2) + (lane & 15);
  const u32x2 dk = *(const u32x2*)(dsk + d);
  const float dkf[4] = {bf_lo(dk[0]), bf_hi(dk[0]), bf_lo(dk[1]), bf_hi(dk[1])};
#pragma unroll
  for (int h = 0; h < 2; ++h) {
    const int m = m0 + 16 * h;
    const u32x2 xv = *(const u32x2*)(x + (size_t)m * DM + d);
    const f32x2 st = *(const f32x2*)(ws + WS_STAT + (size_t)m * 8);
    const float xf[4] = {bf_lo(xv[0]), bf_hi(xv[0]), bf_lo(xv[1]), bf_hi(xv[1])};
    f32x4 o;
#pragma unroll
    for (int e = 0; e < 4; ++e) {
      float s = 0.f;
#pragma unroll
      for (int j = 0; j < 8; ++j) { const u32 q = v[j][2 * h + (e >> 1)]; s += (e & 1) ? bf_hi(q) : bf_lo(q); }
      const float xn = rbf(rbf(xf[e] - st[0]) * st[1]);
      o[e] = xf[e] + rbf(xn * dkf[e]) + s;
    }
    *(f32x4*)(out + (size_t)m * DM + d) = o;
  }
  STAMP(2, 1);
}

extern "C" void kernel_launch(void* const* d_in, const int* in_sizes, int n_in, void* d_out, int out_size, void* d_ws, size_t ws_size,
                              hipStream_t stream) {
  const u16* x = (const u16*)d_in[0];
  char* ws = (char*)d_ws;
  k0_ssm_prep<<<K0_BU_WGS + K0_CP_WGS, 256, 0, stream>>>(x, (const u16*)d_in[1], (const u16*)d_in[2], (const u16*)d_in[3], (const u16*)d_in[4],
                                                         (const u16*)d_in[5], (const u16*)d_in[6], (const u16*)d_in[7], ws);
  k1_main<<<256, 512, 0, stream>>>(x, (const u16*)d_in[10], (const u16*)d_in[11], (const u16*)d_in[12], ws);
  k2_reduce<<<576, 256, 0, stream>>>(x, (const u16*)d_in[8], ws, (float*)d_out);
}
```

```cpp
#include <hip/hip_runtime.h>
#include <hip/hip_bf16.h>
#include <stdint.h>
#include <type_traits>

typedef unsigned short u16;
typedef unsigned int u32;
typedef unsigned long long u64;
typedef __attribute__((ext_vector_type(8))) __bf16 bf16x8;
typedef __attribute__((ext_vector_type(2))) __bf16 bf16x2;
typedef __attribute__((ext_vector_type(4))) short s16x4;
typedef __attribute__((ext_vector_type(8))) short s16x8;
typedef __attribute__((ext_vector_type(4))) float f32x4;
typedef __attribute__((ext_vector_type(2))) float f32x2;
typedef __attribute__((ext_vector_type(4))) u32 u32x4;
typedef __attribute__((ext_vector_type(2))) u32 u32x2;
#define LDSP __attribute__((address_space(3)))
#define DI __device__ __forceinline__
#if defined(__HIP_DEVICE_COMPILE__)
#pragma clang attribute push(__attribute__((target("no-packed-fp32-ops"))), apply_to = function)
#endif

constexpr int DM = 576, FF = 4608;
constexpr int ROWB = DM * 2;
constexpr int WROW = FF * 2;
constexpr size_t WS_EPOCH = 0;
constexpr size_t WS_SX = 4096;
constexpr size_t WS_STAT = WS_SX + (size_t)16 * 576 * 16;
constexpr size_t WS_PART = 262144;
constexpr size_t PART_S = (size_t)16 * 9 * 8 * 2 * 1024;
constexpr size_t WS_STAMP = WS_PART + 16 * PART_S;

#define PFMODE 0

DI float u2f(u32 v) { return __builtin_bit_cast(float, v); }
DI u32 f2u(float v) { return __builtin_bit_cast(u32, v); }
DI float bf_lo(u32 v) { return u2f(v << 16); }
DI float bf_hi(u32 v) { return u2f(v & 0xffff0000u); }
DI float bf2f(u16 v) { return u2f((u32)v << 16); }
DI u32 pack2(float a, float b) { bf16x2 r = {(__bf16)a, (__bf16)b}; return __builtin_bit_cast(u32, r); }
DI float rbf(float a) { return (float)(__bf16)a; }
DI float dot2(u32 a, u32 b, float c) { return __builtin_amdgcn_fdot2_f32_bf16(__builtin_bit_cast(bf16x2, a), __builtin_bit_cast(bf16x2, b), c, false); }
DI u32 centre(u32 a, float m) { const f32x2 d = f32x2{bf_lo(a), bf_hi(a)} - f32x2{m, m}; return pack2(d[0], d[1]); }
DI f32x4 mfma16(s16x8 a, s16x8 b, f32x4 c) {
  f32x4 r = __builtin_amdgcn_mfma_f32_16x16x32_bf16(__builtin_bit_cast(bf16x8, a), __builtin_bit_cast(bf16x8, b), c, 0, 0, 0);
  asm volatile("" : "+v"(r));
  return r;
}
template <int C> DI float dpp(float v) { return u2f(__builtin_amdgcn_update_dpp(0, f2u(v), C, 0xf, 0xf, true)); }
DI float sum8(float s) { s += dpp<0xB1>(s); s += dpp<0x4E>(s); s += dpp<0x141>(s); return s; }

struct cf { float r, i; };
DI cf cmul(cf a, cf b) { return {a.r * b.r - a.i * b.i, a.r * b.i + a.i * b.r}; }
DI cf cfma(cf a, cf b, cf c) { return {fmaf(a.r, b.r, fmaf(-a.i, b.i, c.r)), fmaf(a.r, b.i, fmaf(a.i, b.r, c.i))}; }
DI cf cpow(float zl, float zt, float n) {
  const float e = __builtin_amdgcn_exp2f(n * zl), f = __builtin_amdgcn_fractf(n * zt);
  return {e * __builtin_amdgcn_cosf(f), e * __builtin_amdgcn_sinf(f)};
}

constexpr int L_RING = 0, L_AF2 = 73728, L_SCR = 155648, L_IMG = 16384, L_TOTAL = 163840;
constexpr int L_MISC = L_AF2 + 5 * 8192, L_RS = L_MISC, L_SD = L_MISC + 512, L_ZT = L_MISC + 1024, L_TP = L_MISC + 2048;

template <int OFF> DI void glds16(const char* sbase, u32 voff, u32 lds_base) {
  asm volatile("s_add_u32 m0, %2, %3\n\ts_nop 0\n\tglobal_load_lds_dwordx4 %0, %1" :: "v"(voff), "s"(sbase), "s"(lds_base), "n"(OFF) : "scc");
}
#define PIN(v) asm volatile("" : "+v"(v))
DI u32 touch(const char* a) { return __hip_atomic_load((const __attribute__((address_space(1))) u32*)a, __ATOMIC_RELAXED, __HIP_MEMORY_SCOPE_WAVEFRONT); }
typedef LDSP char* lp;
DI lp pin_lds(const char* p) { u32 a = (u32)(size_t)(lp)p; PIN(a); return (lp)(size_t)a; }
template <class P> DI s16x8 tr_frag(P p) {
  const s16x4 a = __builtin_amdgcn_ds_read_tr16_b64_v4i16((LDSP s16x4*)p);
  const s16x4 b = __builtin_amdgcn_ds_read_tr16_b64_v4i16((LDSP s16x4*)(p + 512));
  return __builtin_shufflevector(a, b, 0, 1, 2, 3, 4, 5, 6, 7);
}
DI s16x8 b64x2_frag(const char* p, int delta) {
  const s16x4 a = *(const s16x4*)p, b = *(const s16x4*)(p + delta);
  return __builtin_shufflevector(a, b, 0, 1, 2, 3, 4, 5, 6, 7);
}
DI int lane_fresh() { int l; asm volatile("v_mbcnt_lo_u32_b32 %0, -1, 0\n\tv_mbcnt_hi_u32_b32 %0, -1, %0" : "=v"(l)); return l; }
#define WAITV(n) asm volatile("s_waitcnt vmcnt(" #n ")" ::: "memory")
#define WAITL0() asm volatile("s_waitcnt lgkmcnt(0)" ::: "memory")
#define BAR() do { __builtin_amdgcn_s_barrier(); asm volatile("" ::: "memory"); } while (0)
#define SB() __builtin_amdgcn_sched_barrier(0)

struct Seg {
  const char *b0, *b4, *b5; u32 st; u32 v0, v4, v5;
};
template <int N> using IC = std::integral_constant<int, N>;
template <int N, class Fn> DI void unroll(Fn&& fn) { if constexpr (N > 0) { unroll<N - 1>(fn); fn(IC<N - 1>{}); } }

__global__ __launch_bounds__(512, 2) void k1_main(const u16* __restrict__ x, const u16* __restrict__ lre, const u16* __restrict__ lim,
                                                  const u16* __restrict__ bre, const u16* __restrict__ bim, const u16* __restrict__ cre,
                                                  const u16* __restrict__ cim, const u16* __restrict__ ldt, const u16* __restrict__ wg,
                                                  const u16* __restrict__ wu, const u16* __restrict__ wd, char* __restrict__ ws) {
  __shared__ __attribute__((aligned(16))) char smem[L_TOTAL];
  const int tid = threadIdx.x, lane = tid & 63, w = __builtin_amdgcn_readfirstlane(tid >> 6);
  const int xc = blockIdx.x & 7, q = blockIdx.x >> 3, ty = q & 1, R = q >> 1;
  const int g = lane >> 4, mm = lane & 15, rg = w & 3, cg = w >> 2;
  const int lr = lane >> 3, lc = lane & 7;
  const int f0 = 576 * xc + (ty ? 320 : 0), npass = ty ? 4 : 5;
  const int P0 = 9 * (xc >> 1) + (ty ? 4 * (xc & 1) : 8), np = ty ? 4 : ((xc & 1) ^ 1), nb = ty ? 2 : np;
  const int hs = 2 * (npass - 1);
  auto swz_ld = [&](int l) { return (((l >> 4) & 1) | ((w & 1) << 1)) << 1; };
  const u32 voff1 = (u32)(lr * WROW + ((lc ^ swz_ld(lane)) << 4));
  auto voff2 = [&](int l) { return (u32)((((l >> 3) & 3) + 16 * (l >> 5)) * ROWB + (((l & 7) ^ swz_ld(l)) << 4)); };
  auto voffb = [&](int l) { return (u32)((l >> 3) * ROWB + (((l & 7) ^ (4 * (w & 1) + (l >> 4))) << 4)); };
  auto voffx = [&](int l) { return (u32)((l >> 1) * ROWB + (((l & 1) ^ ((l >> 4) & 1)) << 4)); };
  const u32 ring_w = (u32)(size_t)(LDSP char*)(smem + L_RING) + w * 1024;
  auto seg_lin = [&](const char* b, u32 st, u32 v) { return Seg{b, b + 4 * (size_t)st, b + 5 * (size_t)st, st, v, v, v}; };
  auto seg_bu = [&](int b) {
    return seg_lin((const char*)((w & 2) ? bim : bre) + (size_t)(16 * (P0 + 2 * b + (ty ? cg : 0)) + 8 * (w & 1)) * ROWB, 128u, (ty || !cg) ? voffb(lane) : 0u);
  };
  auto seg_w1 = [&](const u16* m, int pass) { return seg_lin((const char*)m + (size_t)(8 * w) * WROW + (f0 + 64 * pass) * 2, 64u * WROW, voff1); };
  auto seg_p2 = [&](int pass, int l) {
    Seg s = seg_lin((const char*)wd + (size_t)(f0 + 32 * cg + 4 * rg) * ROWB + pass * 128, 64u * ROWB, voff2(l));
    if (ty) {
      const size_t o = (size_t)(64 * pass + 8 * w) * ROWB + 32 * P0;
      s.b4 = (const char*)cre + o; s.b5 = (const char*)cim + o; s.v4 = s.v5 = voffb(l);
    } else {
      s.b5 = (const char*)((w & 2) ? cim : cre) + (size_t)(64 * pass + 32 * (w & 1)) * ROWB + 32 * P0;
      s.v5 = cg ? 0u : voffx(l);
    }
    return s;
  };
  auto ld_seg = [&](Seg& s, auto ic, u32 lds, auto oc) {
    constexpr int i = decltype(ic)::value, O = decltype(oc)::value;
    if constexpr (i < 4) { glds16<O>(s.b0, s.v0, lds); s.b0 += s.st; }
    else if constexpr (i == 4) glds16<O>(s.b4, s.v4, lds);
    else if constexpr (i == 5) glds16<O>(s.b5, s.v5, lds);
    else { s.b5 += s.st; glds16<O>(s.b5, s.v0, lds); }
  };
  Seg cur = nb ? seg_bu(0) : seg_w1(wg, 0);
  ld_seg(cur, IC<0>{}, ring_w, IC<0>{}); ld_seg(cur, IC<1>{}, ring_w, IC<8192>{});
  u32x4 xv[2][9];
#pragma unroll
  for (int h = 0; h < 2; ++h)
#pragma unroll
    for (int i = 0; i < 9; ++i) xv[h][i] = *(const u32x4*)((const char*)x + (size_t)(128 * R + 16 * w + 8 * h + lr) * ROWB + (lc + 8 * i) * 16);
  const int pl_g = min(16 * P0 + lane, 575);
  const u16 pa = lre[pl_g], pb = lim[pl_g], pc = ldt[pl_g];
  const u32 epoch = __hip_atomic_load((const u32*)(ws + WS_EPOCH), __ATOMIC_RELAXED, __HIP_MEMORY_SCOPE_AGENT);
  u32 pf = 0, pf2 = 0; const char *pfa, *pfa2;
  {
    const int nl = 2 * npass * 36;
    const char* a;
    if (tid < nl) { const int c = tid / 36, rr = tid % 36; a = (const char*)((c & 1) ? wu : wg) + (size_t)(36 * R + rr) * WROW + f0 * 2 + (c >> 1) * 128; }
    else { const int t2 = min(tid - nl, 71), rr = t2 / 9, c = t2 % 9, row = min(16 * P0 + 4 * R + (rr & 3), 575); a = (const char*)((rr & 4) ? bim : bre) + (size_t)row * ROWB + c * 128; }
    pf = touch(a);
    const int n2 = 4 * npass * 9;
    if (tid < n2) a = (const char*)wd + (size_t)(f0 + 4 * npass * R) * ROWB + tid * 128;
    else { const int t2 = min(tid - n2, 143), rr = t2 >> 2; a = (const char*)((t2 & 1) ? cim : cre) + (size_t)(36 * R + rr) * ROWB + 32 * P0 + ((t2 & 2) ? (ty ? 124 : 28) : 0); }
    pf2 = touch(a);
  }
  f32x4 ztab;
  {
    const float dt = rbf(__builtin_amdgcn_exp2f(bf2f(pc) * 1.44269504f)), a = bf2f(pa), b = bf2f(pb);
    const float zl = dt * a * 1.44269504f, zt = dt * b * 0.15915494f;
    const cf l1 = cpow(zl, zt, 1.f);
    const float nr = l1.r - 1.f, ni = l1.i, den = 2.f / (a * a + b * b);
    ztab = f32x4{zl, zt, (nr * a + ni * b) * den, (ni * a - nr * b) * den};
  }
  const u32 tag = __builtin_amdgcn_readfirstlane(epoch) * 0x9E3779B1u + 0x7F4A7C15u;
  const u32 c_one = 0x3f803f80u;
  float mean_[2], mb_[2];
#pragma unroll
  for (int h = 0; h < 2; ++h) {
    float s = 0.f;
#pragma unroll
    for (int i = 0; i < 9; ++i)
#pragma unroll
      for (int e = 0; e < 4; ++e) s = dot2(xv[h][i][e], c_one, s);
    const float mean = sum8(s) * (1.f / DM), mb = rbf(mean);
    mean_[h] = mean; mb_[h] = mb;
    if (h == 0) { SB(); SB(); }
    const int r = 16 * w + 8 * h + lr;
    const int mt = 2 * (r >> 5) + ((r >> 2) & 1), rho = 4 * ((r >> 3) & 3) + (r & 3);
    const int cs = (lc & 3) | ((lc & 4) ? 12 : 0);
    char* dst = smem + L_IMG + ((mt * 18 + (lc >> 2)) << 10) + 256 * (lc & 3) + 16 * (rho ^ cs);
#pragma unroll
    for (int i = 0; i < 9; ++i) {
#pragma unroll
      for (int e = 0; e < 4; ++e) xv[h][i][e] = centre(xv[h][i][e], mb);
      *(u32x4*)(dst + i * 2048) = xv[h][i];
    }
  }
  WAITL0(); BAR();
  s16x8 af[2][18];
  {
    const int o0 = 256 * g + 16 * (mm ^ g), o1 = 256 * g + 16 * (mm ^ (g | 12));
#pragma unroll
    for (int s = 0; s < 18; ++s)
#pragma unroll
      for (int t = 0; t < 2; ++t) af[t][s] = *(const s16x8*)(smem + L_IMG + (((2 * rg + t) * 18 + s) << 10) + ((s & 1) ? o1 : o0));
  }
  float rs_[2], sd_[2];
#pragma unroll
  for (int h = 0; h < 2; ++h) {
    float v = 0.f;
#pragma unroll
    for (int i = 0; i < 9; ++i)
#pragma unroll
      for (int e = 0; e < 4; ++e) v = dot2(xv[h][i][e], xv[h][i][e], v);
    const float dm = mean_[h] - mb_[h];
    const float var = fmaxf(fmaf(-dm, dm, sum8(v) * (1.f / DM)), 0.f);
    sd_[h] = rbf(__builtin_sqrtf(rbf(rbf(var) + 1e-5f)));
    rs_[h] = 1.f / sd_[h];
  }
  WAITL0(); BAR();
  asm volatile("" :: "v"(pf), "v"(pf2));
  unroll<4>([&](auto u) { ld_seg(cur, IC<u + 2>{}, ring_w, IC<(u + 2) * 8192>{}); });
  if (lc == 0) {
#pragma unroll
    for (int h = 0; h < 2; ++h) {
      const int r = 16 * w + 8 * h + lr;
      *(float*)(smem + L_RS + r * 4) = rs_[h]; *(float*)(smem + L_SD + r * 4) = sd_[h];
      if (xc == 0 && ty == 0) *(f32x2*)(ws + WS_STAT + (size_t)(128 * R + r) * 8) = f32x2{mb_[h], rs_[h]};
    }
  }
  if (w == 0) *(f32x4*)(smem + L_ZT + lane * 16) = ztab;
  WAITV(2); WAITL0(); BAR();
  s16x8 F[4];
  f32x4 accA[2][2], accB[2][2];
  auto body9 = [&](auto rd, auto mm_, auto gap, Seg& own, Seg& nxt, bool active, auto hook) {
    unroll<3>([&](auto ivc) {
      constexpr int iv = ivc;
      auto ld = [&](auto uc) {
        constexpr int u = uc;
        if constexpr (u < 3) ld_seg(own, IC<u + 6>{}, ring_w, IC<(u + 6) * 8192>{});
        else ld_seg(nxt, IC<u - 3>{}, ring_w, IC<(u - 3) * 8192>{});
      };
      if (active) {
        unroll<3>([&](auto uc) {
          constexpr int u = 3 * iv + uc;
#pragma unroll
          for (int f = 0; f < 4; ++f) {
            mm_(u, f, 0); SB();
            if (f == 0) ld(IC<u>{});
            if (u == 0 && f == 1) hook(0);
            gap(8 * u + 2 * f); SB();
            mm_(u, f, 1); SB();
            F[f] = rd((u + 1) % 9, f);
            gap(8 * u + 2 * f + 1); SB();
          }
        });
      } else {
        unroll<3>([&](auto uc) { ld(IC<3 * iv + uc>{}); });
      }
      if (iv == 0) hook(1); else WAITV(2);
      BAR(); SB();
    });
  };
  auto nohook = [&](int k) { if (k) WAITV(2); };
  const f32x4 Z4 = {0.f, 0.f, 0.f, 0.f};
  if (nb) {
    const char* rb0 = smem + L_RING + 4096 * cg + (mm >> 3) * 1024 + (mm & 7) * 128;
    const char* rbe[2] = {rb0 + ((g ^ (mm >> 1)) << 4), rb0 + (((4 + g) ^ (mm >> 1)) << 4)};
    const lp rbh[2] = {pin_lds(rbe[0] + 65536), pin_lds(rbe[1] + 65536)};
    auto rd = [&](int u, int f) { return *(LDSP const s16x8*)((u < 8 ? (lp)rbe[f >> 1] + u * 8192 : rbh[f >> 1]) + (f & 1) * 2048); };
    auto mmf = [&](int u, int f, int t) { accA[f & 1][t] = mfma16(af[t][2 * u + (f >> 1)], F[f], (u == 0 && f < 2) ? Z4 : accA[f & 1][t]); };
    auto nogap = [&](int) {};
    const bool active = ty || !cg;
#pragma unroll
    for (int f = 0; f < 4; ++f) F[f] = rd(0, f);
    for (int b = 0; b < nb; ++b) {
      Seg nxt = (b + 1 < nb) ? seg_bu(b + 1) : seg_w1(wg, 0);
      body9(rd, mmf, nogap, cur, nxt, active, nohook);
      cur = nxt;
      if (active) {
        const int pt = 2 * b + cg;
        const f32x4 z = *(const f32x4*)(smem + L_ZT + (16 * pt + mm) * 16);
        const cf l1 = cpow(z[0], z[1], 1.f);
        cf e = {0.f, 0.f};
#pragma unroll
        for (int t = 0; t < 2; ++t) {
          const f32x4 rs = *(const f32x4*)(smem + L_RS + (32 * rg + 8 * g + 4 * t) * 4);
          u32x4 o;
#pragma unroll
          for (int i = 0; i < 4; ++i) {
            const cf v = {accA[0][t][i] * rs[i], accA[1][t][i] * rs[i]};
            e = cfma(l1, e, v);
            o[i] = pack2(v.r, v.i);
          }
          *(u32x4*)(smem + L_AF2 + (hs + pt) * 8192 + (2 * rg + t) * 1024 + lane * 16) = o;

        }
        *(f32x2*)(smem + L_SCR + ((4 * rg + g) * 64 + 16 * pt + mm) * 8) = f32x2{e.r, e.i};
      }
    }
    WAITL0(); BAR();
    if (w == 0 && lane < 16 * np) {
      const cf l8 = cpow(ztab[0], ztab[1], 8.f);
      cf P = {0.f, 0.f};
      f32x2 ev[16];
#pragma unroll
      for (int n = 0; n < 16; ++n) ev[n] = *(const f32x2*)(smem + L_SCR + (n * 64 + lane) * 8);
#pragma unroll
      for (int n = 0; n < 16; ++n) {
        *(f32x2*)(smem + L_SCR + (n * 64 + lane) * 8) = f32x2{P.r, P.i};
        P = cfma(l8, P, cf{ev[n][0], ev[n][1]});
      }
      u64* dst = (u64*)(ws + WS_SX + (size_t)(R * 576 + 16 * P0 + lane) * 16);
      __hip_atomic_store(dst, ((u64)tag << 32) | f2u(P.r), __ATOMIC_RELAXED, __HIP_MEMORY_SCOPE_AGENT);
      __hip_atomic_store(dst + 1, ((u64)tag << 32) | f2u(P.i), __ATOMIC_RELAXED, __HIP_MEMORY_SCOPE_AGENT);
    }
  }
  const int rq = (lane & 15) >> 2, rp = lane & 3;
  const int swz_rd = (((rq >> 1) & 1) | ((g & 1) << 1)) << 1;
  const char* rt0 = smem + L_RING + 1024 * g + 128 * rq + 8 * (rp & 1);
  const char* rtj[2] = {rt0 + (((4 * cg + (rp >> 1)) ^ swz_rd) << 4), rt0 + (((4 * cg + 2 + (rp >> 1)) ^ swz_rd) << 4)};
  float K0[2], K1[2], S2[2];
#pragma unroll
  for (int t = 0; t < 2; ++t) {
    const int r = 32 * rg + 8 * (mm >> 2) + 4 * t + (mm & 3);
    const float rs = *(const float*)(smem + L_RS + r * 4), sd = *(const float*)(smem + L_SD + r * 4);
    K0[t] = -2.3022082f * rs; K1[t] = -0.10294324f * rs * rs * rs; S2[t] = sd * sd;
  }
  float S[16], tmp = 0.f;
  u32x4 opk;
#pragma unroll
  for (int e = 0; e < 16; ++e) S[e] = 0.f;
  accB[0][0] = accB[0][1] = accB[1][0] = accB[1][1] = Z4;
  auto chain = [&](int e, int k, float gv) {
    const int t = e >> 3;
    if (k == 0) tmp = gv * gv;
    else if (k == 1) tmp = fmaf(tmp, K1[t], K0[t]);
    else if (k == 2) tmp = tmp * gv;
    else if (k == 3) tmp = __builtin_amdgcn_exp2f(tmp);
    else if (k == 4) tmp = fmaf(tmp, S2[t], S2[t]);
    else if (k == 5) tmp = __builtin_amdgcn_rcpf(tmp);
    else S[e] = gv * tmp;
    if (k < 6) PIN(tmp); else PIN(S[e]);
  };
  char* act_dst = smem + L_AF2 + cg * 8192 + 2 * rg * 1024 + lane * 16;
  auto fin = [&](int k) {
    const int t = k / 12, c = (k % 12) / 3, s = k % 3, e = 8 * t + 2 * c;
    if (s == 0) { S[e] *= accB[c >> 1][t][2 * (c & 1)]; PIN(S[e]); }
    else if (s == 1) { S[e + 1] *= accB[c >> 1][t][2 * (c & 1) + 1]; PIN(S[e + 1]); }
    else { u32 o = pack2(S[e], S[e + 1]); PIN(o); opk[c] = o; if (c == 3) *(u32x4*)(act_dst + t * 1024) = opk; }
  };
  auto gapA = [&](int n) { if (n < 42) chain(10 + n / 7, n % 7, S[10 + n / 7]); else if (n < 66) fin(n - 42); };
  auto gapB = [&](int n) {
    if (n < 70) { const int e = n / 7; chain(e, n % 7, accA[(e >> 2) & 1][e >> 3][e & 3]); }
    else if (n == 71) {
#pragma unroll
      for (int e = 10; e < 16; ++e) { S[e] = accA[(e >> 2) & 1][e >> 3][e & 3]; PIN(S[e]); }
    }
  };
  const lp rth[2] = {pin_lds(rtj[0] + 65536), pin_lds(rtj[1] + 65536)};
  auto rdt = [&](int u, int f) { return tr_frag((u < 8 ? (lp)rtj[f & 1] + u * 8192 : rth[f & 1]) + (f >> 1) * 4096); };
  auto mmA = [&](int u, int f, int t) { accA[f & 1][t] = mfma16(F[f], af[t][2 * u + (f >> 1)], (u == 0 && f < 2) ? Z4 : accA[f & 1][t]); };
  auto mmB = [&](int u, int f, int t) { accB[f & 1][t] = mfma16(F[f], af[t][2 * u + (f >> 1)], (u == 0 && f < 2) ? Z4 : accB[f & 1][t]); };
#pragma unroll
  for (int f = 0; f < 4; ++f) F[f] = rdt(0, f);
  const char* sxb = ws + WS_SX;
#pragma nounroll
  for (int pass = 0; pass < npass; ++pass) {
    Seg sb = seg_w1(wu, pass);
    body9(rdt, mmA, gapA, cur, sb, true, nohook);
    u32x4 gx[2];
    u32 go[2];
    {
      const int ln = lane_fresh();
      cur = (pass + 1 < npass) ? seg_w1(wg, pass + 1) : seg_p2(0, ln);
      const u32 plo = min(16 * P0 + ln, 575) * 16;
#pragma unroll
      for (int k = 0; k < 2; ++k) go[k] = (pass == 1 && nb && 2 * w + k < R) ? plo + (2 * w + k) * 9216 : 0u;
    }
    auto hook = [&](int k) {
      if (k) { WAITV(4); return; }
      asm volatile("global_load_dwordx4 %0, %1, %2 sc1" : "=v"(gx[0]) : "v"(go[0]), "s"(sxb));
      asm volatile("global_load_dwordx4 %0, %1, %2 sc1" : "=v"(gx[1]) : "v"(go[1]), "s"(sxb));
    };
    body9(rdt, mmB, gapB, sb, cur, true, hook);
    asm volatile("" : "+v"(gx[0]), "+v"(gx[1]));
    const int ln = lane_fresh(), lg = ln >> 4, lm = ln & 15;
    act_dst = smem + L_AF2 + (2 * pass + cg) * 8192 + 2 * rg * 1024 + ln * 16;
    if (pass == 1) {
      if (nb) {
        const f32x4 z = *(const f32x4*)(smem + L_ZT + ln * 16);
        const __amdgpu_buffer_rsrc_t sx_rsrc = __builtin_amdgcn_make_buffer_rsrc(ws + WS_SX, 0, 16 * 576 * 16, 0x00020000);
        const int plo = min(16 * P0 + ln, 575) * 16;
        cf T = {0.f, 0.f};
#pragma unroll
        for (int k = 0; k < 2; ++k)
          if (2 * w + k < R) {
#pragma nounroll
            for (int spin = 0; spin < (1 << 22); ++spin) {
              const bool ok = (gx[k][1] == tag && gx[k][3] == tag) || ln >= 16 * np;
              if (__builtin_amdgcn_ballot_w64(!ok) == 0) break;
              __builtin_amdgcn_s_sleep(4);
              gx[k] = __builtin_amdgcn_raw_buffer_load_b128(sx_rsrc, (2 * w + k) * 9216 + plo, 0, 16);
            }
            T = cfma(cpow(z[0], z[1], (float)(128 * (R - 1 - 2 * w - k))), cf{u2f(gx[k][0]), u2f(gx[k][2])}, T);
          }
        *(f32x2*)(smem + L_TP + (w * 64 + ln) * 8) = f32x2{T.r, T.i};
      }
    } else if (pass == 2) {
      if (nb && (ty || !cg)) {
#pragma nounroll
        for (int b = 0; b < nb; ++b) {
          const int pt = 2 * b + cg, pl = 16 * pt + lm;
          const f32x4 z = *(const f32x4*)(smem + L_ZT + pl * 16);
          cf T0 = {0.f, 0.f};
#pragma unroll
          for (int k = 0; k < 8; ++k) { const f32x2 a = *(const f32x2*)(smem + L_TP + (k * 64 + pl) * 8); T0.r += a[0]; T0.i += a[1]; }
          const f32x2 pp = *(const f32x2*)(smem + L_SCR + ((4 * rg + lg) * 64 + pl) * 8);
          char* hb = smem + L_AF2 + (hs + pt) * 8192 + 2 * rg * 1024;
          const u32x4 v0 = *(const u32x4*)(hb + ln * 16), v1 = *(const u32x4*)(hb + 1024 + ln * 16);
          cf h = cfma(cpow(z[0], z[1], (float)(8 * (4 * rg + lg))), T0, cf{pp[0], pp[1]});
          const cf l1 = cpow(z[0], z[1], 1.f), co = {z[2], z[3]};
          WAITL0();
#pragma unroll
          for (int t = 0; t < 2; ++t) {
            cf o[4];
#pragma unroll
            for (int i = 0; i < 4; ++i) { const u32 v = t ? v1[i] : v0[i]; h = cfma(l1, h, cf{bf_lo(v), bf_hi(v)}); o[i] = cmul(co, h); }
            char* d = hb + t * 1024 + (4 * lm + lg) * 8;
            *(u32x2*)d = u32x2{pack2(o[0].r, o[1].r), pack2(o[2].r, o[3].r)};
            *(u32x2*)(d + 512) = u32x2{pack2(-o[0].i, -o[1].i), pack2(-o[2].i, -o[3].i)};
          }
        }
      }
    }
  }
  const int ln = lane_fresh(), lg = ln >> 4, lm = ln & 15;
  act_dst = smem + L_RING + (6 + cg) * 8192 + 2 * rg * 1024 + ln * 16;
#pragma unroll
  for (int n = 0; n < 66; ++n) gapA(n);
  WAITL0(); BAR();
  char* part = ws + WS_PART + (size_t)(2 * xc + ty) * PART_S + ((size_t)(R * 9) * 8 + w) * 2048;
  const u32 pvo = ln * 16;
  auto phase2 = [&](auto tyc) {
    constexpr int TY = decltype(tyc)::value;
    constexpr int NA = TY ? 8 : 10, NK = TY ? 12 : 11;
#pragma unroll
    for (int s = 0; s < NK; ++s)
#pragma unroll
      for (int t = 0; t < 2; ++t) {
        if (s < NA - 2) af[t][s] = *(const s16x8*)(smem + L_AF2 + s * 8192 + (2 * rg + t) * 1024 + ln * 16);
        else if (s < NA) af[t][s] = *(const s16x8*)(smem + L_RING + (6 + s - (NA - 2)) * 8192 + (2 * rg + t) * 1024 + ln * 16);
        else af[t][s] = tr_frag(smem + L_AF2 + (s - 2) * 8192 + (2 * rg + t) * 1024 + ln * 8);
      }
    if (!TY && !np) af[0][10] = af[1][10] = s16x8{0, 0, 0, 0, 0, 0, 0, 0};
#pragma unroll
    for (int f = 0; f < 4; ++f) F[f] = rdt(0, f);
    WAITL0(); BAR();
    const u32 cy = (u32)((32 * cg + lm) * 128 + 8 * (lg & 1));
    const u32 cyx = (u32)(((lg >> 1) ^ (lm >> 1)) << 4);
    const u32 cx = (u32)(cg * 1024 + lm * 32 + (((lg >> 1) ^ (lm >> 3)) << 4) + 8 * (lg & 1));
    int grp = 0;
    u32x4 o0 = {0, 0, 0, 0}, o1 = {0, 0, 0, 0};
    int sp = 8;
#pragma nounroll
    for (int pass = 0; pass < 9; ++pass) {
      Seg nxt = seg_p2(min(pass + 1, 8), ln);
      if (pass == 8) nxt.v0 = nxt.v4 = nxt.v5 = 0u;
      asm volatile("" : "+s"(sp));
      unroll<2>([&](auto ivc) {
        constexpr int iv = ivc;
        const int g1 = (grp == 2) ? 0 : grp + 1, g2 = (grp == 0) ? 2 : grp - 1;
        const int oc = grp * 24576, on = g1 * 24576;
        const u32 ldb = ring_w + g2 * 24576;
        auto rd = [&](int u, int f) {
          const int off = ((u % 3 == 0) ? on : oc) + (u % 3) * 8192;
          if (u == 6 || u < (TY ? 4 : 5)) return tr_frag(rtj[f & 1] + off + (f >> 1) * 4096);
          if (TY) { const int ks = 2 * (u - 4) + (f >> 1); return b64x2_frag(smem + L_RING + oc + 8192 + cy + (cyx ^ (ks << 5)) + (f & 1) * 2048, 8192); }
          return b64x2_frag(smem + L_RING + off + cx + (f & 1) * 512, 2048);
        };
        unroll<3>([&](auto uc) {
          constexpr int u = 3 * iv + uc;
#pragma unroll
          for (int f = 0; f < 4; ++f) {
            const int ks = 2 * u + (f >> 1);
            if (ks < NK) accA[f & 1][0] = mfma16(F[f], af[0][ks < NK ? ks : 0], (u == 0 && f < 2) ? Z4 : accA[f & 1][0]);
            SB();
            if (f == 0) ld_seg(nxt, IC<u>{}, ldb, IC<uc * 8192>{});
            if (u == 2 && f == 1) *(u32x4*)(part + sp * 16384 + pvo) = o0;
            if (u == 2 && f == 2) *(u32x4*)(part + sp * 16384 + 1024 + pvo) = o1;
            SB();
            if (ks < NK) accA[f & 1][1] = mfma16(F[f], af[1][ks < NK ? ks : 0], (u == 0 && f < 2) ? Z4 : accA[f & 1][1]);
            SB();
            if (!(TY == 0 && u == 4 && f >= 2)) F[f] = rd(u + 1, f);
            SB();
          }
        });
        if (iv == 0) { WAITV(4); } else { WAITV(2); }
        BAR(); SB();
        grp = g1;
      });
      o0 = u32x4{pack2(accA[0][0][0], accA[0][0][1]), pack2(accA[0][0][2], accA[0][0][3]), pack2(accA[1][0][0], accA[1][0][1]), pack2(accA[1][0][2], accA[1][0][3])};
      o1 = u32x4{pack2(accA[0][1][0], accA[0][1][1]), pack2(accA[0][1][2], accA[0][1][3]), pack2(accA[1][1][0], accA[1][1][1]), pack2(accA[1][1][2], accA[1][1][3])};
      sp = pass;
    }
    *(u32x4*)(part + 8 * 16384 + pvo) = o0;
    *(u32x4*)(part + 8 * 16384 + 1024 + pvo) = o1;
  };
  if (ty) phase2(std::integral_constant<int, 1>{}); else phase2(std::integral_constant<int, 0>{});
}

#if defined(__HIP_DEVICE_COMPILE__)
#pragma clang attribute pop
#endif
__global__ __launch_bounds__(256) void k2_reduce(const u16* __restrict__ x, const u16* __restrict__ dsk, char* __restrict__ ws, float* __restrict__ out) {
  const int id = blockIdx.x * 256 + threadIdx.x;
  const int lane = id & 63, t = (id >> 6) & 1, w = (id >> 7) & 7, rp = id >> 10, pass = rp % 9, R = rp / 9;
  u32x4 v[16];
#pragma unroll
  for (int s = 0; s < 16; ++s) v[s] = *(const u32x4*)(ws + WS_PART + s * PART_S + (size_t)id * 16);
  const int mm = lane & 15;
  const int m = 128 * R + 32 * (w & 3) + 8 * (mm >> 2) + 4 * t + (mm & 3);
  const f32x2 st = *(const f32x2*)(ws + WS_STAT + (size_t)m * 8);
  if (id == 0) { u32* ep = (u32*)(ws + WS_EPOCH); *ep = *ep + 1u; }
#pragma unroll
  for (int j = 0; j < 2; ++j) {
    const int d = 64 * pass + 32 * (w >> 2) + 16 * j + 4 * (lane >> 4);
    const u32x2 dk = *(const u32x2*)(dsk + d);
    const u32x2 xv = *(const u32x2*)(x + (size_t)m * DM + d);
    const float dkf[4] = {bf_lo(dk[0]), bf_hi(dk[0]), bf_lo(dk[1]), bf_hi(dk[1])};
    const float xf[4] = {bf_lo(xv[0]), bf_hi(xv[0]), bf_lo(xv[1]), bf_hi(xv[1])};
    f32x4 o;
#pragma unroll
    for (int e = 0; e < 4; ++e) {
      float s = 0.f;
#pragma unroll
      for (int k = 0; k < 16; ++k) { const u32 qv = v[k][2 * j + (e >> 1)]; s += (e & 1) ? bf_hi(qv) : bf_lo(qv); }
      const float xn = rbf(rbf(xf[e] - st[0]) * st[1]);
      o[e] = xf[e] + rbf(xn * dkf[e]) + s;
    }
    *(f32x4*)(out + (size_t)m * DM + d) = o;
  }
}

extern "C" void kernel_launch(void* const* d_in, const int* in_sizes, int n_in, void* d_out, int out_size, void* d_ws, size_t ws_size,
                              hipStream_t stream) {
  const u16* x = (const u16*)d_in[0];
  char* ws = (char*)d_ws;
  k1_main<<<256, 512, 0, stream>>>(x, (const u16*)d_in[1], (const u16*)d_in[2], (const u16*)d_in[3], (const u16*)d_in[4], (const u16*)d_in[5],
                                   (const u16*)d_in[6], (const u16*)d_in[7], (const u16*)d_in[10], (const u16*)d_in[11], (const u16*)d_in[12], ws);
  k2_reduce<<<576, 256, 0, stream>>>(x, (const u16*)d_in[8], ws, (float*)d_out);
}
```

```cpp
#include <hip/hip_runtime.h>
#include <stdint.h>

typedef unsigned short u16;
typedef unsigned int u32;
typedef unsigned long long u64;

constexpr size_t WS_MISC = 0;
constexpr size_t WS_SX = 524288;
constexpr size_t WS_PART = 1048576;
constexpr size_t PART_N = 131072 + (size_t)16 * 10 * 8 * 16384 + (size_t)256 * 12 * 8192;

struct Srd { const void* p; u32 n; u32 f; };
struct Args { Srd d[16]; };

__global__ __launch_bounds__(512, 2) void k1_main(Args a) {
  __shared__ __attribute__((aligned(16))) char smem[163840];
  asm volatile(
R"ASM(.macro qm d,a,b
v_mfma_f32_16x16x32_bf16 v[\d:\d+3],v[\a:\a+3],v[\b:\b+3],v[\d:\d+3]
.endm
.macro qz d,a,b
v_mfma_f32_16x16x32_bf16 v[\d:\d+3],v[\a:\a+3],v[\b:\b+3],0
.endm
.macro qt d,a,o
ds_read_b64_tr_b16 v[\d:\d+1],v\a offset:\o
.endm
.macro qr d,a,o
ds_read_b128 v[\d:\d+3],v\a offset:\o
.endm
.macro qd d,a,o
ds_read_b64 v[\d:\d+1],v\a offset:\o
.endm
.macro qw n
s_waitcnt lgkmcnt(\n)
.endm
.macro ql v,s,o
buffer_load_dwordx4 v\v,s[\s:\s+3],s14 offen offset:\o lds
.endm
s_load_dwordx16 s[36:51],s[0:1],0
s_load_dwordx16 s[52:67],s[0:1],64
s_load_dwordx16 s[68:83],s[0:1],128
s_load_dwordx16 s[84:99],s[0:1],192
s_getpc_b64 s[4:5]
.L_pc:
v_lshrrev_b32 v4,3,v0
v_lshlrev_b32 v3,7,v4
global_load_dword v1,v3,s[4:5]
v_and_b32 v2,4,v4
v_and_b32 v4,7,v4
v_add_u32 v4,v4,v2
v_and_b32 v2,7,v0
v_mul_u32_u24 v5,1152,v4
v_lshl_add_u32 v14,v2,4,v5
v_readfirstlane_b32 s6,v0
s_lshr_b32 s10,s2,4
s_and_b32 s8,s2,7
s_bfe_u32 s9,s2,0x10003
s_lshr_b32 s6,s6,6
s_add_u32 s7,s6,s8
s_and_b32 s7,s7,3
s_lshl_b32 s29,s7,5
s_and_b32 s28,s6,4
s_add_u32 s29,s29,s28
s_lshl_b32 s19,s10,7
s_add_u32 s19,s19,s29
s_mul_i32 s19,s19,1152
s_add_u32 s28,s19,18432
s_cmp_gt_u32 s6,3
qw 0
s_cbranch_scc1 .L_ln1
s_setprio 3
buffer_load_dwordx4 v[184:187],v14,s[36:39],s19 offen offset:0
buffer_load_dwordx4 v[192:195],v14,s[36:39],s19 offen offset:128
buffer_load_dwordx4 v[200:203],v14,s[36:39],s19 offen offset:256
buffer_load_dwordx4 v[208:211],v14,s[36:39],s19 offen offset:384
buffer_load_dwordx4 v[216:219],v14,s[36:39],s19 offen offset:512
buffer_load_dwordx4 v[224:227],v14,s[36:39],s19 offen offset:640
buffer_load_dwordx4 v[232:235],v14,s[36:39],s19 offen offset:768
buffer_load_dwordx4 v[240:243],v14,s[36:39],s19 offen offset:896
buffer_load_dwordx4 v[248:251],v14,s[36:39],s19 offen offset:1024
s_setprio 2
buffer_load_dwordx4 v[188:191],v14,s[36:39],s28 offen offset:0
buffer_load_dwordx4 v[196:199],v14,s[36:39],s28 offen offset:128
buffer_load_dwordx4 v[204:207],v14,s[36:39],s28 offen offset:256
buffer_load_dwordx4 v[212:215],v14,s[36:39],s28 offen offset:384
buffer_load_dwordx4 v[220:223],v14,s[36:39],s28 offen offset:512
buffer_load_dwordx4 v[228:231],v14,s[36:39],s28 offen offset:640
buffer_load_dwordx4 v[236:239],v14,s[36:39],s28 offen offset:768
buffer_load_dwordx4 v[244:247],v14,s[36:39],s28 offen offset:896
buffer_load_dwordx4 v[252:255],v14,s[36:39],s28 offen offset:1024
v_and_b32 v0,63,v0
s_lshl_b32 s13,s7,11
s_mul_i32 s11,s8,576
s_mul_i32 s30,s9,320
s_add_u32 s11,s11,s30
s_lshr_b32 s12,s8,1
s_add_u32 s12,s12,32
s_lshl_b32 s30,s8,2
s_cmp_eq_u32 s9,0
s_cselect_b32 s12,s12,s30
s_lshl_b32 s17,s11,1
s_bitcmp1_b32 s7,0
s_cselect_b64 s[20:21],s[56:57],s[52:53]
s_mov_b64 s[22:23],s[54:55]
s_bitcmp1_b32 s7,1
s_cselect_b64 s[24:25],s[64:65],s[60:61]
s_mov_b64 s[26:27],s[62:63]
s_mov_b32 s31,s8
s_and_b32 s31,s31,1
s_mul_i32 s3,s31,3
s_add_u32 s31,s31,1
s_cmp_eq_u32 s9,0
s_cselect_b32 s9,s31,0
s_cselect_b32 s3,s31,s3
v_lshlrev_b32 v15,1,v0
s_lshl_b32 s54,s12,5
s_setprio 0
v_lshrrev_b32 v6,3,v0
v_lshl_add_u32 v6,s7,4,v6
v_mul_u32_u24 v6,9216,v6
v_lshrrev_b32 v7,3,v0
v_and_b32 v7,2,v7
v_xor_b32 v7,v7,v0
v_and_b32 v7,7,v7
v_lshl_add_u32 v6,v7,4,v6
v_xor_b32 v7,64,v6
v_add_u32 v7,72704,v7
v_lshrrev_b32 v8,3,v0
v_mul_u32_u24 v8,1152,v8
v_lshrrev_b32 v9,4,v0
v_xor_b32 v9,v9,v0
v_and_b32 v9,7,v9
v_lshl_add_u32 v8,v9,4,v8
v_xor_b32 v9,64,v8
v_add_u32 v9,8192,v9
s_lshr_b32 s30,s7,1
s_add_u32 s18,s12,s30
s_mul_i32 s18,s18,18432
s_and_b32 s30,s30,s9
s_cselect_b32 s18,0x7ff00000,s18
s_mov_b64 s[32:33],s[20:21]
s_mov_b64 s[34:35],s[22:23]
s_mov_b32 s14,s18
s_movk_i32 s31,128
s_cmp_lg_u32 s9,2
s_cbranch_scc1 .L_b0
s_mov_b64 s[32:33],s[40:41]
s_mov_b64 s[34:35],s[42:43]
s_mov_b32 s14,s17
s_mov_b32 s31,589824
v_mov_b32 v8,v6
v_mov_b32 v9,v7
.L_b0:
v_lshrrev_b32 v37,3,v0
v_and_b32 v38,7,v0
v_and_b32 v40,4,v38
v_lshl_or_b32 v31,v40,1,v38
v_xor_b32 v39,v37,v31
v_lshlrev_b32 v31,10,v40
v_lshl_add_u32 v31,v39,4,v31
v_and_b32 v39,3,v38
v_lshl_add_u32 v31,v39,8,v31
v_lshl_add_u32 v31,s7,10,v31
v_add_u32 v31,57344,v31
v_xor_b32 v32,0x80,v31
v_lshrrev_b32 v39,4,v0
v_xor_b32 v40,v39,v0
v_and_b32 v40,15,v40
v_lshlrev_b32 v24,8,v39
v_lshl_add_u32 v24,v40,4,v24
v_lshl_add_u32 v24,s7,10,v24
v_add_u32 v24,57344,v24
v_xor_b32 v25,0xc0,v24
v_add_u32 v26,0x10000,v24
v_add_u32 v27,0x10000,v25
v_lshlrev_b32 v18,4,v0
v_add_u32 v18,s13,v18
v_add_u32 v18,81920,v18
v_add_u32 v19,0x10000,v18
v_lshlrev_b32 v2,10,v39
v_bfe_u32 v40,v0,2,2
v_lshl_add_u32 v2,v40,7,v2
v_and_b32 v40,1,v0
v_lshl_add_u32 v2,v40,3,v2
v_lshrrev_b32 v40,2,v0
v_and_b32 v40,6,v40
v_bfe_u32 v37,v0,1,1
v_or_b32 v40,v40,v37
v_xor_b32 v40,0,v40
v_lshl_add_u32 v2,v40,4,v2
v_xor_b32 v3,32,v2
v_add_u32 v4,0x10000,v2
v_add_u32 v5,0x10000,v3
v_and_b32 v40,15,v0
v_lshlrev_b32 v20,7,v40
v_lshrrev_b32 v40,1,v40
v_xor_b32 v40,v40,v39
v_lshl_add_u32 v20,v40,4,v20
v_xor_b32 v21,64,v20
v_add_u32 v22,0x10000,v20
v_add_u32 v23,0x10000,v21
s_mul_i32 s30,s3,.L_code_X1-.L_code_Y
s_add_u32 s30,s30,.L_code_Y0-.L_pc
s_add_u32 s4,s4,s30
s_addc_u32 s5,s5,0
v_mov_b32 v30,0x3f803f80
v_mov_b32 v37,0x3f80
v_mov_b32 v45,1.0
v_add_u32 v38,0x10000,v31
v_add_u32 v39,0x10000,v32
s_waitcnt vmcnt(17)
v_dot2_f32_bf16 v33,v184,v30,0
v_dot2_f32_bf16 v33,v185,v30,v33
v_dot2_f32_bf16 v33,v186,v30,v33
v_dot2_f32_bf16 v33,v187,v30,v33
s_waitcnt vmcnt(16)
v_dot2_f32_bf16 v33,v192,v30,v33
v_dot2_f32_bf16 v33,v193,v30,v33
v_dot2_f32_bf16 v33,v194,v30,v33
v_dot2_f32_bf16 v33,v195,v30,v33
s_waitcnt vmcnt(15)
v_dot2_f32_bf16 v33,v200,v30,v33
v_dot2_f32_bf16 v33,v201,v30,v33
v_dot2_f32_bf16 v33,v202,v30,v33
v_dot2_f32_bf16 v33,v203,v30,v33
s_waitcnt vmcnt(14)
v_dot2_f32_bf16 v33,v208,v30,v33
v_dot2_f32_bf16 v33,v209,v30,v33
v_dot2_f32_bf16 v33,v210,v30,v33
v_dot2_f32_bf16 v33,v211,v30,v33
s_waitcnt vmcnt(13)
v_dot2_f32_bf16 v33,v216,v30,v33
v_dot2_f32_bf16 v33,v217,v30,v33
v_dot2_f32_bf16 v33,v218,v30,v33
v_dot2_f32_bf16 v33,v219,v30,v33
s_waitcnt vmcnt(12)
v_dot2_f32_bf16 v33,v224,v30,v33
v_dot2_f32_bf16 v33,v225,v30,v33
v_dot2_f32_bf16 v33,v226,v30,v33
v_dot2_f32_bf16 v33,v227,v30,v33
s_waitcnt vmcnt(11)
v_dot2_f32_bf16 v33,v232,v30,v33
v_dot2_f32_bf16 v33,v233,v30,v33
v_dot2_f32_bf16 v33,v234,v30,v33
v_dot2_f32_bf16 v33,v235,v30,v33
s_waitcnt vmcnt(10)
v_dot2_f32_bf16 v33,v240,v30,v33
v_dot2_f32_bf16 v33,v241,v30,v33
v_dot2_f32_bf16 v33,v242,v30,v33
v_dot2_f32_bf16 v33,v243,v30,v33
s_waitcnt vmcnt(9)
v_dot2_f32_bf16 v33,v248,v30,v33
v_dot2_f32_bf16 v33,v249,v30,v33
v_dot2_f32_bf16 v33,v250,v30,v33
v_dot2_f32_bf16 v33,v251,v30,v33
s_nop 2
v_add_f32_dpp v33,v33,v33 quad_perm:[1,0,3,2] row_mask:0xf bank_mask:0xf
s_nop 1
v_add_f32_dpp v33,v33,v33 quad_perm:[2,3,0,1] row_mask:0xf bank_mask:0xf
s_nop 1
v_add_f32_dpp v33,v33,v33 row_half_mirror row_mask:0xf bank_mask:0xf
v_mul_f32 v33,0x3ae38e39,v33
v_cvt_pk_bf16_f32 v35,-v33,-v33
v_and_b32 v35,0xffff0000,v35
.p2align 3
v_dot2_f32_bf16 v64,v184,v37,v35
v_dot2_f32_bf16 v65,v184,v45,v35
v_dot2_f32_bf16 v66,v185,v37,v35
v_dot2_f32_bf16 v67,v185,v45,v35
v_dot2_f32_bf16 v68,v186,v37,v35
v_dot2_f32_bf16 v69,v186,v45,v35
v_dot2_f32_bf16 v70,v187,v37,v35
v_dot2_f32_bf16 v71,v187,v45,v35
v_cvt_pk_bf16_f32 v184,v64,v65
v_cvt_pk_bf16_f32 v185,v66,v67
v_cvt_pk_bf16_f32 v186,v68,v69
v_cvt_pk_bf16_f32 v187,v70,v71
ds_write_b128 v31,v[184:187] offset:24576
v_pk_mul_f32 v[16:17],v[64:65],v[64:65]
v_pk_fma_f32 v[16:17],v[66:67],v[66:67],v[16:17]
v_pk_fma_f32 v[16:17],v[68:69],v[68:69],v[16:17]
v_pk_fma_f32 v[16:17],v[70:71],v[70:71],v[16:17]
v_dot2_f32_bf16 v72,v192,v37,v35
v_dot2_f32_bf16 v73,v192,v45,v35
v_dot2_f32_bf16 v74,v193,v37,v35
v_dot2_f32_bf16 v75,v193,v45,v35
v_dot2_f32_bf16 v76,v194,v37,v35
v_dot2_f32_bf16 v77,v194,v45,v35
v_dot2_f32_bf16 v78,v195,v37,v35
v_dot2_f32_bf16 v79,v195,v45,v35
v_cvt_pk_bf16_f32 v192,v72,v73
v_cvt_pk_bf16_f32 v193,v74,v75
v_cvt_pk_bf16_f32 v194,v76,v77
v_cvt_pk_bf16_f32 v195,v78,v79
ds_write_b128 v31,v[192:195] offset:32768
v_pk_fma_f32 v[16:17],v[72:73],v[72:73],v[16:17]
v_pk_fma_f32 v[16:17],v[74:75],v[74:75],v[16:17]
v_pk_fma_f32 v[16:17],v[76:77],v[76:77],v[16:17]
v_pk_fma_f32 v[16:17],v[78:79],v[78:79],v[16:17]
v_dot2_f32_bf16 v64,v200,v37,v35
v_dot2_f32_bf16 v65,v200,v45,v35
v_dot2_f32_bf16 v66,v201,v37,v35
v_dot2_f32_bf16 v67,v201,v45,v35
v_dot2_f32_bf16 v68,v202,v37,v35
v_dot2_f32_bf16 v69,v202,v45,v35
v_dot2_f32_bf16 v70,v203,v37,v35
v_dot2_f32_bf16 v71,v203,v45,v35
v_cvt_pk_bf16_f32 v200,v64,v65
v_cvt_pk_bf16_f32 v201,v66,v67
v_cvt_pk_bf16_f32 v202,v68,v69
v_cvt_pk_bf16_f32 v203,v70,v71
ds_write_b128 v31,v[200:203] offset:40960
v_pk_fma_f32 v[16:17],v[64:65],v[64:65],v[16:17]
v_pk_fma_f32 v[16:17],v[66:67],v[66:67],v[16:17]
v_pk_fma_f32 v[16:17],v[68:69],v[68:69],v[16:17]
v_pk_fma_f32 v[16:17],v[70:71],v[70:71],v[16:17]
v_dot2_f32_bf16 v72,v208,v37,v35
v_dot2_f32_bf16 v73,v208,v45,v35
v_dot2_f32_bf16 v74,v209,v37,v35
v_dot2_f32_bf16 v75,v209,v45,v35
v_dot2_f32_bf16 v76,v210,v37,v35
v_dot2_f32_bf16 v77,v210,v45,v35
v_dot2_f32_bf16 v78,v211,v37,v35
v_dot2_f32_bf16 v79,v211,v45,v35
v_cvt_pk_bf16_f32 v208,v72,v73
v_cvt_pk_bf16_f32 v209,v74,v75
v_cvt_pk_bf16_f32 v210,v76,v77
v_cvt_pk_bf16_f32 v211,v78,v79
ds_write_b128 v31,v[208:211] offset:49152
v_pk_fma_f32 v[16:17],v[72:73],v[72:73],v[16:17]
v_pk_fma_f32 v[16:17],v[74:75],v[74:75],v[16:17]
v_pk_fma_f32 v[16:17],v[76:77],v[76:77],v[16:17]
v_pk_fma_f32 v[16:17],v[78:79],v[78:79],v[16:17]
v_dot2_f32_bf16 v64,v216,v37,v35
v_dot2_f32_bf16 v65,v216,v45,v35
v_dot2_f32_bf16 v66,v217,v37,v35
v_dot2_f32_bf16 v67,v217,v45,v35
v_dot2_f32_bf16 v68,v218,v37,v35
v_dot2_f32_bf16 v69,v218,v45,v35
v_dot2_f32_bf16 v70,v219,v37,v35
v_dot2_f32_bf16 v71,v219,v45,v35
v_cvt_pk_bf16_f32 v216,v64,v65
v_cvt_pk_bf16_f32 v217,v66,v67
v_cvt_pk_bf16_f32 v218,v68,v69
v_cvt_pk_bf16_f32 v219,v70,v71
ds_write_b128 v31,v[216:219] offset:57344
v_pk_fma_f32 v[16:17],v[64:65],v[64:65],v[16:17]
v_pk_fma_f32 v[16:17],v[66:67],v[66:67],v[16:17]
v_pk_fma_f32 v[16:17],v[68:69],v[68:69],v[16:17]
v_pk_fma_f32 v[16:17],v[70:71],v[70:71],v[16:17]
v_dot2_f32_bf16 v72,v224,v37,v35
v_dot2_f32_bf16 v73,v224,v45,v35
v_dot2_f32_bf16 v74,v225,v37,v35
v_dot2_f32_bf16 v75,v225,v45,v35
v_dot2_f32_bf16 v76,v226,v37,v35
v_dot2_f32_bf16 v77,v226,v45,v35
v_dot2_f32_bf16 v78,v227,v37,v35
v_dot2_f32_bf16 v79,v227,v45,v35
v_cvt_pk_bf16_f32 v224,v72,v73
v_cvt_pk_bf16_f32 v225,v74,v75
v_cvt_pk_bf16_f32 v226,v76,v77
v_cvt_pk_bf16_f32 v227,v78,v79
ds_write_b128 v38,v[224:227] offset:8192
v_pk_fma_f32 v[16:17],v[72:73],v[72:73],v[16:17]
v_pk_fma_f32 v[16:17],v[74:75],v[74:75],v[16:17]
v_pk_fma_f32 v[16:17],v[76:77],v[76:77],v[16:17]
v_pk_fma_f32 v[16:17],v[78:79],v[78:79],v[16:17]
v_dot2_f32_bf16 v64,v232,v37,v35
v_dot2_f32_bf16 v65,v232,v45,v35
v_dot2_f32_bf16 v66,v233,v37,v35
v_dot2_f32_bf16 v67,v233,v45,v35
v_dot2_f32_bf16 v68,v234,v37,v35
v_dot2_f32_bf16 v69,v234,v45,v35
v_dot2_f32_bf16 v70,v235,v37,v35
v_dot2_f32_bf16 v71,v235,v45,v35
v_cvt_pk_bf16_f32 v232,v64,v65
v_cvt_pk_bf16_f32 v233,v66,v67
v_cvt_pk_bf16_f32 v234,v68,v69
v_cvt_pk_bf16_f32 v235,v70,v71
ds_write_b128 v38,v[232:235] offset:16384
v_pk_fma_f32 v[16:17],v[64:65],v[64:65],v[16:17]
v_pk_fma_f32 v[16:17],v[66:67],v[66:67],v[16:17]
v_pk_fma_f32 v[16:17],v[68:69],v[68:69],v[16:17]
v_pk_fma_f32 v[16:17],v[70:71],v[70:71],v[16:17]
v_dot2_f32_bf16 v72,v240,v37,v35
v_dot2_f32_bf16 v73,v240,v45,v35
v_dot2_f32_bf16 v74,v241,v37,v35
v_dot2_f32_bf16 v75,v241,v45,v35
v_dot2_f32_bf16 v76,v242,v37,v35
v_dot2_f32_bf16 v77,v242,v45,v35
v_dot2_f32_bf16 v78,v243,v37,v35
v_dot2_f32_bf16 v79,v243,v45,v35
v_cvt_pk_bf16_f32 v240,v72,v73
v_cvt_pk_bf16_f32 v241,v74,v75
v_cvt_pk_bf16_f32 v242,v76,v77
v_cvt_pk_bf16_f32 v243,v78,v79
ds_write_b128 v38,v[240:243] offset:24576
v_pk_fma_f32 v[16:17],v[72:73],v[72:73],v[16:17]
v_pk_fma_f32 v[16:17],v[74:75],v[74:75],v[16:17]
v_pk_fma_f32 v[16:17],v[76:77],v[76:77],v[16:17]
v_pk_fma_f32 v[16:17],v[78:79],v[78:79],v[16:17]
v_dot2_f32_bf16 v64,v248,v37,v35
v_dot2_f32_bf16 v65,v248,v45,v35
v_dot2_f32_bf16 v66,v249,v37,v35
v_dot2_f32_bf16 v67,v249,v45,v35
v_dot2_f32_bf16 v68,v250,v37,v35
v_dot2_f32_bf16 v69,v250,v45,v35
v_dot2_f32_bf16 v70,v251,v37,v35
v_dot2_f32_bf16 v71,v251,v45,v35
v_cvt_pk_bf16_f32 v248,v64,v65
v_cvt_pk_bf16_f32 v249,v66,v67
v_cvt_pk_bf16_f32 v250,v68,v69
v_cvt_pk_bf16_f32 v251,v70,v71
ds_write_b128 v38,v[248:251] offset:32768
v_pk_fma_f32 v[16:17],v[64:65],v[64:65],v[16:17]
v_pk_fma_f32 v[16:17],v[66:67],v[66:67],v[16:17]
v_pk_fma_f32 v[16:17],v[68:69],v[68:69],v[16:17]
v_pk_fma_f32 v[16:17],v[70:71],v[70:71],v[16:17]
s_waitcnt vmcnt(8)
v_dot2_f32_bf16 v34,v188,v30,0
v_dot2_f32_bf16 v34,v189,v30,v34
v_dot2_f32_bf16 v34,v190,v30,v34
v_dot2_f32_bf16 v34,v191,v30,v34
s_waitcnt vmcnt(7)
v_dot2_f32_bf16 v34,v196,v30,v34
v_dot2_f32_bf16 v34,v197,v30,v34
v_dot2_f32_bf16 v34,v198,v30,v34
v_dot2_f32_bf16 v34,v199,v30,v34
s_waitcnt vmcnt(6)
v_dot2_f32_bf16 v34,v204,v30,v34
v_dot2_f32_bf16 v34,v205,v30,v34
v_dot2_f32_bf16 v34,v206,v30,v34
v_dot2_f32_bf16 v34,v207,v30,v34
s_waitcnt vmcnt(5)
v_dot2_f32_bf16 v34,v212,v30,v34
v_dot2_f32_bf16 v34,v213,v30,v34
v_dot2_f32_bf16 v34,v214,v30,v34
v_dot2_f32_bf16 v34,v215,v30,v34
s_waitcnt vmcnt(4)
v_dot2_f32_bf16 v34,v220,v30,v34
v_dot2_f32_bf16 v34,v221,v30,v34
v_dot2_f32_bf16 v34,v222,v30,v34
v_dot2_f32_bf16 v34,v223,v30,v34
s_waitcnt vmcnt(3)
v_dot2_f32_bf16 v34,v228,v30,v34
v_dot2_f32_bf16 v34,v229,v30,v34
v_dot2_f32_bf16 v34,v230,v30,v34
v_dot2_f32_bf16 v34,v231,v30,v34
s_waitcnt vmcnt(2)
v_dot2_f32_bf16 v34,v236,v30,v34
v_dot2_f32_bf16 v34,v237,v30,v34
v_dot2_f32_bf16 v34,v238,v30,v34
v_dot2_f32_bf16 v34,v239,v30,v34
s_waitcnt vmcnt(1)
v_dot2_f32_bf16 v34,v244,v30,v34
v_dot2_f32_bf16 v34,v245,v30,v34
v_dot2_f32_bf16 v34,v246,v30,v34
v_dot2_f32_bf16 v34,v247,v30,v34
s_waitcnt vmcnt(0)
v_dot2_f32_bf16 v34,v252,v30,v34
v_dot2_f32_bf16 v34,v253,v30,v34
v_dot2_f32_bf16 v34,v254,v30,v34
v_dot2_f32_bf16 v34,v255,v30,v34
s_nop 2
v_add_f32_dpp v34,v34,v34 quad_perm:[1,0,3,2] row_mask:0xf bank_mask:0xf
s_nop 1
v_add_f32_dpp v34,v34,v34 quad_perm:[2,3,0,1] row_mask:0xf bank_mask:0xf
s_nop 1
v_add_f32_dpp v34,v34,v34 row_half_mirror row_mask:0xf bank_mask:0xf
v_mul_f32 v34,0x3ae38e39,v34
v_cvt_pk_bf16_f32 v36,-v34,-v34
v_and_b32 v36,0xffff0000,v36
.p2align 3
v_dot2_f32_bf16 v64,v188,v37,v36
v_dot2_f32_bf16 v65,v188,v45,v36
v_dot2_f32_bf16 v66,v189,v37,v36
v_dot2_f32_bf16 v67,v189,v45,v36
v_dot2_f32_bf16 v68,v190,v37,v36
v_dot2_f32_bf16 v69,v190,v45,v36
v_dot2_f32_bf16 v70,v191,v37,v36
v_dot2_f32_bf16 v71,v191,v45,v36
v_cvt_pk_bf16_f32 v188,v64,v65
v_cvt_pk_bf16_f32 v189,v66,v67
v_cvt_pk_bf16_f32 v190,v68,v69
v_cvt_pk_bf16_f32 v191,v70,v71
ds_write_b128 v32,v[188:191] offset:24576
v_pk_mul_f32 v[28:29],v[64:65],v[64:65]
v_pk_fma_f32 v[28:29],v[66:67],v[66:67],v[28:29]
v_pk_fma_f32 v[28:29],v[68:69],v[68:69],v[28:29]
v_pk_fma_f32 v[28:29],v[70:71],v[70:71],v[28:29]
v_dot2_f32_bf16 v72,v196,v37,v36
v_dot2_f32_bf16 v73,v196,v45,v36
v_dot2_f32_bf16 v74,v197,v37,v36
v_dot2_f32_bf16 v75,v197,v45,v36
v_dot2_f32_bf16 v76,v198,v37,v36
v_dot2_f32_bf16 v77,v198,v45,v36
v_dot2_f32_bf16 v78,v199,v37,v36
v_dot2_f32_bf16 v79,v199,v45,v36
v_cvt_pk_bf16_f32 v196,v72,v73
v_cvt_pk_bf16_f32 v197,v74,v75
v_cvt_pk_bf16_f32 v198,v76,v77
v_cvt_pk_bf16_f32 v199,v78,v79
ds_write_b128 v32,v[196:199] offset:32768
v_pk_fma_f32 v[28:29],v[72:73],v[72:73],v[28:29]
v_pk_fma_f32 v[28:29],v[74:75],v[74:75],v[28:29]
v_pk_fma_f32 v[28:29],v[76:77],v[76:77],v[28:29]
v_pk_fma_f32 v[28:29],v[78:79],v[78:79],v[28:29]
v_dot2_f32_bf16 v64,v204,v37,v36
v_dot2_f32_bf16 v65,v204,v45,v36
v_dot2_f32_bf16 v66,v205,v37,v36
v_dot2_f32_bf16 v67,v205,v45,v36
v_dot2_f32_bf16 v68,v206,v37,v36
v_dot2_f32_bf16 v69,v206,v45,v36
v_dot2_f32_bf16 v70,v207,v37,v36
v_dot2_f32_bf16 v71,v207,v45,v36
v_cvt_pk_bf16_f32 v204,v64,v65
v_cvt_pk_bf16_f32 v205,v66,v67
v_cvt_pk_bf16_f32 v206,v68,v69
v_cvt_pk_bf16_f32 v207,v70,v71
ds_write_b128 v32,v[204:207] offset:40960
v_pk_fma_f32 v[28:29],v[64:65],v[64:65],v[28:29]
v_pk_fma_f32 v[28:29],v[66:67],v[66:67],v[28:29]
v_pk_fma_f32 v[28:29],v[68:69],v[68:69],v[28:29]
v_pk_fma_f32 v[28:29],v[70:71],v[70:71],v[28:29]
v_dot2_f32_bf16 v72,v212,v37,v36
v_dot2_f32_bf16 v73,v212,v45,v36
v_dot2_f32_bf16 v74,v213,v37,v36
v_dot2_f32_bf16 v75,v213,v45,v36
v_dot2_f32_bf16 v76,v214,v37,v36
v_dot2_f32_bf16 v77,v214,v45,v36
v_dot2_f32_bf16 v78,v215,v37,v36
v_dot2_f32_bf16 v79,v215,v45,v36
v_cvt_pk_bf16_f32 v212,v72,v73
v_cvt_pk_bf16_f32 v213,v74,v75
v_cvt_pk_bf16_f32 v214,v76,v77
v_cvt_pk_bf16_f32 v215,v78,v79
ds_write_b128 v32,v[212:215] offset:49152
v_pk_fma_f32 v[28:29],v[72:73],v[72:73],v[28:29]
v_pk_fma_f32 v[28:29],v[74:75],v[74:75],v[28:29]
v_pk_fma_f32 v[28:29],v[76:77],v[76:77],v[28:29]
v_pk_fma_f32 v[28:29],v[78:79],v[78:79],v[28:29]
v_dot2_f32_bf16 v64,v220,v37,v36
v_dot2_f32_bf16 v65,v220,v45,v36
v_dot2_f32_bf16 v66,v221,v37,v36
v_dot2_f32_bf16 v67,v221,v45,v36
v_dot2_f32_bf16 v68,v222,v37,v36
v_dot2_f32_bf16 v69,v222,v45,v36
v_dot2_f32_bf16 v70,v223,v37,v36
v_dot2_f32_bf16 v71,v223,v45,v36
v_cvt_pk_bf16_f32 v220,v64,v65
v_cvt_pk_bf16_f32 v221,v66,v67
v_cvt_pk_bf16_f32 v222,v68,v69
v_cvt_pk_bf16_f32 v223,v70,v71
ds_write_b128 v32,v[220:223] offset:57344
v_pk_fma_f32 v[28:29],v[64:65],v[64:65],v[28:29]
v_pk_fma_f32 v[28:29],v[66:67],v[66:67],v[28:29]
v_pk_fma_f32 v[28:29],v[68:69],v[68:69],v[28:29]
v_pk_fma_f32 v[28:29],v[70:71],v[70:71],v[28:29]
v_dot2_f32_bf16 v72,v228,v37,v36
v_dot2_f32_bf16 v73,v228,v45,v36
v_dot2_f32_bf16 v74,v229,v37,v36
v_dot2_f32_bf16 v75,v229,v45,v36
v_dot2_f32_bf16 v76,v230,v37,v36
v_dot2_f32_bf16 v77,v230,v45,v36
v_dot2_f32_bf16 v78,v231,v37,v36
v_dot2_f32_bf16 v79,v231,v45,v36
v_cvt_pk_bf16_f32 v228,v72,v73
v_cvt_pk_bf16_f32 v229,v74,v75
v_cvt_pk_bf16_f32 v230,v76,v77
v_cvt_pk_bf16_f32 v231,v78,v79
ds_write_b128 v39,v[228:231] offset:8192
v_pk_fma_f32 v[28:29],v[72:73],v[72:73],v[28:29]
v_pk_fma_f32 v[28:29],v[74:75],v[74:75],v[28:29]
v_pk_fma_f32 v[28:29],v[76:77],v[76:77],v[28:29]
v_pk_fma_f32 v[28:29],v[78:79],v[78:79],v[28:29]
v_dot2_f32_bf16 v64,v236,v37,v36
v_dot2_f32_bf16 v65,v236,v45,v36
v_dot2_f32_bf16 v66,v237,v37,v36
v_dot2_f32_bf16 v67,v237,v45,v36
v_dot2_f32_bf16 v68,v238,v37,v36
v_dot2_f32_bf16 v69,v238,v45,v36
v_dot2_f32_bf16 v70,v239,v37,v36
v_dot2_f32_bf16 v71,v239,v45,v36
v_cvt_pk_bf16_f32 v236,v64,v65
v_cvt_pk_bf16_f32 v237,v66,v67
v_cvt_pk_bf16_f32 v238,v68,v69
v_cvt_pk_bf16_f32 v239,v70,v71
ds_write_b128 v39,v[236:239] offset:16384
v_pk_fma_f32 v[28:29],v[64:65],v[64:65],v[28:29]
v_pk_fma_f32 v[28:29],v[66:67],v[66:67],v[28:29]
v_pk_fma_f32 v[28:29],v[68:69],v[68:69],v[28:29]
v_pk_fma_f32 v[28:29],v[70:71],v[70:71],v[28:29]
v_dot2_f32_bf16 v72,v244,v37,v36
v_dot2_f32_bf16 v73,v244,v45,v36
v_dot2_f32_bf16 v74,v245,v37,v36
v_dot2_f32_bf16 v75,v245,v45,v36
v_dot2_f32_bf16 v76,v246,v37,v36
v_dot2_f32_bf16 v77,v246,v45,v36
v_dot2_f32_bf16 v78,v247,v37,v36
v_dot2_f32_bf16 v79,v247,v45,v36
v_cvt_pk_bf16_f32 v244,v72,v73
v_cvt_pk_bf16_f32 v245,v74,v75
v_cvt_pk_bf16_f32 v246,v76,v77
v_cvt_pk_bf16_f32 v247,v78,v79
ds_write_b128 v39,v[244:247] offset:24576
v_pk_fma_f32 v[28:29],v[72:73],v[72:73],v[28:29]
v_pk_fma_f32 v[28:29],v[74:75],v[74:75],v[28:29]
v_pk_fma_f32 v[28:29],v[76:77],v[76:77],v[28:29]
v_pk_fma_f32 v[28:29],v[78:79],v[78:79],v[28:29]
v_dot2_f32_bf16 v64,v252,v37,v36
v_dot2_f32_bf16 v65,v252,v45,v36
v_dot2_f32_bf16 v66,v253,v37,v36
v_dot2_f32_bf16 v67,v253,v45,v36
v_dot2_f32_bf16 v68,v254,v37,v36
v_dot2_f32_bf16 v69,v254,v45,v36
v_dot2_f32_bf16 v70,v255,v37,v36
v_dot2_f32_bf16 v71,v255,v45,v36
v_cvt_pk_bf16_f32 v252,v64,v65
v_cvt_pk_bf16_f32 v253,v66,v67
v_cvt_pk_bf16_f32 v254,v68,v69
v_cvt_pk_bf16_f32 v255,v70,v71
ds_write_b128 v39,v[252:255] offset:32768
v_pk_fma_f32 v[28:29],v[64:65],v[64:65],v[28:29]
v_pk_fma_f32 v[28:29],v[66:67],v[66:67],v[28:29]
v_pk_fma_f32 v[28:29],v[68:69],v[68:69],v[28:29]
v_pk_fma_f32 v[28:29],v[70:71],v[70:71],v[28:29]
qr 112,24,24576
qr 116,25,28672
qr 120,24,32768
qr 124,25,36864
qr 128,24,40960
qr 132,25,45056
qr 136,24,49152
qr 140,25,53248
qr 144,24,57344
qr 148,25,61440
qr 152,26,8192
qr 156,27,12288
qr 160,26,16384
qr 164,27,20480
qr 168,26,24576
qr 172,27,28672
qr 176,26,32768
qr 180,27,36864
s_mov_b32 m0,s13
s_nop 0
ql 8,32,0
ql 9,32,1024
s_add_u32 s14,s14,s31
s_add_u32 m0,s13,8192
s_nop 0
ql 8,32,0
ql 9,32,1024
s_add_u32 s14,s14,s31
v_add_f32 v16,v16,v17
v_add_f32 v17,v28,v29
s_nop 0
v_add_f32_dpp v16,v16,v16 quad_perm:[1,0,3,2] row_mask:0xf bank_mask:0xf
v_add_f32 v33,v33,v35
v_add_f32_dpp v17,v17,v17 quad_perm:[1,0,3,2] row_mask:0xf bank_mask:0xf
v_add_f32_dpp v16,v16,v16 quad_perm:[2,3,0,1] row_mask:0xf bank_mask:0xf
v_add_f32 v34,v34,v36
v_add_f32_dpp v17,v17,v17 quad_perm:[2,3,0,1] row_mask:0xf bank_mask:0xf
v_add_f32_dpp v16,v16,v16 row_half_mirror row_mask:0xf bank_mask:0xf
v_lshrrev_b32 v44,3,v0
v_add_f32_dpp v17,v17,v17 row_half_mirror row_mask:0xf bank_mask:0xf
v_mul_f32 v16,0x3ae38e39,v16
v_fma_f32 v16,-v33,v33,v16
v_max_f32 v16,0,v16
v_mul_f32 v17,0x3ae38e39,v17
v_fma_f32 v17,-v34,v34,v17
v_max_f32 v17,0,v17
v_cvt_pk_bf16_f32 v42,v16,v17
v_lshlrev_b32 v16,16,v42
v_and_b32 v17,0xffff0000,v42
v_add_f32 v16,0x3727c5ac,v16
v_add_f32 v17,0x3727c5ac,v17
v_cvt_pk_bf16_f32 v42,v16,v17
v_lshlrev_b32 v16,16,v42
v_and_b32 v17,0xffff0000,v42
v_sqrt_f32 v16,v16
v_sqrt_f32 v17,v17
v_and_b32 v43,4,v44
v_cvt_pk_bf16_f32 v42,v16,v17
v_lshlrev_b32 v16,16,v42
v_and_b32 v17,0xffff0000,v42
v_rcp_f32 v16,v16
v_rcp_f32 v17,v17
v_add3_u32 v44,v43,v44,s29
v_lshlrev_b32 v43,2,v44
v_add_u32 v43,122880,v43
ds_write_b32 v43,v16
ds_write_b32 v43,v17 offset:64
ds_write_b32 v43,v35 offset:512
ds_write_b32 v43,v36 offset:576
s_add_u32 m0,s13,16384
s_nop 0
ql 8,32,0
ql 9,32,1024
s_add_u32 s14,s14,s31
s_add_u32 m0,s13,24576
s_nop 0
ql 8,32,0
ql 9,32,1024
s_add_u32 s14,s14,s31
s_add_u32 m0,s13,32768
s_nop 0
ql 8,32,0
ql 9,32,1024
s_add_u32 s14,s14,s31
s_add_u32 m0,s13,40960
s_nop 0
ql 8,32,0
ql 9,32,1024
s_add_u32 s14,s14,s31
s_add_u32 m0,s13,49152
s_nop 0
ql 8,32,0
ql 9,32,1024
s_add_u32 s14,s14,s31
buffer_load_dword v13,off,s[96:99],0 sc1
s_setpc_b64 s[4:5]
.L_ln1:
v_and_b32 v0,63,v0
s_lshl_b32 s13,s7,11
s_mul_i32 s11,s8,576
s_mul_i32 s30,s9,320
s_add_u32 s11,s11,s30
s_lshr_b32 s12,s8,1
s_add_u32 s12,s12,32
s_lshl_b32 s30,s8,2
s_cmp_eq_u32 s9,0
s_cselect_b32 s12,s12,s30
s_lshl_b32 s17,s11,1
s_bitcmp1_b32 s7,0
s_cselect_b64 s[20:21],s[56:57],s[52:53]
s_mov_b64 s[22:23],s[54:55]
s_bitcmp1_b32 s7,1
s_cselect_b64 s[24:25],s[64:65],s[60:61]
s_mov_b64 s[26:27],s[62:63]
s_mov_b32 s31,s8
s_and_b32 s31,s31,1
s_mul_i32 s3,s31,3
s_add_u32 s31,s31,1
s_cmp_eq_u32 s9,0
s_cselect_b32 s9,s31,0
s_cselect_b32 s3,s31,s3
v_lshlrev_b32 v15,1,v0
s_lshl_b32 s54,s12,5
v_lshlrev_b32 v7,7,v0
s_and_b32 s30,s10,14
s_lshl_b32 s30,s30,1
s_add_u32 s30,s30,s7
s_lshl_b32 s52,s10,2
s_add_u32 s52,s52,s7
s_mul_i32 s52,s52,9
s_mul_i32 s30,s30,2560
s_mul_i32 s31,s3,.L_code_X1-.L_code_Y
s_add_u32 s30,s30,s31
s_add_u32 s53,s30,.L_code_Y-.L_pc
s_mov_b64 s[32:33],s[4:5]
s_mov_b32 s34,.L_end-.L_pc
s_mov_b32 s35,s39
v_min_u32 v8,2432,v7
v_and_b32 v6,31,v0
v_min_u32 v6,8,v6
v_add_u32 v6,s52,v6
v_mul_u32_u24 v6,9216,v6
v_lshrrev_b32 v9,5,v0
v_mul_u32_u24 v9,640,v9
s_lshl_b32 s15,s10,2
s_and_b32 s30,s7,2
s_add_u32 s15,s15,s30
s_and_b32 s31,s10,14
s_lshr_b32 s30,s7,1
s_add_u32 s31,s31,s30
s_cmp_eq_u32 s9,0
s_cselect_b32 s15,s15,s31
s_cselect_b32 s31,17,8
s_lshl_b32 s31,s31,7
s_lshl_b32 s30,s12,4
s_add_u32 s15,s15,s30
s_mul_i32 s15,s15,1152
v_min_u32 v28,s31,v7
s_cmp_eq_u32 s9,2
s_cselect_b32 s15,s17,s15
s_cselect_b64 vcc,-1,0
s_cselect_b64 s[56:57],s[40:41],s[20:21]
s_cselect_b64 s[58:59],s[42:43],s[22:23]
v_cndmask_b32 v28,v28,v6,vcc
v_mov_b32 v33,v6
v_add_u32 v6,v6,v9
s_cmp_eq_u32 s9,0
s_cselect_b32 s18,s17,0x7ff00000
s_and_b32 s30,s10,6
s_lshl_b32 s30,s30,1
s_add_u32 s30,s30,s7
s_mul_i32 s30,s30,384
s_lshr_b32 s31,s10,3
s_lshl_b32 s14,s3,1
s_add_u32 s31,s31,s14
s_mul_i32 s31,s31,.L_code_Y1-.L_code_Y
s_add_u32 s30,s30,s31
s_add_u32 s14,s30,.L_code_Y-.L_pc
v_min_u32 v29,0x100,v7
v_lshrrev_b32 v37,3,v0
v_and_b32 v38,7,v0
v_and_b32 v40,4,v38
v_lshl_or_b32 v31,v40,1,v38
v_xor_b32 v39,v37,v31
v_lshlrev_b32 v31,10,v40
v_lshl_add_u32 v31,v39,4,v31
v_and_b32 v39,3,v38
v_lshl_add_u32 v31,v39,8,v31
v_lshl_add_u32 v31,s7,10,v31
v_add_u32 v31,57344,v31
v_xor_b32 v32,0x80,v31
v_lshrrev_b32 v39,4,v0
v_xor_b32 v40,v39,v0
v_and_b32 v40,15,v40
v_lshlrev_b32 v24,8,v39
v_lshl_add_u32 v24,v40,4,v24
v_lshl_add_u32 v24,s7,10,v24
v_add_u32 v24,57344,v24
v_xor_b32 v25,0xc0,v24
v_add_u32 v26,0x10000,v24
v_add_u32 v27,0x10000,v25
v_lshlrev_b32 v18,4,v0
v_add_u32 v18,s13,v18
v_add_u32 v18,81920,v18
v_add_u32 v19,0x10000,v18
v_lshlrev_b32 v2,10,v39
v_bfe_u32 v40,v0,2,2
v_lshl_add_u32 v2,v40,7,v2
v_and_b32 v40,1,v0
v_lshl_add_u32 v2,v40,3,v2
v_lshrrev_b32 v40,2,v0
v_and_b32 v40,6,v40
v_bfe_u32 v37,v0,1,1
v_or_b32 v40,v40,v37
v_xor_b32 v40,4,v40
v_lshl_add_u32 v2,v40,4,v2
v_xor_b32 v3,32,v2
v_add_u32 v4,0x10000,v2
v_add_u32 v5,0x10000,v3
v_and_b32 v40,15,v0
v_lshlrev_b32 v20,7,v40
v_lshrrev_b32 v40,1,v40
v_xor_b32 v40,v40,v39
v_lshl_add_u32 v20,v40,4,v20
v_xor_b32 v21,64,v20
v_add_u32 v22,0x10000,v20
v_add_u32 v23,0x10000,v21
s_mul_i32 s30,s3,.L_code_X1-.L_code_Y
s_add_u32 s30,s30,.L_code_Y1-.L_pc
s_add_u32 s4,s4,s30
s_addc_u32 s5,s5,0
v_mov_b32 v30,0x3f803f80
v_mov_b32 v37,0x3f80
v_mov_b32 v45,1.0
v_add_u32 v38,0x10000,v31
v_add_u32 v39,0x10000,v32
s_setprio 2
buffer_load_ushort v10,v15,s[68:71],s54 offen
buffer_load_ushort v11,v15,s[72:75],s54 offen
buffer_load_ushort v12,v15,s[76:79],s54 offen
buffer_load_dword v13,off,s[96:99],0 sc1
buffer_load_dword v1,v29,s[32:35],s14 offen
buffer_load_dwordx4 v[184:187],v14,s[36:39],s19 offen offset:0
buffer_load_dwordx4 v[192:195],v14,s[36:39],s19 offen offset:128
buffer_load_dwordx4 v[200:203],v14,s[36:39],s19 offen offset:256
buffer_load_dwordx4 v[208:211],v14,s[36:39],s19 offen offset:384
buffer_load_dwordx4 v[216:219],v14,s[36:39],s19 offen offset:512
buffer_load_dwordx4 v[224:227],v14,s[36:39],s19 offen offset:640
buffer_load_dwordx4 v[232:235],v14,s[36:39],s19 offen offset:768
buffer_load_dwordx4 v[240:243],v14,s[36:39],s19 offen offset:896
buffer_load_dwordx4 v[248:251],v14,s[36:39],s19 offen offset:1024
s_setprio 1
s_waitcnt vmcnt(11)
v_lshlrev_b32 v12,16,v12
v_lshlrev_b32 v10,16,v10
v_lshlrev_b32 v11,16,v11
v_mul_f32 v12,0x3fb8aa3b,v12
v_exp_f32 v12,v12
v_mul_f32 v87,v10,v10
v_cvt_pk_bf16_f32 v12,v12,v12
v_fma_f32 v87,v11,v11,v87
v_and_b32 v12,0xffff0000,v12
v_rcp_f32 v87,v87
v_mul_f32 v80,v12,v10
v_mul_f32 v81,v12,v11
v_mul_f32 v80,0x3fb8aa3b,v80
v_mul_f32 v81,0x3e22f983,v81
v_mul_f32 v86,1.0,v80
v_mul_f32 v85,1.0,v81
v_exp_f32 v86,v86
v_fract_f32 v85,v85
v_cos_f32 v84,v85
v_sin_f32 v85,v85
v_mul_f32 v84,v84,v86
v_mul_f32 v85,v85,v86
v_add_f32 v87,v87,v87
v_add_f32 v86,-1.0,v84
v_mul_f32 v82,v86,v10
v_mul_f32 v83,v85,v10
v_fma_f32 v82,v85,v11,v82
v_fma_f32 v83,-v86,v11,v83
v_mul_f32 v82,v82,v87
v_mul_f32 v83,v83,v87
v_lshlrev_b32 v88,4,v0
v_add_u32 v88,123904,v88
v_lshlrev_b32 v89,3,v0
v_add_u32 v89,124928,v89
ds_write_b128 v88,v[80:83]
ds_write_b64 v89,v[84:85]
s_waitcnt vmcnt(8)
v_dot2_f32_bf16 v33,v184,v30,0
v_dot2_f32_bf16 v33,v185,v30,v33
v_dot2_f32_bf16 v33,v186,v30,v33
v_dot2_f32_bf16 v33,v187,v30,v33
buffer_load_dwordx4 v[188:191],v14,s[36:39],s28 offen offset:0
s_waitcnt vmcnt(8)
v_dot2_f32_bf16 v33,v192,v30,v33
v_dot2_f32_bf16 v33,v193,v30,v33
v_dot2_f32_bf16 v33,v194,v30,v33
v_dot2_f32_bf16 v33,v195,v30,v33
buffer_load_dwordx4 v[196:199],v14,s[36:39],s28 offen offset:128
s_waitcnt vmcnt(8)
v_dot2_f32_bf16 v33,v200,v30,v33
v_dot2_f32_bf16 v33,v201,v30,v33
v_dot2_f32_bf16 v33,v202,v30,v33
v_dot2_f32_bf16 v33,v203,v30,v33
buffer_load_dwordx4 v[204:207],v14,s[36:39],s28 offen offset:256
s_waitcnt vmcnt(8)
v_dot2_f32_bf16 v33,v208,v30,v33
v_dot2_f32_bf16 v33,v209,v30,v33
v_dot2_f32_bf16 v33,v210,v30,v33
v_dot2_f32_bf16 v33,v211,v30,v33
buffer_load_dwordx4 v[212:215],v14,s[36:39],s28 offen offset:384
s_waitcnt vmcnt(8)
v_dot2_f32_bf16 v33,v216,v30,v33
v_dot2_f32_bf16 v33,v217,v30,v33
v_dot2_f32_bf16 v33,v218,v30,v33
v_dot2_f32_bf16 v33,v219,v30,v33
buffer_load_dwordx4 v[220:223],v14,s[36:39],s28 offen offset:512
s_waitcnt vmcnt(8)
v_dot2_f32_bf16 v33,v224,v30,v33
v_dot2_f32_bf16 v33,v225,v30,v33
v_dot2_f32_bf16 v33,v226,v30,v33
v_dot2_f32_bf16 v33,v227,v30,v33
buffer_load_dwordx4 v[228:231],v14,s[36:39],s28 offen offset:640
s_waitcnt vmcnt(8)
v_dot2_f32_bf16 v33,v232,v30,v33
v_dot2_f32_bf16 v33,v233,v30,v33
v_dot2_f32_bf16 v33,v234,v30,v33
v_dot2_f32_bf16 v33,v235,v30,v33
buffer_load_dwordx4 v[236:239],v14,s[36:39],s28 offen offset:768
s_waitcnt vmcnt(8)
v_dot2_f32_bf16 v33,v240,v30,v33
v_dot2_f32_bf16 v33,v241,v30,v33
v_dot2_f32_bf16 v33,v242,v30,v33
v_dot2_f32_bf16 v33,v243,v30,v33
buffer_load_dwordx4 v[244:247],v14,s[36:39],s28 offen offset:896
s_waitcnt vmcnt(8)
v_dot2_f32_bf16 v33,v248,v30,v33
v_dot2_f32_bf16 v33,v249,v30,v33
v_dot2_f32_bf16 v33,v250,v30,v33
v_dot2_f32_bf16 v33,v251,v30,v33
buffer_load_dwordx4 v[252:255],v14,s[36:39],s28 offen offset:1024
s_nop 1
v_add_f32_dpp v33,v33,v33 quad_perm:[1,0,3,2] row_mask:0xf bank_mask:0xf
s_nop 1
v_add_f32_dpp v33,v33,v33 quad_perm:[2,3,0,1] row_mask:0xf bank_mask:0xf
s_nop 1
v_add_f32_dpp v33,v33,v33 row_half_mirror row_mask:0xf bank_mask:0xf
v_mul_f32 v33,0x3ae38e39,v33
v_cvt_pk_bf16_f32 v35,-v33,-v33
v_and_b32 v35,0xffff0000,v35
.p2align 3
v_dot2_f32_bf16 v64,v184,v37,v35
v_dot2_f32_bf16 v65,v184,v45,v35
v_dot2_f32_bf16 v66,v185,v37,v35
v_dot2_f32_bf16 v67,v185,v45,v35
v_dot2_f32_bf16 v68,v186,v37,v35
v_dot2_f32_bf16 v69,v186,v45,v35
v_dot2_f32_bf16 v70,v187,v37,v35
v_dot2_f32_bf16 v71,v187,v45,v35
v_cvt_pk_bf16_f32 v184,v64,v65
v_cvt_pk_bf16_f32 v185,v66,v67
v_cvt_pk_bf16_f32 v186,v68,v69
v_cvt_pk_bf16_f32 v187,v70,v71
ds_write_b128 v31,v[184:187]
v_pk_mul_f32 v[16:17],v[64:65],v[64:65]
v_pk_fma_f32 v[16:17],v[66:67],v[66:67],v[16:17]
v_pk_fma_f32 v[16:17],v[68:69],v[68:69],v[16:17]
v_pk_fma_f32 v[16:17],v[70:71],v[70:71],v[16:17]
v_dot2_f32_bf16 v72,v192,v37,v35
v_dot2_f32_bf16 v73,v192,v45,v35
v_dot2_f32_bf16 v74,v193,v37,v35
v_dot2_f32_bf16 v75,v193,v45,v35
v_dot2_f32_bf16 v76,v194,v37,v35
v_dot2_f32_bf16 v77,v194,v45,v35
v_dot2_f32_bf16 v78,v195,v37,v35
v_dot2_f32_bf16 v79,v195,v45,v35
v_cvt_pk_bf16_f32 v192,v72,v73
v_cvt_pk_bf16_f32 v193,v74,v75
v_cvt_pk_bf16_f32 v194,v76,v77
v_cvt_pk_bf16_f32 v195,v78,v79
ds_write_b128 v31,v[192:195] offset:8192
v_pk_fma_f32 v[16:17],v[72:73],v[72:73],v[16:17]
v_pk_fma_f32 v[16:17],v[74:75],v[74:75],v[16:17]
v_pk_fma_f32 v[16:17],v[76:77],v[76:77],v[16:17]
v_pk_fma_f32 v[16:17],v[78:79],v[78:79],v[16:17]
v_dot2_f32_bf16 v64,v200,v37,v35
v_dot2_f32_bf16 v65,v200,v45,v35
v_dot2_f32_bf16 v66,v201,v37,v35
v_dot2_f32_bf16 v67,v201,v45,v35
v_dot2_f32_bf16 v68,v202,v37,v35
v_dot2_f32_bf16 v69,v202,v45,v35
v_dot2_f32_bf16 v70,v203,v37,v35
v_dot2_f32_bf16 v71,v203,v45,v35
v_cvt_pk_bf16_f32 v200,v64,v65
v_cvt_pk_bf16_f32 v201,v66,v67
v_cvt_pk_bf16_f32 v202,v68,v69
v_cvt_pk_bf16_f32 v203,v70,v71
ds_write_b128 v31,v[200:203] offset:16384
v_pk_fma_f32 v[16:17],v[64:65],v[64:65],v[16:17]
v_pk_fma_f32 v[16:17],v[66:67],v[66:67],v[16:17]
v_pk_fma_f32 v[16:17],v[68:69],v[68:69],v[16:17]
v_pk_fma_f32 v[16:17],v[70:71],v[70:71],v[16:17]
v_dot2_f32_bf16 v72,v208,v37,v35
v_dot2_f32_bf16 v73,v208,v45,v35
v_dot2_f32_bf16 v74,v209,v37,v35
v_dot2_f32_bf16 v75,v209,v45,v35
v_dot2_f32_bf16 v76,v210,v37,v35
v_dot2_f32_bf16 v77,v210,v45,v35
v_dot2_f32_bf16 v78,v211,v37,v35
v_dot2_f32_bf16 v79,v211,v45,v35
v_cvt_pk_bf16_f32 v208,v72,v73
v_cvt_pk_bf16_f32 v209,v74,v75
v_cvt_pk_bf16_f32 v210,v76,v77
v_cvt_pk_bf16_f32 v211,v78,v79
v_pk_fma_f32 v[16:17],v[72:73],v[72:73],v[16:17]
v_pk_fma_f32 v[16:17],v[74:75],v[74:75],v[16:17]
v_pk_fma_f32 v[16:17],v[76:77],v[76:77],v[16:17]
v_pk_fma_f32 v[16:17],v[78:79],v[78:79],v[16:17]
v_dot2_f32_bf16 v64,v216,v37,v35
v_dot2_f32_bf16 v65,v216,v45,v35
v_dot2_f32_bf16 v66,v217,v37,v35
v_dot2_f32_bf16 v67,v217,v45,v35
v_dot2_f32_bf16 v68,v218,v37,v35
v_dot2_f32_bf16 v69,v218,v45,v35
v_dot2_f32_bf16 v70,v219,v37,v35
v_dot2_f32_bf16 v71,v219,v45,v35
v_cvt_pk_bf16_f32 v216,v64,v65
v_cvt_pk_bf16_f32 v217,v66,v67
v_cvt_pk_bf16_f32 v218,v68,v69
v_cvt_pk_bf16_f32 v219,v70,v71
v_pk_fma_f32 v[16:17],v[64:65],v[64:65],v[16:17]
v_pk_fma_f32 v[16:17],v[66:67],v[66:67],v[16:17]
v_pk_fma_f32 v[16:17],v[68:69],v[68:69],v[16:17]
v_pk_fma_f32 v[16:17],v[70:71],v[70:71],v[16:17]
v_dot2_f32_bf16 v72,v224,v37,v35
v_dot2_f32_bf16 v73,v224,v45,v35
v_dot2_f32_bf16 v74,v225,v37,v35
v_dot2_f32_bf16 v75,v225,v45,v35
v_dot2_f32_bf16 v76,v226,v37,v35
v_dot2_f32_bf16 v77,v226,v45,v35
v_dot2_f32_bf16 v78,v227,v37,v35
v_dot2_f32_bf16 v79,v227,v45,v35
v_cvt_pk_bf16_f32 v224,v72,v73
v_cvt_pk_bf16_f32 v225,v74,v75
v_cvt_pk_bf16_f32 v226,v76,v77
v_cvt_pk_bf16_f32 v227,v78,v79
v_pk_fma_f32 v[16:17],v[72:73],v[72:73],v[16:17]
v_pk_fma_f32 v[16:17],v[74:75],v[74:75],v[16:17]
v_pk_fma_f32 v[16:17],v[76:77],v[76:77],v[16:17]
v_pk_fma_f32 v[16:17],v[78:79],v[78:79],v[16:17]
v_dot2_f32_bf16 v64,v232,v37,v35
v_dot2_f32_bf16 v65,v232,v45,v35
v_dot2_f32_bf16 v66,v233,v37,v35
v_dot2_f32_bf16 v67,v233,v45,v35
v_dot2_f32_bf16 v68,v234,v37,v35
v_dot2_f32_bf16 v69,v234,v45,v35
v_dot2_f32_bf16 v70,v235,v37,v35
v_dot2_f32_bf16 v71,v235,v45,v35
v_cvt_pk_bf16_f32 v232,v64,v65
v_cvt_pk_bf16_f32 v233,v66,v67
v_cvt_pk_bf16_f32 v234,v68,v69
v_cvt_pk_bf16_f32 v235,v70,v71
v_pk_fma_f32 v[16:17],v[64:65],v[64:65],v[16:17]
v_pk_fma_f32 v[16:17],v[66:67],v[66:67],v[16:17]
v_pk_fma_f32 v[16:17],v[68:69],v[68:69],v[16:17]
v_pk_fma_f32 v[16:17],v[70:71],v[70:71],v[16:17]
v_dot2_f32_bf16 v72,v240,v37,v35
v_dot2_f32_bf16 v73,v240,v45,v35
v_dot2_f32_bf16 v74,v241,v37,v35
v_dot2_f32_bf16 v75,v241,v45,v35
v_dot2_f32_bf16 v76,v242,v37,v35
v_dot2_f32_bf16 v77,v242,v45,v35
v_dot2_f32_bf16 v78,v243,v37,v35
v_dot2_f32_bf16 v79,v243,v45,v35
v_cvt_pk_bf16_f32 v240,v72,v73
v_cvt_pk_bf16_f32 v241,v74,v75
v_cvt_pk_bf16_f32 v242,v76,v77
v_cvt_pk_bf16_f32 v243,v78,v79
v_pk_fma_f32 v[16:17],v[72:73],v[72:73],v[16:17]
v_pk_fma_f32 v[16:17],v[74:75],v[74:75],v[16:17]
v_pk_fma_f32 v[16:17],v[76:77],v[76:77],v[16:17]
v_pk_fma_f32 v[16:17],v[78:79],v[78:79],v[16:17]
v_dot2_f32_bf16 v64,v248,v37,v35
v_dot2_f32_bf16 v65,v248,v45,v35
v_dot2_f32_bf16 v66,v249,v37,v35
v_dot2_f32_bf16 v67,v249,v45,v35
v_dot2_f32_bf16 v68,v250,v37,v35
v_dot2_f32_bf16 v69,v250,v45,v35
v_dot2_f32_bf16 v70,v251,v37,v35
v_dot2_f32_bf16 v71,v251,v45,v35
v_cvt_pk_bf16_f32 v248,v64,v65
v_cvt_pk_bf16_f32 v249,v66,v67
v_cvt_pk_bf16_f32 v250,v68,v69
v_cvt_pk_bf16_f32 v251,v70,v71
v_pk_fma_f32 v[16:17],v[64:65],v[64:65],v[16:17]
v_pk_fma_f32 v[16:17],v[66:67],v[66:67],v[16:17]
v_pk_fma_f32 v[16:17],v[68:69],v[68:69],v[16:17]
v_pk_fma_f32 v[16:17],v[70:71],v[70:71],v[16:17]
s_waitcnt vmcnt(8)
v_dot2_f32_bf16 v34,v188,v30,0
v_dot2_f32_bf16 v34,v189,v30,v34
v_dot2_f32_bf16 v34,v190,v30,v34
v_dot2_f32_bf16 v34,v191,v30,v34
s_waitcnt vmcnt(7)
v_dot2_f32_bf16 v34,v196,v30,v34
v_dot2_f32_bf16 v34,v197,v30,v34
v_dot2_f32_bf16 v34,v198,v30,v34
v_dot2_f32_bf16 v34,v199,v30,v34
s_waitcnt vmcnt(6)
v_dot2_f32_bf16 v34,v204,v30,v34
v_dot2_f32_bf16 v34,v205,v30,v34
v_dot2_f32_bf16 v34,v206,v30,v34
v_dot2_f32_bf16 v34,v207,v30,v34
s_waitcnt vmcnt(5)
v_dot2_f32_bf16 v34,v212,v30,v34
v_dot2_f32_bf16 v34,v213,v30,v34
v_dot2_f32_bf16 v34,v214,v30,v34
v_dot2_f32_bf16 v34,v215,v30,v34
s_waitcnt vmcnt(4)
v_dot2_f32_bf16 v34,v220,v30,v34
v_dot2_f32_bf16 v34,v221,v30,v34
v_dot2_f32_bf16 v34,v222,v30,v34
v_dot2_f32_bf16 v34,v223,v30,v34
s_waitcnt vmcnt(3)
v_dot2_f32_bf16 v34,v228,v30,v34
v_dot2_f32_bf16 v34,v229,v30,v34
v_dot2_f32_bf16 v34,v230,v30,v34
v_dot2_f32_bf16 v34,v231,v30,v34
s_waitcnt vmcnt(2)
v_dot2_f32_bf16 v34,v236,v30,v34
v_dot2_f32_bf16 v34,v237,v30,v34
v_dot2_f32_bf16 v34,v238,v30,v34
v_dot2_f32_bf16 v34,v239,v30,v34
s_waitcnt vmcnt(1)
v_dot2_f32_bf16 v34,v244,v30,v34
v_dot2_f32_bf16 v34,v245,v30,v34
v_dot2_f32_bf16 v34,v246,v30,v34
v_dot2_f32_bf16 v34,v247,v30,v34
s_waitcnt vmcnt(0)
v_dot2_f32_bf16 v34,v252,v30,v34
v_dot2_f32_bf16 v34,v253,v30,v34
v_dot2_f32_bf16 v34,v254,v30,v34
v_dot2_f32_bf16 v34,v255,v30,v34
s_nop 2
v_add_f32_dpp v34,v34,v34 quad_perm:[1,0,3,2] row_mask:0xf bank_mask:0xf
s_nop 1
v_add_f32_dpp v34,v34,v34 quad_perm:[2,3,0,1] row_mask:0xf bank_mask:0xf
s_nop 1
v_add_f32_dpp v34,v34,v34 row_half_mirror row_mask:0xf bank_mask:0xf
v_mul_f32 v34,0x3ae38e39,v34
v_cvt_pk_bf16_f32 v36,-v34,-v34
v_and_b32 v36,0xffff0000,v36
.p2align 3
v_dot2_f32_bf16 v64,v188,v37,v36
v_dot2_f32_bf16 v65,v188,v45,v36
v_dot2_f32_bf16 v66,v189,v37,v36
v_dot2_f32_bf16 v67,v189,v45,v36
v_dot2_f32_bf16 v68,v190,v37,v36
v_dot2_f32_bf16 v69,v190,v45,v36
v_dot2_f32_bf16 v70,v191,v37,v36
v_dot2_f32_bf16 v71,v191,v45,v36
v_cvt_pk_bf16_f32 v188,v64,v65
v_cvt_pk_bf16_f32 v189,v66,v67
v_cvt_pk_bf16_f32 v190,v68,v69
v_cvt_pk_bf16_f32 v191,v70,v71
ds_write_b128 v32,v[188:191]
v_pk_mul_f32 v[28:29],v[64:65],v[64:65]
v_pk_fma_f32 v[28:29],v[66:67],v[66:67],v[28:29]
v_pk_fma_f32 v[28:29],v[68:69],v[68:69],v[28:29]
v_pk_fma_f32 v[28:29],v[70:71],v[70:71],v[28:29]
v_dot2_f32_bf16 v72,v196,v37,v36
v_dot2_f32_bf16 v73,v196,v45,v36
v_dot2_f32_bf16 v74,v197,v37,v36
v_dot2_f32_bf16 v75,v197,v45,v36
v_dot2_f32_bf16 v76,v198,v37,v36
v_dot2_f32_bf16 v77,v198,v45,v36
v_dot2_f32_bf16 v78,v199,v37,v36
v_dot2_f32_bf16 v79,v199,v45,v36
v_cvt_pk_bf16_f32 v196,v72,v73
v_cvt_pk_bf16_f32 v197,v74,v75
v_cvt_pk_bf16_f32 v198,v76,v77
v_cvt_pk_bf16_f32 v199,v78,v79
ds_write_b128 v32,v[196:199] offset:8192
v_pk_fma_f32 v[28:29],v[72:73],v[72:73],v[28:29]
v_pk_fma_f32 v[28:29],v[74:75],v[74:75],v[28:29]
v_pk_fma_f32 v[28:29],v[76:77],v[76:77],v[28:29]
v_pk_fma_f32 v[28:29],v[78:79],v[78:79],v[28:29]
v_dot2_f32_bf16 v64,v204,v37,v36
v_dot2_f32_bf16 v65,v204,v45,v36
v_dot2_f32_bf16 v66,v205,v37,v36
v_dot2_f32_bf16 v67,v205,v45,v36
v_dot2_f32_bf16 v68,v206,v37,v36
v_dot2_f32_bf16 v69,v206,v45,v36
v_dot2_f32_bf16 v70,v207,v37,v36
v_dot2_f32_bf16 v71,v207,v45,v36
v_cvt_pk_bf16_f32 v204,v64,v65
v_cvt_pk_bf16_f32 v205,v66,v67
v_cvt_pk_bf16_f32 v206,v68,v69
v_cvt_pk_bf16_f32 v207,v70,v71
ds_write_b128 v32,v[204:207] offset:16384
v_pk_fma_f32 v[28:29],v[64:65],v[64:65],v[28:29]
v_pk_fma_f32 v[28:29],v[66:67],v[66:67],v[28:29]
v_pk_fma_f32 v[28:29],v[68:69],v[68:69],v[28:29]
v_pk_fma_f32 v[28:29],v[70:71],v[70:71],v[28:29]
v_dot2_f32_bf16 v72,v212,v37,v36
v_dot2_f32_bf16 v73,v212,v45,v36
v_dot2_f32_bf16 v74,v213,v37,v36
v_dot2_f32_bf16 v75,v213,v45,v36
v_dot2_f32_bf16 v76,v214,v37,v36
v_dot2_f32_bf16 v77,v214,v45,v36
v_dot2_f32_bf16 v78,v215,v37,v36
v_dot2_f32_bf16 v79,v215,v45,v36
v_cvt_pk_bf16_f32 v212,v72,v73
v_cvt_pk_bf16_f32 v213,v74,v75
v_cvt_pk_bf16_f32 v214,v76,v77
v_cvt_pk_bf16_f32 v215,v78,v79
v_pk_fma_f32 v[28:29],v[72:73],v[72:73],v[28:29]
v_pk_fma_f32 v[28:29],v[74:75],v[74:75],v[28:29]
v_pk_fma_f32 v[28:29],v[76:77],v[76:77],v[28:29]
v_pk_fma_f32 v[28:29],v[78:79],v[78:79],v[28:29]
v_dot2_f32_bf16 v64,v220,v37,v36
v_dot2_f32_bf16 v65,v220,v45,v36
v_dot2_f32_bf16 v66,v221,v37,v36
v_dot2_f32_bf16 v67,v221,v45,v36
v_dot2_f32_bf16 v68,v222,v37,v36
v_dot2_f32_bf16 v69,v222,v45,v36
v_dot2_f32_bf16 v70,v223,v37,v36
v_dot2_f32_bf16 v71,v223,v45,v36
v_cvt_pk_bf16_f32 v220,v64,v65
v_cvt_pk_bf16_f32 v221,v66,v67
v_cvt_pk_bf16_f32 v222,v68,v69
v_cvt_pk_bf16_f32 v223,v70,v71
v_pk_fma_f32 v[28:29],v[64:65],v[64:65],v[28:29]
v_pk_fma_f32 v[28:29],v[66:67],v[66:67],v[28:29]
v_pk_fma_f32 v[28:29],v[68:69],v[68:69],v[28:29]
v_pk_fma_f32 v[28:29],v[70:71],v[70:71],v[28:29]
v_dot2_f32_bf16 v72,v228,v37,v36
v_dot2_f32_bf16 v73,v228,v45,v36
v_dot2_f32_bf16 v74,v229,v37,v36
v_dot2_f32_bf16 v75,v229,v45,v36
v_dot2_f32_bf16 v76,v230,v37,v36
v_dot2_f32_bf16 v77,v230,v45,v36
v_dot2_f32_bf16 v78,v231,v37,v36
v_dot2_f32_bf16 v79,v231,v45,v36
v_cvt_pk_bf16_f32 v228,v72,v73
v_cvt_pk_bf16_f32 v229,v74,v75
v_cvt_pk_bf16_f32 v230,v76,v77
v_cvt_pk_bf16_f32 v231,v78,v79
v_pk_fma_f32 v[28:29],v[72:73],v[72:73],v[28:29]
v_pk_fma_f32 v[28:29],v[74:75],v[74:75],v[28:29]
v_pk_fma_f32 v[28:29],v[76:77],v[76:77],v[28:29]
v_pk_fma_f32 v[28:29],v[78:79],v[78:79],v[28:29]
v_dot2_f32_bf16 v64,v236,v37,v36
v_dot2_f32_bf16 v65,v236,v45,v36
v_dot2_f32_bf16 v66,v237,v37,v36
v_dot2_f32_bf16 v67,v237,v45,v36
v_dot2_f32_bf16 v68,v238,v37,v36
v_dot2_f32_bf16 v69,v238,v45,v36
v_dot2_f32_bf16 v70,v239,v37,v36
v_dot2_f32_bf16 v71,v239,v45,v36
v_cvt_pk_bf16_f32 v236,v64,v65
v_cvt_pk_bf16_f32 v237,v66,v67
v_cvt_pk_bf16_f32 v238,v68,v69
v_cvt_pk_bf16_f32 v239,v70,v71
v_pk_fma_f32 v[28:29],v[64:65],v[64:65],v[28:29]
v_pk_fma_f32 v[28:29],v[66:67],v[66:67],v[28:29]
v_pk_fma_f32 v[28:29],v[68:69],v[68:69],v[28:29]
v_pk_fma_f32 v[28:29],v[70:71],v[70:71],v[28:29]
v_dot2_f32_bf16 v72,v244,v37,v36
v_dot2_f32_bf16 v73,v244,v45,v36
v_dot2_f32_bf16 v74,v245,v37,v36
v_dot2_f32_bf16 v75,v245,v45,v36
v_dot2_f32_bf16 v76,v246,v37,v36
v_dot2_f32_bf16 v77,v246,v45,v36
v_dot2_f32_bf16 v78,v247,v37,v36
v_dot2_f32_bf16 v79,v247,v45,v36
v_cvt_pk_bf16_f32 v244,v72,v73
v_cvt_pk_bf16_f32 v245,v74,v75
v_cvt_pk_bf16_f32 v246,v76,v77
v_cvt_pk_bf16_f32 v247,v78,v79
v_pk_fma_f32 v[28:29],v[72:73],v[72:73],v[28:29]
v_pk_fma_f32 v[28:29],v[74:75],v[74:75],v[28:29]
v_pk_fma_f32 v[28:29],v[76:77],v[76:77],v[28:29]
v_pk_fma_f32 v[28:29],v[78:79],v[78:79],v[28:29]
v_dot2_f32_bf16 v64,v252,v37,v36
v_dot2_f32_bf16 v65,v252,v45,v36
v_dot2_f32_bf16 v66,v253,v37,v36
v_dot2_f32_bf16 v67,v253,v45,v36
v_dot2_f32_bf16 v68,v254,v37,v36
v_dot2_f32_bf16 v69,v254,v45,v36
v_dot2_f32_bf16 v70,v255,v37,v36
v_dot2_f32_bf16 v71,v255,v45,v36
v_cvt_pk_bf16_f32 v252,v64,v65
v_cvt_pk_bf16_f32 v253,v66,v67
v_cvt_pk_bf16_f32 v254,v68,v69
v_cvt_pk_bf16_f32 v255,v70,v71
v_pk_fma_f32 v[28:29],v[64:65],v[64:65],v[28:29]
v_pk_fma_f32 v[28:29],v[66:67],v[66:67],v[28:29]
v_pk_fma_f32 v[28:29],v[68:69],v[68:69],v[28:29]
v_pk_fma_f32 v[28:29],v[70:71],v[70:71],v[28:29]
s_setpc_b64 s[4:5]
.p2align 8
.L_code_Y:
.L_code_Y0:
s_waitcnt vmcnt(11) lgkmcnt(0)
s_barrier
qr 184,24,0
qr 96,20,0
qr 100,20,2048
qr 188,25,4096
qr 104,21,0
qr 108,21,2048
qr 192,24,8192
qw 5
qz 48,112,96
qr 196,25,12288
qz 52,184,96
qr 200,24,16384
qr 96,20,8192
qw 7
qz 56,112,100
qr 204,25,20480
qz 60,184,100
qr 100,20,10240
qw 7
qm 48,116,104
qm 52,188,104
qr 104,21,8192
s_add_u32 m0,s13,57344
qw 7
qm 56,116,108
qm 60,188,108
qr 108,21,10240
s_add_u32 s14,s18,896
qw 4
qm 48,120,96
s_waitcnt vmcnt(7) lgkmcnt(3)
s_barrier
qm 52,192,96
qr 96,20,16384
ql 8,20,0
qw 3
qm 56,120,100
qm 60,192,100
qr 100,20,18432
ql 9,20,1024
qw 3
qm 48,124,104
s_add_u32 m0,s13,65536
s_add_u32 s14,s18,1024
qm 52,196,104
qr 104,21,16384
ql 8,20,0
qw 3
qm 56,124,108
qm 60,196,108
qr 108,21,18432
ql 9,20,1024
qw 3
qm 48,128,96
s_add_u32 m0,s13,73728
s_add_u32 s14,s18,36864
qm 52,200,96
qr 96,20,24576
ql 8,20,0
qw 3
qm 56,128,100
qm 60,200,100
qr 100,20,26624
ql 9,20,1024
qw 3
qm 48,132,104
qm 52,204,104
qr 104,21,24576
qw 3
qm 56,132,108
s_waitcnt vmcnt(7)
s_barrier
qr 208,24,24576
qm 60,204,108
qr 212,25,28672
qr 108,21,26624
s_mov_b32 m0,s13
s_add_u32 s14,s18,36992
qw 5
qm 48,136,96
ql 8,20,0
qw 2
qm 52,208,96
qr 216,24,32768
qr 96,20,32768
qm 56,136,100
qr 220,25,36864
ql 9,20,1024
qm 60,208,100
qr 100,20,34816
qm 48,140,104
qr 224,24,40960
s_add_u32 m0,s13,8192
qw 6
qm 52,212,104
qr 228,25,45056
qr 104,21,32768
qw 7
qm 56,140,108
s_add_u32 s14,s18,37120
qm 60,212,108
qr 232,24,49152
qr 108,21,34816
qw 7
qm 48,144,96
qr 236,25,53248
ql 8,20,0
qm 52,216,96
qr 96,20,40960
qw 7
qm 56,144,100
qr 240,24,57344
ql 9,20,1024
qm 60,216,100
qr 244,25,61440
qr 100,20,43008
qw 7
qm 48,148,104
s_add_u32 m0,s13,16384
qm 52,220,104
qr 248,26,8192
qr 104,21,40960
qw 7
qm 56,148,108
qr 252,27,12288
s_add_u32 s14,s18,37248
qm 60,220,108
qr 108,21,43008
qw 7
qm 48,152,96
ql 8,20,0
qm 52,224,96
qr 96,20,49152
qw 5
qm 56,152,100
ql 9,20,1024
qm 60,224,100
qr 100,20,51200
qw 4
qm 48,156,104
s_add_u32 m0,s13,24576
qm 52,228,104
qr 104,21,49152
qw 3
qm 56,156,108
s_waitcnt vmcnt(6)
s_barrier
s_add_u32 s14,s18,37376
qm 60,228,108
qr 108,21,51200
qw 3
qm 48,160,96
ql 8,20,0
qm 52,232,96
qr 96,20,57344
qw 3
qm 56,160,100
ql 9,20,1024
qm 60,232,100
qr 100,20,59392
qw 3
qm 48,164,104
s_add_u32 m0,s13,32768
qm 52,236,104
qr 104,21,57344
qw 3
qm 56,164,108
s_add_u32 s14,s18,37504
qm 60,236,108
qr 108,21,59392
qw 3
qm 48,168,96
ql 8,20,0
qm 52,240,96
qr 96,22,0
qw 3
qm 56,168,100
v_readfirstlane_b32 s3,v13
s_mul_i32 s16,s3,0x9E3779B1
s_add_u32 s16,s16,0x7F4A7C15
ql 9,20,1024
qm 60,240,100
qr 100,22,2048
qw 3
qm 48,172,104
s_add_u32 m0,s13,40960
qm 52,244,104
qr 104,23,0
qw 3
qm 56,172,108
s_add_u32 s14,s18,37632
qm 60,244,108
qr 108,23,2048
qw 3
qm 48,176,96
ql 8,20,0
qm 52,248,96
qr 96,22,8192
qw 3
qm 56,176,100
ql 9,20,1024
qm 60,248,100
qr 100,22,10240
qw 3
qm 48,180,104
s_add_u32 m0,s13,81920
qm 52,252,104
qr 104,23,8192
qw 3
qm 56,180,108
s_waitcnt vmcnt(6)
s_barrier
s_add_u32 s14,s18,37760
qm 60,252,108
qr 108,23,10240
qw 3
qz 64,112,96
v_lshrrev_b32 v42,4,v0
v_and_b32 v43,15,v0
v_bfe_u32 v45,v0,2,2
ql 8,20,0
qz 68,184,96
v_lshl_add_u32 v36,s7,2,v42
v_lshl_add_u32 v45,s7,2,v45
qr 96,20,0
qw 3
qz 72,112,100
v_lshlrev_b32 v42,5,v36
v_and_b32 v14,3,v0
ql 9,20,1024
qz 76,184,100
v_lshlrev_b32 v37,3,v43
v_lshl_add_u32 v45,v45,3,v14
qr 100,20,2048
qw 3
qm 64,116,104
v_lshl_add_u32 v43,v36,9,v37
v_lshlrev_b32 v45,2,v45
s_lshl_b32 s30,s10,3
s_add_u32 s30,s30,s8
s_mul_i32 s30,s30,196608
s_add_u32 s76,s30,21118976
s_add_u32 s77,s30,21020672
s_lshl_b32 s30,s7,13
s_add_u32 s79,s76,s30
s_add_u32 m0,s13,90112
qm 68,188,104
v_add_u32 v42,122880,v42
v_add_u32 v45,122880,v45
qr 104,21,0
qw 3
qm 72,116,108
v_add_u32 v37,124928,v37
ds_read_b32 v10,v45
s_add_u32 s14,s18,37888
qm 76,188,108
v_add_u32 v43,114688,v43
ds_read_b32 v11,v45 offset:16
qr 108,21,2048
qw 5
qm 64,120,96
qr 28,42,0
ql 8,20,0
qm 68,192,96
qr 32,42,16
qr 96,20,8192
qw 7
qm 72,120,100
qd 36,37,0
ql 9,20,1024
qm 76,192,100
s_nop 0
qr 100,20,10240
qw 8
qm 64,124,104
s_nop 0
qw 7
v_mul_f32 v14,v10,v10
s_add_u32 m0,s13,98304
qm 68,196,104
s_nop 0
v_mul_f32 v12,0xbdd2d3e8,v10
qr 104,21,8192
qw 6
qm 72,124,108
qw 5
v_mul_f32 v48,v48,v28
v_mul_f32 v10,0xc0135761,v10
s_mov_b32 s14,s17
qm 76,196,108
v_mul_f32 v56,v56,v28
v_mul_f32 v12,v12,v14
qr 108,21,10240
qw 4
qm 64,128,96
v_mov_b32 v38,v48
v_rcp_f32 v14,v14
ql 6,40,0
qm 68,200,96
v_mov_b32 v39,v56
v_cvt_pk_bf16_f32 v48,v48,v56
v_mul_f32 v15,v11,v11
qr 96,20,16384
qw 3
qm 72,128,100
v_mul_f32 v49,v49,v29
v_mul_f32 v13,0xbdd2d3e8,v11
ql 7,40,1024
qm 76,200,100
v_mul_f32 v57,v57,v29
v_mul_f32 v11,0xc0135761,v11
qr 100,20,18432
qw 3
qm 64,132,104
v_fma_f32 v40,v36,v38,v49
v_mul_f32 v13,v13,v15
s_add_u32 m0,s13,106496
qm 68,204,104
v_fma_f32 v41,v36,v39,v57
v_rcp_f32 v15,v15
qr 104,21,16384
qw 3
qm 72,132,108
s_waitcnt vmcnt(6)
s_barrier
v_fma_f32 v40,-v37,v39,v40
s_add_u32 s14,s17,589824
qm 76,204,108
v_fma_f32 v41,v37,v38,v41
qr 108,21,18432
qw 3
qm 64,136,96
v_cvt_pk_bf16_f32 v49,v49,v57
ql 6,40,0
qm 68,208,96
v_mul_f32 v50,v50,v30
qr 96,20,24576
qw 3
qm 72,136,100
v_mul_f32 v58,v58,v30
ql 7,40,1024
qm 76,208,100
v_fma_f32 v38,v36,v40,v50
qr 100,20,26624
qw 3
qm 64,140,104
v_fma_f32 v39,v36,v41,v58
s_add_u32 m0,s13,49152
qm 68,212,104
v_fma_f32 v38,-v37,v41,v38
qr 104,21,24576
qw 3
qm 72,140,108
v_fma_f32 v39,v37,v40,v39
s_add_u32 s14,s17,1179648
qm 76,212,108
v_cvt_pk_bf16_f32 v50,v50,v58
qr 108,21,26624
qw 3
qm 64,144,96
v_mul_f32 v51,v51,v31
ql 6,40,0
qm 68,216,96
v_mul_f32 v59,v59,v31
qr 96,20,32768
qw 3
qm 72,144,100
v_fma_f32 v40,v36,v38,v51
v_fma_f32 v41,v36,v39,v59
ql 7,40,1024
qm 76,216,100
v_fma_f32 v40,-v37,v39,v40
qr 100,20,34816
qw 3
qm 64,148,104
v_fma_f32 v41,v37,v38,v41
s_add_u32 m0,s13,57344
qm 68,220,104
v_cvt_pk_bf16_f32 v51,v51,v59
qr 104,21,32768
qw 3
qm 72,148,108
ds_write_b128 v18,v[48:51] offset:49152
s_add_u32 s14,s17,1769472
qm 76,220,108
v_mul_f32 v52,v52,v32
qr 108,21,34816
qw 4
qm 64,152,96
v_mul_f32 v60,v60,v32
ql 6,40,0
qm 68,224,96
v_fma_f32 v38,v36,v40,v52
qr 96,20,40960
qw 4
qm 72,152,100
v_fma_f32 v39,v36,v41,v60
ql 7,40,1024
qm 76,224,100
v_fma_f32 v38,-v37,v41,v38
qr 100,20,43008
qw 4
qm 64,156,104
v_fma_f32 v39,v37,v40,v39
s_add_u32 m0,s13,65536
qm 68,228,104
v_cvt_pk_bf16_f32 v52,v52,v60
qr 104,21,40960
qw 3
qm 72,156,108
s_waitcnt vmcnt(6)
s_barrier
v_mul_f32 v53,v53,v33
s_add_u32 s14,s17,2359296
qm 76,228,108
v_mul_f32 v61,v61,v33
qr 108,21,43008
qw 3
qm 64,160,96
v_fma_f32 v40,v36,v38,v53
ql 6,40,0
qm 68,232,96
v_fma_f32 v41,v36,v39,v61
qr 96,22,16384
qw 3
qm 72,160,100
v_fma_f32 v40,-v37,v39,v40
ql 7,40,1024
qm 76,232,100
v_fma_f32 v41,v37,v38,v41
v_cvt_pk_bf16_f32 v53,v53,v61
qr 100,22,18432
qw 3
qm 64,164,104
v_mul_f32 v54,v54,v34
s_add_u32 m0,s13,73728
qm 68,236,104
v_mul_f32 v62,v62,v34
qr 104,23,16384
qw 3
qm 72,164,108
v_fma_f32 v38,v36,v40,v54
s_add_u32 s14,s17,2949120
qm 76,236,108
v_fma_f32 v39,v36,v41,v62
qr 108,23,18432
qw 3
qm 64,168,96
v_fma_f32 v38,-v37,v41,v38
ql 6,40,0
qm 68,240,96
v_fma_f32 v39,v37,v40,v39
qr 96,22,24576
qw 3
qm 72,168,100
v_cvt_pk_bf16_f32 v54,v54,v62
ql 7,40,1024
qm 76,240,100
v_mul_f32 v55,v55,v35
qr 100,22,26624
qw 3
qm 64,172,104
v_mul_f32 v63,v63,v35
s_mov_b32 m0,s13
qm 68,244,104
v_fma_f32 v40,v36,v38,v55
qr 104,23,24576
qw 3
qm 72,172,108
v_fma_f32 v41,v36,v39,v63
s_add_u32 s14,s17,3538944
qm 76,244,108
v_fma_f32 v40,-v37,v39,v40
qr 108,23,26624
qw 3
qm 64,176,96
v_fma_f32 v41,v37,v38,v41
ql 6,40,0
qm 68,248,96
v_cvt_pk_bf16_f32 v55,v55,v63
qt 96,4,32768
qw 3
qm 72,176,100
ds_write_b128 v18,v[52:55] offset:50176
qt 98,4,33280
ql 7,40,1024
qm 76,248,100
ds_write_b64 v43,v[40:41] offset:0
qt 100,5,32768
qw 6
qm 64,180,104
qt 102,5,33280
s_add_u32 m0,s13,81920
qm 68,252,104
qt 104,4,36864
qw 7
qm 72,180,108
s_waitcnt vmcnt(6) lgkmcnt(3)
s_barrier
qt 106,4,37376
s_add_u32 s14,s17,4128768
qm 76,252,108
qt 108,5,36864
qz 48,96,112
v_lshrrev_b32 v42,4,v0
v_and_b32 v43,15,v0
qt 110,5,37376
ql 6,40,0
qz 52,96,184
v_lshl_add_u32 v36,s7,2,v42
v_lshlrev_b32 v42,5,v36
qt 96,4,40960
qw 5
qz 56,100,112
v_lshlrev_b32 v37,3,v43
qt 98,4,41472
ql 7,40,1024
qz 60,100,184
v_lshl_add_u32 v43,v36,9,v37
v_add_u32 v42,122880,v42
qt 100,5,40960
qw 5
qm 48,104,116
v_add_u32 v37,124928,v37
v_add_u32 v43,114688,v43
qt 102,5,41472
s_add_u32 m0,s13,90112
qm 52,104,188
qr 28,42,0
qt 104,4,45056
qw 6
qm 56,108,116
qr 32,42,16
qd 36,37,256
qt 106,4,45568
s_add_u32 s14,s17,4718592
qm 60,108,188
s_nop 0
s_nop 0
qt 108,5,45056
qw 8
qm 48,96,120
s_nop 0
qt 110,5,45568
ql 6,40,0
qm 52,96,192
qw 6
v_mul_f32 v64,v64,v28
v_mul_f32 v72,v72,v28
qt 96,2,49152
qm 56,100,120
v_mov_b32 v38,v64
qt 98,2,49664
ql 7,40,1024
qm 60,100,192
v_mov_b32 v39,v72
v_cvt_pk_bf16_f32 v64,v64,v72
qt 100,3,49152
qw 5
qm 48,104,124
v_mul_f32 v65,v65,v29
v_mul_f32 v73,v73,v29
qt 102,3,49664
s_add_u32 m0,s13,8192
qm 52,104,196
v_fma_f32 v40,v36,v38,v65
qt 104,2,53248
qw 5
qm 56,108,124
v_fma_f32 v41,v36,v39,v73
v_fma_f32 v40,-v37,v39,v40
qt 106,2,53760
s_mov_b32 s14,s17
qm 60,108,196
v_fma_f32 v41,v37,v38,v41
v_cvt_pk_bf16_f32 v65,v65,v73
qt 108,3,53248
qw 5
qm 48,96,128
v_mul_f32 v66,v66,v30
qt 110,3,53760
ql 6,44,0
qm 52,96,200
v_mul_f32 v74,v74,v30
v_fma_f32 v38,v36,v40,v66
qt 96,2,57344
qw 5
qm 56,100,128
v_fma_f32 v39,v36,v41,v74
v_fma_f32 v38,-v37,v41,v38
qt 98,2,57856
ql 7,44,1024
qm 60,100,200
v_fma_f32 v39,v37,v40,v39
qt 100,3,57344
qw 5
qm 48,104,132
v_cvt_pk_bf16_f32 v66,v66,v74
v_mul_f32 v67,v67,v31
qt 102,3,57856
s_add_u32 m0,s13,98304
qm 52,104,204
v_mul_f32 v75,v75,v31
qt 104,2,61440
qw 5
qm 56,108,132
s_waitcnt vmcnt(6)
s_barrier
v_fma_f32 v40,v36,v38,v67
v_fma_f32 v41,v36,v39,v75
qt 106,2,61952
s_add_u32 s14,s17,589824
qm 60,108,204
v_fma_f32 v40,-v37,v39,v40
v_fma_f32 v41,v37,v38,v41
qt 108,3,61440
qw 5
qm 48,96,136
v_cvt_pk_bf16_f32 v67,v67,v75
qt 110,3,61952
ql 6,44,0
qm 52,96,208
ds_write_b128 v19,v[64:67]
v_mul_f32 v68,v68,v32
qt 96,4,0
qw 6
qm 56,100,136
v_mul_f32 v76,v76,v32
v_fma_f32 v38,v36,v40,v68
qt 98,4,512
ql 7,44,1024
qm 60,100,208
v_fma_f32 v39,v36,v41,v76
qt 100,5,0
qw 6
qm 48,104,140
v_fma_f32 v38,-v37,v41,v38
v_fma_f32 v39,v37,v40,v39
qt 102,5,512
s_add_u32 m0,s13,106496
qm 52,104,212
v_cvt_pk_bf16_f32 v68,v68,v76
v_mul_f32 v69,v69,v33
qt 104,4,4096
qw 6
qm 56,108,140
v_mul_f32 v77,v77,v33
qt 106,4,4608
s_add_u32 s14,s17,1179648
qm 60,108,212
v_fma_f32 v40,v36,v38,v69
v_fma_f32 v41,v36,v39,v77
qt 108,5,4096
qw 5
qm 48,96,144
v_fma_f32 v40,-v37,v39,v40
qt 110,5,4608
ql 6,44,0
qm 52,96,216
v_fma_f32 v41,v37,v38,v41
v_cvt_pk_bf16_f32 v69,v69,v77
qt 96,4,8192
qw 5
qm 56,100,144
v_mul_f32 v70,v70,v34
v_mul_f32 v78,v78,v34
qt 98,4,8704
ql 7,44,1024
qm 60,100,216
v_fma_f32 v38,v36,v40,v70
qt 100,5,8192
qw 5
qm 48,104,148
v_fma_f32 v39,v36,v41,v78
v_fma_f32 v38,-v37,v41,v38
qt 102,5,8704
s_add_u32 m0,s13,16384
qm 52,104,220
v_fma_f32 v39,v37,v40,v39
v_cvt_pk_bf16_f32 v70,v70,v78
qt 104,4,12288
qw 5
qm 56,108,148
v_mul_f32 v71,v71,v35
qt 106,4,12800
s_add_u32 s14,s17,1769472
qm 60,108,220
v_mul_f32 v79,v79,v35
v_fma_f32 v40,v36,v38,v71
qt 108,5,12288
qw 5
qm 48,96,152
v_fma_f32 v41,v36,v39,v79
v_fma_f32 v40,-v37,v39,v40
qt 110,5,12800
ql 6,44,0
qm 52,96,224
v_fma_f32 v41,v37,v38,v41
qt 96,2,0
qw 5
qm 56,100,152
v_cvt_pk_bf16_f32 v71,v71,v79
ds_write_b128 v19,v[68:71] offset:1024
qt 98,2,512
ql 7,44,1024
qm 60,100,224
ds_write_b64 v43,v[40:41] offset:256
qt 100,3,0
qw 7
qm 48,104,156
qt 102,3,512
s_add_u32 m0,s13,24576
qm 52,104,228
qt 104,2,4096
qw 7
qm 56,108,156
s_waitcnt vmcnt(6) lgkmcnt(3)
s_barrier
qt 106,2,4608
s_add_u32 s14,s17,2359296
qm 60,108,228
qt 108,3,4096
qm 48,96,160
qt 110,3,4608
ql 6,44,0
qm 52,96,232
qt 96,4,16384
qw 5
qm 56,100,160
qt 98,4,16896
ql 7,44,1024
qm 60,100,232
qt 100,5,16384
qw 5
qm 48,104,164
qt 102,5,16896
s_add_u32 m0,s13,32768
qm 52,104,236
qt 104,4,20480
qw 5
qm 56,108,164
qt 106,4,20992
s_add_u32 s14,s17,2949120
qm 60,108,236
qt 108,5,20480
qw 5
qm 48,96,168
qt 110,5,20992
ql 6,44,0
qm 52,96,240
qt 96,4,24576
qw 5
qm 56,100,168
qt 98,4,25088
ql 7,44,1024
qm 60,100,240
qt 100,5,24576
qw 5
qm 48,104,172
qt 102,5,25088
s_add_u32 m0,s13,40960
qm 52,104,244
qt 104,4,28672
qw 5
qm 56,108,172
qt 106,4,29184
s_add_u32 s14,s17,3538944
qm 60,108,244
qt 108,5,28672
qw 5
qm 48,96,176
qt 110,5,29184
ql 6,44,0
qm 52,96,248
qt 96,2,8192
qw 5
qm 56,100,176
qt 98,2,8704
ql 7,44,1024
qm 60,100,248
qt 100,3,8192
qw 5
qm 48,104,180
qt 102,3,8704
s_add_u32 m0,s13,81920
qm 52,104,252
qt 104,2,12288
qw 5
qm 56,108,180
s_waitcnt vmcnt(6)
s_barrier
qt 106,2,12800
s_add_u32 s14,s17,4128768
qm 60,108,252
qt 108,3,12288
qw 5
qz 80,96,112
qt 110,3,12800
ql 6,44,0
qz 84,96,184
v_mul_f32 v16,v48,v48
qt 96,4,32768
qw 5
qz 88,100,112
v_fma_f32 v16,v16,v12,v10
qt 98,4,33280
ql 7,44,1024
qz 92,100,184
v_mul_f32 v16,v16,v48
qt 100,5,32768
qw 5
qm 80,104,116
v_exp_f32 v16,v16
qt 102,5,33280
s_add_u32 m0,s13,90112
qm 84,104,188
v_fma_f32 v16,v16,v14,v14
qt 104,4,36864
qw 5
qm 88,108,116
v_rcp_f32 v16,v16
qt 106,4,37376
s_add_u32 s14,s17,4718592
qm 92,108,188
v_mul_f32 v48,v48,v16
qt 108,5,36864
qw 5
qm 80,96,120
v_mul_f32 v16,v49,v49
qt 110,5,37376
ql 6,44,0
qm 84,96,192
v_fma_f32 v16,v16,v12,v10
qt 96,4,40960
qw 5
qm 88,100,120
v_mul_f32 v16,v16,v49
qt 98,4,41472
ql 7,44,1024
qm 92,100,192
v_exp_f32 v16,v16
qt 100,5,40960
qw 5
qm 80,104,124
v_fma_f32 v16,v16,v14,v14
qt 102,5,41472
s_add_u32 m0,s13,49152
qm 84,104,196
v_rcp_f32 v16,v16
qt 104,4,45056
qw 5
qm 88,108,124
v_mul_f32 v49,v49,v16
qt 106,4,45568
s_add_u32 s14,s17,128
qm 92,108,196
v_mul_f32 v16,v50,v50
qt 108,5,45056
qw 5
qm 80,96,128
v_fma_f32 v16,v16,v12,v10
qt 110,5,45568
ql 6,40,0
qm 84,96,200
v_mul_f32 v16,v16,v50
qt 96,2,16384
qw 5
qm 88,100,128
v_exp_f32 v16,v16
qt 98,2,16896
ql 7,40,1024
qm 92,100,200
v_fma_f32 v16,v16,v14,v14
qt 100,3,16384
qw 5
qm 80,104,132
v_rcp_f32 v16,v16
qt 102,3,16896
s_add_u32 m0,s13,98304
qm 84,104,204
v_mul_f32 v50,v50,v16
qt 104,2,20480
qw 5
qm 88,108,132
s_waitcnt vmcnt(6)
s_barrier
v_mul_f32 v16,v51,v51
qt 106,2,20992
s_add_u32 s14,s17,589952
qm 92,108,204
v_fma_f32 v16,v16,v12,v10
qt 108,3,20480
qw 5
qm 80,96,136
v_mul_f32 v16,v16,v51
qt 110,3,20992
ql 6,40,0
qm 84,96,208
v_exp_f32 v16,v16
qt 96,2,24576
qw 5
qm 88,100,136
v_fma_f32 v16,v16,v14,v14
qt 98,2,25088
ql 7,40,1024
qm 92,100,208
v_rcp_f32 v16,v16
qt 100,3,24576
qw 5
qm 80,104,140
v_mul_f32 v51,v51,v16
qt 102,3,25088
s_add_u32 m0,s13,106496
qm 84,104,212
v_mul_f32 v16,v56,v56
qt 104,2,28672
qw 5
qm 88,108,140
v_fma_f32 v16,v16,v12,v10
qt 106,2,29184
s_add_u32 s14,s17,1179776
qm 92,108,212
v_mul_f32 v16,v16,v56
qt 108,3,28672
qw 5
qm 80,96,144
v_exp_f32 v16,v16
qt 110,3,29184
ql 6,40,0
qm 84,96,216
v_fma_f32 v16,v16,v14,v14
qt 96,2,32768
qw 5
qm 88,100,144
v_rcp_f32 v16,v16
qt 98,2,33280
ql 7,40,1024
qm 92,100,216
v_mul_f32 v56,v56,v16
qt 100,3,32768
qw 5
qm 80,104,148
v_mul_f32 v16,v57,v57
qt 102,3,33280
s_add_u32 m0,s13,57344
qm 84,104,220
v_fma_f32 v16,v16,v12,v10
qt 104,2,36864
qw 5
qm 88,108,148
v_mul_f32 v16,v16,v57
qt 106,2,37376
s_add_u32 s14,s17,1769600
qm 92,108,220
v_exp_f32 v16,v16
qt 108,3,36864
qw 5
qm 80,96,152
v_fma_f32 v16,v16,v14,v14
qt 110,3,37376
ql 6,40,0
qm 84,96,224
v_rcp_f32 v16,v16
qt 96,2,40960
qw 5
qm 88,100,152
v_mul_f32 v57,v57,v16
qt 98,2,41472
ql 7,40,1024
qm 92,100,224
v_mul_f32 v16,v58,v58
qt 100,3,40960
qw 5
qm 80,104,156
v_fma_f32 v16,v16,v12,v10
qt 102,3,41472
s_add_u32 m0,s13,65536
qm 84,104,228
v_mul_f32 v16,v16,v58
qt 104,2,45056
qw 5
qm 88,108,156
s_waitcnt vmcnt(6)
s_barrier
v_exp_f32 v16,v16
qt 106,2,45568
s_add_u32 s14,s17,2359424
qm 92,108,228
v_fma_f32 v16,v16,v14,v14
qt 108,3,45056
qw 5
qm 80,96,160
v_rcp_f32 v16,v16
qt 110,3,45568
ql 6,40,0
qm 84,96,232
v_mul_f32 v58,v58,v16
qt 96,4,16384
qw 5
qm 88,100,160
v_mul_f32 v16,v59,v59
qt 98,4,16896
ql 7,40,1024
qm 92,100,232
v_fma_f32 v16,v16,v12,v10
qt 100,5,16384
qw 5
qm 80,104,164
v_mul_f32 v16,v16,v59
qt 102,5,16896
s_add_u32 m0,s13,73728
qm 84,104,236
v_exp_f32 v16,v16
qt 104,4,20480
qw 5
qm 88,108,164
v_fma_f32 v16,v16,v14,v14
qt 106,4,20992
s_add_u32 s14,s17,2949248
qm 92,108,236
v_rcp_f32 v16,v16
qt 108,5,20480
qw 5
qm 80,96,168
v_mul_f32 v59,v59,v16
qt 110,5,20992
ql 6,40,0
qm 84,96,240
v_mul_f32 v16,v52,v52
qt 96,4,24576
qw 5
qm 88,100,168
v_fma_f32 v16,v16,v13,v11
qt 98,4,25088
ql 7,40,1024
qm 92,100,240
v_mul_f32 v16,v16,v52
qt 100,5,24576
qw 5
qm 80,104,172
v_exp_f32 v16,v16
qt 102,5,25088
s_mov_b32 m0,s13
qm 84,104,244
v_fma_f32 v16,v16,v15,v15
qt 104,4,28672
qw 5
qm 88,108,172
v_rcp_f32 v16,v16
qt 106,4,29184
s_add_u32 s14,s17,3539072
qm 92,108,244
v_mul_f32 v52,v52,v16
qt 108,5,28672
qw 5
qm 80,96,176
v_mul_f32 v16,v53,v53
qt 110,5,29184
ql 6,40,0
qm 84,96,248
v_fma_f32 v16,v16,v13,v11
qt 96,2,49152
qw 5
qm 88,100,176
v_mul_f32 v16,v16,v53
qt 98,2,49664
ql 7,40,1024
qm 92,100,248
v_exp_f32 v16,v16
qt 100,3,49152
qw 5
qm 80,104,180
v_fma_f32 v16,v16,v15,v15
qt 102,3,49664
s_add_u32 m0,s13,8192
qm 84,104,252
v_rcp_f32 v16,v16
qt 104,2,53248
qw 5
qm 88,108,180
s_waitcnt vmcnt(6)
s_barrier
v_mul_f32 v53,v53,v16
qt 106,2,53760
s_add_u32 s14,s17,4128896
qm 92,108,252
qt 108,3,53248
qw 5
qz 64,96,112
v_mul_f32 v16,v54,v54
qt 110,3,53760
ql 6,40,0
qz 68,96,184
v_fma_f32 v16,v16,v13,v11
qt 96,4,32768
qw 5
qz 72,100,112
v_mul_f32 v16,v16,v54
qt 98,4,33280
ql 7,40,1024
qz 76,100,184
v_exp_f32 v16,v16
qt 100,5,32768
qw 5
qm 64,104,116
v_fma_f32 v16,v16,v15,v15
qt 102,5,33280
s_add_u32 m0,s13,16384
qm 68,104,188
v_rcp_f32 v16,v16
qt 104,4,36864
qw 5
qm 72,108,116
v_mul_f32 v54,v54,v16
qt 106,4,37376
s_add_u32 s14,s17,4718720
qm 76,108,188
v_mul_f32 v16,v55,v55
qt 108,5,36864
qw 5
qm 64,96,120
v_fma_f32 v16,v16,v13,v11
qt 110,5,37376
ql 6,40,0
qm 68,96,192
v_mul_f32 v16,v16,v55
qt 96,4,40960
qw 5
qm 72,100,120
v_exp_f32 v16,v16
qt 98,4,41472
ql 7,40,1024
qm 76,100,192
v_fma_f32 v16,v16,v15,v15
qt 100,5,40960
qw 5
qm 64,104,124
v_rcp_f32 v16,v16
qt 102,5,41472
s_add_u32 m0,s13,24576
qm 68,104,196
v_mul_f32 v55,v55,v16
qt 104,4,45056
qw 5
qm 72,108,124
v_mul_f32 v16,v60,v60
qt 106,4,45568
s_add_u32 s14,s17,128
qm 76,108,196
v_fma_f32 v16,v16,v13,v11
qt 108,5,45056
qw 5
qm 64,96,128
v_mul_f32 v16,v16,v60
qt 110,5,45568
ql 6,44,0
qm 68,96,200
v_exp_f32 v16,v16
qt 96,2,57344
qw 5
qm 72,100,128
v_fma_f32 v16,v16,v15,v15
qt 98,2,57856
ql 7,44,1024
qm 76,100,200
v_rcp_f32 v16,v16
qt 100,3,57344
qw 5
qm 64,104,132
v_mul_f32 v60,v60,v16
qt 102,3,57856
s_add_u32 m0,s13,98304
qm 68,104,204
)ASM"
R"ASM(v_mul_f32 v16,v61,v61
qt 104,2,61440
qw 5
qm 72,108,132
s_waitcnt vmcnt(6)
s_barrier
v_fma_f32 v16,v16,v13,v11
qt 106,2,61952
s_add_u32 s14,s17,589952
qm 76,108,204
v_mul_f32 v16,v16,v61
qt 108,3,61440
qw 5
qm 64,96,136
v_exp_f32 v16,v16
qt 110,3,61952
ql 6,44,0
qm 68,96,208
v_fma_f32 v16,v16,v15,v15
qt 96,4,0
qw 5
qm 72,100,136
v_rcp_f32 v16,v16
qt 98,4,512
ql 7,44,1024
qm 76,100,208
v_mul_f32 v61,v61,v16
qt 100,5,0
qw 5
qm 64,104,140
v_mul_f32 v16,v62,v62
qt 102,5,512
s_add_u32 m0,s13,106496
qm 68,104,212
v_fma_f32 v16,v16,v13,v11
qt 104,4,4096
qw 5
qm 72,108,140
v_mul_f32 v16,v16,v62
qt 106,4,4608
s_add_u32 s14,s17,1179776
qm 76,108,212
v_exp_f32 v16,v16
qt 108,5,4096
qw 5
qm 64,96,144
v_fma_f32 v16,v16,v15,v15
qt 110,5,4608
ql 6,44,0
qm 68,96,216
v_rcp_f32 v16,v16
qt 96,4,8192
qw 5
qm 72,100,144
v_mul_f32 v62,v62,v16
qt 98,4,8704
ql 7,44,1024
qm 76,100,216
v_mul_f32 v16,v63,v63
qt 100,5,8192
qw 5
qm 64,104,148
v_fma_f32 v16,v16,v13,v11
qt 102,5,8704
s_add_u32 m0,s13,32768
qm 68,104,220
v_mul_f32 v16,v16,v63
qt 104,4,12288
qw 5
qm 72,108,148
v_exp_f32 v16,v16
qt 106,4,12800
s_add_u32 s14,s17,1769600
qm 76,108,220
v_fma_f32 v16,v16,v15,v15
qt 108,5,12288
qw 5
qm 64,96,152
v_rcp_f32 v16,v16
qt 110,5,12800
ql 6,44,0
qm 68,96,224
v_mul_f32 v63,v63,v16
qt 96,2,0
qw 5
qm 72,100,152
v_mul_f32 v48,v48,v80
qt 98,2,512
ql 7,44,1024
qm 76,100,224
v_mul_f32 v49,v49,v81
qt 100,3,0
qw 5
qm 64,104,156
v_cvt_pk_bf16_f32 v48,v48,v49
qt 102,3,512
s_add_u32 m0,s13,40960
qm 68,104,228
v_mul_f32 v50,v50,v82
qt 104,2,4096
qw 5
qm 72,108,156
s_waitcnt vmcnt(6)
s_barrier
v_mul_f32 v51,v51,v83
qt 106,2,4608
s_add_u32 s14,s17,2359424
qm 76,108,228
v_cvt_pk_bf16_f32 v49,v50,v51
qt 108,3,4096
qw 5
qm 64,96,160
v_mul_f32 v56,v56,v88
qt 110,3,4608
ql 6,44,0
qm 68,96,232
v_mul_f32 v57,v57,v89
qt 96,2,8192
qw 5
qm 72,100,160
v_cvt_pk_bf16_f32 v50,v56,v57
qt 98,2,8704
ql 7,44,1024
qm 76,100,232
v_mul_f32 v58,v58,v90
qt 100,3,8192
qw 5
qm 64,104,164
v_mul_f32 v59,v59,v91
qt 102,3,8704
s_add_u32 m0,s13,49152
qm 68,104,236
v_cvt_pk_bf16_f32 v51,v58,v59
qt 104,2,12288
qw 5
qm 72,108,164
ds_write_b128 v18,v[48:51]
v_mul_f32 v52,v52,v84
qt 106,2,12800
s_add_u32 s14,s17,2949248
qm 76,108,236
s_add_u32 s15,s76,0
buffer_store_dwordx4 v[48:51],v18,s[88:91],s15 offen offset:0 sc1
v_mul_f32 v53,v53,v85
qt 108,3,12288
qw 6
qm 64,96,168
v_cvt_pk_bf16_f32 v52,v52,v53
qt 110,3,12800
ql 6,44,0
qm 68,96,240
v_mul_f32 v54,v54,v86
qt 96,2,16384
qw 6
qm 72,100,168
v_mul_f32 v55,v55,v87
qt 98,2,16896
ql 7,44,1024
qm 76,100,240
v_cvt_pk_bf16_f32 v53,v54,v55
qt 100,3,16384
qw 5
qm 64,104,172
v_mul_f32 v60,v60,v92
qt 102,3,16896
s_add_u32 m0,s13,57344
qm 68,104,244
v_mul_f32 v61,v61,v93
qt 104,2,20480
qw 5
qm 72,108,172
v_cvt_pk_bf16_f32 v54,v60,v61
qt 106,2,20992
s_add_u32 s14,s17,3539072
qm 76,108,244
v_mul_f32 v62,v62,v94
qt 108,3,20480
qw 5
qm 64,96,176
v_mul_f32 v63,v63,v95
qt 110,3,20992
ql 6,44,0
qm 68,96,248
v_cvt_pk_bf16_f32 v55,v62,v63
qt 96,2,24576
qw 5
qm 72,100,176
ds_write_b128 v18,v[52:55] offset:1024
qt 98,2,25088
ql 7,44,1024
qm 76,100,248
s_add_u32 s15,s76,0
buffer_store_dwordx4 v[52:55],v18,s[88:91],s15 offen offset:1024 sc1
qt 100,3,24576
qw 6
qm 64,104,180
qt 102,3,25088
s_add_u32 m0,s13,65536
qm 68,104,252
qt 104,2,28672
qw 6
qm 72,108,180
s_waitcnt vmcnt(8) lgkmcnt(4)
s_barrier
qt 106,2,29184
s_add_u32 s14,s17,4128896
qm 76,108,252
qt 108,3,28672
qw 5
qz 80,96,112
qt 110,3,29184
ql 6,44,0
qz 84,96,184
v_mul_f32 v16,v64,v64
qt 96,4,32768
qw 5
qz 88,100,112
v_fma_f32 v16,v16,v12,v10
qt 98,4,33280
ql 7,44,1024
qz 92,100,184
v_mul_f32 v16,v16,v64
qt 100,5,32768
qw 5
qm 80,104,116
v_exp_f32 v16,v16
qt 102,5,33280
s_add_u32 m0,s13,73728
qm 84,104,188
v_fma_f32 v16,v16,v14,v14
qt 104,4,36864
qw 5
qm 88,108,116
v_rcp_f32 v16,v16
qt 106,4,37376
s_add_u32 s14,s17,4718720
qm 92,108,188
v_mul_f32 v64,v64,v16
qt 108,5,36864
qw 5
qm 80,96,120
v_mul_f32 v16,v65,v65
qt 110,5,37376
ql 6,44,0
qm 84,96,192
v_fma_f32 v16,v16,v12,v10
qt 96,4,40960
qw 5
qm 88,100,120
v_mul_f32 v16,v16,v65
qt 98,4,41472
ql 7,44,1024
qm 92,100,192
v_exp_f32 v16,v16
qt 100,5,40960
qw 5
qm 80,104,124
v_fma_f32 v16,v16,v14,v14
qt 102,5,41472
s_mov_b32 m0,s13
qm 84,104,196
v_rcp_f32 v16,v16
qt 104,4,45056
qw 5
qm 88,108,124
v_mul_f32 v65,v65,v16
qt 106,4,45568
s_add_u32 s14,s17,256
qm 92,108,196
v_mul_f32 v16,v66,v66
qt 108,5,45056
qw 5
qm 80,96,128
v_fma_f32 v16,v16,v12,v10
qt 110,5,45568
ql 6,40,0
qm 84,96,200
v_mul_f32 v16,v16,v66
qt 96,2,32768
qw 5
qm 88,100,128
v_exp_f32 v16,v16
qt 98,2,33280
ql 7,40,1024
qm 92,100,200
v_fma_f32 v16,v16,v14,v14
qt 100,3,32768
qw 5
qm 80,104,132
v_rcp_f32 v16,v16
qt 102,3,33280
s_add_u32 m0,s13,98304
qm 84,104,204
v_mul_f32 v66,v66,v16
qt 104,2,36864
qw 5
qm 88,108,132
s_waitcnt vmcnt(7)
s_barrier
v_mul_f32 v16,v67,v67
qt 106,2,37376
s_add_u32 s14,s17,590080
qm 92,108,204
v_fma_f32 v16,v16,v12,v10
qt 108,3,36864
qw 5
qm 80,96,136
v_mul_f32 v16,v16,v67
qt 110,3,37376
ql 6,40,0
qm 84,96,208
v_exp_f32 v16,v16
qt 96,2,40960
qw 5
qm 88,100,136
v_fma_f32 v16,v16,v14,v14
qt 98,2,41472
ql 7,40,1024
qm 92,100,208
v_rcp_f32 v16,v16
qt 100,3,40960
qw 5
qm 80,104,140
v_mul_f32 v67,v67,v16
qt 102,3,41472
s_add_u32 m0,s13,106496
qm 84,104,212
v_mul_f32 v16,v72,v72
qt 104,2,45056
qw 5
qm 88,108,140
v_fma_f32 v16,v16,v12,v10
qt 106,2,45568
s_add_u32 s14,s17,1179904
qm 92,108,212
v_mul_f32 v16,v16,v72
qt 108,3,45056
qw 5
qm 80,96,144
v_exp_f32 v16,v16
qt 110,3,45568
ql 6,40,0
qm 84,96,216
v_fma_f32 v16,v16,v14,v14
qt 96,2,49152
qw 5
qm 88,100,144
v_rcp_f32 v16,v16
qt 98,2,49664
ql 7,40,1024
qm 92,100,216
v_mul_f32 v72,v72,v16
qt 100,3,49152
qw 5
qm 80,104,148
v_mul_f32 v16,v73,v73
qt 102,3,49664
s_add_u32 m0,s13,8192
qm 84,104,220
v_fma_f32 v16,v16,v12,v10
qt 104,2,53248
qw 5
qm 88,108,148
v_mul_f32 v16,v16,v73
qt 106,2,53760
s_add_u32 s14,s17,1769728
qm 92,108,220
v_exp_f32 v16,v16
qt 108,3,53248
qw 5
qm 80,96,152
v_fma_f32 v16,v16,v14,v14
qt 110,3,53760
ql 6,40,0
qm 84,96,224
v_rcp_f32 v16,v16
qt 96,2,57344
qw 5
qm 88,100,152
v_mul_f32 v73,v73,v16
qt 98,2,57856
ql 7,40,1024
qm 92,100,224
v_mul_f32 v16,v74,v74
qt 100,3,57344
qw 5
qm 80,104,156
v_fma_f32 v16,v16,v12,v10
qt 102,3,57856
s_add_u32 m0,s13,16384
qm 84,104,228
v_mul_f32 v16,v16,v74
qt 104,2,61440
qw 5
qm 88,108,156
s_waitcnt vmcnt(6)
s_barrier
v_exp_f32 v16,v16
qt 106,2,61952
s_add_u32 s14,s17,2359552
qm 92,108,228
v_fma_f32 v16,v16,v14,v14
qt 108,3,61440
qw 5
qm 80,96,160
v_rcp_f32 v16,v16
qt 110,3,61952
ql 6,40,0
qm 84,96,232
v_mul_f32 v74,v74,v16
qt 96,4,0
qw 5
qm 88,100,160
v_mul_f32 v16,v75,v75
qt 98,4,512
ql 7,40,1024
qm 92,100,232
v_fma_f32 v16,v16,v12,v10
qt 100,5,0
qw 5
qm 80,104,164
v_mul_f32 v16,v16,v75
qt 102,5,512
s_add_u32 m0,s13,24576
qm 84,104,236
v_exp_f32 v16,v16
qt 104,4,4096
qw 5
qm 88,108,164
v_fma_f32 v16,v16,v14,v14
qt 106,4,4608
s_add_u32 s14,s17,2949376
qm 92,108,236
v_rcp_f32 v16,v16
qt 108,5,4096
qw 5
qm 80,96,168
v_mul_f32 v75,v75,v16
qt 110,5,4608
ql 6,40,0
qm 84,96,240
v_mul_f32 v16,v68,v68
qt 96,4,8192
qw 5
qm 88,100,168
v_fma_f32 v16,v16,v13,v11
qt 98,4,8704
ql 7,40,1024
qm 92,100,240
v_mul_f32 v16,v16,v68
qt 100,5,8192
qw 5
qm 80,104,172
v_exp_f32 v16,v16
qt 102,5,8704
s_add_u32 m0,s13,32768
qm 84,104,244
v_fma_f32 v16,v16,v15,v15
qt 104,4,12288
qw 5
qm 88,108,172
v_rcp_f32 v16,v16
qt 106,4,12800
s_add_u32 s14,s17,3539200
qm 92,108,244
v_mul_f32 v68,v68,v16
qt 108,5,12288
qw 5
qm 80,96,176
v_mul_f32 v16,v69,v69
qt 110,5,12800
ql 6,40,0
qm 84,96,248
v_fma_f32 v16,v16,v13,v11
qt 96,2,0
qw 5
qm 88,100,176
v_mul_f32 v16,v16,v69
qt 98,2,512
ql 7,40,1024
qm 92,100,248
v_exp_f32 v16,v16
qt 100,3,0
qw 5
qm 80,104,180
v_fma_f32 v16,v16,v15,v15
qt 102,3,512
s_add_u32 m0,s13,40960
qm 84,104,252
v_rcp_f32 v16,v16
qt 104,2,4096
qw 5
qm 88,108,180
s_waitcnt vmcnt(6)
s_barrier
v_mul_f32 v69,v69,v16
qt 106,2,4608
s_add_u32 s14,s17,4129024
qm 92,108,252
qt 108,3,4096
qw 5
qz 48,96,112
v_mul_f32 v16,v70,v70
qt 110,3,4608
ql 6,40,0
qz 52,96,184
v_fma_f32 v16,v16,v13,v11
qt 96,4,32768
qw 5
qz 56,100,112
v_mul_f32 v16,v16,v70
qt 98,4,33280
ql 7,40,1024
qz 60,100,184
v_exp_f32 v16,v16
qt 100,5,32768
qw 5
qm 48,104,116
v_fma_f32 v16,v16,v15,v15
qt 102,5,33280
s_add_u32 m0,s13,49152
qm 52,104,188
v_rcp_f32 v16,v16
qt 104,4,36864
qw 5
qm 56,108,116
v_mul_f32 v70,v70,v16
qt 106,4,37376
s_add_u32 s14,s17,4718848
qm 60,108,188
v_mul_f32 v16,v71,v71
qt 108,5,36864
qw 5
qm 48,96,120
v_fma_f32 v16,v16,v13,v11
qt 110,5,37376
ql 6,40,0
qm 52,96,192
v_mul_f32 v16,v16,v71
qt 96,4,40960
qw 5
qm 56,100,120
v_exp_f32 v16,v16
qt 98,4,41472
ql 7,40,1024
qm 60,100,192
v_fma_f32 v16,v16,v15,v15
qt 100,5,40960
qw 5
qm 48,104,124
v_rcp_f32 v16,v16
qt 102,5,41472
s_add_u32 m0,s13,57344
qm 52,104,196
v_mul_f32 v71,v71,v16
qt 104,4,45056
qw 5
qm 56,108,124
v_mul_f32 v16,v76,v76
qt 106,4,45568
s_add_u32 s14,s17,256
qm 60,108,196
v_fma_f32 v16,v16,v13,v11
qt 108,5,45056
qw 5
qm 48,96,128
v_mul_f32 v16,v16,v76
qt 110,5,45568
ql 6,44,0
qm 52,96,200
v_exp_f32 v16,v16
qt 96,2,8192
qw 5
qm 56,100,128
v_fma_f32 v16,v16,v15,v15
qt 98,2,8704
ql 7,44,1024
qm 60,100,200
v_rcp_f32 v16,v16
qt 100,3,8192
qw 5
qm 48,104,132
v_mul_f32 v76,v76,v16
qt 102,3,8704
s_add_u32 m0,s13,65536
qm 52,104,204
v_mul_f32 v16,v77,v77
qt 104,2,12288
qw 5
qm 56,108,132
s_waitcnt vmcnt(6)
s_barrier
v_fma_f32 v16,v16,v13,v11
qt 106,2,12800
s_add_u32 s14,s17,590080
qm 60,108,204
v_mul_f32 v16,v16,v77
qt 108,3,12288
qw 5
qm 48,96,136
v_exp_f32 v16,v16
qt 110,3,12800
ql 6,44,0
qm 52,96,208
v_fma_f32 v16,v16,v15,v15
qt 96,2,16384
qw 5
qm 56,100,136
v_rcp_f32 v16,v16
qt 98,2,16896
ql 7,44,1024
qm 60,100,208
v_mul_f32 v77,v77,v16
qt 100,3,16384
qw 5
qm 48,104,140
v_mul_f32 v16,v78,v78
qt 102,3,16896
s_add_u32 m0,s13,73728
qm 52,104,212
v_fma_f32 v16,v16,v13,v11
qt 104,2,20480
qw 5
qm 56,108,140
v_mul_f32 v16,v16,v78
qt 106,2,20992
s_add_u32 s14,s17,1179904
qm 60,108,212
v_exp_f32 v16,v16
qt 108,3,20480
qw 5
qm 48,96,144
v_fma_f32 v16,v16,v15,v15
qt 110,3,20992
ql 6,44,0
qm 52,96,216
v_rcp_f32 v16,v16
qt 96,2,24576
qw 5
qm 56,100,144
v_mul_f32 v78,v78,v16
qt 98,2,25088
ql 7,44,1024
qm 60,100,216
v_mul_f32 v16,v79,v79
qt 100,3,24576
qw 5
qm 48,104,148
v_fma_f32 v16,v16,v13,v11
qt 102,3,25088
s_mov_b32 m0,s13
qm 52,104,220
v_mul_f32 v16,v16,v79
qt 104,2,28672
qw 5
qm 56,108,148
v_exp_f32 v16,v16
qt 106,2,29184
s_add_u32 s14,s17,1769728
qm 60,108,220
v_fma_f32 v16,v16,v15,v15
qt 108,3,28672
qw 5
qm 48,96,152
v_rcp_f32 v16,v16
qt 110,3,29184
ql 6,44,0
qm 52,96,224
v_mul_f32 v79,v79,v16
qt 96,2,32768
qw 5
qm 56,100,152
v_mul_f32 v64,v64,v80
qt 98,2,33280
ql 7,44,1024
qm 60,100,224
v_mul_f32 v65,v65,v81
qt 100,3,32768
qw 5
qm 48,104,156
v_cvt_pk_bf16_f32 v64,v64,v65
qt 102,3,33280
s_add_u32 m0,s13,8192
qm 52,104,228
v_mul_f32 v66,v66,v82
qt 104,2,36864
qw 5
qm 56,108,156
s_waitcnt vmcnt(6)
s_barrier
v_mul_f32 v67,v67,v83
qt 106,2,37376
s_add_u32 s14,s17,2359552
qm 60,108,228
v_cvt_pk_bf16_f32 v65,v66,v67
qt 108,3,36864
qw 5
qm 48,96,160
v_mul_f32 v72,v72,v88
v_and_b32 v44,15,v0
v_lshrrev_b32 v45,4,v0
qt 110,3,37376
ql 6,44,0
qm 52,96,232
v_mul_f32 v73,v73,v89
v_lshl_add_u32 v17,v44,2,v45
qt 96,2,40960
qw 5
qm 56,100,160
v_cvt_pk_bf16_f32 v66,v72,v73
v_lshl_add_u32 v45,s7,2,v45
qt 98,2,41472
ql 7,44,1024
qm 60,100,232
v_mul_f32 v74,v74,v90
v_lshlrev_b32 v28,4,v44
qt 100,3,40960
qw 5
qm 48,104,164
v_mul_f32 v75,v75,v91
v_add_u32 v28,123904,v28
qt 102,3,41472
s_add_u32 m0,s13,16384
qm 52,104,236
v_cvt_pk_bf16_f32 v67,v74,v75
v_lshlrev_b32 v44,3,v44
v_lshl_add_u32 v42,v45,9,v44
qt 104,2,45056
qw 5
qm 56,108,164
ds_write_b128 v18,v[64:67] offset:16384
v_mul_f32 v68,v68,v84
v_add_u32 v42,114688,v42
qt 106,2,45568
s_add_u32 s14,s17,2949376
qm 60,108,236
s_add_u32 s15,s76,16384
buffer_store_dwordx4 v[64:67],v18,s[88:91],s15 offen offset:0 sc1
v_mul_f32 v69,v69,v85
v_add_u32 v44,124928,v44
qt 108,3,45056
qw 6
qm 48,96,168
v_cvt_pk_bf16_f32 v68,v68,v69
qr 28,28,0
qt 110,3,45568
ql 6,44,0
qm 52,96,240
v_mul_f32 v70,v70,v86
qd 32,44,0
qt 96,2,49152
qw 8
qm 56,100,168
v_mul_f32 v71,v71,v87
qd 34,44,512
qd 36,44,1024
qt 98,2,49664
ql 7,44,1024
qm 60,100,240
v_cvt_pk_bf16_f32 v69,v70,v71
qd 38,44,1536
qt 100,3,49152
qw 10
qm 48,104,172
v_mul_f32 v76,v76,v92
qd 40,44,2048
qt 102,3,49664
s_add_u32 m0,s13,24576
qm 52,104,244
v_mul_f32 v77,v77,v93
qd 42,42,0
qt 104,2,53248
qw 11
qm 56,108,172
v_cvt_pk_bf16_f32 v70,v76,v77
qr 20,18,49152
qt 106,2,53760
s_add_u32 s14,s17,3539200
qm 60,108,244
v_mul_f32 v78,v78,v94
qr 24,18,50176
v_lshlrev_b32 v17,3,v17
qt 108,3,53248
qw 10
qm 48,96,176
v_mul_f32 v79,v79,v95
v_add_u32 v17,s13,v17
qt 110,3,53760
ql 6,44,0
qm 52,96,248
v_cvt_pk_bf16_f32 v71,v78,v79
v_add_u32 v17,131072,v17
qt 96,2,57344
qw 8
qm 56,100,176
ds_write_b128 v18,v[68:71] offset:17408
v_lshlrev_b32 v45,3,v45
qt 98,2,57856
ql 7,44,1024
qm 60,100,248
s_add_u32 s15,s76,16384
buffer_store_dwordx4 v[68:71],v18,s[88:91],s15 offen offset:1024 sc1
v_cvt_f32_u32 v45,v45
qt 100,3,57344
qw 7
qm 48,104,180
qt 102,3,57856
s_add_u32 m0,s13,32768
qm 52,104,252
qt 104,2,61440
qw 6
qm 56,108,180
s_waitcnt vmcnt(8) lgkmcnt(4)
s_barrier
qt 106,2,61952
s_add_u32 s14,s17,4129024
qm 60,108,252
qt 108,3,61440
v_add_f32 v34,v34,v36
v_add_f32 v35,v35,v37
v_add_f32 v38,v38,v40
v_add_f32 v39,v39,v41
v_add_f32 v34,v34,v38
v_add_f32 v35,v35,v39
v_mul_f32 v44,v45,v28
v_mul_f32 v37,v45,v29
v_exp_f32 v44,v44
v_fract_f32 v37,v37
v_cos_f32 v36,v37
v_sin_f32 v37,v37
v_mul_f32 v36,v36,v44
v_mul_f32 v37,v37,v44
v_fma_f32 v38,v36,v34,v42
v_fma_f32 v39,v36,v35,v43
v_fma_f32 v38,-v37,v35,v38
v_fma_f32 v39,v37,v34,v39
v_lshlrev_b32 v34,16,v20
v_and_b32 v35,0xffff0000,v20
v_fma_f32 v40,v32,v38,v34
v_fma_f32 v41,v32,v39,v35
v_fma_f32 v40,-v33,v39,v40
v_fma_f32 v41,v33,v38,v41
v_mul_f32 v36,v30,v40
v_mul_f32 v42,v30,v41
v_fma_f32 v36,-v31,v41,v36
v_fma_f32 v42,v31,v40,v42
v_lshlrev_b32 v34,16,v21
v_and_b32 v35,0xffff0000,v21
v_fma_f32 v38,v32,v40,v34
v_fma_f32 v39,v32,v41,v35
v_fma_f32 v38,-v33,v41,v38
v_fma_f32 v39,v33,v40,v39
v_mul_f32 v37,v30,v38
v_mul_f32 v43,v30,v39
v_fma_f32 v37,-v31,v39,v37
v_fma_f32 v43,v31,v38,v43
v_cvt_pk_bf16_f32 v20,v36,v37
v_cvt_pk_bf16_f32 v28,-v42,-v43
v_lshlrev_b32 v34,16,v22
v_and_b32 v35,0xffff0000,v22
v_fma_f32 v40,v32,v38,v34
v_fma_f32 v41,v32,v39,v35
v_fma_f32 v40,-v33,v39,v40
v_fma_f32 v41,v33,v38,v41
v_mul_f32 v36,v30,v40
v_mul_f32 v42,v30,v41
v_fma_f32 v36,-v31,v41,v36
v_fma_f32 v42,v31,v40,v42
v_lshlrev_b32 v34,16,v23
v_and_b32 v35,0xffff0000,v23
v_fma_f32 v38,v32,v40,v34
v_fma_f32 v39,v32,v41,v35
v_fma_f32 v38,-v33,v41,v38
v_fma_f32 v39,v33,v40,v39
v_mul_f32 v37,v30,v38
v_mul_f32 v43,v30,v39
v_fma_f32 v37,-v31,v39,v37
v_fma_f32 v43,v31,v38,v43
v_cvt_pk_bf16_f32 v21,v36,v37
v_cvt_pk_bf16_f32 v29,-v42,-v43
ds_write_b64 v17,v[20:21] offset:0
ds_write_b64 v17,v[28:29] offset:512
v_lshlrev_b32 v34,16,v24
v_and_b32 v35,0xffff0000,v24
v_fma_f32 v40,v32,v38,v34
v_fma_f32 v41,v32,v39,v35
v_fma_f32 v40,-v33,v39,v40
v_fma_f32 v41,v33,v38,v41
v_mul_f32 v36,v30,v40
v_mul_f32 v42,v30,v41
v_fma_f32 v36,-v31,v41,v36
v_fma_f32 v42,v31,v40,v42
v_lshlrev_b32 v34,16,v25
v_and_b32 v35,0xffff0000,v25
v_fma_f32 v38,v32,v40,v34
v_fma_f32 v39,v32,v41,v35
v_fma_f32 v38,-v33,v41,v38
v_fma_f32 v39,v33,v40,v39
v_mul_f32 v37,v30,v38
v_mul_f32 v43,v30,v39
v_fma_f32 v37,-v31,v39,v37
v_fma_f32 v43,v31,v38,v43
v_cvt_pk_bf16_f32 v24,v36,v37
v_cvt_pk_bf16_f32 v28,-v42,-v43
v_lshlrev_b32 v34,16,v26
v_and_b32 v35,0xffff0000,v26
v_fma_f32 v40,v32,v38,v34
v_fma_f32 v41,v32,v39,v35
v_fma_f32 v40,-v33,v39,v40
v_fma_f32 v41,v33,v38,v41
v_mul_f32 v36,v30,v40
v_mul_f32 v42,v30,v41
v_fma_f32 v36,-v31,v41,v36
v_fma_f32 v42,v31,v40,v42
v_lshlrev_b32 v34,16,v27
v_and_b32 v35,0xffff0000,v27
v_fma_f32 v38,v32,v40,v34
v_fma_f32 v39,v32,v41,v35
v_fma_f32 v38,-v33,v41,v38
v_fma_f32 v39,v33,v40,v39
v_mul_f32 v37,v30,v38
v_mul_f32 v43,v30,v39
v_fma_f32 v37,-v31,v39,v37
v_fma_f32 v43,v31,v38,v43
v_cvt_pk_bf16_f32 v25,v36,v37
v_cvt_pk_bf16_f32 v29,-v42,-v43
ds_write_b64 v17,v[24:25] offset:1024
ds_write_b64 v17,v[28:29] offset:1536
qw 9
qz 80,96,112
qt 110,3,61952
ql 6,44,0
qz 84,96,184
v_mul_f32 v16,v48,v48
qt 96,4,0
qw 9
qz 88,100,112
v_fma_f32 v16,v16,v12,v10
v_lshlrev_b32 v36,3,v0
v_add_u32 v36,s13,v36
qt 98,4,512
ql 7,44,1024
qz 92,100,184
v_mul_f32 v16,v16,v48
v_add_u32 v36,131072,v36
qt 100,5,0
qw 9
qm 80,104,116
v_exp_f32 v16,v16
qt 28,36,0
qt 102,5,512
s_add_u32 m0,s13,40960
qm 84,104,188
v_fma_f32 v16,v16,v14,v14
qt 30,36,512
qt 104,4,4096
qw 7
qm 88,108,116
v_rcp_f32 v16,v16
qt 32,36,1024
qt 106,4,4608
s_add_u32 s14,s17,4718848
qm 92,108,188
v_mul_f32 v48,v48,v16
qt 34,36,1536
qt 108,5,4096
qw 9
qm 80,96,120
v_mul_f32 v16,v49,v49
qt 110,5,4608
ql 6,44,0
qm 84,96,192
v_fma_f32 v16,v16,v12,v10
qt 96,4,8192
qw 8
qm 88,100,120
v_mul_f32 v16,v16,v49
qt 98,4,8704
ql 7,44,1024
qm 92,100,192
v_exp_f32 v16,v16
qt 100,5,8192
qw 6
qm 80,104,124
v_fma_f32 v16,v16,v14,v14
qt 102,5,8704
s_add_u32 m0,s13,49152
qm 84,104,196
v_rcp_f32 v16,v16
qt 104,4,12288
qw 5
qm 88,108,124
v_mul_f32 v49,v49,v16
qt 106,4,12800
s_add_u32 s14,s17,384
qm 92,108,196
v_mul_f32 v16,v50,v50
s_add_u32 s15,s76,65536
buffer_store_dwordx4 v[28:31],v18,s[88:91],s15 offen offset:0 sc1
qt 108,5,12288
qw 5
qm 80,96,128
v_fma_f32 v16,v16,v12,v10
s_add_u32 s15,s76,65536
buffer_store_dwordx4 v[32:35],v18,s[88:91],s15 offen offset:1024 sc1
qt 110,5,12800
ql 6,40,0
qm 84,96,200
v_mul_f32 v16,v16,v50
qt 96,2,0
qw 5
qm 88,100,128
v_exp_f32 v16,v16
qt 98,2,512
ql 7,40,1024
qm 92,100,200
v_fma_f32 v16,v16,v14,v14
qt 100,3,0
qw 5
qm 80,104,132
v_rcp_f32 v16,v16
qt 102,3,512
s_add_u32 m0,s13,57344
qm 84,104,204
v_mul_f32 v50,v50,v16
qt 104,2,4096
qw 5
qm 88,108,132
s_waitcnt vmcnt(9)
s_barrier
v_mul_f32 v16,v51,v51
qt 106,2,4608
s_add_u32 s14,s17,590208
qm 92,108,204
v_fma_f32 v16,v16,v12,v10
qt 108,3,4096
qw 5
qm 80,96,136
v_mul_f32 v16,v16,v51
qt 110,3,4608
ql 6,40,0
qm 84,96,208
v_exp_f32 v16,v16
qt 96,2,8192
qw 5
qm 88,100,136
v_fma_f32 v16,v16,v14,v14
qt 98,2,8704
ql 7,40,1024
qm 92,100,208
v_rcp_f32 v16,v16
qt 100,3,8192
qw 5
qm 80,104,140
v_mul_f32 v51,v51,v16
qt 102,3,8704
s_add_u32 m0,s13,65536
qm 84,104,212
v_mul_f32 v16,v56,v56
qt 104,2,12288
qw 5
qm 88,108,140
v_fma_f32 v16,v16,v12,v10
qt 106,2,12800
s_add_u32 s14,s17,1180032
qm 92,108,212
v_mul_f32 v16,v16,v56
qt 108,3,12288
qw 5
qm 80,96,144
v_exp_f32 v16,v16
qt 110,3,12800
ql 6,40,0
qm 84,96,216
v_fma_f32 v16,v16,v14,v14
qt 96,2,16384
qw 5
qm 88,100,144
v_rcp_f32 v16,v16
qt 98,2,16896
ql 7,40,1024
qm 92,100,216
v_mul_f32 v56,v56,v16
qt 100,3,16384
qw 5
qm 80,104,148
v_mul_f32 v16,v57,v57
qt 102,3,16896
s_add_u32 m0,s13,73728
qm 84,104,220
v_fma_f32 v16,v16,v12,v10
qt 104,2,20480
qw 5
qm 88,108,148
v_mul_f32 v16,v16,v57
qt 106,2,20992
s_add_u32 s14,s17,1769856
qm 92,108,220
v_exp_f32 v16,v16
qt 108,3,20480
qw 5
qm 80,96,152
v_fma_f32 v16,v16,v14,v14
qt 110,3,20992
ql 6,40,0
qm 84,96,224
v_rcp_f32 v16,v16
qt 96,2,24576
qw 5
qm 88,100,152
v_mul_f32 v57,v57,v16
qt 98,2,25088
ql 7,40,1024
qm 92,100,224
v_mul_f32 v16,v58,v58
qt 100,3,24576
qw 5
qm 80,104,156
v_fma_f32 v16,v16,v12,v10
qt 102,3,25088
s_mov_b32 m0,s13
qm 84,104,228
v_mul_f32 v16,v16,v58
qt 104,2,28672
qw 5
qm 88,108,156
s_waitcnt vmcnt(6)
s_barrier
v_exp_f32 v16,v16
qt 106,2,29184
s_add_u32 s14,s17,2359680
qm 92,108,228
v_fma_f32 v16,v16,v14,v14
qt 108,3,28672
qw 5
qm 80,96,160
v_rcp_f32 v16,v16
v_and_b32 v44,15,v0
v_lshrrev_b32 v45,4,v0
qt 110,3,29184
ql 6,40,0
qm 84,96,232
v_mul_f32 v58,v58,v16
v_lshl_add_u32 v17,v44,2,v45
qt 96,2,32768
qw 5
qm 88,100,160
v_mul_f32 v16,v59,v59
v_lshl_add_u32 v45,s7,2,v45
qt 98,2,33280
ql 7,40,1024
qm 92,100,232
v_fma_f32 v16,v16,v12,v10
v_lshlrev_b32 v28,4,v44
qt 100,3,32768
qw 5
qm 80,104,164
v_mul_f32 v16,v16,v59
v_add_u32 v28,123904,v28
qt 102,3,33280
s_add_u32 m0,s13,8192
qm 84,104,236
v_exp_f32 v16,v16
v_lshlrev_b32 v44,3,v44
v_lshl_add_u32 v42,v45,9,v44
qt 104,2,36864
qw 5
qm 88,108,164
v_fma_f32 v16,v16,v14,v14
v_add_u32 v42,114688,v42
qt 106,2,37376
s_add_u32 s14,s17,2949504
qm 92,108,236
v_rcp_f32 v16,v16
v_add_u32 v44,124928,v44
qt 108,3,36864
qw 5
qm 80,96,168
v_mul_f32 v59,v59,v16
qr 28,28,512
qt 110,3,37376
ql 6,40,0
qm 84,96,240
v_mul_f32 v16,v52,v52
qd 32,44,256
qt 96,2,40960
qw 7
qm 88,100,168
v_fma_f32 v16,v16,v13,v11
qd 34,44,768
qd 36,44,1280
qt 98,2,41472
ql 7,40,1024
qm 92,100,240
v_mul_f32 v16,v16,v52
qd 38,44,1792
qt 100,3,40960
qw 10
qm 80,104,172
v_exp_f32 v16,v16
qd 40,44,2304
qt 102,3,41472
s_add_u32 m0,s13,16384
qm 84,104,244
v_fma_f32 v16,v16,v15,v15
qd 42,42,256
qt 104,2,45056
qw 11
qm 88,108,172
v_rcp_f32 v16,v16
qr 20,19,0
qt 106,2,45568
s_add_u32 s14,s17,3539328
qm 92,108,244
v_mul_f32 v52,v52,v16
qr 24,19,1024
v_lshlrev_b32 v17,3,v17
qt 108,3,45056
qw 10
qm 80,96,176
v_mul_f32 v16,v53,v53
v_add_u32 v17,s13,v17
qt 110,3,45568
ql 6,40,0
qm 84,96,248
v_fma_f32 v16,v16,v13,v11
v_add_u32 v17,147456,v17
qt 96,2,49152
qw 8
qm 88,100,176
v_mul_f32 v16,v16,v53
v_lshlrev_b32 v45,3,v45
qt 98,2,49664
ql 7,40,1024
qm 92,100,248
v_exp_f32 v16,v16
v_cvt_f32_u32 v45,v45
qt 100,3,49152
qw 6
qm 80,104,180
v_fma_f32 v16,v16,v15,v15
qt 102,3,49664
s_add_u32 m0,s13,24576
qm 84,104,252
v_rcp_f32 v16,v16
qt 104,2,53248
qw 5
qm 88,108,180
s_waitcnt vmcnt(6)
s_barrier
v_mul_f32 v53,v53,v16
qt 106,2,53760
s_add_u32 s14,s17,4129152
qm 92,108,252
qt 108,3,53248
v_add_f32 v34,v34,v36
v_add_f32 v35,v35,v37
v_add_f32 v38,v38,v40
v_add_f32 v39,v39,v41
v_add_f32 v34,v34,v38
v_add_f32 v35,v35,v39
v_mul_f32 v44,v45,v28
v_mul_f32 v37,v45,v29
v_exp_f32 v44,v44
v_fract_f32 v37,v37
v_cos_f32 v36,v37
v_sin_f32 v37,v37
v_mul_f32 v36,v36,v44
v_mul_f32 v37,v37,v44
v_fma_f32 v38,v36,v34,v42
v_fma_f32 v39,v36,v35,v43
v_fma_f32 v38,-v37,v35,v38
v_fma_f32 v39,v37,v34,v39
v_lshlrev_b32 v34,16,v20
v_and_b32 v35,0xffff0000,v20
v_fma_f32 v40,v32,v38,v34
v_fma_f32 v41,v32,v39,v35
v_fma_f32 v40,-v33,v39,v40
v_fma_f32 v41,v33,v38,v41
v_mul_f32 v36,v30,v40
v_mul_f32 v42,v30,v41
v_fma_f32 v36,-v31,v41,v36
v_fma_f32 v42,v31,v40,v42
v_lshlrev_b32 v34,16,v21
v_and_b32 v35,0xffff0000,v21
v_fma_f32 v38,v32,v40,v34
v_fma_f32 v39,v32,v41,v35
v_fma_f32 v38,-v33,v41,v38
v_fma_f32 v39,v33,v40,v39
v_mul_f32 v37,v30,v38
v_mul_f32 v43,v30,v39
v_fma_f32 v37,-v31,v39,v37
v_fma_f32 v43,v31,v38,v43
v_cvt_pk_bf16_f32 v20,v36,v37
v_cvt_pk_bf16_f32 v28,-v42,-v43
v_lshlrev_b32 v34,16,v22
v_and_b32 v35,0xffff0000,v22
v_fma_f32 v40,v32,v38,v34
v_fma_f32 v41,v32,v39,v35
v_fma_f32 v40,-v33,v39,v40
v_fma_f32 v41,v33,v38,v41
v_mul_f32 v36,v30,v40
v_mul_f32 v42,v30,v41
v_fma_f32 v36,-v31,v41,v36
v_fma_f32 v42,v31,v40,v42
v_lshlrev_b32 v34,16,v23
v_and_b32 v35,0xffff0000,v23
v_fma_f32 v38,v32,v40,v34
v_fma_f32 v39,v32,v41,v35
v_fma_f32 v38,-v33,v41,v38
v_fma_f32 v39,v33,v40,v39
v_mul_f32 v37,v30,v38
v_mul_f32 v43,v30,v39
v_fma_f32 v37,-v31,v39,v37
v_fma_f32 v43,v31,v38,v43
v_cvt_pk_bf16_f32 v21,v36,v37
v_cvt_pk_bf16_f32 v29,-v42,-v43
ds_write_b64 v17,v[20:21] offset:0
ds_write_b64 v17,v[28:29] offset:512
v_lshlrev_b32 v34,16,v24
v_and_b32 v35,0xffff0000,v24
v_fma_f32 v40,v32,v38,v34
v_fma_f32 v41,v32,v39,v35
v_fma_f32 v40,-v33,v39,v40
v_fma_f32 v41,v33,v38,v41
v_mul_f32 v36,v30,v40
v_mul_f32 v42,v30,v41
v_fma_f32 v36,-v31,v41,v36
v_fma_f32 v42,v31,v40,v42
v_lshlrev_b32 v34,16,v25
v_and_b32 v35,0xffff0000,v25
v_fma_f32 v38,v32,v40,v34
v_fma_f32 v39,v32,v41,v35
v_fma_f32 v38,-v33,v41,v38
v_fma_f32 v39,v33,v40,v39
v_mul_f32 v37,v30,v38
v_mul_f32 v43,v30,v39
v_fma_f32 v37,-v31,v39,v37
v_fma_f32 v43,v31,v38,v43
v_cvt_pk_bf16_f32 v24,v36,v37
v_cvt_pk_bf16_f32 v28,-v42,-v43
v_lshlrev_b32 v34,16,v26
v_and_b32 v35,0xffff0000,v26
v_fma_f32 v40,v32,v38,v34
v_fma_f32 v41,v32,v39,v35
v_fma_f32 v40,-v33,v39,v40
v_fma_f32 v41,v33,v38,v41
v_mul_f32 v36,v30,v40
v_mul_f32 v42,v30,v41
v_fma_f32 v36,-v31,v41,v36
v_fma_f32 v42,v31,v40,v42
v_lshlrev_b32 v34,16,v27
v_and_b32 v35,0xffff0000,v27
v_fma_f32 v38,v32,v40,v34
v_fma_f32 v39,v32,v41,v35
v_fma_f32 v38,-v33,v41,v38
v_fma_f32 v39,v33,v40,v39
v_mul_f32 v37,v30,v38
v_mul_f32 v43,v30,v39
v_fma_f32 v37,-v31,v39,v37
v_fma_f32 v43,v31,v38,v43
v_cvt_pk_bf16_f32 v25,v36,v37
v_cvt_pk_bf16_f32 v29,-v42,-v43
ds_write_b64 v17,v[24:25] offset:1024
ds_write_b64 v17,v[28:29] offset:1536
qw 9
qz 64,96,112
v_mul_f32 v16,v54,v54
qt 110,3,53760
ql 6,40,0
qz 68,96,184
v_fma_f32 v16,v16,v13,v11
v_bfe_u32 v44,v0,2,2
qt 96,2,57344
qw 9
qz 72,100,112
v_mul_f32 v16,v16,v54
v_lshlrev_b32 v36,3,v0
v_add_u32 v36,s13,v36
v_mov_b32 v38,s16
v_and_b32 v37,48,v0
qt 98,2,57856
ql 7,40,1024
qz 76,100,184
v_exp_f32 v16,v16
v_add_u32 v36,131072,v36
v_lshlrev_b32 v37,24,v37
qt 100,3,57344
qw 9
qm 64,104,116
v_fma_f32 v16,v16,v15,v15
qt 28,36,16384
v_lshl_or_b32 v37,v0,7,v37
qt 102,3,57856
s_add_u32 m0,s13,32768
qm 68,104,188
v_rcp_f32 v16,v16
qt 30,36,16896
s_mul_i32 s72,s10,2048
s_and_b32 s33,s8,3
s_lshl_b32 s33,s33,2
s_add_u32 s33,s33,s7
s_lshl_b32 s33,s33,3
s_lshl_b32 s33,s33,2
s_add_u32 s33,s33,122896
v_lshl_add_u32 v45,v44,2,s33
qt 104,2,61440
qw 7
qm 72,108,116
v_mul_f32 v54,v54,v16
qt 32,36,17408
s_add_u32 s72,s72,8192
qt 106,2,61952
s_add_u32 s14,s17,4718976
qm 76,108,188
v_mul_f32 v16,v55,v55
qt 34,36,17920
s_and_b32 s30,s8,3
qt 108,3,61440
qw 9
qm 64,96,120
v_fma_f32 v16,v16,v13,v11
s_lshl_b32 s30,s30,4
qt 110,3,61952
ql 6,40,0
qm 68,96,192
v_mul_f32 v16,v16,v55
s_and_b32 s31,s8,4
qt 96,4,0
qw 8
qm 72,100,120
v_exp_f32 v16,v16
s_add_u32 s30,s30,s31
ds_read_b32 v40,v45 offset:0
qt 98,4,512
ql 7,40,1024
qm 76,100,192
v_fma_f32 v16,v16,v15,v15
s_add_u32 s68,s30,s72
qt 100,5,0
qw 7
qm 64,104,124
v_rcp_f32 v16,v16
s_lshl_b32 s31,s8,8
qt 102,5,512
s_add_u32 m0,s13,40960
qm 68,104,196
v_mul_f32 v55,v55,v16
s_add_u32 s78,s68,s31
qt 104,4,4096
qw 6
qm 72,108,124
v_mul_f32 v16,v60,v60
s_add_u32 s78,s78,128
ds_read_b32 v41,v45 offset:512
qt 106,4,4608
s_add_u32 s14,s17,384
qm 76,108,196
v_fma_f32 v16,v16,v13,v11
s_add_u32 s15,s76,81920
buffer_store_dwordx4 v[28:31],v18,s[88:91],s15 offen offset:0 sc1
s_add_u32 s68,s68,8
s_cmp_eq_u32 s6,4
s_cselect_b32 s68,s68,0x7ff00000
qt 108,5,4096
qw 6
qm 64,96,128
v_mul_f32 v16,v16,v60
s_add_u32 s15,s76,81920
buffer_store_dwordx4 v[32:35],v18,s[88:91],s15 offen offset:1024 sc1
s_add_u32 s72,s72,s31
qt 110,5,4608
ql 6,44,0
qm 68,96,200
v_exp_f32 v16,v16
s_add_u32 s72,s72,128
qt 96,4,8192
qw 6
qm 72,100,128
v_fma_f32 v16,v16,v15,v15
s_add_u32 s74,s16,8
qt 98,4,8704
ql 7,44,1024
qm 76,100,200
v_rcp_f32 v16,v16
v_bfe_u32 v20,v0,3,2
qt 100,5,8192
qw 5
qm 64,104,132
v_mul_f32 v60,v60,v16
v_lshrrev_b32 v21,1,v0
qt 102,5,8704
s_add_u32 m0,s13,49152
qm 68,104,204
v_mul_f32 v16,v61,v61
v_and_or_b32 v20,v21,16,v20
qt 104,4,12288
qw 5
qm 72,108,132
s_waitcnt vmcnt(8)
s_barrier
v_fma_f32 v16,v16,v13,v11
v_mul_u32_u24 v20,1152,v20
qt 106,4,12800
s_add_u32 s14,s17,590208
qm 76,108,204
v_mul_f32 v16,v16,v61
v_lshrrev_b32 v21,3,v0
qt 108,5,12288
qw 5
qm 64,96,136
v_exp_f32 v16,v16
v_and_b32 v21,2,v21
qt 110,5,12800
ql 6,44,0
qm 68,96,208
v_fma_f32 v16,v16,v15,v15
v_xor_b32 v21,v21,v0
qt 96,2,0
qw 5
qm 72,100,136
v_rcp_f32 v16,v16
v_and_b32 v21,7,v21
qt 98,2,512
ql 7,44,1024
qm 76,100,208
v_mul_f32 v61,v61,v16
v_lshl_add_u32 v20,v21,4,v20
qt 100,3,0
s_add_u32 m0,s13,57344
s_add_u32 s14,s17,1180032
qw 5
qm 64,104,140
v_mul_f32 v16,v62,v62
v_xor_b32 v21,64,v20
qt 102,3,512
ql 6,44,0
qm 68,104,212
v_fma_f32 v16,v16,v13,v11
v_add_u32 v21,3584,v21
v_and_b32 v42,15,v0
qt 104,2,4096
qw 5
qm 72,108,140
v_mul_f32 v16,v16,v62
v_lshrrev_b32 v43,5,v0
qt 106,2,4608
ql 7,44,1024
qm 76,108,212
v_exp_f32 v16,v16
v_bfe_u32 v25,v0,4,1
qt 108,3,4096
s_add_u32 m0,s13,65536
s_add_u32 s14,s17,1769856
qw 5
qm 64,96,144
v_fma_f32 v16,v16,v15,v15
v_lshlrev_b32 v25,3,v25
qt 110,3,4608
ql 6,44,0
qm 68,96,216
v_rcp_f32 v16,v16
v_add_u32 v25,0,v25
qt 96,2,8192
qw 5
qm 72,100,144
v_mul_f32 v62,v62,v16
v_lshl_add_u32 v24,v42,7,v25
qt 98,2,8704
ql 7,44,1024
qm 76,100,216
v_mul_f32 v16,v63,v63
v_lshl_add_u32 v22,v42,5,v25
qt 100,3,8192
qw 5
qm 64,104,148
v_fma_f32 v16,v16,v13,v11
v_lshrrev_b32 v42,1,v42
qt 102,3,8704
qm 68,104,220
v_mul_f32 v16,v16,v63
v_xor_b32 v25,v42,v43
qt 104,2,12288
qw 5
qm 72,108,148
v_exp_f32 v16,v16
v_lshl_add_u32 v24,v25,4,v24
qt 106,2,12800
qm 76,108,220
v_fma_f32 v16,v16,v15,v15
v_lshrrev_b32 v42,2,v42
qt 108,3,12288
qw 5
qm 64,96,152
v_rcp_f32 v16,v16
v_xor_b32 v42,v42,v43
qt 110,3,12800
qm 68,96,224
v_mul_f32 v63,v63,v16
v_lshl_add_u32 v22,v42,4,v22
qt 96,2,16384
qw 5
qm 72,100,152
v_mul_f32 v48,v48,v80
v_add_u32 v23,0x10000,v22
v_xor_b32 v25,32,v24
qt 98,2,16896
qm 76,100,224
v_mul_f32 v49,v49,v81
v_xor_b32 v26,64,v24
qt 100,3,16384
qw 5
qm 64,104,156
v_cvt_pk_bf16_f32 v48,v48,v49
v_xor_b32 v27,0x60,v24
qt 102,3,16896
s_add_u32 m0,s13,73728
qm 68,104,228
v_mul_f32 v50,v50,v82
v_lshlrev_b32 v36,3,v0
qt 104,2,20480
qw 5
qm 72,108,156
s_waitcnt vmcnt(0)
s_barrier
v_mul_f32 v51,v51,v83
v_add_u32 v36,s13,v36
qt 106,2,20992
s_add_u32 s14,s17,2359680
qm 76,108,228
v_cvt_pk_bf16_f32 v49,v50,v51
v_add_u32 v36,131072,v36
qt 108,3,20480
qw 5
qm 64,96,160
v_mul_f32 v56,v56,v88
v_lshrrev_b32 v8,3,v0
qt 110,3,20992
ql 6,44,0
qm 68,96,232
v_mul_f32 v57,v57,v89
v_mul_u32_u24 v8,1152,v8
qt 96,2,24576
qw 5
qm 72,100,160
v_cvt_pk_bf16_f32 v50,v56,v57
v_lshrrev_b32 v9,4,v0
qt 98,2,25088
ql 7,44,1024
qm 76,100,232
v_mul_f32 v58,v58,v90
v_xor_b32 v9,v9,v0
qt 100,3,24576
qw 5
qm 64,104,164
v_mul_f32 v59,v59,v91
v_and_b32 v9,7,v9
qt 102,3,25088
s_mov_b32 m0,s13
qm 68,104,236
v_cvt_pk_bf16_f32 v51,v58,v59
v_lshl_add_u32 v8,v9,4,v8
qt 104,2,28672
qw 5
qm 72,108,164
ds_write_b128 v18,v[48:51] offset:32768
v_mul_f32 v52,v52,v84
v_xor_b32 v9,64,v8
qt 106,2,29184
s_add_u32 s14,s17,2949504
qm 76,108,236
s_add_u32 s15,s76,32768
buffer_store_dwordx4 v[48:51],v18,s[88:91],s15 offen offset:0 sc1
v_mul_f32 v53,v53,v85
v_add_u32 v9,8192,v9
qt 108,3,28672
qw 6
qm 64,96,168
v_cvt_pk_bf16_f32 v52,v52,v53
s_lshr_b32 s30,s7,1
s_lshl_b32 s30,s30,5
qt 110,3,29184
ql 6,44,0
qm 68,96,240
v_mul_f32 v54,v54,v86
s_and_b32 s31,s7,1
qt 96,2,32768
qw 6
qm 72,100,168
v_mul_f32 v55,v55,v87
s_lshl_b32 s32,s31,3
qt 98,2,33280
ql 7,44,1024
qm 76,100,240
v_cvt_pk_bf16_f32 v53,v54,v55
s_add_u32 s30,s30,s32
qt 100,3,32768
qw 5
qm 64,104,172
v_mul_f32 v60,v60,v92
s_mul_i32 s32,s8,576
qt 102,3,33280
s_add_u32 m0,s13,8192
qm 68,104,244
v_mul_f32 v61,v61,v93
s_add_u32 s30,s30,s32
qt 104,2,36864
qw 5
qm 72,108,172
v_cvt_pk_bf16_f32 v54,v60,v61
s_mul_i32 s19,s30,1152
qt 106,2,37376
s_add_u32 s14,s17,3539328
qm 76,108,244
v_mul_f32 v62,v62,v94
s_mul_i32 s31,s31,36864
qt 108,3,36864
qw 5
qm 64,96,176
v_mul_f32 v63,v63,v95
s_and_b32 s30,s8,6
qt 110,3,37376
ql 6,44,0
qm 68,96,248
v_cvt_pk_bf16_f32 v55,v62,v63
s_lshl_b32 s30,s30,4
qt 96,2,40960
qw 5
qm 72,100,176
ds_write_b128 v18,v[52:55] offset:33792
s_add_u32 s18,s31,s30
qt 98,2,41472
ql 7,44,1024
qm 76,100,248
s_add_u32 s15,s76,32768
buffer_store_dwordx4 v[52:55],v18,s[88:91],s15 offen offset:1024 sc1
s_add_u32 s18,s18,1024
qt 100,3,40960
qw 6
qm 64,104,180
s_lshl_b32 s30,s8,7
qt 102,3,41472
s_add_u32 m0,s13,16384
qm 68,104,252
s_add_u32 s28,s31,s30
qt 104,2,45056
qw 6
qm 72,108,180
qw 4
s_barrier
qt 106,2,45568
s_add_u32 s14,s17,4129152
qm 76,108,252
qt 108,3,45056
qw 5
qz 80,96,112
qt 110,3,45568
ql 6,44,0
qz 84,96,184
v_mul_f32 v16,v64,v64
qt 96,2,49152
qw 5
qz 88,100,112
v_fma_f32 v16,v16,v12,v10
qt 98,2,49664
ql 7,44,1024
qz 92,100,184
v_mul_f32 v16,v16,v64
qt 100,3,49152
qw 5
qm 80,104,116
v_exp_f32 v16,v16
qt 102,3,49664
s_add_u32 m0,s13,24576
qm 84,104,188
v_fma_f32 v16,v16,v14,v14
qt 104,2,53248
qw 5
qm 88,108,116
v_rcp_f32 v16,v16
qt 106,2,53760
s_add_u32 s14,s17,4718976
qm 92,108,188
v_mul_f32 v64,v64,v16
qt 108,3,53248
qw 5
qm 80,96,120
v_mul_f32 v16,v65,v65
qt 110,3,53760
ql 6,44,0
qm 84,96,192
v_fma_f32 v16,v16,v12,v10
qr 112,18,0
qt 96,2,57344
qw 6
qm 88,100,120
v_mul_f32 v16,v16,v65
qt 98,2,57856
ql 7,44,1024
qm 92,100,192
v_exp_f32 v16,v16
qr 184,18,1024
qt 100,3,57344
qw 7
qm 80,104,124
v_fma_f32 v16,v16,v14,v14
qt 102,3,57856
s_add_u32 m0,s13,32768
qm 84,104,196
v_rcp_f32 v16,v16
qr 116,18,8192
qt 104,2,61440
qw 8
qm 88,108,124
v_mul_f32 v65,v65,v16
qt 106,2,61952
s_add_u32 s14,s28,368640
qm 92,108,196
v_mul_f32 v16,v66,v66
qr 188,18,9216
qt 108,3,61440
qw 8
qm 80,96,128
v_fma_f32 v16,v16,v12,v10
qt 110,3,61952
ql 8,24,0
qm 84,96,200
v_mul_f32 v16,v16,v66
qt 96,4,0
qw 7
qm 88,100,128
v_exp_f32 v16,v16
qt 98,4,512
ql 9,24,1024
qm 92,100,200
v_fma_f32 v16,v16,v14,v14
qt 100,5,0
qw 6
qm 80,104,132
v_rcp_f32 v16,v16
qt 102,5,512
s_add_u32 m0,s13,40960
qm 84,104,204
v_mul_f32 v66,v66,v16
qt 104,4,4096
qw 5
qm 88,108,132
s_waitcnt vmcnt(7)
s_barrier
v_mul_f32 v16,v67,v67
qt 106,4,4608
s_add_u32 s14,s28,387072
qm 92,108,204
v_fma_f32 v16,v16,v12,v10
qt 108,5,4096
qw 5
qm 80,96,136
v_mul_f32 v16,v16,v67
v_lshrrev_b32 v6,1,v0
v_add_u32 v28,0x10000,v24
qt 110,5,4608
ql 8,24,0
qm 84,96,208
v_exp_f32 v16,v16
v_add_u32 v29,0x10000,v25
qt 128,36,0
qt 96,4,8192
qw 6
qm 88,100,136
v_fma_f32 v16,v16,v14,v14
v_mul_u32_u24 v6,1152,v6
qt 130,36,512
qt 98,4,8704
ql 9,24,1024
qm 92,100,208
v_rcp_f32 v16,v16
v_add_u32 v30,0x10000,v26
qt 200,36,1024
s_add_u32 s15,s77,81920
buffer_load_dwordx4 v[32:35],v18,s[88:91],s15 offen offset:0 sc1
qt 100,5,8192
qw 8
qm 80,104,140
v_mul_f32 v67,v67,v16
v_lshrrev_b32 v7,4,v0
v_add_u32 v31,0x10000,v27
qt 202,36,1536
qt 102,5,8704
s_add_u32 m0,s13,49152
qm 84,104,212
v_mul_f32 v16,v72,v72
qt 132,36,8192
qt 104,4,12288
qw 10
qm 88,108,140
v_fma_f32 v16,v16,v12,v10
v_xor_b32 v7,v7,v0
qt 134,36,8704
qt 106,4,12800
s_add_u32 s14,s28,442368
qm 92,108,212
v_mul_f32 v16,v16,v72
qt 204,36,9216
qt 108,5,12288
qw 10
qm 80,96,144
v_exp_f32 v16,v16
qt 206,36,9728
qt 110,5,12800
ql 8,24,0
qm 84,96,216
v_fma_f32 v16,v16,v14,v14
v_and_b32 v7,1,v7
qt 136,36,16384
qt 96,2,0
qw 10
qm 88,100,144
v_rcp_f32 v16,v16
qt 138,36,16896
qt 98,2,512
ql 9,24,1024
qm 92,100,216
v_mul_f32 v72,v72,v16
v_lshl_add_u32 v6,v7,4,v6
qt 208,36,17408
qt 100,3,0
qw 10
qm 80,104,148
v_mul_f32 v16,v73,v73
qt 210,36,17920
qt 102,3,512
s_add_u32 m0,s13,57344
qm 84,104,220
v_fma_f32 v16,v16,v12,v10
v_add_u32 v7,72704,v6
qt 140,36,24576
s_add_u32 s15,s77,81920
buffer_load_dwordx4 v[42:45],v18,s[88:91],s15 offen offset:1024 sc1
qt 104,2,4096
qw 10
qm 88,108,148
v_mul_f32 v16,v16,v73
qt 142,36,25088
qt 106,2,4608
s_add_u32 s14,s28,460800
qm 92,108,220
v_exp_f32 v16,v16
qt 212,36,25600
qt 108,3,4096
qw 10
qm 80,96,152
v_fma_f32 v16,v16,v14,v14
qt 214,36,26112
qt 110,3,4608
ql 8,24,0
qm 84,96,224
v_rcp_f32 v16,v16
qt 96,2,8192
qw 9
qm 88,100,152
v_mul_f32 v73,v73,v16
s_mul_i32 s30,s10,1310720
qt 98,2,8704
ql 9,24,1024
qm 92,100,224
v_mul_f32 v16,v74,v74
s_lshl_b32 s31,s8,14
qt 100,3,8192
qw 7
qm 80,104,156
v_fma_f32 v16,v16,v12,v10
qt 102,3,8704
s_add_u32 m0,s13,65536
qm 84,104,228
v_mul_f32 v16,v16,v74
s_add_u32 s73,s30,s31
qt 104,2,12288
qw 5
qm 88,108,156
s_waitcnt vmcnt(8)
s_barrier
v_exp_f32 v16,v16
qt 106,2,12800
s_add_u32 s14,s28,294912
qm 92,108,228
v_fma_f32 v16,v16,v14,v14
s_add_u32 s73,s73,49152
s_add_u32 s15,s77,0
buffer_load_dwordx4 v[48:51],v18,s[88:91],s15 offen offset:0 sc1
qt 108,3,12288
qw 5
qm 80,96,160
v_rcp_f32 v16,v16
qt 110,3,12800
ql 8,24,0
qm 84,96,232
v_mul_f32 v74,v74,v16
s_lshl_b32 s31,s8,11
qt 96,2,16384
qw 5
qm 88,100,160
v_mul_f32 v16,v75,v75
s_add_u32 s69,s30,s31
qt 98,2,16896
ql 9,24,1024
qm 92,100,232
v_fma_f32 v16,v16,v12,v10
qt 100,3,16384
qw 5
qm 80,104,164
v_mul_f32 v16,v16,v75
s_lshl_b32 s31,s7,8
qt 102,3,16896
s_add_u32 m0,s13,73728
qm 84,104,236
v_exp_f32 v16,v16
qt 104,2,20480
qw 5
qm 88,108,164
v_fma_f32 v16,v16,v14,v14
s_add_u32 s69,s69,s31
qt 106,2,20992
s_add_u32 s14,s28,313344
qm 92,108,236
v_rcp_f32 v16,v16
qt 108,3,20480
qw 5
qm 80,96,168
v_mul_f32 v75,v75,v16
s_add_u32 s69,s69,132096
qt 110,3,20992
ql 8,24,0
qm 84,96,240
v_mul_f32 v16,v68,v68
s_and_b32 s75,s8,4
s_add_u32 s15,s77,0
buffer_load_dwordx4 v[56:59],v18,s[88:91],s15 offen offset:1024 sc1
qt 96,2,24576
qw 5
qm 88,100,168
v_fma_f32 v16,v16,v13,v11
qt 98,2,25088
ql 9,24,1024
qm 92,100,240
v_mul_f32 v16,v16,v68
s_lshl_b32 s75,s75,4
qt 100,3,24576
qw 5
qm 80,104,172
v_exp_f32 v16,v16
qt 102,3,25088
s_mov_b32 m0,s13
qm 84,104,244
v_fma_f32 v16,v16,v15,v15
s_and_b32 s33,s8,3
s_lshl_b32 s33,s33,2
s_add_u32 s33,s33,s7
s_lshl_b32 s33,s33,3
s_lshl_b32 s30,s10,7
s_add_u32 s30,s30,s33
qt 104,2,28672
qw 5
qm 88,108,172
v_rcp_f32 v16,v16
qt 106,2,29184
s_add_u32 s14,s19,369280
qm 92,108,244
v_mul_f32 v68,v68,v16
s_add_u32 s30,s30,4
qt 108,3,28672
qw 5
qm 80,96,176
v_mul_f32 v16,v69,v69
s_mul_i32 s30,s30,1152
qt 110,3,29184
ql 20,48,0
qm 84,96,248
v_fma_f32 v16,v16,v13,v11
qd 96,24,32768
qw 5
qm 88,100,176
v_mul_f32 v16,v16,v69
s_add_u32 s70,s30,s75
qd 98,24,36864
ql 21,48,1024
qm 92,100,248
v_exp_f32 v16,v16
s_add_u32 s15,s77,8192
buffer_load_dwordx4 v[52:55],v18,s[88:91],s15 offen offset:0 sc1
qd 100,25,32768
qw 5
qm 80,104,180
v_fma_f32 v16,v16,v15,v15
s_lshl_b32 s71,s70,1
qd 102,25,36864
s_add_u32 m0,s13,8192
qm 84,104,252
v_rcp_f32 v16,v16
qd 104,26,32768
qw 5
qm 88,108,180
s_waitcnt vmcnt(9)
s_barrier
v_mul_f32 v69,v69,v16
qd 106,26,36864
s_add_u32 s14,s19,369408
qm 92,108,252
qd 108,27,32768
qw 5
qz 152,96,128
v_mul_f32 v16,v70,v70
v_mul_f32 v17,v71,v71
v_fma_f32 v16,v16,v13,v11
qd 110,27,36864
ql 20,48,0
qz 224,96,200
v_fma_f32 v17,v17,v13,v11
v_mul_f32 v16,v16,v70
qd 96,24,40960
qw 5
qm 152,100,132
v_mul_f32 v17,v17,v71
v_exp_f32 v16,v16
qd 98,24,45056
ql 21,48,1024
qm 224,100,204
v_exp_f32 v17,v17
v_fma_f32 v16,v16,v15,v15
v_fma_f32 v17,v17,v15,v15
qd 100,25,40960
qw 5
qm 152,104,136
v_rcp_f32 v16,v16
v_rcp_f32 v17,v17
qd 102,25,45056
s_add_u32 m0,s13,16384
qm 224,104,208
v_mul_f32 v70,v70,v16
v_mul_f32 v71,v71,v17
qd 104,26,40960
qw 5
qm 152,108,140
v_mul_f32 v16,v76,v76
v_mul_f32 v17,v77,v77
v_fma_f32 v16,v16,v13,v11
s_add_u32 s15,s77,8192
buffer_load_dwordx4 v[60:63],v18,s[88:91],s15 offen offset:1024 sc1
qd 106,26,45056
s_add_u32 s14,s19,369152
qm 224,108,212
v_fma_f32 v17,v17,v13,v11
v_mul_f32 v16,v16,v76
qd 108,27,40960
qw 5
qz 156,96,128
v_mul_f32 v17,v17,v77
v_exp_f32 v16,v16
qd 110,27,45056
ql 20,48,0
qz 228,96,200
v_exp_f32 v17,v17
v_fma_f32 v16,v16,v15,v15
v_fma_f32 v17,v17,v15,v15
qd 96,24,49152
qw 5
qm 156,100,132
v_rcp_f32 v16,v16
v_rcp_f32 v17,v17
qd 98,24,53248
ql 21,48,1024
qm 228,100,204
v_mul_f32 v76,v76,v16
v_mul_f32 v77,v77,v17
qd 100,25,49152
qw 5
qm 156,104,136
v_mul_f32 v16,v78,v78
v_mul_f32 v17,v79,v79
v_fma_f32 v16,v16,v13,v11
qd 102,25,53248
s_add_u32 m0,s13,24576
qm 228,104,208
v_fma_f32 v17,v17,v13,v11
v_mul_f32 v16,v16,v78
qd 104,26,49152
qw 5
qm 156,108,140
v_mul_f32 v17,v17,v79
v_exp_f32 v16,v16
qd 106,26,53248
s_add_u32 s14,s19,443008
qm 228,108,212
v_exp_f32 v17,v17
v_fma_f32 v16,v16,v15,v15
v_fma_f32 v17,v17,v15,v15
qd 108,27,49152
qw 5
qz 160,96,128
v_rcp_f32 v16,v16
v_rcp_f32 v17,v17
s_add_u32 s15,s77,16384
buffer_load_dwordx4 v[120:123],v18,s[88:91],s15 offen offset:0 sc1
qd 110,27,53248
ql 20,48,0
qz 232,96,200
v_mul_f32 v78,v78,v16
v_mul_f32 v79,v79,v17
qd 96,24,57344
qw 5
qm 160,100,132
v_mul_f32 v64,v64,v80
v_mul_f32 v65,v65,v81
v_cvt_pk_bf16_f32 v64,v64,v65
qd 98,24,61440
ql 21,48,1024
qm 232,100,204
v_mul_f32 v66,v66,v82
v_mul_f32 v67,v67,v83
qd 100,25,57344
qw 5
qm 160,104,136
v_cvt_pk_bf16_f32 v65,v66,v67
v_mul_f32 v72,v72,v88
qd 102,25,61440
s_add_u32 m0,s13,32768
qm 232,104,208
v_mul_f32 v73,v73,v89
v_cvt_pk_bf16_f32 v66,v72,v73
v_mul_f32 v74,v74,v90
qd 104,26,57344
qw 5
qm 160,108,140
s_waitcnt vmcnt(9)
s_barrier
v_mul_f32 v75,v75,v91
v_cvt_pk_bf16_f32 v67,v74,v75
qd 106,26,61440
s_add_u32 s14,s19,443136
qm 232,108,212
s_add_u32 s15,s76,49152
buffer_store_dwordx4 v[64:67],v18,s[88:91],s15 offen offset:0 sc1
ds_write_b128 v18,v[64:67]
qd 108,27,57344
qw 6
qz 164,96,128
v_mul_f32 v68,v68,v84
v_mul_f32 v69,v69,v85
v_cvt_pk_bf16_f32 v68,v68,v69
qd 110,27,61440
ql 20,48,0
qz 236,96,200
v_mul_f32 v70,v70,v86
v_mul_f32 v71,v71,v87
qd 96,28,0
qw 6
qm 164,100,132
v_cvt_pk_bf16_f32 v69,v70,v71
v_mul_f32 v76,v76,v92
s_add_u32 s15,s77,16384
buffer_load_dwordx4 v[192:195],v18,s[88:91],s15 offen offset:1024 sc1
qd 98,28,4096
ql 21,48,1024
qm 236,100,204
v_mul_f32 v77,v77,v93
v_cvt_pk_bf16_f32 v70,v76,v77
v_mul_f32 v78,v78,v94
qd 100,29,0
qw 6
qm 164,104,136
v_mul_f32 v79,v79,v95
v_cvt_pk_bf16_f32 v71,v78,v79
qd 102,29,4096
s_add_u32 m0,s13,40960
qm 236,104,208
s_add_u32 s15,s76,49152
buffer_store_dwordx4 v[68:71],v18,s[88:91],s15 offen offset:1024 sc1
ds_write_b128 v18,v[68:71] offset:1024
qd 104,30,0
qw 6
qm 164,108,140
qd 106,30,4096
s_add_u32 s14,s19,442880
qm 236,108,212
qd 108,31,0
qw 6
qz 144,96,128
qd 110,31,4096
ql 20,48,0
qz 216,96,200
qr 176,18,16384
qd 96,28,8192
qw 7
qm 144,100,132
qd 98,28,12288
ql 21,48,1024
qm 216,100,204
qr 248,18,17408
qd 100,29,8192
qw 7
qm 144,104,136
s_add_u32 s15,s77,24576
buffer_load_dwordx4 v[124:127],v18,s[88:91],s15 offen offset:0 sc1
qd 102,29,12288
s_add_u32 m0,s13,49152
qm 216,104,208
qr 180,18,24576
qd 104,30,8192
qw 8
qm 144,108,140
qd 106,30,12288
s_add_u32 s14,s19,516736
qm 216,108,212
qr 252,18,25600
qd 108,31,8192
qw 8
qz 148,96,128
qd 110,31,12288
ql 20,48,0
qz 220,96,200
qt 96,2,0
qw 7
qm 148,100,132
qt 98,2,512
ql 21,48,1024
qm 220,100,204
qt 100,3,0
qw 6
qm 148,104,136
qt 102,3,512
s_add_u32 m0,s13,57344
qm 220,104,208
qt 104,2,4096
qw 5
qm 148,108,140
s_waitcnt vmcnt(10)
s_barrier
s_add_u32 s15,s77,24576
buffer_load_dwordx4 v[196:199],v18,s[88:91],s15 offen offset:1024 sc1
qt 106,2,4608
s_add_u32 s14,s19,516864
qm 220,108,212
qt 108,3,4096
qw 5
qm 152,96,112
qt 110,3,4608
ql 20,48,0
qm 224,96,184
qr 168,18,32768
qt 96,2,8192
qw 6
qm 156,100,112
qt 98,2,8704
ql 21,48,1024
qm 228,100,184
qr 240,18,33792
qt 100,3,8192
s_add_u32 m0,s13,65536
s_add_u32 s14,s19,516608
qw 7
qm 152,104,116
qt 102,3,8704
ql 20,48,0
qm 224,104,188
qr 172,18,40960
qt 104,2,12288
qw 8
qm 156,108,116
qt 106,2,12800
ql 21,48,1024
qm 228,108,188
qr 244,18,41984
qt 108,3,12288
s_add_u32 m0,s13,73728
s_add_u32 s14,s19,590336
qw 8
qm 160,96,112
qt 110,3,12800
ql 20,48,0
qm 232,96,184
qt 96,2,16384
qw 7
qm 164,100,112
qt 98,2,16896
ql 21,48,1024
qm 236,100,184
qt 100,3,16384
qw 6
qm 160,104,116
qt 102,3,16896
qm 232,104,188
qt 104,2,20480
qw 5
qm 164,108,116
qt 106,2,20992
qm 236,108,188
qt 108,3,20480
qw 5
qm 144,96,112
qt 110,3,20992
qm 216,96,184
qt 96,2,24576
qw 5
qm 148,100,112
qt 98,2,25088
qm 220,100,184
qt 100,3,24576
qw 5
qm 144,104,116
qt 102,3,25088
s_mov_b32 m0,s13
qm 216,104,188
qt 104,2,28672
qw 5
qm 148,108,116
s_waitcnt vmcnt(0)
s_barrier
qt 106,2,29184
s_add_u32 s14,s19,590464
qm 220,108,188
qt 108,3,28672
qw 5
qm 152,96,176
v_and_b32 v1,0xfffffc3f,v18
qt 110,3,29184
ql 20,48,0
qm 224,96,248
qr 64,18,0
qt 96,2,32768
qw 6
qm 156,100,176
qt 98,2,33280
ql 21,48,1024
qm 228,100,248
qr 72,18,1024
qt 100,3,32768
qw 7
qm 152,104,180
v_lshrrev_b32 v16,4,v0
qt 102,3,33280
s_add_u32 m0,s13,8192
qm 224,104,252
qr 68,18,8192
qt 104,2,36864
qw 8
qm 156,108,180
qt 106,2,37376
s_add_u32 s14,s19,590592
qm 228,108,252
qr 76,18,9216
s_add_u32 s15,s77,32768
buffer_load_dwordx4 v[80:83],v18,s[88:91],s15 offen offset:0 sc1
qt 108,3,36864
qw 8
qm 160,96,176
qt 110,3,37376
ql 20,48,0
qm 232,96,248
v_lshl_add_u32 v1,v16,6,v1
qt 96,2,40960
qw 7
qm 164,100,176
qt 98,2,41472
ql 21,48,1024
qm 236,100,248
qt 100,3,40960
qw 6
qm 160,104,180
qt 102,3,41472
s_add_u32 m0,s13,16384
qm 232,104,252
qt 104,2,45056
qw 5
qm 164,108,180
v_and_b32 v16,12,v0
qt 106,2,45568
s_add_u32 s14,s18,368640
qm 236,108,252
qt 108,3,45056
qw 5
qm 144,96,176
qt 110,3,45568
ql 6,24,0
qm 216,96,248
s_add_u32 s15,s77,32768
buffer_load_dwordx4 v[88:91],v18,s[88:91],s15 offen offset:1024 sc1
qt 96,2,49152
qw 5
qm 148,100,176
qt 98,2,49664
ql 7,24,1024
qm 220,100,248
v_lshl_add_u32 v1,v16,6,v1
qt 100,3,49152
qw 5
qm 144,104,180
qt 102,3,49664
s_add_u32 m0,s13,24576
qm 216,104,252
qt 104,2,53248
qw 5
qm 148,108,180
s_barrier
qt 106,2,53760
s_add_u32 s14,s19,640
qm 220,108,252
qt 108,3,53248
qw 5
qm 152,96,168
qt 110,3,53760
ql 20,48,0
qm 224,96,240
qt 96,2,57344
qw 5
qm 156,100,168
qt 98,2,57856
ql 21,48,1024
qm 228,100,240
s_add_u32 s15,s77,40960
buffer_load_dwordx4 v[84:87],v18,s[88:91],s15 offen offset:0 sc1
qt 100,3,57344
qw 5
qm 152,104,172
qt 102,3,57856
s_add_u32 m0,s13,32768
qm 224,104,244
qt 104,2,61440
qw 5
qm 156,108,172
qt 106,2,61952
s_add_u32 s14,s19,768
qm 228,108,244
qt 108,3,61440
qw 5
qm 160,96,168
qt 110,3,61952
ql 20,48,0
qm 232,96,240
qt 96,4,0
qw 5
qm 164,100,168
qt 98,4,512
ql 21,48,1024
qm 236,100,240
qt 100,5,0
qw 5
qm 160,104,172
qt 102,5,512
s_add_u32 m0,s13,40960
qm 232,104,244
s_add_u32 s15,s77,40960
buffer_load_dwordx4 v[92:95],v18,s[88:91],s15 offen offset:1024 sc1
qt 104,4,4096
qw 5
qm 164,108,172
qt 106,4,4608
s_add_u32 s14,s19,74368
qm 236,108,244
qt 108,5,4096
qw 5
qm 144,96,168
qt 110,5,4608
ql 20,48,0
qm 216,96,240
qt 96,4,8192
qw 5
qm 148,100,168
qt 98,4,8704
ql 21,48,1024
qm 220,100,240
qt 100,5,8192
qw 5
qm 144,104,172
qt 102,5,8704
s_add_u32 m0,s13,49152
qm 216,104,244
qt 104,4,12288
qw 5
qm 148,108,172
s_waitcnt vmcnt(8)
s_barrier
qt 106,4,12800
s_add_u32 s14,s19,74496
qm 220,108,244
qt 108,5,12288
qw 5
qm 144,96,64
s_add_u32 s15,s77,49152
buffer_load_dwordx4 v[128:131],v18,s[88:91],s15 offen offset:0 sc1
qt 110,5,12800
ql 20,48,0
qm 216,96,72
qt 96,2,0
qw 5
qm 148,100,64
qt 98,2,512
ql 21,48,1024
qm 220,100,72
qt 100,3,0
qw 5
qm 144,104,68
qt 102,3,512
s_add_u32 m0,s13,57344
qm 216,104,76
qt 104,2,4096
qw 5
qm 148,108,68
qt 106,2,4608
s_add_u32 s14,s19,148096
qm 220,108,76
qt 108,3,4096
qw 5
qm 152,96,64
s_add_u32 s15,s77,49152
buffer_load_dwordx4 v[200:203],v18,s[88:91],s15 offen offset:1024 sc1
qt 110,3,4608
ql 20,48,0
qm 224,96,72
qt 96,2,8192
qw 5
qm 156,100,64
qt 98,2,8704
ql 21,48,1024
qm 228,100,72
v_cvt_pk_bf16_f32 v144,v144,v145
qt 100,3,8192
qw 5
qm 152,104,68
v_cvt_pk_bf16_f32 v145,v146,v147
qt 102,3,8704
s_add_u32 m0,s13,65536
qm 224,104,76
v_cvt_pk_bf16_f32 v146,v148,v149
qt 104,2,12288
qw 5
qm 156,108,68
v_cvt_pk_bf16_f32 v147,v150,v151
qt 106,2,12800
s_add_u32 s14,s19,148224
qm 228,108,76
qt 108,3,12288
qw 5
qm 160,96,64
s_add_u32 s15,s77,57344
buffer_load_dwordx4 v[132:135],v18,s[88:91],s15 offen offset:0 sc1
v_cvt_pk_bf16_f32 v216,v216,v217
qt 110,3,12800
ql 20,48,0
qm 232,96,72
v_cvt_pk_bf16_f32 v217,v218,v219
qd 96,22,16384
qw 5
qm 164,100,64
v_cvt_pk_bf16_f32 v218,v220,v221
qd 98,22,20480
ql 21,48,1024
qm 236,100,72
v_cvt_pk_bf16_f32 v219,v222,v223
qd 100,22,16896
qw 5
qm 160,104,68
qd 102,22,20992
s_add_u32 m0,s13,73728
qm 232,104,76
qd 104,22,17408
qw 5
qm 164,108,68
s_waitcnt vmcnt(9)
s_barrier
qd 106,22,21504
s_add_u32 s14,s19,221824
qm 236,108,76
qd 108,22,17920
qw 5
qm 152,96,32
s_add_u32 s15,s77,57344
buffer_load_dwordx4 v[204:207],v18,s[88:91],s15 offen offset:1024 sc1
v_and_b32 v15,48,v0
qd 110,22,22016
ql 20,48,0
qm 224,96,42
qt 96,2,24576
qw 5
qm 156,100,32
v_lshlrev_b32 v10,10,v15
qt 98,2,25088
ql 21,48,1024
qm 228,100,42
qt 100,3,24576
qw 5
qm 160,104,32
qt 102,3,25088
s_mov_b32 m0,s13
qm 232,104,42
v_and_b32 v16,16,v0
qt 104,2,28672
qw 5
qm 164,108,32
qt 106,2,29184
s_add_u32 s14,s19,295552
qm 236,108,42
qt 108,3,28672
qw 5
qm 152,96,48
s_add_u32 s15,s77,65536
buffer_load_dwordx4 v[136:139],v18,s[88:91],s15 offen offset:0 sc1
v_lshrrev_b32 v17,5,v0
qt 110,3,29184
ql 20,48,0
qm 224,96,56
qt 96,2,32768
qw 5
qm 156,100,48
v_lshl_add_u32 v12,v17,6,v16
qt 98,2,33280
ql 21,48,1024
qm 228,100,56
qt 100,3,32768
qw 5
qm 152,104,52
qt 102,3,33280
s_add_u32 m0,s13,8192
qm 224,104,60
v_and_b32 v16,3,v0
qt 104,2,36864
qw 5
qm 156,108,52
qt 106,2,37376
s_add_u32 s14,s19,221952
qm 228,108,60
qt 108,3,36864
qw 5
qm 160,96,48
s_add_u32 s15,s77,65536
buffer_load_dwordx4 v[208:211],v18,s[88:91],s15 offen offset:1024 sc1
v_lshl_add_u32 v12,v16,2,v12
qt 110,3,37376
ql 20,48,0
qm 232,96,56
qt 96,2,40960
qw 5
qm 164,100,48
v_lshl_add_u32 v10,v16,6,v10
qt 98,2,41472
ql 21,48,1024
qm 236,100,56
qt 100,3,40960
qw 5
qm 160,104,52
qt 102,3,41472
s_add_u32 m0,s13,16384
qm 232,104,60
v_bfe_u32 v11,v0,2,2
qt 104,2,45056
qw 5
qm 164,108,52
s_waitcnt vmcnt(9)
s_barrier
qt 106,2,45568
s_add_u32 s14,s19,295680
qm 236,108,60
qt 108,3,45056
qw 5
qm 152,96,120
s_add_u32 s15,s77,73728
buffer_load_dwordx4 v[140:143],v18,s[88:91],s15 offen offset:0 sc1
v_lshl_add_u32 v10,v11,4,v10
qt 110,3,45568
ql 20,48,0
qm 224,96,192
qt 96,2,49152
qw 5
qm 156,100,120
v_lshlrev_b32 v12,1,v12
qt 98,2,49664
ql 21,48,1024
qm 228,100,192
qt 100,3,49152
qw 5
qm 152,104,124
qt 102,3,49664
s_add_u32 m0,s13,24576
qm 224,104,196
v_mul_u32_u24 v11,1152,v11
qt 104,2,53248
qw 5
qm 156,108,124
qt 106,2,53760
s_add_u32 s14,s19,369536
qm 228,108,196
qt 108,3,53248
qw 5
qm 160,96,120
s_add_u32 s15,s77,73728
buffer_load_dwordx4 v[212:215],v18,s[88:91],s15 offen offset:1024 sc1
v_add_u32 v11,v11,v12
qt 110,3,53760
ql 20,48,0
qm 232,96,192
qt 96,2,57344
qw 5
qm 164,100,120
v_lshlrev_b32 v13,1,v11
qt 98,2,57856
ql 21,48,1024
qm 236,100,192
qt 100,3,57344
qw 5
qm 160,104,124
qt 102,3,57856
s_add_u32 m0,s13,32768
qm 232,104,196
v_lshl_or_b32 v14,v17,30,v13
qt 104,2,61440
qw 5
qm 164,108,124
qt 106,2,61952
s_add_u32 s14,s19,369664
qm 236,108,196
qt 108,3,61440
qw 5
qm 152,96,80
s_and_b32 s30,s8,3
s_lshl_b32 s30,s30,4
s_mov_b64 s[32:33],0xffff
s_lshl_b64 s[32:33],s[32:33],s30
s_bitcmp1_b32 s8,2
s_cselect_b64 s[40:41],0,s[32:33]
s_cselect_b64 s[42:43],s[32:33],0
qt 110,3,61952
ql 20,48,0
qm 224,96,88
qt 96,4,0
qw 5
qm 156,100,80
s_mov_b64 s[52:53],0
qt 98,4,512
ql 21,48,1024
qm 228,100,88
qt 100,5,0
qw 5
qm 152,104,84
qt 102,5,512
s_add_u32 m0,s13,40960
qm 224,104,92
s_add_u32 s15,s73,1179648
buffer_store_dwordx4 v[144:147],v1,s[88:91],s15 offen offset:0 sc1
s_mov_b64 s[54:55],0
qt 104,4,4096
qw 5
qm 156,108,84
s_waitcnt vmcnt(9)
s_barrier
qt 106,4,4608
s_add_u32 s14,s18,516096
qm 228,108,92
qt 108,5,4096
qw 5
qm 160,96,80
s_mov_b64 s[56:57],0
qt 110,5,4608
ql 6,24,0
qm 232,96,88
qt 96,4,8192
qw 5
qm 164,100,80
s_mov_b64 s[58:59],0
qt 98,4,8704
ql 7,24,1024
qm 236,100,88
qt 100,5,8192
qw 5
qm 160,104,84
qt 102,5,8704
s_add_u32 m0,s13,49152
qm 232,104,92
s_add_u32 s15,s73,1179648
buffer_store_dwordx4 v[216:219],v1,s[88:91],s15 offen offset:1024 sc1
s_mov_b32 s76,0
qt 104,4,12288
qw 5
qm 164,108,84
qt 106,4,12800
s_add_u32 s14,s19,896
qm 236,108,92
qt 108,5,12288
qw 5
qm 152,96,128
qt 110,5,12800
ql 20,48,0
qm 224,96,200
qt 96,2,0
qw 5
qm 156,100,128
qt 98,2,512
ql 21,48,1024
qm 228,100,200
qt 100,3,0
qw 5
qm 152,104,132
qt 102,3,512
s_add_u32 m0,s13,57344
qm 224,104,204
qt 104,2,4096
qw 5
qm 156,108,132
qt 106,2,4608
s_add_u32 s14,s19,1024
qm 228,108,204
qt 108,3,4096
qw 5
qm 152,96,136
qt 110,3,4608
ql 20,48,0
qm 224,96,208
qt 96,2,8192
qw 5
qm 156,100,136
qt 98,2,8704
ql 21,48,1024
qm 228,100,208
qt 100,3,8192
s_waitcnt vmcnt(15) lgkmcnt(5)
qm 152,104,140
qt 102,3,8704
s_add_u32 m0,s13,65536
s_waitcnt vmcnt(12)
qm 224,104,212
qt 104,2,12288
qw 5
qm 156,108,140
s_waitcnt vmcnt(8)
s_barrier
qt 106,2,12800
s_add_u32 s14,s19,74624
qm 228,108,212
qt 108,3,12288
qw 5
qm 160,96,128
qt 110,3,12800
ql 20,48,0
qm 232,96,200
qt 96,2,16384
qw 5
qm 164,100,128
qt 98,2,16896
ql 21,48,1024
qm 236,100,200
v_cvt_pk_bf16_f32 v152,v152,v153
qt 100,3,16384
qw 5
qm 160,104,132
v_cvt_pk_bf16_f32 v153,v154,v155
qt 102,3,16896
s_add_u32 m0,s13,73728
qm 232,104,204
v_cvt_pk_bf16_f32 v154,v156,v157
qt 104,2,20480
qw 5
qm 164,108,132
v_cvt_pk_bf16_f32 v155,v158,v159
qt 106,2,20992
s_add_u32 s14,s19,74752
qm 236,108,204
qt 108,3,20480
qw 5
qm 160,96,136
v_cvt_pk_bf16_f32 v224,v224,v225
qt 110,3,20992
ql 20,48,0
qm 232,96,208
v_cvt_pk_bf16_f32 v225,v226,v227
qt 96,2,24576
qw 5
qm 164,100,136
v_cvt_pk_bf16_f32 v226,v228,v229
qt 98,2,25088
ql 21,48,1024
qm 236,100,208
v_cvt_pk_bf16_f32 v227,v230,v231
qt 100,3,24576
qw 5
qm 160,104,140
s_add_u32 s15,s73,655360
buffer_store_dwordx4 v[152:155],v1,s[88:91],s15 offen offset:0 sc1
qt 102,3,25088
s_mov_b32 m0,s13
qm 232,104,212
qt 104,2,28672
qw 5
qm 164,108,140
qt 106,2,29184
s_add_u32 s14,s19,148352
qm 236,108,212
qt 108,3,28672
qw 5
qz 168,96,112
qt 110,3,29184
ql 20,48,0
qz 240,96,184
qt 96,2,32768
qw 5
qz 172,100,112
qt 98,2,33280
ql 21,48,1024
qz 244,100,184
v_cvt_pk_bf16_f32 v160,v160,v161
qt 100,3,32768
qw 5
qm 168,104,116
s_add_u32 s15,s73,655360
buffer_store_dwordx4 v[224:227],v1,s[88:91],s15 offen offset:1024 sc1
v_cvt_pk_bf16_f32 v161,v162,v163
qt 102,3,33280
s_add_u32 m0,s13,8192
qm 240,104,188
v_cvt_pk_bf16_f32 v162,v164,v165
qt 104,2,36864
qw 5
qm 172,108,116
s_waitcnt vmcnt(8)
s_barrier
v_cvt_pk_bf16_f32 v163,v166,v167
qt 106,2,37376
s_add_u32 s14,s19,148480
qm 244,108,188
qt 108,3,36864
qw 5
qz 176,96,112
v_cvt_pk_bf16_f32 v232,v232,v233
qt 110,3,37376
ql 20,48,0
qz 248,96,184
v_cvt_pk_bf16_f32 v233,v234,v235
qd 96,22,40960
qw 5
qz 180,100,112
v_cvt_pk_bf16_f32 v234,v236,v237
qd 98,22,45056
ql 21,48,1024
qz 252,100,184
v_cvt_pk_bf16_f32 v235,v238,v239
qd 100,22,41472
qw 5
qm 176,104,116
s_add_u32 s15,s73,786432
buffer_store_dwordx4 v[160:163],v1,s[88:91],s15 offen offset:0 sc1
qd 102,22,45568
s_add_u32 m0,s13,16384
qm 248,104,188
v_and_b32 v16,1,v0
qd 104,22,41984
qw 5
qm 180,108,116
qd 106,22,46080
s_add_u32 s14,s19,222080
qm 252,108,188
v_lshlrev_b32 v16,4,v16
qd 108,22,42496
qw 5
qm 168,96,32
qd 110,22,46592
ql 20,48,0
qm 240,96,42
v_mov_b32 v17,0x3f80
qt 96,2,49152
qw 5
qm 172,100,32
qt 98,2,49664
ql 21,48,1024
qm 244,100,42
v_lshlrev_b32 v16,v16,v17
qt 100,3,49152
qw 5
qm 176,104,32
s_add_u32 s15,s73,786432
buffer_store_dwordx4 v[232:235],v1,s[88:91],s15 offen offset:1024 sc1
qt 102,3,49664
s_add_u32 m0,s13,24576
qm 248,104,42
v_bfe_u32 v17,v0,1,3
qt 104,2,53248
qw 5
qm 180,108,32
qt 106,2,53760
s_add_u32 s14,s19,222208
qm 252,108,42
v_cmp_eq_u32 vcc,0,v17
s_nop 1
v_cndmask_b32 v152,0,v16,vcc
qt 108,3,53248
qw 5
qm 168,96,48
qt 110,3,53760
ql 20,48,0
qm 240,96,56
v_cmp_eq_u32 vcc,1,v17
s_nop 1
v_cndmask_b32 v153,0,v16,vcc
qt 96,2,57344
qw 5
qm 172,100,48
qt 98,2,57856
ql 21,48,1024
qm 244,100,56
v_cmp_eq_u32 vcc,2,v17
s_nop 1
v_cndmask_b32 v154,0,v16,vcc
qt 100,3,57344
qw 5
qm 168,104,52
qt 102,3,57856
s_add_u32 m0,s13,32768
qm 240,104,60
v_cmp_eq_u32 vcc,3,v17
s_nop 1
v_cndmask_b32 v155,0,v16,vcc
qt 104,2,61440
qw 5
qm 172,108,52
s_waitcnt vmcnt(9)
s_barrier
qt 106,2,61952
s_add_u32 s14,s19,295808
qm 244,108,60
v_cmp_eq_u32 vcc,4,v17
s_nop 1
v_cndmask_b32 v156,0,v16,vcc
qt 108,3,61440
qw 5
qm 176,96,48
qt 110,3,61952
ql 20,48,0
qm 248,96,56
v_cmp_eq_u32 vcc,5,v17
s_nop 1
v_cndmask_b32 v157,0,v16,vcc
qt 96,4,0
qw 5
qm 180,100,48
qt 98,4,512
ql 21,48,1024
qm 252,100,56
v_cmp_eq_u32 vcc,6,v17
s_nop 1
v_cndmask_b32 v158,0,v16,vcc
qt 100,5,0
s_add_u32 m0,s13,40960
s_add_u32 s14,s19,295936
qw 5
qm 176,104,52
qt 102,5,512
ql 20,48,0
qm 248,104,60
v_cmp_eq_u32 vcc,7,v17
s_nop 1
v_cndmask_b32 v159,0,v16,vcc
qt 104,4,4096
qw 5
qm 180,108,52
qt 106,4,4608
ql 21,48,1024
qm 252,108,60
qt 108,5,4096
s_add_u32 m0,s13,49152
s_add_u32 s14,s19,590720
qw 5
qm 168,96,120
qt 110,5,4608
ql 20,48,0
qm 240,96,192
qt 96,4,8192
qw 5
qm 172,100,120
qt 98,4,8704
ql 21,48,1024
qm 244,100,192
qt 100,5,8192
qw 5
qm 168,104,124
qt 102,5,8704
qm 240,104,196
qt 104,4,12288
qw 5
qm 172,108,124
qt 106,4,12800
qm 244,108,196
qt 108,5,12288
qw 5
qm 176,96,120
qt 110,5,12800
qm 248,96,192
qt 96,2,0
qw 5
qm 180,100,120
qt 98,2,512
qm 252,100,192
qt 100,3,0
qw 5
qm 176,104,124
qt 102,3,512
s_add_u32 m0,s13,57344
qm 248,104,196
qt 104,2,4096
qw 5
qm 180,108,124
s_waitcnt vmcnt(0)
s_barrier
qt 106,2,4608
s_add_u32 s14,s19,443264
qm 252,108,196
qt 108,3,4096
qw 5
qm 168,96,80
buffer_load_dwordx2 v[224:225],v11,s[36:39],s70 offen offset:256
qt 110,3,4608
ql 20,48,0
qm 240,96,88
qr 120,18,16384
qt 96,2,8192
qw 6
qm 172,100,80
qt 98,2,8704
ql 21,48,1024
qm 244,100,88
qr 192,18,17408
qt 100,3,8192
qw 7
qm 168,104,84
qt 102,3,8704
s_add_u32 m0,s13,65536
qm 240,104,92
qr 124,18,24576
qt 104,2,12288
qw 8
qm 172,108,84
qt 106,2,12800
s_add_u32 s14,s28,516096
qm 244,108,92
qr 196,18,25600
qt 108,3,12288
qw 8
qm 176,96,80
qt 110,3,12800
ql 8,24,0
qm 248,96,88
buffer_load_dwordx2 v[226:227],v12,s[80:83],s75 offen offset:256
qt 96,2,16384
qw 7
qm 180,100,80
qt 98,2,16896
ql 9,24,1024
qm 252,100,88
qt 100,3,16384
qw 6
qm 176,104,84
qt 102,3,16896
s_add_u32 m0,s13,73728
qm 248,104,92
qt 104,2,20480
qw 5
qm 180,108,84
qt 106,2,20992
s_add_u32 s14,s28,534528
qm 252,108,92
qt 108,3,20480
qw 5
qm 168,96,128
qt 110,3,20992
ql 8,24,0
qm 240,96,200
qt 80,36,0
qt 96,2,24576
qw 6
qm 172,100,128
qt 82,36,512
buffer_load_dwordx2 v[228:229],v11,s[36:39],s70 offen offset:896
qt 98,2,25088
ql 9,24,1024
qm 244,100,200
qt 88,36,1024
qt 100,3,24576
qw 8
qm 168,104,132
qt 90,36,1536
qt 102,3,25088
s_mov_b32 m0,s13
qm 240,104,204
qt 84,36,8192
qt 104,2,28672
qw 10
qm 172,108,132
qw 1
s_barrier
qt 86,36,8704
qt 106,2,29184
s_add_u32 s14,s19,516992
qm 244,108,204
qt 92,36,9216
qt 108,3,28672
qm 176,96,128
qt 94,36,9728
qt 110,3,29184
ql 20,48,0
qm 248,96,200
qt 96,2,32768
qm 180,100,128
qt 98,2,33280
ql 21,48,1024
qm 252,100,200
buffer_load_dwordx2 v[230:231],v12,s[80:83],s75 offen offset:896
qt 100,3,32768
qw 7
qm 176,104,132
qt 102,3,33280
s_add_u32 m0,s13,8192
qm 248,104,204
qt 104,2,36864
qw 5
qm 180,108,132
qt 106,2,37376
s_add_u32 s14,s19,590848
qm 252,108,204
qt 108,3,36864
qw 5
qm 168,96,136
qt 110,3,37376
ql 20,48,0
qm 240,96,208
qt 128,36,16384
qt 96,2,40960
qw 6
qm 172,100,136
qt 130,36,16896
qt 98,2,41472
ql 21,48,1024
qm 244,100,208
qt 200,36,17408
qt 100,3,40960
qw 8
qm 168,104,140
qt 202,36,17920
qt 102,3,41472
s_add_u32 m0,s13,16384
qm 240,104,212
qt 132,36,24576
qt 104,2,45056
qw 10
qm 172,108,140
qt 134,36,25088
qt 106,2,45568
s_add_u32 s14,s19,443392
qm 244,108,212
qt 204,36,25600
qt 108,3,45056
qw 10
qm 176,96,136
qt 206,36,26112
qt 110,3,45568
ql 20,48,0
qm 248,96,208
qt 96,2,49152
qw 9
qm 180,100,136
qt 98,2,49664
ql 21,48,1024
qm 252,100,208
qt 100,3,49152
qw 7
qm 176,104,140
qt 102,3,49664
s_add_u32 m0,s13,24576
qm 248,104,212
qt 104,2,53248
qw 5
qm 180,108,140
s_waitcnt vmcnt(7)
s_barrier
qt 106,2,53760
s_add_u32 s14,s28,589824
qm 252,108,212
qt 108,3,53248
qw 5
qm 168,96,64
qt 110,3,53760
ql 8,24,0
qm 240,96,72
qr 136,18,32768
qt 96,2,57344
qw 6
qm 172,100,64
qt 98,2,57856
ql 9,24,1024
qm 244,100,72
qr 208,18,33792
qt 100,3,57344
qw 7
qm 168,104,68
qt 102,3,57856
s_add_u32 m0,s13,32768
qm 240,104,76
qr 140,18,40960
qt 104,2,61440
qw 8
qm 172,108,68
qt 106,2,61952
s_add_u32 s14,s28,608256
qm 244,108,76
qr 212,18,41984
qt 108,3,61440
qw 8
qm 168,96,120
qt 110,3,61952
ql 8,24,0
qm 240,96,192
qd 96,28,0
qw 7
qm 172,100,120
qd 98,28,4096
ql 9,24,1024
qm 244,100,192
qd 100,29,0
qw 6
qm 168,104,124
qd 102,29,4096
s_add_u32 m0,s13,40960
qm 240,104,196
qd 104,30,0
qw 5
qm 172,108,124
qd 106,30,4096
s_add_u32 s14,s19,517120
qm 244,108,196
qd 108,31,0
qw 5
qm 168,96,80
qd 110,31,4096
ql 20,48,0
qm 240,96,88
qd 96,28,8192
qw 5
qm 168,100,84
qd 98,28,12288
ql 21,48,1024
qm 240,100,92
qd 100,29,8192
qw 5
qm 168,104,128
qd 102,29,12288
qm 240,104,200
qd 104,30,8192
qw 5
qm 168,108,132
s_waitcnt vmcnt(6)
s_barrier
qd 106,30,12288
qm 240,108,204
qd 108,31,8192
qw 5
qm 172,96,80
qd 110,31,12288
qm 244,96,88
qt 96,2,0
qw 5
qm 172,100,84
qt 98,2,512
qm 244,100,92
qt 100,3,0
qw 5
qm 172,104,128
qt 102,3,512
qm 244,104,200
qt 104,2,4096
qw 5
qm 172,108,132
qt 106,2,4608
qm 244,108,204
qt 108,3,4096
qw 5
qm 168,96,136
qt 110,3,4608
qm 240,96,208
qt 96,2,8192
qw 5
qm 172,100,136
qt 98,2,8704
qm 244,100,208
qt 100,3,8192
qw 5
qm 168,104,140
qt 102,3,8704
qm 240,104,212
qt 104,2,12288
qw 5
qm 172,108,140
qt 106,2,12800
qm 244,108,212
qt 108,3,12288
qw 5
qm 176,96,64
qt 110,3,12800
qm 248,96,72
qt 96,2,16384
qw 5
qm 180,100,64
qt 98,2,16896
qm 252,100,72
v_cvt_pk_bf16_f32 v168,v168,v169
qt 100,3,16384
qw 5
qm 176,104,68
v_cvt_pk_bf16_f32 v169,v170,v171
qt 102,3,16896
qm 248,104,76
v_cvt_pk_bf16_f32 v170,v172,v173
qt 104,2,20480
qw 5
qm 180,108,68
s_waitcnt vmcnt(0)
s_barrier
v_cvt_pk_bf16_f32 v171,v174,v175
qt 106,2,20992
qm 252,108,76
qt 108,3,20480
qw 5
qm 176,96,120
v_cvt_pk_bf16_f32 v240,v240,v241
qt 110,3,20992
qm 248,96,192
v_cvt_pk_bf16_f32 v241,v242,v243
qd 96,24,24576
qw 5
qm 180,100,120
v_cvt_pk_bf16_f32 v242,v244,v245
qd 98,24,28672
qm 252,100,192
v_cvt_pk_bf16_f32 v243,v246,v247
qd 100,25,24576
qw 5
qm 176,104,124
s_add_u32 s15,s73,917504
buffer_store_dwordx4 v[168:171],v1,s[88:91],s15 offen offset:0 sc1
qd 102,25,28672
qm 248,104,196
qd 104,26,24576
qw 5
qm 180,108,124
qd 106,26,28672
qm 252,108,196
qd 108,27,24576
qw 5
qm 176,96,80
)ASM"
R"ASM(qd 110,27,28672
qm 248,96,88
qd 96,24,32768
qw 5
qm 176,100,84
qd 98,24,36864
qm 248,100,92
qd 100,25,32768
qw 5
qm 176,104,128
s_add_u32 s15,s73,917504
buffer_store_dwordx4 v[240:243],v1,s[88:91],s15 offen offset:1024 sc1
qd 102,25,36864
qm 248,104,200
qd 104,26,32768
qw 5
qm 176,108,132
qd 106,26,36864
qm 248,108,204
qd 108,27,32768
qw 5
qm 180,96,80
qd 110,27,36864
qm 252,96,88
qt 96,2,40960
qw 5
qm 180,100,84
qt 98,2,41472
qm 252,100,92
qt 100,3,40960
qw 5
qm 180,104,128
qt 102,3,41472
qm 252,104,200
qt 104,2,45056
qw 5
qm 180,108,132
s_barrier
qt 106,2,45568
qm 252,108,204
qt 108,3,45056
qw 5
qm 176,96,136
qt 110,3,45568
qm 248,96,208
qw 4
qm 180,100,136
qm 252,100,208
qw 2
qm 176,104,140
qm 248,104,212
qw 0
qm 180,108,140
qm 252,108,212
s_nop 3
v_cvt_pk_bf16_f32 v176,v176,v177
v_cvt_pk_bf16_f32 v177,v178,v179
s_nop 0
v_cvt_pk_bf16_f32 v178,v180,v181
v_cvt_pk_bf16_f32 v179,v182,v183
s_add_u32 s15,s73,1048576
buffer_store_dwordx4 v[176:179],v1,s[88:91],s15 offen offset:0 sc1
v_cvt_pk_bf16_f32 v248,v248,v249
v_cvt_pk_bf16_f32 v249,v250,v251
v_cvt_pk_bf16_f32 v250,v252,v253
v_cvt_pk_bf16_f32 v251,v254,v255
s_add_u32 s15,s73,1048576
buffer_store_dwordx4 v[248:251],v1,s[88:91],s15 offen offset:1024 sc1
v_lshlrev_b32 v16,16,v226
v_lshlrev_b32 v144,16,v224
v_mul_f32 v16,v16,v40
v_add_f32 v17,v144,v41
v_fma_f32 v144,v17,v16,v144
v_and_b32 v16,0xffff0000,v226
v_and_b32 v145,0xffff0000,v224
v_mul_f32 v16,v16,v40
v_add_f32 v17,v145,v41
v_fma_f32 v145,v17,v16,v145
v_lshlrev_b32 v16,16,v227
v_lshlrev_b32 v146,16,v225
v_mul_f32 v16,v16,v40
v_add_f32 v17,v146,v41
v_fma_f32 v146,v17,v16,v146
v_and_b32 v16,0xffff0000,v227
v_and_b32 v147,0xffff0000,v225
v_mul_f32 v16,v16,v40
v_add_f32 v17,v147,v41
v_fma_f32 v147,v17,v16,v147
v_lshlrev_b32 v16,16,v230
v_lshlrev_b32 v148,16,v228
v_mul_f32 v16,v16,v40
v_add_f32 v17,v148,v41
v_fma_f32 v148,v17,v16,v148
v_and_b32 v16,0xffff0000,v230
v_and_b32 v149,0xffff0000,v228
v_mul_f32 v16,v16,v40
v_add_f32 v17,v149,v41
v_fma_f32 v149,v17,v16,v149
v_lshlrev_b32 v16,16,v231
v_lshlrev_b32 v150,16,v229
v_mul_f32 v16,v16,v40
v_add_f32 v17,v150,v41
v_fma_f32 v150,v17,v16,v150
v_and_b32 v16,0xffff0000,v231
v_and_b32 v151,0xffff0000,v229
v_mul_f32 v16,v16,v40
v_add_f32 v17,v151,v41
v_fma_f32 v151,v17,v16,v151
buffer_load_dwordx4 v[220:223],v15,s[96:99],s72 offen sc1
s_waitcnt vmcnt(0)
s_barrier
.L_tY0:
v_sub_u32 v220,s74,v220
v_sub_u32 v221,s74,v221
v_sub_u32 v222,s74,v222
v_sub_u32 v223,s74,v223
v_cndmask_b32 v222,v222,0,s[40:41]
v_cndmask_b32 v223,v223,0,s[42:43]
v_cmp_ge_u32 vcc,0,v220
s_nop 1
s_andn2_b64 exec,vcc,s[52:53]
s_cbranch_scc0 .L_tY0r10
s_add_u32 s31,s69,262144
buffer_load_dwordx4 v[160:163],v10,s[88:91],s31 offen sc1
s_add_u32 s31,s69,393216
buffer_load_dwordx4 v[232:235],v10,s[88:91],s31 offen sc1
.L_tY0r10:
s_mov_b64 exec,-1
s_or_b64 s[52:53],s[52:53],vcc
v_cmp_ge_u32 vcc,0,v221
s_nop 1
s_andn2_b64 exec,vcc,s[54:55]
s_cbranch_scc0 .L_tY0r11
s_add_u32 s31,s69,327680
buffer_load_dwordx4 v[164:167],v10,s[88:91],s31 offen sc1
s_add_u32 s31,s69,458752
buffer_load_dwordx4 v[236:239],v10,s[88:91],s31 offen sc1
.L_tY0r11:
s_mov_b64 exec,-1
s_or_b64 s[54:55],s[54:55],vcc
v_cmp_ge_u32 vcc,0,v222
s_nop 1
s_andn2_b64 exec,vcc,s[56:57]
s_cbranch_scc0 .L_tY0r12
s_add_u32 s31,s69,917504
buffer_load_dwordx4 v[112:115],v10,s[88:91],s31 offen sc1
s_add_u32 s31,s69,1048576
buffer_load_dwordx4 v[184:187],v10,s[88:91],s31 offen sc1
.L_tY0r12:
s_mov_b64 exec,-1
s_or_b64 s[56:57],s[56:57],vcc
v_cmp_ge_u32 vcc,0,v223
s_nop 1
s_andn2_b64 exec,vcc,s[58:59]
s_cbranch_scc0 .L_tY0r13
s_add_u32 s31,s69,983040
buffer_load_dwordx4 v[116:119],v10,s[88:91],s31 offen sc1
s_add_u32 s31,s69,1114112
buffer_load_dwordx4 v[188:191],v10,s[88:91],s31 offen sc1
.L_tY0r13:
s_mov_b64 exec,-1
s_or_b64 s[58:59],s[58:59],vcc
.L_tY0c:
s_and_b64 s[32:33],s[52:53],s[54:55]
s_and_b64 s[32:33],s[32:33],s[56:57]
s_and_b64 s[32:33],s[32:33],s[58:59]
s_cmp_eq_u64 s[32:33],-1
s_cbranch_scc1 .L_tY0x
s_add_u32 s76,s76,1
s_cmp_gt_u32 s76,1000000
s_cbranch_scc1 .L_tY0x
s_sleep 4
buffer_load_dwordx4 v[220:223],v15,s[96:99],s72 offen sc1
s_waitcnt vmcnt(0)
s_branch .L_tY0
.L_tY0x:
s_waitcnt vmcnt(0)
qm 144,152,160
qm 148,152,112
qm 144,152,164
qm 148,152,116
qm 144,156,232
qm 148,156,184
qm 144,156,236
qm 148,156,188
s_nop 6
buffer_store_dwordx4 v[144:147],v13,s[92:95],s71 offen offset:512 sc1
buffer_store_dwordx4 v[148:151],v13,s[92:95],s71 offen offset:1792 sc1
s_endpgm
.org .L_code_Y+40960
.L_code_Y1:
qw 0
s_barrier
qr 112,24,24576
qr 184,24,0
qr 96,20,4096
qr 100,20,6144
qr 116,25,28672
qr 188,25,4096
qr 104,21,4096
qr 108,21,6144
qr 120,24,32768
qr 192,24,8192
qw 7
qz 48,112,96
qr 124,25,36864
qr 196,25,12288
ds_write_b128 v31,v[208:211] offset:24576
qz 52,184,96
qr 200,24,16384
ds_write_b128 v32,v[212:215] offset:24576
qr 96,20,12288
qw 12
qz 56,112,100
qr 128,24,40960
qr 204,25,20480
s_sub_u32 s15,s17,640
buffer_load_dword v1,v6,s[40:43],s15 offen
qz 60,184,100
ds_write_b128 v31,v[216:219] offset:32768
qr 100,20,14336
qw 13
qm 48,116,104
qr 132,25,45056
ds_write_b128 v32,v[220:223] offset:32768
qm 52,188,104
qr 104,21,12288
qw 15
qm 56,116,108
qr 136,24,49152
ds_write_b128 v31,v[224:227] offset:40960
qm 60,188,108
ds_write_b128 v32,v[228:231] offset:40960
qr 108,21,14336
qw 11
qm 48,120,96
qw 9
s_barrier
qr 140,25,53248
qm 52,192,96
ds_write_b128 v31,v[232:235] offset:49152
qr 96,20,20480
qw 10
qm 56,120,100
qr 144,24,57344
buffer_load_dword v1,v8,s[32:35],s53 offen
qm 60,192,100
ds_write_b128 v32,v[236:239] offset:49152
qr 100,20,22528
qw 10
qm 48,124,104
qr 148,25,61440
qm 52,196,104
ds_write_b128 v31,v[240:243] offset:57344
qr 104,21,20480
qw 9
qm 56,124,108
qr 152,26,8192
qm 60,196,108
ds_write_b128 v32,v[244:247] offset:57344
qr 108,21,22528
qw 9
qm 48,128,96
qr 156,27,12288
qm 52,200,96
ds_write_b128 v38,v[248:251] offset:8192
qr 96,20,28672
qw 9
qm 56,128,100
qr 160,26,16384
buffer_load_dword v1,v8,s[80:83],0 offen
qm 60,200,100
ds_write_b128 v39,v[252:255] offset:8192
qr 100,20,30720
qw 9
qm 48,132,104
qr 164,27,20480
qm 52,204,104
qr 104,21,28672
qw 8
qm 56,132,108
qw 1
s_barrier
qr 168,26,24576
qr 208,24,24576
qm 60,204,108
qr 212,25,28672
qr 108,21,30720
qm 48,136,96
qr 172,27,28672
v_add_f32 v16,v16,v17
qw 3
qm 52,208,96
qr 216,24,32768
v_add_f32 v17,v28,v29
qr 96,20,36864
qm 56,136,100
qr 176,26,32768
qr 220,25,36864
v_add_f32_dpp v16,v16,v16 quad_perm:[1,0,3,2] row_mask:0xf bank_mask:0xf
qm 60,208,100
v_add_f32 v33,v33,v35
qr 100,20,38912
qm 48,140,104
qr 180,27,36864
qr 224,24,40960
v_add_f32_dpp v17,v17,v17 quad_perm:[1,0,3,2] row_mask:0xf bank_mask:0xf
qw 9
qm 52,212,104
qr 228,25,45056
v_add_f32_dpp v16,v16,v16 quad_perm:[2,3,0,1] row_mask:0xf bank_mask:0xf
qr 104,21,36864
qw 10
qm 56,140,108
v_add_f32 v34,v34,v36
qm 60,212,108
qr 232,24,49152
v_add_f32_dpp v17,v17,v17 quad_perm:[2,3,0,1] row_mask:0xf bank_mask:0xf
qr 108,21,38912
qw 9
qm 48,144,96
qr 236,25,53248
qm 52,216,96
v_add_f32_dpp v16,v16,v16 row_half_mirror row_mask:0xf bank_mask:0xf
qr 96,20,45056
qw 8
qm 56,144,100
qr 240,24,57344
v_lshrrev_b32 v44,3,v0
qm 60,216,100
qr 244,25,61440
v_add_f32_dpp v17,v17,v17 row_half_mirror row_mask:0xf bank_mask:0xf
qr 100,20,47104
qw 7
qm 48,148,104
v_mul_f32 v16,0x3ae38e39,v16
v_mov_b32 v9,v6
qm 52,220,104
qr 248,26,8192
v_fma_f32 v16,-v33,v33,v16
qr 104,21,45056
qw 7
qm 56,148,108
qr 252,27,12288
v_max_f32 v16,0,v16
qm 60,220,108
v_mul_f32 v17,0x3ae38e39,v17
qr 108,21,47104
qw 7
qm 48,152,96
v_fma_f32 v17,-v34,v34,v17
qm 52,224,96
qr 96,20,53248
qw 5
qm 56,152,100
v_max_f32 v17,0,v17
qm 60,224,100
v_cvt_pk_bf16_f32 v42,v16,v17
qr 100,20,55296
qw 4
qm 48,156,104
v_lshlrev_b32 v16,16,v42
qm 52,228,104
v_and_b32 v17,0xffff0000,v42
qr 104,21,53248
qw 3
qm 56,156,108
s_barrier
v_add_f32 v16,0x3727c5ac,v16
qm 60,228,108
v_add_f32 v17,0x3727c5ac,v17
qr 108,21,55296
qw 3
qm 48,160,96
v_cvt_pk_bf16_f32 v42,v16,v17
qm 52,232,96
v_lshlrev_b32 v16,16,v42
qr 96,20,61440
qw 3
qm 56,160,100
qm 60,232,100
v_and_b32 v17,0xffff0000,v42
qr 100,20,63488
qw 3
qm 48,164,104
v_sqrt_f32 v16,v16
qm 52,236,104
v_sqrt_f32 v17,v17
qr 104,21,61440
qw 3
qm 56,164,108
v_and_b32 v43,4,v44
qm 60,236,108
v_cvt_pk_bf16_f32 v42,v16,v17
qr 108,21,63488
qw 3
qm 48,168,96
v_lshlrev_b32 v16,16,v42
qm 52,240,96
v_and_b32 v17,0xffff0000,v42
qr 96,22,4096
qw 3
qm 56,168,100
v_readfirstlane_b32 s3,v13
s_mul_i32 s16,s3,0x9E3779B1
s_add_u32 s16,s16,0x7F4A7C15
v_rcp_f32 v16,v16
qm 60,240,100
qr 100,22,6144
qw 3
qm 48,172,104
v_rcp_f32 v17,v17
qm 52,244,104
v_add3_u32 v44,v43,v44,s29
qr 104,23,4096
qw 3
qm 56,172,108
v_lshlrev_b32 v43,2,v44
qm 60,244,108
v_add_u32 v43,122880,v43
qr 108,23,6144
qw 3
qm 48,176,96
ds_write_b32 v43,v16
qm 52,248,96
ds_write_b32 v43,v17 offset:64
qr 96,22,12288
qw 5
qm 56,176,100
ds_write_b32 v43,v35 offset:512
qm 60,248,100
ds_write_b32 v43,v36 offset:576
qr 100,22,14336
qw 7
qm 48,180,104
qm 52,252,104
qr 104,23,12288
qw 7
qm 56,180,108
qw 2
s_barrier
qm 60,252,108
qr 108,23,14336
qz 64,112,96
v_lshrrev_b32 v42,4,v0
v_and_b32 v43,15,v0
v_bfe_u32 v45,v0,2,2
s_setprio 0
qz 68,184,96
v_lshl_add_u32 v36,s7,2,v42
v_lshl_add_u32 v45,s7,2,v45
qr 96,20,4096
qw 3
qz 72,112,100
v_lshlrev_b32 v42,5,v36
v_and_b32 v14,3,v0
qz 76,184,100
v_lshlrev_b32 v37,3,v43
v_lshl_add_u32 v45,v45,3,v14
qr 100,20,6144
s_add_u32 s15,s17,-512
buffer_load_dword v1,v9,s[44:47],s15 offen
qw 3
qm 64,116,104
v_lshl_add_u32 v43,v36,9,v37
v_lshlrev_b32 v45,2,v45
s_lshl_b32 s30,s10,3
s_add_u32 s30,s30,s8
s_mul_i32 s30,s30,196608
s_add_u32 s76,s30,21118976
s_add_u32 s77,s30,21020672
s_lshl_b32 s30,s7,13
s_add_u32 s79,s76,s30
qm 68,188,104
v_add_u32 v42,122880,v42
v_add_u32 v45,122880,v45
qr 104,21,4096
qw 3
qm 72,116,108
v_add_u32 v37,124928,v37
ds_read_b32 v10,v45
qm 76,188,108
v_add_u32 v43,114688,v43
ds_read_b32 v11,v45 offset:16
qr 108,21,6144
qw 5
qm 64,120,96
qr 28,42,0
qm 68,192,96
qr 32,42,16
qr 96,20,12288
qw 7
qm 72,120,100
qd 36,37,128
qm 76,192,100
s_nop 0
qr 100,20,14336
qw 8
qm 64,124,104
s_nop 0
qw 7
v_mul_f32 v14,v10,v10
qm 68,196,104
s_nop 0
v_mul_f32 v12,0xbdd2d3e8,v10
qr 104,21,12288
qw 6
qm 72,124,108
qw 5
v_mul_f32 v48,v48,v28
v_mul_f32 v10,0xc0135761,v10
qm 76,196,108
v_mul_f32 v56,v56,v28
v_mul_f32 v12,v12,v14
qr 108,21,14336
qw 4
qm 64,128,96
v_mov_b32 v38,v48
v_rcp_f32 v14,v14
qm 68,200,96
v_mov_b32 v39,v56
v_cvt_pk_bf16_f32 v48,v48,v56
v_mul_f32 v15,v11,v11
qr 96,20,20480
qw 3
qm 72,128,100
v_mul_f32 v49,v49,v29
v_mul_f32 v13,0xbdd2d3e8,v11
qm 76,200,100
v_mul_f32 v57,v57,v29
v_mul_f32 v11,0xc0135761,v11
qr 100,20,22528
qw 3
qm 64,132,104
v_fma_f32 v40,v36,v38,v49
v_mul_f32 v13,v13,v15
qm 68,204,104
v_fma_f32 v41,v36,v39,v57
v_rcp_f32 v15,v15
qr 104,21,20480
qw 3
qm 72,132,108
s_barrier
v_fma_f32 v40,-v37,v39,v40
qm 76,204,108
v_fma_f32 v41,v37,v38,v41
qr 108,21,22528
qw 3
qm 64,136,96
v_cvt_pk_bf16_f32 v49,v49,v57
qm 68,208,96
v_mul_f32 v50,v50,v30
qr 96,20,28672
qw 3
qm 72,136,100
v_mul_f32 v58,v58,v30
qm 76,208,100
v_fma_f32 v38,v36,v40,v50
qr 100,20,30720
qw 3
qm 64,140,104
v_fma_f32 v39,v36,v41,v58
qm 68,212,104
v_fma_f32 v38,-v37,v41,v38
qr 104,21,28672
qw 3
qm 72,140,108
v_fma_f32 v39,v37,v40,v39
qm 76,212,108
v_cvt_pk_bf16_f32 v50,v50,v58
qr 108,21,30720
qw 3
qm 64,144,96
v_mul_f32 v51,v51,v31
qm 68,216,96
v_mul_f32 v59,v59,v31
qr 96,20,36864
qw 3
qm 72,144,100
v_fma_f32 v40,v36,v38,v51
v_fma_f32 v41,v36,v39,v59
qm 76,216,100
v_fma_f32 v40,-v37,v39,v40
qr 100,20,38912
qw 3
qm 64,148,104
v_fma_f32 v41,v37,v38,v41
qm 68,220,104
v_cvt_pk_bf16_f32 v51,v51,v59
qr 104,21,36864
qw 3
qm 72,148,108
ds_write_b128 v18,v[48:51] offset:57344
qm 76,220,108
v_mul_f32 v52,v52,v32
qr 108,21,38912
qw 4
qm 64,152,96
v_mul_f32 v60,v60,v32
qm 68,224,96
v_fma_f32 v38,v36,v40,v52
qr 96,20,45056
qw 4
qm 72,152,100
v_fma_f32 v39,v36,v41,v60
s_mov_b32 s53,s10
qm 76,224,100
v_fma_f32 v38,-v37,v41,v38
qr 100,20,47104
qw 4
qm 64,156,104
v_fma_f32 v39,v37,v40,v39
s_lshl_b32 s54,s53,2
qm 68,228,104
v_cvt_pk_bf16_f32 v52,v52,v60
qr 104,21,45056
qw 3
qm 72,156,108
s_barrier
v_mul_f32 v53,v53,v33
s_add_u32 s54,s54,s7
qm 76,228,108
v_mul_f32 v61,v61,v33
qr 108,21,47104
qw 3
qm 64,160,96
v_fma_f32 v40,v36,v38,v53
s_mul_i32 s54,s54,3456
qm 68,232,96
v_fma_f32 v41,v36,v39,v61
qr 96,22,20480
qw 3
qm 72,160,100
v_fma_f32 v40,-v37,v39,v40
s_mul_i32 s52,s8,663552
qm 76,232,100
v_fma_f32 v41,v37,v38,v41
v_cvt_pk_bf16_f32 v53,v53,v61
qr 100,22,22528
qw 3
qm 64,164,104
v_mul_f32 v54,v54,v34
qm 68,236,104
v_mul_f32 v62,v62,v34
s_add_u32 s54,s54,s52
qr 104,23,20480
qw 3
qm 72,164,108
v_fma_f32 v38,v36,v40,v54
qm 76,236,108
v_fma_f32 v39,v36,v41,v62
s_and_b32 s53,s53,3
qr 108,23,22528
qw 3
qm 64,168,96
v_fma_f32 v38,-v37,v41,v38
qm 68,240,96
v_fma_f32 v39,v37,v40,v39
v_min_u32 v8,53,v0
qr 96,22,28672
qw 3
qm 72,168,100
v_cvt_pk_bf16_f32 v54,v54,v62
qm 76,240,100
v_mul_f32 v55,v55,v35
v_lshlrev_b32 v8,7,v8
qr 100,22,30720
qw 3
qm 64,172,104
v_mul_f32 v63,v63,v35
qm 68,244,104
v_fma_f32 v40,v36,v38,v55
qr 104,23,28672
qw 3
qm 72,172,108
v_fma_f32 v41,v36,v39,v63
qm 76,244,108
v_fma_f32 v40,-v37,v39,v40
qr 108,23,30720
qw 3
qm 64,176,96
v_fma_f32 v41,v37,v38,v41
qm 68,248,96
v_cvt_pk_bf16_f32 v55,v55,v63
qt 96,4,32768
qw 3
qm 72,176,100
ds_write_b128 v18,v[52:55] offset:58368
qt 98,4,33280
qm 76,248,100
ds_write_b64 v43,v[40:41] offset:128
qt 100,5,32768
qw 6
qm 64,180,104
qt 102,5,33280
qm 68,252,104
qt 104,4,36864
qw 7
qm 72,180,108
qw 3
s_barrier
qt 106,4,37376
qm 76,252,108
qt 108,5,36864
qz 48,96,112
v_lshrrev_b32 v42,4,v0
v_and_b32 v43,15,v0
qt 110,5,37376
qz 52,96,184
v_lshl_add_u32 v36,s7,2,v42
v_lshlrev_b32 v42,5,v36
qt 96,4,40960
qw 5
qz 56,100,112
v_lshlrev_b32 v37,3,v43
qt 98,4,41472
qz 60,100,184
v_lshl_add_u32 v43,v36,9,v37
v_add_u32 v42,122880,v42
qt 100,5,40960
s_add_u32 s15,s17,-384
buffer_load_dword v1,v9,s[44:47],s15 offen
qw 5
qm 48,104,116
v_add_u32 v37,124928,v37
v_add_u32 v43,114688,v43
qt 102,5,41472
qm 52,104,188
qr 28,42,0
qt 104,4,45056
qw 6
qm 56,108,116
qr 32,42,16
qd 36,37,384
qt 106,4,45568
qm 60,108,188
s_nop 0
s_nop 0
qt 108,5,45056
qw 8
qm 48,96,120
s_nop 0
qt 110,5,45568
qm 52,96,192
qw 6
v_mul_f32 v64,v64,v28
v_mul_f32 v72,v72,v28
qt 96,2,49152
qm 56,100,120
v_mov_b32 v38,v64
qt 98,2,49664
qm 60,100,192
v_mov_b32 v39,v72
v_cvt_pk_bf16_f32 v64,v64,v72
qt 100,3,49152
qw 5
qm 48,104,124
v_mul_f32 v65,v65,v29
v_mul_f32 v73,v73,v29
qt 102,3,49664
qm 52,104,196
v_fma_f32 v40,v36,v38,v65
qt 104,2,53248
qw 5
qm 56,108,124
v_fma_f32 v41,v36,v39,v73
v_fma_f32 v40,-v37,v39,v40
qt 106,2,53760
qm 60,108,196
v_fma_f32 v41,v37,v38,v41
v_cvt_pk_bf16_f32 v65,v65,v73
qt 108,3,53248
qw 5
qm 48,96,128
v_mul_f32 v66,v66,v30
qt 110,3,53760
qm 52,96,200
v_mul_f32 v74,v74,v30
v_fma_f32 v38,v36,v40,v66
qt 96,2,57344
qw 5
qm 56,100,128
v_fma_f32 v39,v36,v41,v74
v_fma_f32 v38,-v37,v41,v38
qt 98,2,57856
qm 60,100,200
v_fma_f32 v39,v37,v40,v39
qt 100,3,57344
qw 5
qm 48,104,132
v_cvt_pk_bf16_f32 v66,v66,v74
v_mul_f32 v67,v67,v31
qt 102,3,57856
qm 52,104,204
v_mul_f32 v75,v75,v31
qt 104,2,61440
qw 5
qm 56,108,132
s_barrier
v_fma_f32 v40,v36,v38,v67
v_fma_f32 v41,v36,v39,v75
qt 106,2,61952
qm 60,108,204
v_fma_f32 v40,-v37,v39,v40
v_fma_f32 v41,v37,v38,v41
qt 108,3,61440
qw 5
qm 48,96,136
v_cvt_pk_bf16_f32 v67,v67,v75
qt 110,3,61952
qm 52,96,208
ds_write_b128 v19,v[64:67] offset:8192
v_mul_f32 v68,v68,v32
qt 96,4,0
qw 6
qm 56,100,136
v_mul_f32 v76,v76,v32
v_fma_f32 v38,v36,v40,v68
qt 98,4,512
qm 60,100,208
v_fma_f32 v39,v36,v41,v76
qt 100,5,0
qw 6
qm 48,104,140
v_fma_f32 v38,-v37,v41,v38
v_fma_f32 v39,v37,v40,v39
qt 102,5,512
qm 52,104,212
v_cvt_pk_bf16_f32 v68,v68,v76
v_mul_f32 v69,v69,v33
qt 104,4,4096
qw 6
qm 56,108,140
v_mul_f32 v77,v77,v33
qt 106,4,4608
qm 60,108,212
v_fma_f32 v40,v36,v38,v69
v_fma_f32 v41,v36,v39,v77
qt 108,5,4096
qw 5
qm 48,96,144
v_fma_f32 v40,-v37,v39,v40
qt 110,5,4608
qm 52,96,216
v_fma_f32 v41,v37,v38,v41
v_cvt_pk_bf16_f32 v69,v69,v77
qt 96,4,8192
qw 5
qm 56,100,144
v_mul_f32 v70,v70,v34
v_mul_f32 v78,v78,v34
qt 98,4,8704
qm 60,100,216
v_fma_f32 v38,v36,v40,v70
qt 100,5,8192
qw 5
qm 48,104,148
v_fma_f32 v39,v36,v41,v78
v_fma_f32 v38,-v37,v41,v38
qt 102,5,8704
qm 52,104,220
v_fma_f32 v39,v37,v40,v39
v_cvt_pk_bf16_f32 v70,v70,v78
qt 104,4,12288
qw 5
qm 56,108,148
v_mul_f32 v71,v71,v35
qt 106,4,12800
qm 60,108,220
v_mul_f32 v79,v79,v35
v_fma_f32 v40,v36,v38,v71
qt 108,5,12288
qw 5
qm 48,96,152
v_fma_f32 v41,v36,v39,v79
v_fma_f32 v40,-v37,v39,v40
qt 110,5,12800
qm 52,96,224
v_fma_f32 v41,v37,v38,v41
qt 96,2,0
qw 5
qm 56,100,152
v_cvt_pk_bf16_f32 v71,v71,v79
ds_write_b128 v19,v[68:71] offset:9216
qt 98,2,512
qm 60,100,224
ds_write_b64 v43,v[40:41] offset:384
qt 100,3,0
qw 7
qm 48,104,156
qt 102,3,512
qm 52,104,228
qt 104,2,4096
qw 7
qm 56,108,156
qw 3
s_barrier
qt 106,2,4608
qm 60,108,228
v_lshrrev_b32 v43,4,v0
v_and_b32 v76,15,v0
qt 108,3,4096
qm 48,96,160
v_lshl_add_u32 v76,s7,4,v76
qt 110,3,4608
qm 52,96,232
v_lshlrev_b32 v43,11,v43
v_lshl_add_u32 v43,v76,3,v43
qt 96,4,16384
qw 5
qm 56,100,160
v_add_u32 v43,114688,v43
qt 98,4,16896
qm 60,100,232
v_lshlrev_b32 v76,4,v76
v_add_u32 v42,123904,v76
qt 100,5,16384
qw 5
qm 48,104,164
qd 36,42,0
qt 102,5,16896
qm 52,104,236
qd 28,43,0
qd 30,43,512
qt 104,4,20480
qw 8
qm 56,108,164
qd 32,43,1024
qt 106,4,20992
qm 60,108,236
qd 34,43,1536
s_mov_b32 s30,0
qt 108,5,20480
qw 10
qm 48,96,168
s_mov_b32 s31,-1
qt 110,5,20992
qm 52,96,240
s_mov_b32 s32,0xffff0000
s_mov_b32 s33,0xffff0000
qt 96,4,24576
qw 9
qm 56,100,168
v_mov_b32 v73,s16
qt 98,4,25088
qm 60,100,240
v_mov_b32 v75,s16
qt 100,5,24576
qw 6
qm 48,104,172
v_mul_f32 v42,0x41000000,v36
v_mul_f32 v44,0x42000000,v36
qt 102,5,25088
qm 52,104,244
v_mul_f32 v39,0x41000000,v37
qt 104,4,28672
qw 5
qm 56,108,172
v_mul_f32 v41,0x42000000,v37
v_exp_f32 v42,v42
qt 106,4,29184
qm 60,108,244
v_exp_f32 v44,v44
qt 108,5,28672
qw 5
qm 48,96,176
v_fract_f32 v39,v39
v_fract_f32 v41,v41
qt 110,5,29184
qm 52,96,248
v_cos_f32 v38,v39
qt 96,2,8192
qw 5
qm 56,100,176
v_cos_f32 v40,v41
v_sin_f32 v39,v39
qt 98,2,8704
qm 60,100,248
v_sin_f32 v41,v41
qt 100,3,8192
qw 5
qm 48,104,180
v_mul_f32 v38,v38,v42
v_mul_f32 v40,v40,v44
qt 102,3,8704
qm 52,104,252
v_mul_f32 v39,v39,v42
qt 104,2,12288
qw 5
qm 56,108,180
s_barrier
v_mul_f32 v41,v41,v44
v_fma_f32 v44,v38,v28,v30
qt 106,2,12800
qm 60,108,252
v_fma_f32 v45,v38,v29,v31
qt 108,3,12288
qw 5
qz 80,96,112
v_fma_f32 v44,-v39,v29,v44
qt 110,3,12800
qz 84,96,184
v_fma_f32 v45,v39,v28,v45
v_fma_f32 v64,v38,v44,v32
v_mul_f32 v16,v48,v48
qt 96,4,32768
qw 5
qz 88,100,112
v_fma_f32 v65,v38,v45,v33
v_fma_f32 v16,v16,v12,v10
v_and_b32 v6,31,v0
qt 98,4,33280
qz 92,100,184
v_fma_f32 v64,-v39,v45,v64
v_fma_f32 v65,v39,v44,v65
v_mul_f32 v16,v16,v48
qt 100,5,32768
qw 5
qm 80,104,116
v_fma_f32 v44,v38,v64,v34
v_exp_f32 v16,v16
v_min_u32 v6,8,v6
qt 102,5,33280
qm 84,104,188
v_fma_f32 v45,v38,v65,v35
v_fma_f32 v44,-v39,v65,v44
v_fma_f32 v16,v16,v14,v14
qt 104,4,36864
qw 5
qm 88,108,116
v_fma_f32 v45,v39,v64,v45
v_rcp_f32 v16,v16
v_mul_u32_u24 v6,1152,v6
qt 106,4,37376
qm 92,108,188
v_mov_b32 v64,v44
v_mov_b32 v65,v45
v_mul_f32 v48,v48,v16
qt 108,5,36864
qw 5
qm 80,96,120
v_permlane16_swap_b32 v44,v64
v_mul_f32 v16,v49,v49
s_lshl_b32 s52,s8,7
qt 110,5,37376
qm 84,96,192
v_permlane16_swap_b32 v45,v65
v_fma_f32 v66,v40,v44,v64
v_fma_f32 v16,v16,v12,v10
qt 96,4,40960
qw 5
qm 88,100,120
v_fma_f32 v67,v40,v45,v65
v_mul_f32 v16,v16,v49
v_add_u32 v6,s52,v6
qt 98,4,41472
qm 92,100,192
v_fma_f32 v66,-v41,v45,v66
v_fma_f32 v67,v41,v44,v67
v_exp_f32 v16,v16
qt 100,5,40960
qw 5
qm 80,104,124
v_mov_b32 v64,v66
v_fma_f32 v16,v16,v14,v14
s_sub_u32 s52,0x400,s52
qt 102,5,41472
qm 84,104,196
v_mov_b32 v65,v67
v_rcp_f32 v16,v16
qt 104,4,45056
qw 5
qm 88,108,124
v_permlane32_swap_b32 v66,v64
v_permlane32_swap_b32 v67,v65
v_mul_f32 v49,v49,v16
qt 106,4,45568
qm 92,108,196
v_cndmask_b32 v66,0,v66,s[30:31]
v_mul_f32 v16,v50,v50
v_lshrrev_b32 v7,5,v0
qt 108,5,45056
qw 5
qm 80,96,128
v_cndmask_b32 v67,0,v67,s[30:31]
v_fma_f32 v64,v40,v66,v44
v_fma_f32 v16,v16,v12,v10
qt 110,5,45568
qm 84,96,200
v_fma_f32 v65,v40,v67,v45
v_mul_f32 v16,v16,v50
v_mul_u32_u24 v7,s52,v7
qt 96,2,16384
qw 5
qm 88,100,128
v_fma_f32 v64,-v41,v67,v64
v_fma_f32 v65,v41,v66,v65
v_exp_f32 v16,v16
qt 98,2,16896
qm 92,100,200
v_cndmask_b32 v66,v66,v64,s[32:33]
v_fma_f32 v16,v16,v14,v14
v_add_u32 v6,v6,v7
qt 100,3,16384
qw 5
qm 80,104,132
v_cndmask_b32 v67,v67,v65,s[32:33]
ds_write_b64 v43,v[66:67] offset:0
v_rcp_f32 v16,v16
qt 102,3,16896
qm 84,104,204
v_fma_f32 v64,v38,v66,v28
v_mul_f32 v50,v50,v16
s_lshl_b32 s52,s10,2
qt 104,2,20480
qw 6
qm 88,108,132
qw 2
s_barrier
v_fma_f32 v65,v38,v67,v29
v_fma_f32 v64,-v39,v67,v64
v_mul_f32 v16,v51,v51
qt 106,2,20992
qm 92,108,204
v_fma_f32 v65,v39,v66,v65
v_fma_f32 v16,v16,v12,v10
s_add_u32 s52,s52,s7
qt 108,3,20480
qm 80,96,136
ds_write_b64 v43,v[64:65] offset:512
v_fma_f32 v44,v38,v64,v30
v_mul_f32 v16,v16,v51
qt 110,3,20992
qm 84,96,208
v_fma_f32 v45,v38,v65,v31
v_exp_f32 v16,v16
s_mul_i32 s52,s52,10368
qt 96,2,24576
qw 6
qm 88,100,136
v_fma_f32 v44,-v39,v65,v44
v_fma_f32 v16,v16,v14,v14
qt 98,2,25088
qm 92,100,208
v_fma_f32 v45,v39,v64,v45
ds_write_b64 v43,v[44:45] offset:1024
v_rcp_f32 v16,v16
qt 100,3,24576
qw 7
qm 80,104,140
v_fma_f32 v68,v38,v44,v32
v_mul_f32 v51,v51,v16
qt 102,3,25088
qm 84,104,212
v_fma_f32 v69,v38,v45,v33
v_fma_f32 v68,-v39,v45,v68
v_mul_f32 v16,v56,v56
qt 104,2,28672
qw 6
qm 88,108,140
v_fma_f32 v69,v39,v44,v69
v_fma_f32 v16,v16,v12,v10
qt 106,2,29184
qm 92,108,212
ds_write_b64 v43,v[68:69] offset:1536
v_fma_f32 v72,v38,v68,v34
v_mul_f32 v16,v16,v56
qt 108,3,28672
qw 7
qm 80,96,144
v_fma_f32 v74,v38,v69,v35
v_exp_f32 v16,v16
qt 110,3,29184
qm 84,96,216
v_fma_f32 v72,-v39,v69,v72
v_fma_f32 v74,v39,v68,v74
v_fma_f32 v16,v16,v14,v14
qt 96,2,32768
qw 6
qm 88,100,144
v_cmp_gt_u32 vcc,48,v0
v_rcp_f32 v16,v16
qt 98,2,33280
qm 92,100,216
v_mov_b32 v42,0x7ff00000
s_mul_i32 s30,s10,9216
v_mul_f32 v56,v56,v16
qt 100,3,32768
qw 6
qm 80,104,148
s_lshl_b32 s31,s12,8
v_mul_f32 v16,v57,v57
qt 102,3,33280
qm 84,104,220
s_add_u32 s30,s30,s31
v_cndmask_b32 v76,v76,v42,vcc
v_fma_f32 v16,v16,v12,v10
qt 104,2,36864
qw 5
qm 88,108,148
buffer_store_dwordx2 v[72:73],v76,s[84:87],s30 offen sc1
v_mul_f32 v16,v16,v57
qt 106,2,37376
qm 92,108,220
buffer_store_dwordx2 v[74:75],v76,s[84:87],s30 offen offset:8 sc1
v_exp_f32 v16,v16
qt 108,3,36864
qw 5
qm 80,96,152
v_fma_f32 v16,v16,v14,v14
qt 110,3,37376
qm 84,96,224
v_rcp_f32 v16,v16
qt 96,2,40960
qw 5
qm 88,100,152
v_mul_f32 v57,v57,v16
qt 98,2,41472
qm 92,100,224
v_mul_f32 v16,v58,v58
qt 100,3,40960
qw 5
qm 80,104,156
v_fma_f32 v16,v16,v12,v10
qt 102,3,41472
qm 84,104,228
v_mul_f32 v16,v16,v58
qt 104,2,45056
qw 5
qm 88,108,156
s_barrier
v_exp_f32 v16,v16
qt 106,2,45568
qm 92,108,228
v_fma_f32 v16,v16,v14,v14
qt 108,3,45056
qw 5
qm 80,96,160
v_rcp_f32 v16,v16
qt 110,3,45568
qm 84,96,232
v_mul_f32 v58,v58,v16
qt 96,4,16384
qw 5
qm 88,100,160
v_mul_f32 v16,v59,v59
qt 98,4,16896
qm 92,100,232
v_fma_f32 v16,v16,v12,v10
qt 100,5,16384
qw 5
qm 80,104,164
v_mul_f32 v16,v16,v59
qt 102,5,16896
qm 84,104,236
v_exp_f32 v16,v16
qt 104,4,20480
qw 5
qm 88,108,164
v_fma_f32 v16,v16,v14,v14
qt 106,4,20992
qm 92,108,236
v_rcp_f32 v16,v16
qt 108,5,20480
qw 5
qm 80,96,168
v_mul_f32 v59,v59,v16
qt 110,5,20992
qm 84,96,240
v_mul_f32 v16,v52,v52
qt 96,4,24576
qw 5
qm 88,100,168
v_fma_f32 v16,v16,v13,v11
qt 98,4,25088
qm 92,100,240
v_mul_f32 v16,v16,v52
qt 100,5,24576
qw 5
qm 80,104,172
v_exp_f32 v16,v16
qt 102,5,25088
qm 84,104,244
v_fma_f32 v16,v16,v15,v15
qt 104,4,28672
qw 5
qm 88,108,172
v_rcp_f32 v16,v16
qt 106,4,29184
qm 92,108,244
v_mul_f32 v52,v52,v16
qt 108,5,28672
qw 5
qm 80,96,176
v_mul_f32 v16,v53,v53
qt 110,5,29184
qm 84,96,248
v_fma_f32 v16,v16,v13,v11
qt 96,2,49152
qw 5
qm 88,100,176
v_mul_f32 v16,v16,v53
qt 98,2,49664
qm 92,100,248
v_exp_f32 v16,v16
qt 100,3,49152
qw 5
qm 80,104,180
v_fma_f32 v16,v16,v15,v15
qt 102,3,49664
qm 84,104,252
v_rcp_f32 v16,v16
qt 104,2,53248
qw 5
qm 88,108,180
s_barrier
v_mul_f32 v53,v53,v16
qt 106,2,53760
qm 92,108,252
qt 108,3,53248
qw 5
qz 64,96,112
v_mul_f32 v16,v54,v54
qt 110,3,53760
qz 68,96,184
v_fma_f32 v16,v16,v13,v11
qt 96,4,32768
qw 5
qz 72,100,112
v_mul_f32 v16,v16,v54
qt 98,4,33280
qz 76,100,184
v_exp_f32 v16,v16
qt 100,5,32768
qw 5
qm 64,104,116
v_fma_f32 v16,v16,v15,v15
qt 102,5,33280
qm 68,104,188
v_rcp_f32 v16,v16
qt 104,4,36864
qw 5
qm 72,108,116
v_mul_f32 v54,v54,v16
qt 106,4,37376
qm 76,108,188
v_mul_f32 v16,v55,v55
qt 108,5,36864
qw 5
qm 64,96,120
v_fma_f32 v16,v16,v13,v11
qt 110,5,37376
qm 68,96,192
v_mul_f32 v16,v16,v55
qt 96,4,40960
qw 5
qm 72,100,120
v_exp_f32 v16,v16
qt 98,4,41472
qm 76,100,192
v_fma_f32 v16,v16,v15,v15
qt 100,5,40960
qw 5
qm 64,104,124
v_rcp_f32 v16,v16
qt 102,5,41472
qm 68,104,196
v_mul_f32 v55,v55,v16
qt 104,4,45056
qw 5
qm 72,108,124
v_mul_f32 v16,v60,v60
qt 106,4,45568
qm 76,108,196
v_fma_f32 v16,v16,v13,v11
qt 108,5,45056
qw 5
qm 64,96,128
v_mul_f32 v16,v16,v60
qt 110,5,45568
qm 68,96,200
v_exp_f32 v16,v16
qt 96,2,57344
qw 5
qm 72,100,128
v_fma_f32 v16,v16,v15,v15
qt 98,2,57856
qm 76,100,200
v_rcp_f32 v16,v16
qt 100,3,57344
qw 5
qm 64,104,132
v_mul_f32 v60,v60,v16
qt 102,3,57856
qm 68,104,204
v_mul_f32 v16,v61,v61
qt 104,2,61440
qw 5
qm 72,108,132
s_barrier
v_fma_f32 v16,v16,v13,v11
qt 106,2,61952
qm 76,108,204
v_mul_f32 v16,v16,v61
qt 108,3,61440
qw 5
qm 64,96,136
v_exp_f32 v16,v16
qt 110,3,61952
qm 68,96,208
v_fma_f32 v16,v16,v15,v15
qt 96,4,0
qw 5
qm 72,100,136
v_rcp_f32 v16,v16
qt 98,4,512
qm 76,100,208
v_mul_f32 v61,v61,v16
qt 100,5,0
qw 5
qm 64,104,140
v_mul_f32 v16,v62,v62
qt 102,5,512
qm 68,104,212
v_fma_f32 v16,v16,v13,v11
qt 104,4,4096
qw 5
qm 72,108,140
v_mul_f32 v16,v16,v62
qt 106,4,4608
qm 76,108,212
v_exp_f32 v16,v16
qt 108,5,4096
qw 5
qm 64,96,144
v_fma_f32 v16,v16,v15,v15
qt 110,5,4608
qm 68,96,216
v_rcp_f32 v16,v16
qt 96,4,8192
qw 5
qm 72,100,144
v_mul_f32 v62,v62,v16
qt 98,4,8704
qm 76,100,216
v_mul_f32 v16,v63,v63
qt 100,5,8192
qw 5
qm 64,104,148
v_fma_f32 v16,v16,v13,v11
qt 102,5,8704
qm 68,104,220
v_mul_f32 v16,v16,v63
qt 104,4,12288
qw 5
qm 72,108,148
v_exp_f32 v16,v16
qt 106,4,12800
qm 76,108,220
v_fma_f32 v16,v16,v15,v15
qt 108,5,12288
qw 5
qm 64,96,152
v_rcp_f32 v16,v16
qt 110,5,12800
qm 68,96,224
v_mul_f32 v63,v63,v16
qt 96,2,0
qw 5
qm 72,100,152
v_mul_f32 v48,v48,v80
qt 98,2,512
qm 76,100,224
v_mul_f32 v49,v49,v81
qt 100,3,0
qw 5
qm 64,104,156
v_cvt_pk_bf16_f32 v48,v48,v49
qt 102,3,512
qm 68,104,228
v_mul_f32 v50,v50,v82
qt 104,2,4096
qw 5
qm 72,108,156
s_barrier
v_mul_f32 v51,v51,v83
qt 106,2,4608
qm 76,108,228
v_cvt_pk_bf16_f32 v49,v50,v51
qt 108,3,4096
qw 5
qm 64,96,160
v_mul_f32 v56,v56,v88
qt 110,3,4608
qm 68,96,232
v_mul_f32 v57,v57,v89
qt 96,2,8192
qw 5
qm 72,100,160
v_cvt_pk_bf16_f32 v50,v56,v57
qt 98,2,8704
qm 76,100,232
v_mul_f32 v58,v58,v90
qt 100,3,8192
qw 5
qm 64,104,164
v_mul_f32 v59,v59,v91
qt 102,3,8704
qm 68,104,236
v_cvt_pk_bf16_f32 v51,v58,v59
qt 104,2,12288
qw 5
qm 72,108,164
ds_write_b128 v18,v[48:51] offset:8192
v_mul_f32 v52,v52,v84
qt 106,2,12800
qm 76,108,236
s_add_u32 s15,s76,8192
buffer_store_dwordx4 v[48:51],v18,s[88:91],s15 offen offset:0 sc1
v_mul_f32 v53,v53,v85
qt 108,3,12288
qw 6
qm 64,96,168
v_cvt_pk_bf16_f32 v52,v52,v53
qt 110,3,12800
qm 68,96,240
v_mul_f32 v54,v54,v86
qt 96,2,16384
qw 6
qm 72,100,168
v_mul_f32 v55,v55,v87
qt 98,2,16896
qm 76,100,240
v_cvt_pk_bf16_f32 v53,v54,v55
qt 100,3,16384
qw 5
qm 64,104,172
v_mul_f32 v60,v60,v92
qt 102,3,16896
qm 68,104,244
v_mul_f32 v61,v61,v93
qt 104,2,20480
qw 5
qm 72,108,172
v_cvt_pk_bf16_f32 v54,v60,v61
qt 106,2,20992
qm 76,108,244
v_mul_f32 v62,v62,v94
qt 108,3,20480
qw 5
qm 64,96,176
v_mul_f32 v63,v63,v95
qt 110,3,20992
qm 68,96,248
v_cvt_pk_bf16_f32 v55,v62,v63
qt 96,2,24576
qw 5
qm 72,100,176
ds_write_b128 v18,v[52:55] offset:9216
qt 98,2,25088
qm 76,100,248
s_add_u32 s15,s76,8192
buffer_store_dwordx4 v[52:55],v18,s[88:91],s15 offen offset:1024 sc1
qt 100,3,24576
qw 6
qm 64,104,180
qt 102,3,25088
qm 68,104,252
qt 104,2,28672
qw 6
qm 72,108,180
qw 4
s_barrier
qt 106,2,29184
qm 76,108,252
qt 108,3,28672
qw 5
qz 80,96,112
qt 110,3,29184
qz 84,96,184
v_mul_f32 v16,v64,v64
qt 96,4,32768
qw 5
qz 88,100,112
v_fma_f32 v16,v16,v12,v10
v_lshlrev_b32 v44,4,v0
qt 98,4,33280
qz 92,100,184
v_mul_f32 v16,v16,v64
s_lshl_b32 s30,s7,2
qt 100,5,32768
qw 5
qm 80,104,116
v_exp_f32 v16,v16
s_sub_i32 s30,s10,s30
qt 102,5,33280
qm 84,104,188
v_fma_f32 v16,v16,v14,v14
qt 104,4,36864
qw 5
qm 88,108,116
v_rcp_f32 v16,v16
s_lshl_b32 s31,s12,8
qt 106,4,37376
qm 92,108,188
v_mul_f32 v64,v64,v16
s_sub_i32 s30,s30,1
qt 108,5,36864
qw 5
qm 80,96,120
v_mul_f32 v16,v65,v65
qt 110,5,37376
qm 84,96,192
v_fma_f32 v16,v16,v12,v10
s_mul_i32 s68,s30,9216
qt 96,4,40960
qw 5
qm 88,100,120
v_mul_f32 v16,v16,v65
s_add_u32 s68,s68,s31
qt 98,4,41472
qm 92,100,192
v_exp_f32 v16,v16
s_cmp_ge_i32 s30,0
s_cselect_b32 s68,s68,0x7ff00000
s_cselect_b32 s72,s16,0
qt 100,5,40960
qw 5
qm 80,104,124
v_fma_f32 v16,v16,v14,v14
qt 102,5,41472
qm 84,104,196
v_rcp_f32 v16,v16
s_sub_i32 s30,s30,1
qt 104,4,45056
qw 5
qm 88,108,124
v_mul_f32 v65,v65,v16
s_mul_i32 s69,s30,9216
qt 106,4,45568
qm 92,108,196
v_mul_f32 v16,v66,v66
qt 108,5,45056
qw 5
qm 80,96,128
v_fma_f32 v16,v16,v12,v10
s_add_u32 s69,s69,s31
qt 110,5,45568
qm 84,96,200
v_mul_f32 v16,v16,v66
s_cmp_ge_i32 s30,0
s_cselect_b32 s69,s69,0x7ff00000
s_cselect_b32 s73,s16,0
qt 96,2,32768
qw 5
qm 88,100,128
v_exp_f32 v16,v16
s_sub_i32 s30,s30,1
qt 98,2,33280
qm 92,100,200
v_fma_f32 v16,v16,v14,v14
qt 100,3,32768
qw 5
qm 80,104,132
v_rcp_f32 v16,v16
s_mul_i32 s70,s30,9216
qt 102,3,33280
qm 84,104,204
v_mul_f32 v66,v66,v16
s_add_u32 s70,s70,s31
qt 104,2,36864
qw 5
qm 88,108,132
s_barrier
v_mul_f32 v16,v67,v67
qt 106,2,37376
qm 92,108,204
v_fma_f32 v16,v16,v12,v10
s_cmp_ge_i32 s30,0
s_cselect_b32 s70,s70,0x7ff00000
s_cselect_b32 s74,s16,0
qt 108,3,36864
qw 5
qm 80,96,136
v_mul_f32 v16,v16,v67
s_sub_i32 s30,s30,1
qt 110,3,37376
qm 84,96,208
v_exp_f32 v16,v16
s_mul_i32 s71,s30,9216
qt 96,2,40960
qw 5
qm 88,100,136
v_fma_f32 v16,v16,v14,v14
qt 98,2,41472
qm 92,100,208
v_rcp_f32 v16,v16
s_add_u32 s71,s71,s31
qt 100,3,40960
qw 5
qm 80,104,140
v_mul_f32 v67,v67,v16
s_cmp_ge_i32 s30,0
s_cselect_b32 s71,s71,0x7ff00000
s_cselect_b32 s75,s16,0
qt 102,3,41472
qm 84,104,212
v_mul_f32 v16,v72,v72
qt 104,2,45056
qw 5
qm 88,108,140
v_fma_f32 v16,v16,v12,v10
buffer_load_dwordx4 v[28:31],v44,s[84:87],s68 offen sc1
qt 106,2,45568
qm 92,108,212
v_mul_f32 v16,v16,v72
qt 108,3,45056
qw 5
qm 80,96,144
v_exp_f32 v16,v16
buffer_load_dwordx4 v[32:35],v44,s[84:87],s69 offen sc1
qt 110,3,45568
qm 84,96,216
v_fma_f32 v16,v16,v14,v14
qt 96,2,49152
qw 5
qm 88,100,144
v_rcp_f32 v16,v16
buffer_load_dwordx4 v[36:39],v44,s[84:87],s70 offen sc1
qt 98,2,49664
qm 92,100,216
v_mul_f32 v72,v72,v16
qt 100,3,49152
qw 5
qm 80,104,148
v_mul_f32 v16,v73,v73
buffer_load_dwordx4 v[40:43],v44,s[84:87],s71 offen sc1
qt 102,3,49664
qm 84,104,220
v_fma_f32 v16,v16,v12,v10
qt 104,2,53248
qw 5
qm 88,108,148
v_mul_f32 v16,v16,v73
qt 106,2,53760
qm 92,108,220
v_exp_f32 v16,v16
qt 108,3,53248
qw 5
qm 80,96,152
v_fma_f32 v16,v16,v14,v14
v_add_u32 v26,123904,v44
qd 20,26,0
qt 110,3,53760
qm 84,96,224
v_rcp_f32 v16,v16
qt 96,2,57344
qw 6
qm 88,100,152
v_mul_f32 v73,v73,v16
qt 98,2,57856
qm 92,100,224
v_mul_f32 v16,v74,v74
qt 100,3,57344
qw 6
qm 80,104,156
v_fma_f32 v16,v16,v12,v10
qt 102,3,57856
qm 84,104,228
v_mul_f32 v16,v16,v74
qt 104,2,61440
qw 5
qm 88,108,156
s_barrier
v_exp_f32 v16,v16
s_lshl_b32 s30,s7,9
v_cvt_f32_u32 v17,s30
qt 106,2,61952
qm 92,108,228
v_fma_f32 v16,v16,v14,v14
v_mul_f32 v26,0x43000000,v20
qt 108,3,61440
qw 5
qm 80,96,160
v_rcp_f32 v16,v16
v_mul_f32 v27,v17,v20
qt 110,3,61952
qm 84,96,232
v_mul_f32 v74,v74,v16
v_mul_f32 v23,0x43000000,v21
qt 96,4,0
qw 5
qm 88,100,160
v_mul_f32 v16,v75,v75
qt 98,4,512
qm 92,100,232
v_fma_f32 v16,v16,v12,v10
v_mul_f32 v25,v17,v21
qt 100,5,0
qw 5
qm 80,104,164
v_mul_f32 v16,v16,v75
v_exp_f32 v26,v26
qt 102,5,512
qm 84,104,236
v_exp_f32 v16,v16
v_exp_f32 v27,v27
qt 104,4,4096
qw 5
qm 88,108,164
v_fma_f32 v16,v16,v14,v14
qt 106,4,4608
qm 92,108,236
v_rcp_f32 v16,v16
v_fract_f32 v23,v23
qt 108,5,4096
qw 5
qm 80,96,168
v_mul_f32 v75,v75,v16
v_fract_f32 v25,v25
qt 110,5,4608
qm 84,96,240
v_mul_f32 v16,v68,v68
v_cos_f32 v22,v23
qt 96,4,8192
qw 5
qm 88,100,168
v_fma_f32 v16,v16,v13,v11
v_cos_f32 v24,v25
qt 98,4,8704
qm 92,100,240
v_mul_f32 v16,v16,v68
qt 100,5,8192
qw 5
qm 80,104,172
v_exp_f32 v16,v16
v_sin_f32 v23,v23
qt 102,5,8704
qm 84,104,244
v_fma_f32 v16,v16,v15,v15
v_sin_f32 v25,v25
qt 104,4,12288
qw 5
qm 88,108,172
v_rcp_f32 v16,v16
v_mul_f32 v22,v22,v26
qt 106,4,12800
qm 92,108,244
v_mul_f32 v68,v68,v16
qt 108,5,12288
qw 5
qm 80,96,176
v_mul_f32 v16,v69,v69
v_mul_f32 v24,v24,v27
qt 110,5,12800
qm 84,96,248
v_fma_f32 v16,v16,v13,v11
v_mul_f32 v23,v23,v26
qt 96,2,0
qw 5
qm 88,100,176
v_mul_f32 v16,v16,v69
v_mul_f32 v25,v25,v27
qt 98,2,512
qm 92,100,248
v_exp_f32 v16,v16
qt 100,3,0
qw 5
qm 80,104,180
v_fma_f32 v16,v16,v15,v15
qt 102,3,512
qm 84,104,252
v_rcp_f32 v16,v16
qt 104,2,4096
qw 5
qm 88,108,180
s_barrier
v_mul_f32 v69,v69,v16
qt 106,2,4608
qm 92,108,252
qt 108,3,4096
s_mov_b32 s34,0
s_mov_b32 s32,-1
s_mov_b32 s33,-1
s_waitcnt vmcnt(0)
.L_gY:
s_waitcnt vmcnt(0)
v_xor_b32 v26,s72,v29
v_xor_b32 v27,s72,v31
v_or3_b32 v17,v26,v27,0
v_xor_b32 v26,s73,v33
v_xor_b32 v27,s73,v35
v_or3_b32 v17,v26,v27,v17
v_xor_b32 v26,s74,v37
v_xor_b32 v27,s74,v39
v_or3_b32 v17,v26,v27,v17
v_xor_b32 v26,s75,v41
v_xor_b32 v27,s75,v43
v_or3_b32 v17,v26,v27,v17
v_cmp_ne_u32 vcc,0,v17
s_nop 1
s_and_b64 s[30:31],vcc,s[32:33]
s_cbranch_scc0 .L_gdY
s_add_u32 s34,s34,1
s_cmp_gt_u32 s34,1000000
s_cbranch_scc1 .L_gdY
s_sleep 4
buffer_load_dwordx4 v[28:31],v44,s[84:87],s68 offen sc1
buffer_load_dwordx4 v[32:35],v44,s[84:87],s69 offen sc1
buffer_load_dwordx4 v[36:39],v44,s[84:87],s70 offen sc1
buffer_load_dwordx4 v[40:43],v44,s[84:87],s71 offen sc1
s_branch .L_gY
.L_gdY:
qw 5
qz 48,96,112
v_mul_f32 v16,v70,v70
v_fma_f32 v36,v22,v40,v36
v_fma_f32 v38,v22,v42,v38
qt 110,3,4608
qz 52,96,184
v_fma_f32 v16,v16,v13,v11
v_fma_f32 v36,-v23,v42,v36
qt 96,4,32768
qw 5
qz 56,100,112
v_mul_f32 v16,v16,v70
v_fma_f32 v38,v23,v40,v38
qt 98,4,33280
qz 60,100,184
v_exp_f32 v16,v16
v_fma_f32 v32,v22,v36,v32
qt 100,5,32768
qw 5
qm 48,104,116
v_fma_f32 v16,v16,v15,v15
v_fma_f32 v34,v22,v38,v34
qt 102,5,33280
qm 52,104,188
v_rcp_f32 v16,v16
v_fma_f32 v32,-v23,v38,v32
qt 104,4,36864
qw 5
qm 56,108,116
v_mul_f32 v70,v70,v16
v_fma_f32 v34,v23,v36,v34
qt 106,4,37376
qm 60,108,188
v_mul_f32 v16,v71,v71
v_fma_f32 v28,v22,v32,v28
qt 108,5,36864
qw 5
qm 48,96,120
v_fma_f32 v16,v16,v13,v11
v_fma_f32 v30,v22,v34,v30
qt 110,5,37376
qm 52,96,192
v_mul_f32 v16,v16,v71
v_fma_f32 v28,-v23,v34,v28
v_fma_f32 v30,v23,v32,v30
qt 96,4,40960
qw 5
qm 56,100,120
v_exp_f32 v16,v16
v_mul_f32 v20,v24,v28
qt 98,4,41472
qm 60,100,192
v_fma_f32 v16,v16,v15,v15
v_mul_f32 v21,v24,v30
qt 100,5,40960
qw 5
qm 48,104,124
v_rcp_f32 v16,v16
v_fma_f32 v20,-v25,v30,v20
qt 102,5,41472
qm 52,104,196
v_mul_f32 v71,v71,v16
v_fma_f32 v21,v25,v28,v21
qt 104,4,45056
qw 5
qm 56,108,124
v_mul_f32 v16,v76,v76
v_lshlrev_b32 v26,3,v0
qt 106,4,45568
qm 60,108,196
v_fma_f32 v16,v16,v13,v11
v_lshl_add_u32 v26,s7,9,v26
qt 108,5,45056
qw 5
qm 48,96,128
v_mul_f32 v16,v16,v76
v_add_u32 v26,125440,v26
qt 110,5,45568
qm 52,96,200
v_exp_f32 v16,v16
ds_write_b64 v26,v[20:21]
qt 96,2,8192
qw 6
qm 56,100,128
v_fma_f32 v16,v16,v15,v15
qt 98,2,8704
qm 60,100,200
v_rcp_f32 v16,v16
qt 100,3,8192
qw 6
qm 48,104,132
v_mul_f32 v76,v76,v16
qt 102,3,8704
qm 52,104,204
v_mul_f32 v16,v77,v77
qt 104,2,12288
qw 6
qm 56,108,132
qw 5
s_barrier
v_fma_f32 v16,v16,v13,v11
qt 106,2,12800
qm 60,108,204
v_mul_f32 v16,v16,v77
qt 108,3,12288
qw 5
qm 48,96,136
v_exp_f32 v16,v16
qt 110,3,12800
qm 52,96,208
v_fma_f32 v16,v16,v15,v15
qt 96,2,16384
qw 5
qm 56,100,136
v_rcp_f32 v16,v16
qt 98,2,16896
qm 60,100,208
v_mul_f32 v77,v77,v16
qt 100,3,16384
qw 5
qm 48,104,140
v_mul_f32 v16,v78,v78
qt 102,3,16896
qm 52,104,212
v_fma_f32 v16,v16,v13,v11
qt 104,2,20480
qw 5
qm 56,108,140
v_mul_f32 v16,v16,v78
qt 106,2,20992
qm 60,108,212
v_exp_f32 v16,v16
qt 108,3,20480
qw 5
qm 48,96,144
v_fma_f32 v16,v16,v15,v15
qt 110,3,20992
qm 52,96,216
v_rcp_f32 v16,v16
qt 96,2,24576
qw 5
qm 56,100,144
v_mul_f32 v78,v78,v16
qt 98,2,25088
qm 60,100,216
v_mul_f32 v16,v79,v79
qt 100,3,24576
qw 5
qm 48,104,148
v_fma_f32 v16,v16,v13,v11
qt 102,3,25088
qm 52,104,220
v_mul_f32 v16,v16,v79
qt 104,2,28672
qw 5
qm 56,108,148
v_exp_f32 v16,v16
qt 106,2,29184
qm 60,108,220
v_fma_f32 v16,v16,v15,v15
qt 108,3,28672
qw 5
qm 48,96,152
v_rcp_f32 v16,v16
qt 110,3,29184
qm 52,96,224
v_mul_f32 v79,v79,v16
qt 96,2,32768
qw 5
qm 56,100,152
v_mul_f32 v64,v64,v80
qt 98,2,33280
qm 60,100,224
v_mul_f32 v65,v65,v81
qt 100,3,32768
qw 5
qm 48,104,156
v_cvt_pk_bf16_f32 v64,v64,v65
qt 102,3,33280
qm 52,104,228
v_mul_f32 v66,v66,v82
qt 104,2,36864
qw 5
qm 56,108,156
s_barrier
v_mul_f32 v67,v67,v83
qt 106,2,37376
qm 60,108,228
v_cvt_pk_bf16_f32 v65,v66,v67
qt 108,3,36864
qw 5
qm 48,96,160
v_mul_f32 v72,v72,v88
v_and_b32 v44,15,v0
v_lshrrev_b32 v45,4,v0
qt 110,3,37376
qm 52,96,232
v_mul_f32 v73,v73,v89
v_lshl_add_u32 v17,v44,2,v45
qt 96,2,40960
qw 5
qm 56,100,160
v_cvt_pk_bf16_f32 v66,v72,v73
v_lshl_add_u32 v45,s7,2,v45
qt 98,2,41472
qm 60,100,232
v_mul_f32 v74,v74,v90
v_lshlrev_b32 v28,4,v44
qt 100,3,40960
qw 5
qm 48,104,164
v_mul_f32 v75,v75,v91
v_add_u32 v28,123904,v28
qt 102,3,41472
qm 52,104,236
v_cvt_pk_bf16_f32 v67,v74,v75
v_lshlrev_b32 v44,3,v44
v_lshl_add_u32 v42,v45,9,v44
qt 104,2,45056
qw 5
qm 56,108,164
ds_write_b128 v18,v[64:67] offset:24576
v_mul_f32 v68,v68,v84
v_add_u32 v42,114688,v42
qt 106,2,45568
qm 60,108,236
s_add_u32 s15,s76,24576
buffer_store_dwordx4 v[64:67],v18,s[88:91],s15 offen offset:0 sc1
v_mul_f32 v69,v69,v85
v_add_u32 v44,124928,v44
qt 108,3,45056
qw 6
qm 48,96,168
v_cvt_pk_bf16_f32 v68,v68,v69
qr 28,28,256
qt 110,3,45568
qm 52,96,240
v_mul_f32 v70,v70,v86
qd 32,44,128
qt 96,2,49152
qw 8
qm 56,100,168
v_mul_f32 v71,v71,v87
qd 34,44,640
qd 36,44,1152
qt 98,2,49664
v_min_u32 v8,1664,v8
buffer_load_dword v1,v8,s[48:51],s54 offen
s_add_u32 s54,s54,1792
buffer_load_dword v1,v6,s[60:63],s52 offen
qm 60,100,240
v_cvt_pk_bf16_f32 v69,v70,v71
qd 38,44,1664
qt 100,3,49152
qw 10
qm 48,104,172
v_mul_f32 v76,v76,v92
qd 40,44,2176
qt 102,3,49664
qm 52,104,244
v_mul_f32 v77,v77,v93
qd 42,42,128
qt 104,2,53248
qw 11
qm 56,108,172
v_cvt_pk_bf16_f32 v70,v76,v77
qr 20,18,57344
qt 106,2,53760
qm 60,108,244
v_mul_f32 v78,v78,v94
qr 24,18,58368
v_lshlrev_b32 v17,3,v17
qt 108,3,53248
qw 10
qm 48,96,176
v_mul_f32 v79,v79,v95
v_add_u32 v17,s13,v17
qt 110,3,53760
qm 52,96,248
v_cvt_pk_bf16_f32 v71,v78,v79
v_add_u32 v17,139264,v17
qt 96,2,57344
qw 8
qm 56,100,176
ds_write_b128 v18,v[68:71] offset:25600
v_lshlrev_b32 v45,3,v45
qt 98,2,57856
qm 60,100,248
s_add_u32 s15,s76,24576
buffer_store_dwordx4 v[68:71],v18,s[88:91],s15 offen offset:1024 sc1
v_cvt_f32_u32 v45,v45
qt 100,3,57344
qw 7
qm 48,104,180
qt 102,3,57856
qm 52,104,252
qt 104,2,61440
qw 6
qm 56,108,180
qw 4
s_barrier
qt 106,2,61952
qm 60,108,252
qt 108,3,61440
v_add_f32 v34,v34,v36
v_add_f32 v35,v35,v37
v_add_f32 v38,v38,v40
v_add_f32 v39,v39,v41
v_add_f32 v34,v34,v38
v_add_f32 v35,v35,v39
v_mul_f32 v44,v45,v28
v_mul_f32 v37,v45,v29
v_exp_f32 v44,v44
v_fract_f32 v37,v37
v_cos_f32 v36,v37
v_sin_f32 v37,v37
v_mul_f32 v36,v36,v44
v_mul_f32 v37,v37,v44
v_fma_f32 v38,v36,v34,v42
v_fma_f32 v39,v36,v35,v43
v_fma_f32 v38,-v37,v35,v38
v_fma_f32 v39,v37,v34,v39
v_lshlrev_b32 v34,16,v20
v_and_b32 v35,0xffff0000,v20
v_fma_f32 v40,v32,v38,v34
v_fma_f32 v41,v32,v39,v35
v_fma_f32 v40,-v33,v39,v40
v_fma_f32 v41,v33,v38,v41
v_mul_f32 v36,v30,v40
v_mul_f32 v42,v30,v41
v_fma_f32 v36,-v31,v41,v36
v_fma_f32 v42,v31,v40,v42
v_lshlrev_b32 v34,16,v21
v_and_b32 v35,0xffff0000,v21
v_fma_f32 v38,v32,v40,v34
v_fma_f32 v39,v32,v41,v35
v_fma_f32 v38,-v33,v41,v38
v_fma_f32 v39,v33,v40,v39
v_mul_f32 v37,v30,v38
v_mul_f32 v43,v30,v39
v_fma_f32 v37,-v31,v39,v37
v_fma_f32 v43,v31,v38,v43
v_cvt_pk_bf16_f32 v20,v36,v37
v_cvt_pk_bf16_f32 v28,-v42,-v43
v_lshlrev_b32 v34,16,v22
v_and_b32 v35,0xffff0000,v22
v_fma_f32 v40,v32,v38,v34
v_fma_f32 v41,v32,v39,v35
v_fma_f32 v40,-v33,v39,v40
v_fma_f32 v41,v33,v38,v41
v_mul_f32 v36,v30,v40
v_mul_f32 v42,v30,v41
v_fma_f32 v36,-v31,v41,v36
v_fma_f32 v42,v31,v40,v42
v_lshlrev_b32 v34,16,v23
v_and_b32 v35,0xffff0000,v23
v_fma_f32 v38,v32,v40,v34
v_fma_f32 v39,v32,v41,v35
v_fma_f32 v38,-v33,v41,v38
v_fma_f32 v39,v33,v40,v39
v_mul_f32 v37,v30,v38
v_mul_f32 v43,v30,v39
v_fma_f32 v37,-v31,v39,v37
v_fma_f32 v43,v31,v38,v43
v_cvt_pk_bf16_f32 v21,v36,v37
v_cvt_pk_bf16_f32 v29,-v42,-v43
ds_write_b64 v17,v[20:21] offset:0
ds_write_b64 v17,v[28:29] offset:512
v_lshlrev_b32 v34,16,v24
v_and_b32 v35,0xffff0000,v24
v_fma_f32 v40,v32,v38,v34
v_fma_f32 v41,v32,v39,v35
v_fma_f32 v40,-v33,v39,v40
v_fma_f32 v41,v33,v38,v41
v_mul_f32 v36,v30,v40
v_mul_f32 v42,v30,v41
v_fma_f32 v36,-v31,v41,v36
v_fma_f32 v42,v31,v40,v42
v_lshlrev_b32 v34,16,v25
v_and_b32 v35,0xffff0000,v25
v_fma_f32 v38,v32,v40,v34
v_fma_f32 v39,v32,v41,v35
v_fma_f32 v38,-v33,v41,v38
v_fma_f32 v39,v33,v40,v39
v_mul_f32 v37,v30,v38
v_mul_f32 v43,v30,v39
v_fma_f32 v37,-v31,v39,v37
v_fma_f32 v43,v31,v38,v43
v_cvt_pk_bf16_f32 v24,v36,v37
v_cvt_pk_bf16_f32 v28,-v42,-v43
v_lshlrev_b32 v34,16,v26
v_and_b32 v35,0xffff0000,v26
v_fma_f32 v40,v32,v38,v34
v_fma_f32 v41,v32,v39,v35
v_fma_f32 v40,-v33,v39,v40
v_fma_f32 v41,v33,v38,v41
v_mul_f32 v36,v30,v40
v_mul_f32 v42,v30,v41
v_fma_f32 v36,-v31,v41,v36
v_fma_f32 v42,v31,v40,v42
v_lshlrev_b32 v34,16,v27
v_and_b32 v35,0xffff0000,v27
v_fma_f32 v38,v32,v40,v34
v_fma_f32 v39,v32,v41,v35
v_fma_f32 v38,-v33,v41,v38
v_fma_f32 v39,v33,v40,v39
v_mul_f32 v37,v30,v38
v_mul_f32 v43,v30,v39
v_fma_f32 v37,-v31,v39,v37
v_fma_f32 v43,v31,v38,v43
v_cvt_pk_bf16_f32 v25,v36,v37
v_cvt_pk_bf16_f32 v29,-v42,-v43
ds_write_b64 v17,v[24:25] offset:1024
ds_write_b64 v17,v[28:29] offset:1536
qw 9
qz 80,96,112
qt 110,3,61952
qz 84,96,184
v_mul_f32 v16,v48,v48
qt 96,4,0
qw 9
qz 88,100,112
v_fma_f32 v16,v16,v12,v10
v_lshlrev_b32 v36,3,v0
v_add_u32 v36,s13,v36
qt 98,4,512
qz 92,100,184
v_mul_f32 v16,v16,v48
v_add_u32 v36,131072,v36
qt 100,5,0
qw 9
qm 80,104,116
v_exp_f32 v16,v16
qt 28,36,8192
qt 102,5,512
qm 84,104,188
v_fma_f32 v16,v16,v14,v14
qt 30,36,8704
qt 104,4,4096
qw 7
qm 88,108,116
v_rcp_f32 v16,v16
qt 32,36,9216
qt 106,4,4608
qm 92,108,188
v_mul_f32 v48,v48,v16
qt 34,36,9728
qt 108,5,4096
qw 9
qm 80,96,120
v_mul_f32 v16,v49,v49
qt 110,5,4608
qm 84,96,192
v_fma_f32 v16,v16,v12,v10
qt 96,4,8192
qw 8
qm 88,100,120
v_mul_f32 v16,v16,v49
qt 98,4,8704
qm 92,100,192
v_exp_f32 v16,v16
qt 100,5,8192
qw 6
qm 80,104,124
v_fma_f32 v16,v16,v14,v14
qt 102,5,8704
qm 84,104,196
v_rcp_f32 v16,v16
qt 104,4,12288
qw 5
qm 88,108,124
v_mul_f32 v49,v49,v16
qt 106,4,12800
qm 92,108,196
v_mul_f32 v16,v50,v50
s_add_u32 s15,s76,73728
buffer_store_dwordx4 v[28:31],v18,s[88:91],s15 offen offset:0 sc1
qt 108,5,12288
qw 5
qm 80,96,128
v_fma_f32 v16,v16,v12,v10
s_add_u32 s15,s76,73728
buffer_store_dwordx4 v[32:35],v18,s[88:91],s15 offen offset:1024 sc1
qt 110,5,12800
qm 84,96,200
v_mul_f32 v16,v16,v50
qt 96,2,0
qw 5
qm 88,100,128
v_exp_f32 v16,v16
qt 98,2,512
qm 92,100,200
v_fma_f32 v16,v16,v14,v14
qt 100,3,0
qw 5
qm 80,104,132
v_rcp_f32 v16,v16
qt 102,3,512
qm 84,104,204
v_mul_f32 v50,v50,v16
qt 104,2,4096
qw 5
qm 88,108,132
s_barrier
v_mul_f32 v16,v51,v51
qt 106,2,4608
qm 92,108,204
v_fma_f32 v16,v16,v12,v10
qt 108,3,4096
qw 5
qm 80,96,136
v_mul_f32 v16,v16,v51
qt 110,3,4608
qm 84,96,208
v_exp_f32 v16,v16
qt 96,2,8192
qw 5
qm 88,100,136
v_fma_f32 v16,v16,v14,v14
qt 98,2,8704
qm 92,100,208
v_rcp_f32 v16,v16
qt 100,3,8192
qw 5
qm 80,104,140
v_mul_f32 v51,v51,v16
qt 102,3,8704
qm 84,104,212
v_mul_f32 v16,v56,v56
qt 104,2,12288
qw 5
qm 88,108,140
v_fma_f32 v16,v16,v12,v10
qt 106,2,12800
qm 92,108,212
v_mul_f32 v16,v16,v56
qt 108,3,12288
qw 5
qm 80,96,144
v_exp_f32 v16,v16
qt 110,3,12800
qm 84,96,216
v_fma_f32 v16,v16,v14,v14
qt 96,2,16384
qw 5
qm 88,100,144
v_rcp_f32 v16,v16
qt 98,2,16896
qm 92,100,216
v_mul_f32 v56,v56,v16
qt 100,3,16384
qw 5
qm 80,104,148
v_mul_f32 v16,v57,v57
qt 102,3,16896
qm 84,104,220
v_fma_f32 v16,v16,v12,v10
qt 104,2,20480
qw 5
qm 88,108,148
v_mul_f32 v16,v16,v57
qt 106,2,20992
qm 92,108,220
v_exp_f32 v16,v16
qt 108,3,20480
qw 5
qm 80,96,152
v_fma_f32 v16,v16,v14,v14
qt 110,3,20992
qm 84,96,224
v_rcp_f32 v16,v16
qt 96,2,24576
qw 5
qm 88,100,152
v_mul_f32 v57,v57,v16
qt 98,2,25088
qm 92,100,224
v_mul_f32 v16,v58,v58
qt 100,3,24576
qw 5
qm 80,104,156
v_fma_f32 v16,v16,v12,v10
qt 102,3,25088
qm 84,104,228
v_mul_f32 v16,v16,v58
qt 104,2,28672
qw 5
qm 88,108,156
s_barrier
v_exp_f32 v16,v16
qt 106,2,29184
qm 92,108,228
v_fma_f32 v16,v16,v14,v14
qt 108,3,28672
qw 5
qm 80,96,160
v_rcp_f32 v16,v16
v_and_b32 v44,15,v0
v_lshrrev_b32 v45,4,v0
qt 110,3,29184
qm 84,96,232
v_mul_f32 v58,v58,v16
v_lshl_add_u32 v17,v44,2,v45
qt 96,2,32768
qw 5
qm 88,100,160
v_mul_f32 v16,v59,v59
v_lshl_add_u32 v45,s7,2,v45
qt 98,2,33280
qm 92,100,232
v_fma_f32 v16,v16,v12,v10
v_lshlrev_b32 v28,4,v44
qt 100,3,32768
qw 5
qm 80,104,164
v_mul_f32 v16,v16,v59
v_add_u32 v28,123904,v28
qt 102,3,33280
qm 84,104,236
v_exp_f32 v16,v16
v_lshlrev_b32 v44,3,v44
v_lshl_add_u32 v42,v45,9,v44
qt 104,2,36864
qw 5
qm 88,108,164
v_fma_f32 v16,v16,v14,v14
v_add_u32 v42,114688,v42
qt 106,2,37376
qm 92,108,236
v_rcp_f32 v16,v16
v_add_u32 v44,124928,v44
qt 108,3,36864
qw 5
qm 80,96,168
v_mul_f32 v59,v59,v16
qr 28,28,768
qt 110,3,37376
qm 84,96,240
v_mul_f32 v16,v52,v52
qd 32,44,384
qt 96,2,40960
qw 7
qm 88,100,168
v_fma_f32 v16,v16,v13,v11
qd 34,44,896
qd 36,44,1408
qt 98,2,41472
buffer_load_dword v1,v8,s[48:51],s54 offen
buffer_load_dword v1,v6,s[64:67],s52 offen
qm 92,100,240
v_mul_f32 v16,v16,v52
qd 38,44,1920
qt 100,3,40960
qw 10
qm 80,104,172
v_exp_f32 v16,v16
qd 40,44,2432
qt 102,3,41472
qm 84,104,244
v_fma_f32 v16,v16,v15,v15
qd 42,42,384
qt 104,2,45056
qw 11
qm 88,108,172
v_rcp_f32 v16,v16
qr 20,19,8192
qt 106,2,45568
qm 92,108,244
v_mul_f32 v52,v52,v16
qr 24,19,9216
v_lshlrev_b32 v17,3,v17
qt 108,3,45056
qw 10
qm 80,96,176
v_mul_f32 v16,v53,v53
v_add_u32 v17,s13,v17
qt 110,3,45568
qm 84,96,248
v_fma_f32 v16,v16,v13,v11
v_add_u32 v17,155648,v17
qt 96,2,49152
qw 8
qm 88,100,176
v_mul_f32 v16,v16,v53
v_lshlrev_b32 v45,3,v45
qt 98,2,49664
qm 92,100,248
v_exp_f32 v16,v16
v_cvt_f32_u32 v45,v45
qt 100,3,49152
qw 6
qm 80,104,180
v_fma_f32 v16,v16,v15,v15
qt 102,3,49664
qm 84,104,252
v_rcp_f32 v16,v16
qt 104,2,53248
qw 5
qm 88,108,180
s_barrier
v_mul_f32 v53,v53,v16
qt 106,2,53760
qm 92,108,252
qt 108,3,53248
v_add_f32 v34,v34,v36
v_add_f32 v35,v35,v37
v_add_f32 v38,v38,v40
v_add_f32 v39,v39,v41
v_add_f32 v34,v34,v38
v_add_f32 v35,v35,v39
v_mul_f32 v44,v45,v28
v_mul_f32 v37,v45,v29
v_exp_f32 v44,v44
v_fract_f32 v37,v37
v_cos_f32 v36,v37
v_sin_f32 v37,v37
v_mul_f32 v36,v36,v44
v_mul_f32 v37,v37,v44
v_fma_f32 v38,v36,v34,v42
v_fma_f32 v39,v36,v35,v43
v_fma_f32 v38,-v37,v35,v38
v_fma_f32 v39,v37,v34,v39
v_lshlrev_b32 v34,16,v20
v_and_b32 v35,0xffff0000,v20
v_fma_f32 v40,v32,v38,v34
v_fma_f32 v41,v32,v39,v35
v_fma_f32 v40,-v33,v39,v40
v_fma_f32 v41,v33,v38,v41
v_mul_f32 v36,v30,v40
v_mul_f32 v42,v30,v41
v_fma_f32 v36,-v31,v41,v36
v_fma_f32 v42,v31,v40,v42
v_lshlrev_b32 v34,16,v21
v_and_b32 v35,0xffff0000,v21
v_fma_f32 v38,v32,v40,v34
v_fma_f32 v39,v32,v41,v35
v_fma_f32 v38,-v33,v41,v38
v_fma_f32 v39,v33,v40,v39
v_mul_f32 v37,v30,v38
v_mul_f32 v43,v30,v39
v_fma_f32 v37,-v31,v39,v37
v_fma_f32 v43,v31,v38,v43
v_cvt_pk_bf16_f32 v20,v36,v37
v_cvt_pk_bf16_f32 v28,-v42,-v43
v_lshlrev_b32 v34,16,v22
v_and_b32 v35,0xffff0000,v22
v_fma_f32 v40,v32,v38,v34
v_fma_f32 v41,v32,v39,v35
v_fma_f32 v40,-v33,v39,v40
v_fma_f32 v41,v33,v38,v41
v_mul_f32 v36,v30,v40
v_mul_f32 v42,v30,v41
v_fma_f32 v36,-v31,v41,v36
v_fma_f32 v42,v31,v40,v42
v_lshlrev_b32 v34,16,v23
v_and_b32 v35,0xffff0000,v23
v_fma_f32 v38,v32,v40,v34
v_fma_f32 v39,v32,v41,v35
v_fma_f32 v38,-v33,v41,v38
v_fma_f32 v39,v33,v40,v39
v_mul_f32 v37,v30,v38
v_mul_f32 v43,v30,v39
v_fma_f32 v37,-v31,v39,v37
v_fma_f32 v43,v31,v38,v43
v_cvt_pk_bf16_f32 v21,v36,v37
v_cvt_pk_bf16_f32 v29,-v42,-v43
ds_write_b64 v17,v[20:21] offset:0
ds_write_b64 v17,v[28:29] offset:512
v_lshlrev_b32 v34,16,v24
v_and_b32 v35,0xffff0000,v24
v_fma_f32 v40,v32,v38,v34
v_fma_f32 v41,v32,v39,v35
v_fma_f32 v40,-v33,v39,v40
v_fma_f32 v41,v33,v38,v41
v_mul_f32 v36,v30,v40
v_mul_f32 v42,v30,v41
v_fma_f32 v36,-v31,v41,v36
v_fma_f32 v42,v31,v40,v42
v_lshlrev_b32 v34,16,v25
v_and_b32 v35,0xffff0000,v25
v_fma_f32 v38,v32,v40,v34
v_fma_f32 v39,v32,v41,v35
v_fma_f32 v38,-v33,v41,v38
v_fma_f32 v39,v33,v40,v39
v_mul_f32 v37,v30,v38
v_mul_f32 v43,v30,v39
v_fma_f32 v37,-v31,v39,v37
v_fma_f32 v43,v31,v38,v43
v_cvt_pk_bf16_f32 v24,v36,v37
v_cvt_pk_bf16_f32 v28,-v42,-v43
v_lshlrev_b32 v34,16,v26
v_and_b32 v35,0xffff0000,v26
v_fma_f32 v40,v32,v38,v34
v_fma_f32 v41,v32,v39,v35
v_fma_f32 v40,-v33,v39,v40
v_fma_f32 v41,v33,v38,v41
v_mul_f32 v36,v30,v40
v_mul_f32 v42,v30,v41
v_fma_f32 v36,-v31,v41,v36
v_fma_f32 v42,v31,v40,v42
v_lshlrev_b32 v34,16,v27
v_and_b32 v35,0xffff0000,v27
v_fma_f32 v38,v32,v40,v34
v_fma_f32 v39,v32,v41,v35
v_fma_f32 v38,-v33,v41,v38
v_fma_f32 v39,v33,v40,v39
v_mul_f32 v37,v30,v38
v_mul_f32 v43,v30,v39
v_fma_f32 v37,-v31,v39,v37
v_fma_f32 v43,v31,v38,v43
v_cvt_pk_bf16_f32 v25,v36,v37
v_cvt_pk_bf16_f32 v29,-v42,-v43
ds_write_b64 v17,v[24:25] offset:1024
ds_write_b64 v17,v[28:29] offset:1536
qw 9
qz 64,96,112
v_mul_f32 v16,v54,v54
qt 110,3,53760
qz 68,96,184
v_fma_f32 v16,v16,v13,v11
v_bfe_u32 v44,v0,2,2
qt 96,2,57344
qw 9
qz 72,100,112
v_mul_f32 v16,v16,v54
v_lshlrev_b32 v36,3,v0
v_add_u32 v36,s13,v36
v_mov_b32 v38,s16
qt 98,2,57856
qz 76,100,184
v_exp_f32 v16,v16
v_add_u32 v36,131072,v36
v_and_b32 v37,48,v0
qt 100,3,57344
qw 9
qm 64,104,116
v_fma_f32 v16,v16,v15,v15
qt 28,36,24576
qt 102,3,57856
qm 68,104,188
v_rcp_f32 v16,v16
qt 30,36,25088
v_lshlrev_b32 v37,24,v37
s_and_b32 s33,s8,3
s_lshl_b32 s33,s33,2
s_add_u32 s33,s33,s7
s_lshl_b32 s33,s33,3
s_lshl_b32 s33,s33,2
s_add_u32 s33,s33,122896
v_lshl_add_u32 v45,v44,2,s33
qt 104,2,61440
qw 7
qm 72,108,116
v_mul_f32 v54,v54,v16
qt 32,36,25600
qt 106,2,61952
qm 76,108,188
v_mul_f32 v16,v55,v55
qt 34,36,26112
v_lshl_or_b32 v37,v0,7,v37
qt 108,3,61440
qw 9
qm 64,96,120
v_fma_f32 v16,v16,v13,v11
s_mul_i32 s72,s10,2048
qt 110,3,61952
qm 68,96,192
v_mul_f32 v16,v16,v55
qt 96,4,0
qw 8
qm 72,100,120
v_exp_f32 v16,v16
s_add_u32 s72,s72,8192
ds_read_b32 v40,v45 offset:0
qt 98,4,512
qm 76,100,192
v_fma_f32 v16,v16,v15,v15
qt 100,5,0
qw 7
qm 64,104,124
v_rcp_f32 v16,v16
s_and_b32 s30,s8,3
qt 102,5,512
qm 68,104,196
v_mul_f32 v55,v55,v16
qt 104,4,4096
qw 6
qm 72,108,124
v_mul_f32 v16,v60,v60
s_lshl_b32 s30,s30,4
ds_read_b32 v41,v45 offset:512
qt 106,4,4608
qm 76,108,196
v_fma_f32 v16,v16,v13,v11
s_add_u32 s15,s76,90112
buffer_store_dwordx4 v[28:31],v18,s[88:91],s15 offen offset:0 sc1
s_and_b32 s31,s8,4
qt 108,5,4096
qw 6
qm 64,96,128
v_mul_f32 v16,v16,v60
s_add_u32 s15,s76,90112
buffer_store_dwordx4 v[32:35],v18,s[88:91],s15 offen offset:1024 sc1
qt 110,5,4608
qm 68,96,200
v_exp_f32 v16,v16
s_add_u32 s30,s30,s31
qt 96,4,8192
qw 6
qm 72,100,128
v_fma_f32 v16,v16,v15,v15
qt 98,4,8704
qm 76,100,200
v_rcp_f32 v16,v16
s_add_u32 s68,s30,s72
qt 100,5,8192
qw 5
qm 64,104,132
v_mul_f32 v60,v60,v16
qt 102,5,8704
qm 68,104,204
v_mul_f32 v16,v61,v61
s_lshl_b32 s31,s8,8
qt 104,4,12288
qw 5
qm 72,108,132
s_barrier
v_fma_f32 v16,v16,v13,v11
s_add_u32 s78,s68,s31
qt 106,4,12800
qm 76,108,204
v_mul_f32 v16,v16,v61
qt 108,5,12288
qw 5
qm 64,96,136
v_exp_f32 v16,v16
s_add_u32 s78,s78,128
qt 110,5,12800
qm 68,96,208
v_fma_f32 v16,v16,v15,v15
qt 96,2,0
qw 5
qm 72,100,136
v_rcp_f32 v16,v16
s_add_u32 s68,s68,8
qt 98,2,512
qm 76,100,208
v_mul_f32 v61,v61,v16
qt 100,3,0
qw 5
qm 64,104,140
v_mul_f32 v16,v62,v62
s_cmp_eq_u32 s6,4
s_cselect_b32 s68,s68,0x7ff00000
qt 102,3,512
qm 68,104,212
v_fma_f32 v16,v16,v13,v11
s_add_u32 s72,s72,s31
qt 104,2,4096
qw 5
qm 72,108,140
v_mul_f32 v16,v16,v62
qt 106,2,4608
qm 76,108,212
v_exp_f32 v16,v16
s_add_u32 s72,s72,128
qt 108,3,4096
qw 5
qm 64,96,144
v_fma_f32 v16,v16,v15,v15
qt 110,3,4608
qm 68,96,216
v_rcp_f32 v16,v16
s_add_u32 s74,s16,8
qt 96,2,8192
qw 5
qm 72,100,144
v_mul_f32 v62,v62,v16
qt 98,2,8704
qm 76,100,216
v_mul_f32 v16,v63,v63
v_and_b32 v42,15,v0
qt 100,3,8192
qw 5
qm 64,104,148
v_fma_f32 v16,v16,v13,v11
v_lshrrev_b32 v43,5,v0
qt 102,3,8704
qm 68,104,220
v_mul_f32 v16,v16,v63
qt 104,2,12288
qw 5
qm 72,108,148
v_exp_f32 v16,v16
v_bfe_u32 v25,v0,4,1
qt 106,2,12800
qm 76,108,220
v_fma_f32 v16,v16,v15,v15
qt 108,3,12288
qw 5
qm 64,96,152
v_rcp_f32 v16,v16
v_lshlrev_b32 v25,3,v25
qt 110,3,12800
qm 68,96,224
v_mul_f32 v63,v63,v16
qt 96,2,16384
qw 5
qm 72,100,152
v_mul_f32 v48,v48,v80
v_add_u32 v25,2048,v25
qt 98,2,16896
qm 76,100,224
v_mul_f32 v49,v49,v81
v_lshl_add_u32 v24,v42,7,v25
qt 100,3,16384
qw 5
qm 64,104,156
v_cvt_pk_bf16_f32 v48,v48,v49
qt 102,3,16896
qm 68,104,228
v_mul_f32 v50,v50,v82
v_lshl_add_u32 v22,v42,5,v25
qt 104,2,20480
qw 5
qm 72,108,156
s_waitcnt vmcnt(0)
s_barrier
v_mul_f32 v51,v51,v83
v_add_u32 v38,1,v38
buffer_store_dword v38,v37,s[96:99],s68 offen sc1
qt 106,2,20992
qm 76,108,228
v_cvt_pk_bf16_f32 v49,v50,v51
v_lshrrev_b32 v42,1,v42
qt 108,3,20480
qw 5
qm 64,96,160
v_mul_f32 v56,v56,v88
qt 110,3,20992
qm 68,96,232
v_mul_f32 v57,v57,v89
v_xor_b32 v25,v42,v43
qt 96,2,24576
qw 5
qm 72,100,160
v_cvt_pk_bf16_f32 v50,v56,v57
v_lshl_add_u32 v24,v25,4,v24
qt 98,2,25088
qm 76,100,232
v_mul_f32 v58,v58,v90
qt 100,3,24576
qw 5
qm 64,104,164
v_mul_f32 v59,v59,v91
v_lshrrev_b32 v42,2,v42
qt 102,3,25088
qm 68,104,236
v_cvt_pk_bf16_f32 v51,v58,v59
qt 104,2,28672
qw 5
qm 72,108,164
ds_write_b128 v18,v[48:51] offset:40960
v_mul_f32 v52,v52,v84
v_xor_b32 v42,v42,v43
qt 106,2,29184
qm 76,108,236
s_add_u32 s15,s76,40960
buffer_store_dwordx4 v[48:51],v18,s[88:91],s15 offen offset:0 sc1
v_mul_f32 v53,v53,v85
qt 108,3,28672
qw 6
qm 64,96,168
v_cvt_pk_bf16_f32 v52,v52,v53
v_lshl_add_u32 v22,v42,4,v22
qt 110,3,29184
qm 68,96,240
v_mul_f32 v54,v54,v86
v_add_u32 v23,0x10000,v22
qt 96,2,32768
qw 6
qm 72,100,168
v_mul_f32 v55,v55,v87
qt 98,2,33280
qm 76,100,240
v_cvt_pk_bf16_f32 v53,v54,v55
v_xor_b32 v25,32,v24
qt 100,3,32768
qw 5
qm 64,104,172
v_mul_f32 v60,v60,v92
qt 102,3,33280
qm 68,104,244
v_mul_f32 v61,v61,v93
v_xor_b32 v26,64,v24
qt 104,2,36864
qw 5
qm 72,108,172
v_cvt_pk_bf16_f32 v54,v60,v61
qt 106,2,37376
qm 76,108,244
v_mul_f32 v62,v62,v94
v_xor_b32 v27,0x60,v24
qt 108,3,36864
qw 5
qm 64,96,176
v_mul_f32 v63,v63,v95
v_lshlrev_b32 v36,3,v0
qt 110,3,37376
qm 68,96,248
v_cvt_pk_bf16_f32 v55,v62,v63
qt 96,2,40960
qw 5
qm 72,100,176
ds_write_b128 v18,v[52:55] offset:41984
v_add_u32 v36,s13,v36
qt 98,2,41472
qm 76,100,248
s_add_u32 s15,s76,40960
buffer_store_dwordx4 v[52:55],v18,s[88:91],s15 offen offset:1024 sc1
qt 100,3,40960
qw 6
qm 64,104,180
buffer_load_dword v39,off,s[96:99],s78 sc1
v_add_u32 v36,131072,v36
qt 102,3,41472
qm 68,104,252
qt 104,2,45056
qw 6
qm 72,108,180
qw 4
s_barrier
qt 106,2,45568
qm 76,108,252
qt 108,3,45056
qw 5
qz 80,96,112
qt 110,3,45568
qz 84,96,184
v_mul_f32 v16,v64,v64
qt 96,2,49152
qw 5
qz 88,100,112
v_fma_f32 v16,v16,v12,v10
qt 98,2,49664
qz 92,100,184
v_mul_f32 v16,v16,v64
qt 100,3,49152
qw 5
qm 80,104,116
v_exp_f32 v16,v16
qt 102,3,49664
qm 84,104,188
v_fma_f32 v16,v16,v14,v14
qt 104,2,53248
qw 5
qm 88,108,116
v_rcp_f32 v16,v16
qt 106,2,53760
qm 92,108,188
v_mul_f32 v64,v64,v16
qt 108,3,53248
qw 5
qm 80,96,120
v_mul_f32 v16,v65,v65
qt 110,3,53760
qm 84,96,192
v_fma_f32 v16,v16,v12,v10
qr 112,18,0
qt 96,2,57344
qw 6
qm 88,100,120
v_mul_f32 v16,v16,v65
qt 98,2,57856
qm 92,100,192
v_exp_f32 v16,v16
qr 184,18,1024
qt 100,3,57344
qw 7
qm 80,104,124
v_fma_f32 v16,v16,v14,v14
qt 102,3,57856
qm 84,104,196
v_rcp_f32 v16,v16
qr 116,18,8192
qt 104,2,61440
qw 8
qm 88,108,124
v_mul_f32 v65,v65,v16
qt 106,2,61952
qm 92,108,196
v_mul_f32 v16,v66,v66
qr 188,18,9216
qt 108,3,61440
qw 8
qm 80,96,128
v_fma_f32 v16,v16,v12,v10
qt 110,3,61952
qm 84,96,200
v_mul_f32 v16,v16,v66
qt 96,4,0
qw 7
qm 88,100,128
v_exp_f32 v16,v16
qt 98,4,512
qm 92,100,200
v_fma_f32 v16,v16,v14,v14
qt 100,5,0
qw 6
qm 80,104,132
v_rcp_f32 v16,v16
qt 102,5,512
qm 84,104,204
v_mul_f32 v66,v66,v16
qt 104,4,4096
qw 5
qm 88,108,132
s_waitcnt vmcnt(0)
s_mov_b32 s35,0
.L_wY1:
v_readfirstlane_b32 s34,v39
s_sub_u32 s34,s74,s34
s_cmp_le_u32 s34,7
s_cbranch_scc1 .L_wY1d
s_add_u32 s35,s35,1
s_cmp_gt_u32 s35,1000000
s_cbranch_scc1 .L_wY1d
s_sleep 2
buffer_load_dword v39,off,s[96:99],s78 sc1
s_waitcnt vmcnt(0)
s_branch .L_wY1
.L_wY1d:
s_barrier
v_mul_f32 v16,v67,v67
qt 106,4,4608
qm 92,108,204
v_fma_f32 v16,v16,v12,v10
qt 108,5,4096
qw 5
qm 80,96,136
v_mul_f32 v16,v16,v67
v_add_u32 v28,0x10000,v24
qt 110,5,4608
qm 84,96,208
v_exp_f32 v16,v16
v_add_u32 v29,0x10000,v25
qt 128,36,0
qt 96,4,8192
qw 6
qm 88,100,136
v_fma_f32 v16,v16,v14,v14
qt 130,36,512
qt 98,4,8704
qm 92,100,208
v_rcp_f32 v16,v16
v_add_u32 v30,0x10000,v26
qt 200,36,1024
s_add_u32 s15,s77,81920
buffer_load_dwordx4 v[32:35],v18,s[88:91],s15 offen offset:0 sc1
qt 100,5,8192
qw 8
qm 80,104,140
v_mul_f32 v67,v67,v16
v_add_u32 v31,0x10000,v27
qt 202,36,1536
qt 102,5,8704
qm 84,104,212
v_mul_f32 v16,v72,v72
qt 132,36,8192
qt 104,4,12288
qw 10
qm 88,108,140
v_fma_f32 v16,v16,v12,v10
qt 134,36,8704
qt 106,4,12800
qm 92,108,212
v_mul_f32 v16,v16,v72
qt 204,36,9216
qt 108,5,12288
qw 10
qm 80,96,144
v_exp_f32 v16,v16
qt 206,36,9728
qt 110,5,12800
qm 84,96,216
v_fma_f32 v16,v16,v14,v14
qt 136,36,16384
qt 96,2,0
qw 10
qm 88,100,144
v_rcp_f32 v16,v16
qt 138,36,16896
qt 98,2,512
qm 92,100,216
v_mul_f32 v72,v72,v16
qt 208,36,17408
qt 100,3,0
qw 10
qm 80,104,148
v_mul_f32 v16,v73,v73
qt 210,36,17920
qt 102,3,512
qm 84,104,220
v_fma_f32 v16,v16,v12,v10
qt 140,36,24576
s_add_u32 s15,s77,81920
buffer_load_dwordx4 v[42:45],v18,s[88:91],s15 offen offset:1024 sc1
qt 104,2,4096
qw 10
qm 88,108,148
v_mul_f32 v16,v16,v73
qt 142,36,25088
qt 106,2,4608
qm 92,108,220
v_exp_f32 v16,v16
qt 212,36,25600
qt 108,3,4096
qw 10
qm 80,96,152
v_fma_f32 v16,v16,v14,v14
qt 214,36,26112
qt 110,3,4608
qm 84,96,224
v_rcp_f32 v16,v16
qt 96,2,8192
qw 9
qm 88,100,152
v_mul_f32 v73,v73,v16
s_mul_i32 s30,s10,1310720
qt 98,2,8704
qm 92,100,224
v_mul_f32 v16,v74,v74
s_lshl_b32 s31,s8,14
qt 100,3,8192
qw 7
qm 80,104,156
v_fma_f32 v16,v16,v12,v10
qt 102,3,8704
qm 84,104,228
v_mul_f32 v16,v16,v74
s_add_u32 s73,s30,s31
qt 104,2,12288
qw 5
qm 88,108,156
s_barrier
v_exp_f32 v16,v16
qt 106,2,12800
qm 92,108,228
v_fma_f32 v16,v16,v14,v14
s_add_u32 s73,s73,57344
s_add_u32 s15,s77,0
buffer_load_dwordx4 v[48:51],v18,s[88:91],s15 offen offset:0 sc1
qt 108,3,12288
qw 5
qm 80,96,160
v_rcp_f32 v16,v16
qt 110,3,12800
qm 84,96,232
v_mul_f32 v74,v74,v16
s_lshl_b32 s31,s8,11
qt 96,2,16384
qw 5
qm 88,100,160
v_mul_f32 v16,v75,v75
s_add_u32 s69,s30,s31
qt 98,2,16896
qm 92,100,232
v_fma_f32 v16,v16,v12,v10
qt 100,3,16384
qw 5
qm 80,104,164
v_mul_f32 v16,v16,v75
s_lshl_b32 s31,s7,8
qt 102,3,16896
qm 84,104,236
v_exp_f32 v16,v16
qt 104,2,20480
qw 5
qm 88,108,164
v_fma_f32 v16,v16,v14,v14
s_add_u32 s69,s69,s31
qt 106,2,20992
qm 92,108,236
v_rcp_f32 v16,v16
qt 108,3,20480
qw 5
qm 80,96,168
v_mul_f32 v75,v75,v16
s_add_u32 s69,s69,132096
qt 110,3,20992
qm 84,96,240
v_mul_f32 v16,v68,v68
s_and_b32 s75,s8,4
s_add_u32 s15,s77,0
buffer_load_dwordx4 v[56:59],v18,s[88:91],s15 offen offset:1024 sc1
qt 96,2,24576
qw 5
qm 88,100,168
v_fma_f32 v16,v16,v13,v11
qt 98,2,25088
qm 92,100,240
v_mul_f32 v16,v16,v68
s_lshl_b32 s75,s75,4
qt 100,3,24576
qw 5
qm 80,104,172
v_exp_f32 v16,v16
qt 102,3,25088
qm 84,104,244
v_fma_f32 v16,v16,v15,v15
s_and_b32 s33,s8,3
s_lshl_b32 s33,s33,2
s_add_u32 s33,s33,s7
s_lshl_b32 s33,s33,3
s_lshl_b32 s30,s10,7
s_add_u32 s30,s30,s33
qt 104,2,28672
qw 5
qm 88,108,172
v_rcp_f32 v16,v16
qt 106,2,29184
qm 92,108,244
v_mul_f32 v68,v68,v16
s_add_u32 s30,s30,4
qt 108,3,28672
qw 5
qm 80,96,176
v_mul_f32 v16,v69,v69
s_mul_i32 s30,s30,1152
qt 110,3,29184
qm 84,96,248
v_fma_f32 v16,v16,v13,v11
qd 96,24,32768
qw 5
qm 88,100,176
v_mul_f32 v16,v16,v69
s_add_u32 s70,s30,s75
qd 98,24,36864
qm 92,100,248
v_exp_f32 v16,v16
s_add_u32 s15,s77,8192
buffer_load_dwordx4 v[52:55],v18,s[88:91],s15 offen offset:0 sc1
)ASM"
R"ASM(qd 100,25,32768
qw 5
qm 80,104,180
v_fma_f32 v16,v16,v15,v15
s_lshl_b32 s71,s70,1
qd 102,25,36864
qm 84,104,252
v_rcp_f32 v16,v16
qd 104,26,32768
qw 5
qm 88,108,180
s_barrier
v_mul_f32 v69,v69,v16
qd 106,26,36864
qm 92,108,252
qd 108,27,32768
qw 5
qz 152,96,128
v_mul_f32 v16,v70,v70
v_mul_f32 v17,v71,v71
v_fma_f32 v16,v16,v13,v11
qd 110,27,36864
qz 224,96,200
v_fma_f32 v17,v17,v13,v11
v_mul_f32 v16,v16,v70
qd 96,24,40960
qw 5
qm 152,100,132
v_mul_f32 v17,v17,v71
v_exp_f32 v16,v16
qd 98,24,45056
qm 224,100,204
v_exp_f32 v17,v17
v_fma_f32 v16,v16,v15,v15
v_fma_f32 v17,v17,v15,v15
qd 100,25,40960
qw 5
qm 152,104,136
v_rcp_f32 v16,v16
v_rcp_f32 v17,v17
qd 102,25,45056
qm 224,104,208
v_mul_f32 v70,v70,v16
v_mul_f32 v71,v71,v17
qd 104,26,40960
qw 5
qm 152,108,140
v_mul_f32 v16,v76,v76
v_mul_f32 v17,v77,v77
v_fma_f32 v16,v16,v13,v11
s_add_u32 s15,s77,8192
buffer_load_dwordx4 v[60:63],v18,s[88:91],s15 offen offset:1024 sc1
qd 106,26,45056
qm 224,108,212
v_fma_f32 v17,v17,v13,v11
v_mul_f32 v16,v16,v76
qd 108,27,40960
qw 5
qz 156,96,128
v_mul_f32 v17,v17,v77
v_exp_f32 v16,v16
qd 110,27,45056
qz 228,96,200
v_exp_f32 v17,v17
v_fma_f32 v16,v16,v15,v15
v_fma_f32 v17,v17,v15,v15
qd 96,24,49152
qw 5
qm 156,100,132
v_rcp_f32 v16,v16
v_rcp_f32 v17,v17
qd 98,24,53248
qm 228,100,204
v_mul_f32 v76,v76,v16
v_mul_f32 v77,v77,v17
qd 100,25,49152
qw 5
qm 156,104,136
v_mul_f32 v16,v78,v78
v_mul_f32 v17,v79,v79
v_fma_f32 v16,v16,v13,v11
qd 102,25,53248
qm 228,104,208
v_fma_f32 v17,v17,v13,v11
v_mul_f32 v16,v16,v78
qd 104,26,49152
qw 5
qm 156,108,140
v_mul_f32 v17,v17,v79
v_exp_f32 v16,v16
qd 106,26,53248
qm 228,108,212
v_exp_f32 v17,v17
v_fma_f32 v16,v16,v15,v15
v_fma_f32 v17,v17,v15,v15
qd 108,27,49152
qw 5
qz 160,96,128
v_rcp_f32 v16,v16
v_rcp_f32 v17,v17
s_add_u32 s15,s77,16384
buffer_load_dwordx4 v[120:123],v18,s[88:91],s15 offen offset:0 sc1
qd 110,27,53248
qz 232,96,200
v_mul_f32 v78,v78,v16
v_mul_f32 v79,v79,v17
qd 96,24,57344
qw 5
qm 160,100,132
v_mul_f32 v64,v64,v80
v_mul_f32 v65,v65,v81
v_cvt_pk_bf16_f32 v64,v64,v65
qd 98,24,61440
qm 232,100,204
v_mul_f32 v66,v66,v82
v_mul_f32 v67,v67,v83
qd 100,25,57344
qw 5
qm 160,104,136
v_cvt_pk_bf16_f32 v65,v66,v67
v_mul_f32 v72,v72,v88
qd 102,25,61440
qm 232,104,208
v_mul_f32 v73,v73,v89
v_cvt_pk_bf16_f32 v66,v72,v73
v_mul_f32 v74,v74,v90
qd 104,26,57344
qw 5
qm 160,108,140
s_barrier
v_mul_f32 v75,v75,v91
v_cvt_pk_bf16_f32 v67,v74,v75
qd 106,26,61440
qm 232,108,212
s_add_u32 s15,s76,57344
buffer_store_dwordx4 v[64:67],v18,s[88:91],s15 offen offset:0 sc1
ds_write_b128 v18,v[64:67] offset:8192
qd 108,27,57344
qw 6
qz 164,96,128
v_mul_f32 v68,v68,v84
v_mul_f32 v69,v69,v85
v_cvt_pk_bf16_f32 v68,v68,v69
qd 110,27,61440
qz 236,96,200
v_mul_f32 v70,v70,v86
v_mul_f32 v71,v71,v87
qd 96,28,0
qw 6
qm 164,100,132
v_cvt_pk_bf16_f32 v69,v70,v71
v_mul_f32 v76,v76,v92
s_add_u32 s15,s77,16384
buffer_load_dwordx4 v[192:195],v18,s[88:91],s15 offen offset:1024 sc1
qd 98,28,4096
qm 236,100,204
v_mul_f32 v77,v77,v93
v_cvt_pk_bf16_f32 v70,v76,v77
v_mul_f32 v78,v78,v94
qd 100,29,0
qw 6
qm 164,104,136
v_mul_f32 v79,v79,v95
v_cvt_pk_bf16_f32 v71,v78,v79
qd 102,29,4096
qm 236,104,208
s_add_u32 s15,s76,57344
buffer_store_dwordx4 v[68:71],v18,s[88:91],s15 offen offset:1024 sc1
ds_write_b128 v18,v[68:71] offset:9216
qd 104,30,0
qw 6
qm 164,108,140
qd 106,30,4096
qm 236,108,212
qd 108,31,0
qw 6
qz 144,96,128
qd 110,31,4096
qz 216,96,200
qr 176,18,16384
qd 96,28,8192
qw 7
qm 144,100,132
qd 98,28,12288
qm 216,100,204
qr 248,18,17408
qd 100,29,8192
qw 7
qm 144,104,136
s_add_u32 s15,s77,24576
buffer_load_dwordx4 v[124:127],v18,s[88:91],s15 offen offset:0 sc1
qd 102,29,12288
qm 216,104,208
qr 180,18,24576
qd 104,30,8192
qw 8
qm 144,108,140
qd 106,30,12288
qm 216,108,212
qr 252,18,25600
qd 108,31,8192
qw 8
qz 148,96,128
qd 110,31,12288
qz 220,96,200
qt 96,2,0
qw 7
qm 148,100,132
qt 98,2,512
qm 220,100,204
qt 100,3,0
qw 6
qm 148,104,136
qt 102,3,512
qm 220,104,208
qt 104,2,4096
qw 5
qm 148,108,140
s_barrier
s_add_u32 s15,s77,24576
buffer_load_dwordx4 v[196:199],v18,s[88:91],s15 offen offset:1024 sc1
qt 106,2,4608
qm 220,108,212
qt 108,3,4096
qw 5
qm 152,96,112
qt 110,3,4608
qm 224,96,184
qr 168,18,32768
qt 96,2,8192
qw 6
qm 156,100,112
qt 98,2,8704
qm 228,100,184
qr 240,18,33792
qt 100,3,8192
qw 7
qm 152,104,116
qt 102,3,8704
qm 224,104,188
qr 172,18,40960
qt 104,2,12288
qw 8
qm 156,108,116
qt 106,2,12800
qm 228,108,188
qr 244,18,41984
qt 108,3,12288
qw 8
qm 160,96,112
qt 110,3,12800
qm 232,96,184
qt 96,2,16384
qw 7
qm 164,100,112
qt 98,2,16896
qm 236,100,184
qt 100,3,16384
qw 6
qm 160,104,116
qt 102,3,16896
qm 232,104,188
qt 104,2,20480
qw 5
qm 164,108,116
qt 106,2,20992
qm 236,108,188
qt 108,3,20480
qw 5
qm 144,96,112
qt 110,3,20992
qm 216,96,184
qt 96,2,24576
qw 5
qm 148,100,112
qt 98,2,25088
qm 220,100,184
qt 100,3,24576
qw 5
qm 144,104,116
qt 102,3,25088
qm 216,104,188
qt 104,2,28672
qw 5
qm 148,108,116
s_waitcnt vmcnt(0)
s_barrier
v_add_u32 v38,1,v38
buffer_store_dword v38,v37,s[96:99],s68 offen sc1
qt 106,2,29184
qm 220,108,188
qt 108,3,28672
qw 5
qm 152,96,176
v_and_b32 v1,0xfffffc3f,v18
qt 110,3,29184
qm 224,96,248
qr 64,18,0
qt 96,2,32768
qw 6
qm 156,100,176
qt 98,2,33280
qm 228,100,248
qr 72,18,1024
qt 100,3,32768
qw 7
qm 152,104,180
v_lshrrev_b32 v16,4,v0
qt 102,3,33280
qm 224,104,252
qr 68,18,8192
qt 104,2,36864
qw 8
qm 156,108,180
qt 106,2,37376
qm 228,108,252
qr 76,18,9216
s_add_u32 s15,s77,32768
buffer_load_dwordx4 v[80:83],v18,s[88:91],s15 offen offset:0 sc1
qt 108,3,36864
qw 8
qm 160,96,176
qt 110,3,37376
qm 232,96,248
v_lshl_add_u32 v1,v16,6,v1
qt 96,2,40960
qw 7
qm 164,100,176
qt 98,2,41472
qm 236,100,248
qt 100,3,40960
qw 6
qm 160,104,180
qt 102,3,41472
qm 232,104,252
qt 104,2,45056
qw 5
qm 164,108,180
v_and_b32 v16,12,v0
qt 106,2,45568
qm 236,108,252
qt 108,3,45056
qw 5
qm 144,96,176
qt 110,3,45568
qm 216,96,248
s_add_u32 s15,s77,32768
buffer_load_dwordx4 v[88:91],v18,s[88:91],s15 offen offset:1024 sc1
qt 96,2,49152
qw 5
qm 148,100,176
qt 98,2,49664
qm 220,100,248
v_lshl_add_u32 v1,v16,6,v1
qt 100,3,49152
qw 5
qm 144,104,180
buffer_load_dword v39,off,s[96:99],s78 sc1
qt 102,3,49664
qm 216,104,252
qt 104,2,53248
qw 5
qm 148,108,180
s_barrier
qt 106,2,53760
qm 220,108,252
qt 108,3,53248
qw 5
qm 152,96,168
qt 110,3,53760
qm 224,96,240
qt 96,2,57344
qw 5
qm 156,100,168
qt 98,2,57856
qm 228,100,240
s_add_u32 s15,s77,40960
buffer_load_dwordx4 v[84:87],v18,s[88:91],s15 offen offset:0 sc1
qt 100,3,57344
qw 5
qm 152,104,172
qt 102,3,57856
qm 224,104,244
qt 104,2,61440
qw 5
qm 156,108,172
qt 106,2,61952
qm 228,108,244
qt 108,3,61440
qw 5
qm 160,96,168
qt 110,3,61952
qm 232,96,240
qt 96,4,0
qw 5
qm 164,100,168
qt 98,4,512
qm 236,100,240
qt 100,5,0
qw 5
qm 160,104,172
qt 102,5,512
qm 232,104,244
s_add_u32 s15,s77,40960
buffer_load_dwordx4 v[92:95],v18,s[88:91],s15 offen offset:1024 sc1
qt 104,4,4096
qw 5
qm 164,108,172
qt 106,4,4608
qm 236,108,244
qt 108,5,4096
qw 5
qm 144,96,168
qt 110,5,4608
qm 216,96,240
qt 96,4,8192
qw 5
qm 148,100,168
qt 98,4,8704
qm 220,100,240
qt 100,5,8192
qw 5
qm 144,104,172
qt 102,5,8704
qm 216,104,244
qt 104,4,12288
qw 5
qm 148,108,172
s_waitcnt vmcnt(2)
s_mov_b32 s35,0
.L_wY2:
v_readfirstlane_b32 s34,v39
s_sub_u32 s34,s74,s34
s_cmp_le_u32 s34,6
s_cbranch_scc1 .L_wY2d
s_add_u32 s35,s35,1
s_cmp_gt_u32 s35,1000000
s_cbranch_scc1 .L_wY2d
s_sleep 2
buffer_load_dword v39,off,s[96:99],s78 sc1
s_waitcnt vmcnt(0)
s_branch .L_wY2
.L_wY2d:
s_barrier
qt 106,4,12800
qm 220,108,244
qt 108,5,12288
qw 5
qm 144,96,64
s_add_u32 s15,s77,49152
buffer_load_dwordx4 v[128:131],v18,s[88:91],s15 offen offset:0 sc1
qt 110,5,12800
qm 216,96,72
qt 96,2,0
qw 5
qm 148,100,64
qt 98,2,512
qm 220,100,72
qt 100,3,0
qw 5
qm 144,104,68
qt 102,3,512
qm 216,104,76
qt 104,2,4096
qw 5
qm 148,108,68
qt 106,2,4608
qm 220,108,76
qt 108,3,4096
qw 5
qm 152,96,64
s_add_u32 s15,s77,49152
buffer_load_dwordx4 v[200:203],v18,s[88:91],s15 offen offset:1024 sc1
qt 110,3,4608
qm 224,96,72
qt 96,2,8192
qw 5
qm 156,100,64
qt 98,2,8704
qm 228,100,72
v_cvt_pk_bf16_f32 v144,v144,v145
qt 100,3,8192
qw 5
qm 152,104,68
v_cvt_pk_bf16_f32 v145,v146,v147
qt 102,3,8704
qm 224,104,76
v_cvt_pk_bf16_f32 v146,v148,v149
qt 104,2,12288
qw 5
qm 156,108,68
v_cvt_pk_bf16_f32 v147,v150,v151
qt 106,2,12800
qm 228,108,76
qt 108,3,12288
qw 5
qm 160,96,64
s_add_u32 s15,s77,57344
buffer_load_dwordx4 v[132:135],v18,s[88:91],s15 offen offset:0 sc1
v_cvt_pk_bf16_f32 v216,v216,v217
qt 110,3,12800
qm 232,96,72
v_cvt_pk_bf16_f32 v217,v218,v219
qd 96,22,16384
qw 5
qm 164,100,64
v_cvt_pk_bf16_f32 v218,v220,v221
qd 98,22,20480
qm 236,100,72
v_cvt_pk_bf16_f32 v219,v222,v223
qd 100,22,16896
qw 5
qm 160,104,68
qd 102,22,20992
qm 232,104,76
qd 104,22,17408
qw 5
qm 164,108,68
s_barrier
qd 106,22,21504
qm 236,108,76
qd 108,22,17920
qw 5
qm 152,96,32
s_add_u32 s15,s77,57344
buffer_load_dwordx4 v[204:207],v18,s[88:91],s15 offen offset:1024 sc1
v_and_b32 v15,48,v0
qd 110,22,22016
qm 224,96,42
qt 96,2,24576
qw 5
qm 156,100,32
v_lshlrev_b32 v10,10,v15
qt 98,2,25088
qm 228,100,42
qt 100,3,24576
qw 5
qm 160,104,32
qt 102,3,25088
qm 232,104,42
v_and_b32 v16,16,v0
qt 104,2,28672
qw 5
qm 164,108,32
qt 106,2,29184
qm 236,108,42
qt 108,3,28672
qw 5
qm 152,96,48
s_add_u32 s15,s77,65536
buffer_load_dwordx4 v[136:139],v18,s[88:91],s15 offen offset:0 sc1
v_lshrrev_b32 v17,5,v0
qt 110,3,29184
qm 224,96,56
qt 96,2,32768
qw 5
qm 156,100,48
v_lshl_add_u32 v12,v17,6,v16
qt 98,2,33280
qm 228,100,56
qt 100,3,32768
qw 5
qm 152,104,52
qt 102,3,33280
qm 224,104,60
v_and_b32 v16,3,v0
qt 104,2,36864
qw 5
qm 156,108,52
qt 106,2,37376
qm 228,108,60
qt 108,3,36864
qw 5
qm 160,96,48
s_add_u32 s15,s77,65536
buffer_load_dwordx4 v[208:211],v18,s[88:91],s15 offen offset:1024 sc1
v_lshl_add_u32 v12,v16,2,v12
qt 110,3,37376
qm 232,96,56
qt 96,2,40960
qw 5
qm 164,100,48
v_lshl_add_u32 v10,v16,6,v10
qt 98,2,41472
qm 236,100,56
qt 100,3,40960
qw 5
qm 160,104,52
qt 102,3,41472
qm 232,104,60
v_bfe_u32 v11,v0,2,2
qt 104,2,45056
qw 5
qm 164,108,52
s_barrier
qt 106,2,45568
qm 236,108,60
qt 108,3,45056
qw 5
qm 152,96,120
s_add_u32 s15,s77,73728
buffer_load_dwordx4 v[140:143],v18,s[88:91],s15 offen offset:0 sc1
v_lshl_add_u32 v10,v11,4,v10
qt 110,3,45568
qm 224,96,192
qt 96,2,49152
qw 5
qm 156,100,120
v_lshlrev_b32 v12,1,v12
qt 98,2,49664
qm 228,100,192
qt 100,3,49152
qw 5
qm 152,104,124
qt 102,3,49664
qm 224,104,196
v_mul_u32_u24 v11,1152,v11
qt 104,2,53248
qw 5
qm 156,108,124
qt 106,2,53760
qm 228,108,196
qt 108,3,53248
qw 5
qm 160,96,120
s_add_u32 s15,s77,73728
buffer_load_dwordx4 v[212:215],v18,s[88:91],s15 offen offset:1024 sc1
v_add_u32 v11,v11,v12
qt 110,3,53760
qm 232,96,192
qt 96,2,57344
qw 5
qm 164,100,120
v_lshlrev_b32 v13,1,v11
qt 98,2,57856
qm 236,100,192
qt 100,3,57344
qw 5
qm 160,104,124
qt 102,3,57856
qm 232,104,196
v_lshl_or_b32 v14,v17,30,v13
qt 104,2,61440
qw 5
qm 164,108,124
qt 106,2,61952
qm 236,108,196
qt 108,3,61440
qw 5
qm 152,96,80
s_and_b32 s30,s8,3
s_lshl_b32 s30,s30,4
s_mov_b64 s[32:33],0xffff
s_lshl_b64 s[32:33],s[32:33],s30
s_bitcmp1_b32 s8,2
s_cselect_b64 s[40:41],0,s[32:33]
s_cselect_b64 s[42:43],s[32:33],0
qt 110,3,61952
qm 224,96,88
s_add_u32 s15,s73,1179648
buffer_store_dwordx4 v[144:147],v1,s[88:91],s15 offen offset:0 sc1
qt 96,4,0
qw 5
qm 156,100,80
s_mov_b64 s[52:53],0
qt 98,4,512
qm 228,100,88
qt 100,5,0
s_waitcnt vmcnt(10) lgkmcnt(5)
qm 152,104,84
qt 102,5,512
s_waitcnt vmcnt(9)
qm 224,104,92
s_mov_b64 s[54:55],0
qt 104,4,4096
qw 5
qm 156,108,84
s_barrier
qt 106,4,4608
qm 228,108,92
qt 108,5,4096
qw 5
qm 160,96,80
s_mov_b64 s[56:57],0
qt 110,5,4608
qm 232,96,88
s_add_u32 s15,s73,1179648
buffer_store_dwordx4 v[216:219],v1,s[88:91],s15 offen offset:1024 sc1
qt 96,4,8192
qw 5
qm 164,100,80
s_mov_b64 s[58:59],0
qt 98,4,8704
qm 236,100,88
qt 100,5,8192
qw 5
qm 160,104,84
qt 102,5,8704
qm 232,104,92
s_mov_b32 s76,0
qt 104,4,12288
qw 5
qm 164,108,84
qt 106,4,12800
qm 236,108,92
qt 108,5,12288
s_waitcnt vmcnt(9) lgkmcnt(5)
qm 152,96,128
qt 110,5,12800
s_waitcnt vmcnt(8)
qm 224,96,200
qt 96,2,0
qw 5
qm 156,100,128
qt 98,2,512
qm 228,100,200
qt 100,3,0
s_waitcnt vmcnt(7) lgkmcnt(5)
qm 152,104,132
qt 102,3,512
s_waitcnt vmcnt(6)
qm 224,104,204
qt 104,2,4096
qw 5
qm 156,108,132
qt 106,2,4608
qm 228,108,204
qt 108,3,4096
s_waitcnt vmcnt(5) lgkmcnt(5)
qm 152,96,136
qt 110,3,4608
s_waitcnt vmcnt(4)
qm 224,96,208
qt 96,2,8192
qw 5
qm 156,100,136
qt 98,2,8704
qm 228,100,208
qt 100,3,8192
s_waitcnt vmcnt(3) lgkmcnt(5)
qm 152,104,140
qt 102,3,8704
s_waitcnt vmcnt(2)
qm 224,104,212
qt 104,2,12288
qw 5
qm 156,108,140
s_barrier
qt 106,2,12800
qm 228,108,212
qt 108,3,12288
qw 5
qm 160,96,128
qt 110,3,12800
qm 232,96,200
qt 96,2,16384
qw 5
qm 164,100,128
qt 98,2,16896
qm 236,100,200
v_cvt_pk_bf16_f32 v152,v152,v153
qt 100,3,16384
qw 5
qm 160,104,132
v_cvt_pk_bf16_f32 v153,v154,v155
qt 102,3,16896
qm 232,104,204
v_cvt_pk_bf16_f32 v154,v156,v157
qt 104,2,20480
qw 5
qm 164,108,132
v_cvt_pk_bf16_f32 v155,v158,v159
qt 106,2,20992
qm 236,108,204
qt 108,3,20480
qw 5
qm 160,96,136
v_cvt_pk_bf16_f32 v224,v224,v225
s_add_u32 s15,s73,655360
buffer_store_dwordx4 v[152:155],v1,s[88:91],s15 offen offset:0 sc1
qt 110,3,20992
qm 232,96,208
v_cvt_pk_bf16_f32 v225,v226,v227
qt 96,2,24576
qw 5
qm 164,100,136
v_cvt_pk_bf16_f32 v226,v228,v229
qt 98,2,25088
qm 236,100,208
v_cvt_pk_bf16_f32 v227,v230,v231
qt 100,3,24576
qw 5
qm 160,104,140
qt 102,3,25088
qm 232,104,212
qt 104,2,28672
qw 5
qm 164,108,140
qt 106,2,29184
qm 236,108,212
qt 108,3,28672
qw 5
qz 168,96,112
s_add_u32 s15,s73,655360
buffer_store_dwordx4 v[224:227],v1,s[88:91],s15 offen offset:1024 sc1
qt 110,3,29184
qz 240,96,184
qt 96,2,32768
qw 5
qz 172,100,112
qt 98,2,33280
qz 244,100,184
v_cvt_pk_bf16_f32 v160,v160,v161
qt 100,3,32768
qw 5
qm 168,104,116
v_cvt_pk_bf16_f32 v161,v162,v163
qt 102,3,33280
qm 240,104,188
v_cvt_pk_bf16_f32 v162,v164,v165
qt 104,2,36864
qw 5
qm 172,108,116
s_barrier
v_cvt_pk_bf16_f32 v163,v166,v167
qt 106,2,37376
qm 244,108,188
qt 108,3,36864
qw 5
qz 176,96,112
v_cvt_pk_bf16_f32 v232,v232,v233
s_add_u32 s15,s73,786432
buffer_store_dwordx4 v[160:163],v1,s[88:91],s15 offen offset:0 sc1
qt 110,3,37376
qz 248,96,184
v_cvt_pk_bf16_f32 v233,v234,v235
qd 96,22,40960
qw 5
qz 180,100,112
v_cvt_pk_bf16_f32 v234,v236,v237
qd 98,22,45056
qz 252,100,184
v_cvt_pk_bf16_f32 v235,v238,v239
qd 100,22,41472
qw 5
qm 176,104,116
qd 102,22,45568
qm 248,104,188
v_and_b32 v16,1,v0
qd 104,22,41984
qw 5
qm 180,108,116
qd 106,22,46080
qm 252,108,188
v_lshlrev_b32 v16,4,v16
qd 108,22,42496
qw 5
qm 168,96,32
s_add_u32 s15,s73,786432
buffer_store_dwordx4 v[232:235],v1,s[88:91],s15 offen offset:1024 sc1
qd 110,22,46592
qm 240,96,42
v_mov_b32 v17,0x3f80
qt 96,2,49152
qw 5
qm 172,100,32
qt 98,2,49664
qm 244,100,42
v_lshlrev_b32 v16,v16,v17
qt 100,3,49152
qw 5
qm 176,104,32
qt 102,3,49664
qm 248,104,42
v_bfe_u32 v17,v0,1,3
qt 104,2,53248
qw 5
qm 180,108,32
qt 106,2,53760
qm 252,108,42
v_cmp_eq_u32 vcc,0,v17
s_nop 1
v_cndmask_b32 v152,0,v16,vcc
qt 108,3,53248
qw 5
qm 168,96,48
qt 110,3,53760
qm 240,96,56
v_cmp_eq_u32 vcc,1,v17
s_nop 1
v_cndmask_b32 v153,0,v16,vcc
qt 96,2,57344
qw 5
qm 172,100,48
qt 98,2,57856
qm 244,100,56
v_cmp_eq_u32 vcc,2,v17
s_nop 1
v_cndmask_b32 v154,0,v16,vcc
qt 100,3,57344
qw 5
qm 168,104,52
qt 102,3,57856
qm 240,104,60
v_cmp_eq_u32 vcc,3,v17
s_nop 1
v_cndmask_b32 v155,0,v16,vcc
qt 104,2,61440
qw 5
qm 172,108,52
s_barrier
qt 106,2,61952
qm 244,108,60
v_cmp_eq_u32 vcc,4,v17
s_nop 1
v_cndmask_b32 v156,0,v16,vcc
qt 108,3,61440
qw 5
qm 176,96,48
qt 110,3,61952
qm 248,96,56
v_cmp_eq_u32 vcc,5,v17
s_nop 1
v_cndmask_b32 v157,0,v16,vcc
qt 96,4,0
qw 5
qm 180,100,48
qt 98,4,512
qm 252,100,56
v_cmp_eq_u32 vcc,6,v17
s_nop 1
v_cndmask_b32 v158,0,v16,vcc
qt 100,5,0
qw 5
qm 176,104,52
qt 102,5,512
qm 248,104,60
v_cmp_eq_u32 vcc,7,v17
s_nop 1
v_cndmask_b32 v159,0,v16,vcc
qt 104,4,4096
qw 5
qm 180,108,52
qt 106,4,4608
qm 252,108,60
qt 108,5,4096
qw 5
qm 168,96,120
qt 110,5,4608
qm 240,96,192
qt 96,4,8192
qw 5
qm 172,100,120
qt 98,4,8704
qm 244,100,192
qt 100,5,8192
qw 5
qm 168,104,124
qt 102,5,8704
qm 240,104,196
qt 104,4,12288
qw 5
qm 172,108,124
qt 106,4,12800
qm 244,108,196
qt 108,5,12288
qw 5
qm 176,96,120
qt 110,5,12800
qm 248,96,192
qt 96,2,0
qw 5
qm 180,100,120
qt 98,2,512
qm 252,100,192
qt 100,3,0
qw 5
qm 176,104,124
qt 102,3,512
qm 248,104,196
qt 104,2,4096
qw 5
qm 180,108,124
s_waitcnt vmcnt(0)
s_barrier
v_add_u32 v38,1,v38
buffer_store_dword v38,v37,s[96:99],s68 offen sc1
qt 106,2,4608
qm 252,108,196
qt 108,3,4096
qw 5
qm 168,96,80
buffer_load_dwordx2 v[224:225],v11,s[36:39],s70 offen offset:512
qt 110,3,4608
qm 240,96,88
qr 120,18,16384
qt 96,2,8192
qw 6
qm 172,100,80
qt 98,2,8704
qm 244,100,88
qr 192,18,17408
qt 100,3,8192
qw 7
qm 168,104,84
qt 102,3,8704
qm 240,104,92
qr 124,18,24576
qt 104,2,12288
qw 8
qm 172,108,84
buffer_load_dwordx2 v[226:227],v12,s[80:83],s75 offen offset:512
qt 106,2,12800
qm 244,108,92
qr 196,18,25600
qt 108,3,12288
qw 8
qm 176,96,80
qt 110,3,12800
qm 248,96,88
qt 96,2,16384
qw 7
qm 180,100,80
qt 98,2,16896
qm 252,100,88
qt 100,3,16384
qw 6
qm 176,104,84
buffer_load_dwordx2 v[228:229],v11,s[36:39],s70 offen offset:0
qt 102,3,16896
qm 248,104,92
qt 104,2,20480
qw 5
qm 180,108,84
qt 106,2,20992
qm 252,108,92
qt 108,3,20480
qw 5
qm 168,96,128
buffer_load_dwordx4 v[220:223],v15,s[96:99],s72 offen sc1
qt 110,3,20992
qm 240,96,200
qt 80,36,0
qt 96,2,24576
qw 6
qm 172,100,128
qt 82,36,512
buffer_load_dwordx2 v[230:231],v12,s[80:83],s75 offen offset:0
qt 98,2,25088
qm 244,100,200
qt 88,36,1024
qt 100,3,24576
qw 8
qm 168,104,132
qt 90,36,1536
qt 102,3,25088
qm 240,104,204
qt 84,36,8192
qt 104,2,28672
qw 10
qm 172,108,132
qw 1
s_barrier
qt 86,36,8704
qt 106,2,29184
qm 244,108,204
qt 92,36,9216
qt 108,3,28672
qm 176,96,128
qt 94,36,9728
buffer_load_dwordx2 v[42:43],v11,s[36:39],s70 offen offset:640
qt 110,3,29184
qm 248,96,200
qt 96,2,32768
qm 180,100,128
qt 98,2,33280
qm 252,100,200
qt 100,3,32768
qw 7
qm 176,104,132
qt 102,3,33280
qm 248,104,204
qt 104,2,36864
qw 5
qm 180,108,132
buffer_load_dwordx2 v[44:45],v12,s[80:83],s75 offen offset:640
qt 106,2,37376
qm 252,108,204
qt 108,3,36864
qw 5
qm 168,96,136
qt 110,3,37376
qm 240,96,208
qt 128,36,16384
qt 96,2,40960
qw 6
qm 172,100,136
qt 130,36,16896
qt 98,2,41472
qm 244,100,208
qt 200,36,17408
qt 100,3,40960
qw 8
qm 168,104,140
qt 202,36,17920
qt 102,3,41472
qm 240,104,212
qt 132,36,24576
qt 104,2,45056
qw 10
qm 172,108,140
qt 134,36,25088
qt 106,2,45568
qm 244,108,212
qt 204,36,25600
qt 108,3,45056
v_mov_b32 v16,0x7ff00000
s_waitcnt vmcnt(3)
v_sub_u32 v220,s74,v220
v_sub_u32 v221,s74,v221
v_sub_u32 v222,s74,v222
v_sub_u32 v223,s74,v223
v_cndmask_b32 v222,v222,0,s[40:41]
v_cndmask_b32 v223,v223,0,s[42:43]
qw 10
qm 176,96,136
qt 206,36,26112
qt 110,3,45568
v_cmp_ge_u32 s[52:53],5,v220
v_cmp_ge_u32 s[54:55],5,v221
v_cmp_ge_u32 s[56:57],5,v222
v_cmp_ge_u32 s[58:59],5,v223
qm 248,96,208
qt 96,2,49152
v_cndmask_b32 v32,v16,v10,s[52:53]
v_cndmask_b32 v33,v16,v10,s[54:55]
v_cndmask_b32 v34,v16,v10,s[56:57]
v_cndmask_b32 v35,v16,v10,s[58:59]
qw 9
qm 180,100,136
s_add_u32 s31,s69,524288
buffer_load_dwordx4 v[160:163],v32,s[88:91],s31 offen sc1
qt 98,2,49664
qm 252,100,208
qt 100,3,49152
qw 7
qm 176,104,140
qt 102,3,49664
qm 248,104,212
qt 104,2,53248
qw 5
qm 180,108,140
s_barrier
s_add_u32 s31,s69,0
buffer_load_dwordx4 v[112:115],v32,s[88:91],s31 offen sc1
qt 106,2,53760
qm 252,108,212
qt 108,3,53248
qw 5
qm 168,96,64
qt 110,3,53760
qm 240,96,72
qr 136,18,32768
qt 96,2,57344
qw 6
qm 172,100,64
s_add_u32 s31,s69,131072
buffer_load_dwordx4 v[184:187],v32,s[88:91],s31 offen sc1
qt 98,2,57856
qm 244,100,72
qr 208,18,33792
qt 100,3,57344
qw 7
qm 168,104,68
qt 102,3,57856
qm 240,104,76
qr 140,18,40960
qt 104,2,61440
qw 8
qm 172,108,68
s_add_u32 s31,s69,589824
buffer_load_dwordx4 v[164:167],v33,s[88:91],s31 offen sc1
qt 106,2,61952
qm 244,108,76
qr 212,18,41984
qt 108,3,61440
qw 8
qm 168,96,120
qt 110,3,61952
qm 240,96,192
qd 96,28,0
qw 7
qm 172,100,120
s_add_u32 s31,s69,65536
buffer_load_dwordx4 v[116:119],v33,s[88:91],s31 offen sc1
qd 98,28,4096
qm 244,100,192
qd 100,29,0
qw 6
qm 168,104,124
qd 102,29,4096
qm 240,104,196
qd 104,30,0
qw 5
qm 172,108,124
s_add_u32 s31,s69,196608
buffer_load_dwordx4 v[188:191],v33,s[88:91],s31 offen sc1
qd 106,30,4096
qm 244,108,196
qd 108,31,0
qw 5
qm 168,96,80
qd 110,31,4096
qm 240,96,88
qd 96,28,8192
qw 5
qm 168,100,84
s_add_u32 s31,s69,1179648
buffer_load_dwordx4 v[232:235],v34,s[88:91],s31 offen sc1
qd 98,28,12288
qm 240,100,92
qd 100,29,8192
qw 5
qm 168,104,128
qd 102,29,12288
qm 240,104,200
qd 104,30,8192
qw 5
qm 168,108,132
s_barrier
s_add_u32 s31,s69,655360
buffer_load_dwordx4 v[48:51],v34,s[88:91],s31 offen sc1
qd 106,30,12288
qm 240,108,204
qd 108,31,8192
qw 5
qm 172,96,80
qd 110,31,12288
qm 244,96,88
qt 96,2,0
qw 5
qm 172,100,84
s_add_u32 s31,s69,786432
buffer_load_dwordx4 v[56:59],v34,s[88:91],s31 offen sc1
qt 98,2,512
qm 244,100,92
qt 100,3,0
qw 5
qm 172,104,128
qt 102,3,512
qm 244,104,200
qt 104,2,4096
qw 5
qm 172,108,132
s_add_u32 s31,s69,1245184
buffer_load_dwordx4 v[236:239],v35,s[88:91],s31 offen sc1
qt 106,2,4608
qm 244,108,204
qt 108,3,4096
qw 5
qm 168,96,136
qt 110,3,4608
qm 240,96,208
qt 96,2,8192
qw 5
qm 172,100,136
s_add_u32 s31,s69,720896
buffer_load_dwordx4 v[52:55],v35,s[88:91],s31 offen sc1
qt 98,2,8704
qm 244,100,208
qt 100,3,8192
qw 5
qm 168,104,140
qt 102,3,8704
qm 240,104,212
qt 104,2,12288
qw 5
qm 172,108,140
s_add_u32 s31,s69,851968
buffer_load_dwordx4 v[60:63],v35,s[88:91],s31 offen sc1
qt 106,2,12800
qm 244,108,212
qt 108,3,12288
qw 5
qm 176,96,64
qt 110,3,12800
qm 248,96,72
qt 96,2,16384
qw 5
qm 180,100,64
qt 98,2,16896
qm 252,100,72
v_cvt_pk_bf16_f32 v168,v168,v169
qt 100,3,16384
qw 5
qm 176,104,68
v_cvt_pk_bf16_f32 v169,v170,v171
qt 102,3,16896
qm 248,104,76
v_cvt_pk_bf16_f32 v170,v172,v173
qt 104,2,20480
qw 5
qm 180,108,68
s_barrier
v_cvt_pk_bf16_f32 v171,v174,v175
qt 106,2,20992
qm 252,108,76
qt 108,3,20480
qw 5
qm 176,96,120
v_cvt_pk_bf16_f32 v240,v240,v241
s_add_u32 s15,s73,917504
buffer_store_dwordx4 v[168:171],v1,s[88:91],s15 offen offset:0 sc1
qt 110,3,20992
qm 248,96,192
v_cvt_pk_bf16_f32 v241,v242,v243
qd 96,24,24576
qw 5
qm 180,100,120
v_cvt_pk_bf16_f32 v242,v244,v245
qd 98,24,28672
qm 252,100,192
v_cvt_pk_bf16_f32 v243,v246,v247
qd 100,25,24576
qw 5
qm 176,104,124
qd 102,25,28672
qm 248,104,196
qd 104,26,24576
qw 5
qm 180,108,124
qd 106,26,28672
qm 252,108,196
qd 108,27,24576
qw 5
qm 176,96,80
s_add_u32 s15,s73,917504
buffer_store_dwordx4 v[240:243],v1,s[88:91],s15 offen offset:1024 sc1
qd 110,27,28672
qm 248,96,88
qd 96,24,32768
qw 5
qm 176,100,84
qd 98,24,36864
qm 248,100,92
qd 100,25,32768
qw 5
qm 176,104,128
qd 102,25,36864
qm 248,104,200
qd 104,26,32768
qw 5
qm 176,108,132
qd 106,26,36864
qm 248,108,204
qd 108,27,32768
qw 5
qm 180,96,80
qd 110,27,36864
qm 252,96,88
qt 96,2,40960
qw 5
qm 180,100,84
qt 98,2,41472
qm 252,100,92
qt 100,3,40960
qw 5
qm 180,104,128
qt 102,3,41472
qm 252,104,200
qt 104,2,45056
qw 5
qm 180,108,132
s_barrier
qt 106,2,45568
qm 252,108,204
qt 108,3,45056
qw 5
qm 176,96,136
qt 110,3,45568
qm 248,96,208
qw 4
qm 180,100,136
qm 252,100,208
qw 2
qm 176,104,140
qm 248,104,212
qw 0
qm 180,108,140
qm 252,108,212
s_nop 3
v_cvt_pk_bf16_f32 v176,v176,v177
v_cvt_pk_bf16_f32 v177,v178,v179
s_nop 0
v_cvt_pk_bf16_f32 v178,v180,v181
v_cvt_pk_bf16_f32 v179,v182,v183
s_add_u32 s15,s73,1048576
buffer_store_dwordx4 v[176:179],v1,s[88:91],s15 offen offset:0 sc1
v_cvt_pk_bf16_f32 v248,v248,v249
v_cvt_pk_bf16_f32 v249,v250,v251
v_cvt_pk_bf16_f32 v250,v252,v253
v_cvt_pk_bf16_f32 v251,v254,v255
s_add_u32 s15,s73,1048576
buffer_store_dwordx4 v[248:251],v1,s[88:91],s15 offen offset:1024 sc1
v_lshlrev_b32 v16,16,v226
v_lshlrev_b32 v144,16,v224
v_mul_f32 v16,v16,v40
v_add_f32 v17,v144,v41
v_fma_f32 v144,v17,v16,v144
v_and_b32 v16,0xffff0000,v226
v_and_b32 v145,0xffff0000,v224
v_mul_f32 v16,v16,v40
v_add_f32 v17,v145,v41
v_fma_f32 v145,v17,v16,v145
v_lshlrev_b32 v16,16,v227
v_lshlrev_b32 v146,16,v225
v_mul_f32 v16,v16,v40
v_add_f32 v17,v146,v41
v_fma_f32 v146,v17,v16,v146
v_and_b32 v16,0xffff0000,v227
v_and_b32 v147,0xffff0000,v225
v_mul_f32 v16,v16,v40
v_add_f32 v17,v147,v41
v_fma_f32 v147,v17,v16,v147
s_waitcnt vmcnt(18)
v_lshlrev_b32 v16,16,v230
v_lshlrev_b32 v148,16,v228
v_mul_f32 v16,v16,v40
v_add_f32 v17,v148,v41
v_fma_f32 v148,v17,v16,v148
v_and_b32 v16,0xffff0000,v230
v_and_b32 v149,0xffff0000,v228
v_mul_f32 v16,v16,v40
v_add_f32 v17,v149,v41
v_fma_f32 v149,v17,v16,v149
v_lshlrev_b32 v16,16,v231
v_lshlrev_b32 v150,16,v229
v_mul_f32 v16,v16,v40
v_add_f32 v17,v150,v41
v_fma_f32 v150,v17,v16,v150
v_and_b32 v16,0xffff0000,v231
v_and_b32 v151,0xffff0000,v229
v_mul_f32 v16,v16,v40
v_add_f32 v17,v151,v41
v_fma_f32 v151,v17,v16,v151
s_waitcnt vmcnt(16)
v_lshlrev_b32 v16,16,v44
v_lshlrev_b32 v216,16,v42
v_mul_f32 v16,v16,v40
v_add_f32 v17,v216,v41
v_fma_f32 v216,v17,v16,v216
v_and_b32 v16,0xffff0000,v44
v_and_b32 v217,0xffff0000,v42
v_mul_f32 v16,v16,v40
v_add_f32 v17,v217,v41
v_fma_f32 v217,v17,v16,v217
v_lshlrev_b32 v16,16,v45
v_lshlrev_b32 v218,16,v43
v_mul_f32 v16,v16,v40
v_add_f32 v17,v218,v41
v_fma_f32 v218,v17,v16,v218
v_and_b32 v16,0xffff0000,v45
v_and_b32 v219,0xffff0000,v43
v_mul_f32 v16,v16,v40
v_add_f32 v17,v219,v41
v_fma_f32 v219,v17,v16,v219
buffer_load_dwordx4 v[220:223],v15,s[96:99],s72 offen sc1
v_add_u32 v38,5,v38
s_setprio 3
s_waitcnt vmcnt(0)
s_barrier
buffer_store_dword v38,v37,s[96:99],s68 offen sc1
s_setprio 0
.L_tY1:
v_sub_u32 v220,s74,v220
v_sub_u32 v221,s74,v221
v_sub_u32 v222,s74,v222
v_sub_u32 v223,s74,v223
v_cndmask_b32 v222,v222,0,s[40:41]
v_cndmask_b32 v223,v223,0,s[42:43]
v_cmp_ge_u32 vcc,5,v220
s_nop 1
s_andn2_b64 exec,vcc,s[52:53]
s_cbranch_scc0 .L_tY1r10
s_add_u32 s31,s69,524288
buffer_load_dwordx4 v[160:163],v10,s[88:91],s31 offen sc1
s_add_u32 s31,s69,0
buffer_load_dwordx4 v[112:115],v10,s[88:91],s31 offen sc1
s_add_u32 s31,s69,131072
buffer_load_dwordx4 v[184:187],v10,s[88:91],s31 offen sc1
.L_tY1r10:
s_mov_b64 exec,-1
s_or_b64 s[52:53],s[52:53],vcc
v_cmp_ge_u32 vcc,5,v221
s_nop 1
s_andn2_b64 exec,vcc,s[54:55]
s_cbranch_scc0 .L_tY1r11
s_add_u32 s31,s69,589824
buffer_load_dwordx4 v[164:167],v10,s[88:91],s31 offen sc1
s_add_u32 s31,s69,65536
buffer_load_dwordx4 v[116:119],v10,s[88:91],s31 offen sc1
s_add_u32 s31,s69,196608
buffer_load_dwordx4 v[188:191],v10,s[88:91],s31 offen sc1
.L_tY1r11:
s_mov_b64 exec,-1
s_or_b64 s[54:55],s[54:55],vcc
v_cmp_ge_u32 vcc,5,v222
s_nop 1
s_andn2_b64 exec,vcc,s[56:57]
s_cbranch_scc0 .L_tY1r12
s_add_u32 s31,s69,1179648
buffer_load_dwordx4 v[232:235],v10,s[88:91],s31 offen sc1
s_add_u32 s31,s69,655360
buffer_load_dwordx4 v[48:51],v10,s[88:91],s31 offen sc1
s_add_u32 s31,s69,786432
buffer_load_dwordx4 v[56:59],v10,s[88:91],s31 offen sc1
.L_tY1r12:
s_mov_b64 exec,-1
s_or_b64 s[56:57],s[56:57],vcc
v_cmp_ge_u32 vcc,5,v223
s_nop 1
s_andn2_b64 exec,vcc,s[58:59]
s_cbranch_scc0 .L_tY1r13
s_add_u32 s31,s69,1245184
buffer_load_dwordx4 v[236:239],v10,s[88:91],s31 offen sc1
s_add_u32 s31,s69,720896
buffer_load_dwordx4 v[52:55],v10,s[88:91],s31 offen sc1
s_add_u32 s31,s69,851968
buffer_load_dwordx4 v[60:63],v10,s[88:91],s31 offen sc1
.L_tY1r13:
s_mov_b64 exec,-1
s_or_b64 s[58:59],s[58:59],vcc
.L_tY1c:
s_and_b64 s[32:33],s[52:53],s[54:55]
s_and_b64 s[32:33],s[32:33],s[56:57]
s_and_b64 s[32:33],s[32:33],s[58:59]
s_cmp_eq_u64 s[32:33],-1
s_cbranch_scc1 .L_tY1x
s_add_u32 s76,s76,1
s_cmp_gt_u32 s76,1000000
s_cbranch_scc1 .L_tY1x
s_sleep 4
buffer_load_dwordx4 v[220:223],v15,s[96:99],s72 offen sc1
s_waitcnt vmcnt(0)
s_branch .L_tY1
.L_tY1x:
s_waitcnt vmcnt(0)
qm 144,152,160
qm 148,152,112
qm 216,152,48
qm 144,152,164
qm 148,152,116
qm 216,152,52
qm 144,152,232
qm 148,156,184
qm 216,156,56
qm 144,152,236
qm 148,156,188
qm 216,156,60
s_nop 5
buffer_store_dwordx4 v[144:147],v14,s[92:95],s71 offen offset:1024 sc1
buffer_store_dwordx4 v[148:151],v13,s[92:95],s71 offen offset:0 sc1
buffer_store_dwordx4 v[216:219],v13,s[92:95],s71 offen offset:1280 sc1
s_endpgm
.org .L_code_Y+81920
.L_code_X1:
.L_code_X10:
s_waitcnt vmcnt(11) lgkmcnt(0)
s_barrier
qr 184,24,0
qr 188,25,4096
qr 192,24,8192
qr 196,25,12288
qr 200,24,16384
qr 204,25,20480
s_add_u32 m0,s13,57344
s_add_u32 s14,s18,896
s_waitcnt vmcnt(7) lgkmcnt(0)
s_barrier
ql 8,20,0
ql 9,20,1024
s_add_u32 m0,s13,65536
s_add_u32 s14,s18,1024
ql 8,20,0
ql 9,20,1024
s_add_u32 m0,s13,73728
s_mov_b32 s14,s17
ql 6,40,0
ql 7,40,1024
s_waitcnt vmcnt(7)
s_barrier
qr 208,24,24576
qr 212,25,28672
s_mov_b32 m0,s13
s_add_u32 s14,s17,589824
ql 6,40,0
qr 216,24,32768
qr 220,25,36864
ql 7,40,1024
qr 224,24,40960
s_add_u32 m0,s13,8192
qr 228,25,45056
s_add_u32 s14,s17,1179648
qr 232,24,49152
qr 236,25,53248
ql 6,40,0
qr 240,24,57344
ql 7,40,1024
qr 244,25,61440
s_add_u32 m0,s13,16384
qr 248,26,8192
qr 252,27,12288
s_add_u32 s14,s17,1769472
ql 6,40,0
ql 7,40,1024
s_add_u32 m0,s13,24576
s_waitcnt vmcnt(6) lgkmcnt(0)
s_barrier
s_add_u32 s14,s17,2359296
ql 6,40,0
ql 7,40,1024
s_add_u32 m0,s13,32768
s_add_u32 s14,s17,2949120
ql 6,40,0
v_readfirstlane_b32 s3,v13
s_mul_i32 s16,s3,0x9E3779B1
s_add_u32 s16,s16,0x7F4A7C15
ql 7,40,1024
s_add_u32 m0,s13,40960
s_add_u32 s14,s17,3538944
ql 6,40,0
qt 96,4,8192
qt 98,4,8704
ql 7,40,1024
qt 100,5,8192
qt 102,5,8704
s_add_u32 m0,s13,81920
qt 104,4,12288
s_waitcnt vmcnt(6)
s_barrier
qt 106,4,12800
s_add_u32 s14,s17,4128768
qt 108,5,12288
qw 5
qz 64,96,112
v_bfe_u32 v45,v0,2,2
qt 110,5,12800
ql 6,40,0
qz 68,96,184
v_lshl_add_u32 v45,s7,2,v45
qt 96,2,0
qw 5
qz 72,100,112
v_and_b32 v14,3,v0
qt 98,2,512
ql 7,40,1024
qz 76,100,184
v_lshl_add_u32 v45,v45,3,v14
qt 100,3,0
qw 5
qm 64,104,116
v_lshlrev_b32 v45,2,v45
s_lshl_b32 s30,s10,3
s_add_u32 s30,s30,s8
s_mul_i32 s30,s30,196608
s_add_u32 s76,s30,21020672
s_add_u32 s77,s30,21118976
s_lshl_b32 s30,s7,13
s_add_u32 s79,s76,s30
qt 102,3,512
s_add_u32 m0,s13,90112
qm 68,104,188
v_add_u32 v45,122880,v45
qt 104,2,4096
qw 5
qm 72,108,116
ds_read_b32 v10,v45
qt 106,2,4608
s_add_u32 s14,s17,4718592
qm 76,108,188
ds_read_b32 v11,v45 offset:16
qt 108,3,4096
qw 7
qm 64,96,120
qt 110,3,4608
ql 6,40,0
qm 68,96,192
qt 96,2,8192
qw 7
qm 72,100,120
qt 98,2,8704
ql 7,40,1024
qm 76,100,192
qt 100,3,8192
qw 6
qm 64,104,124
v_mul_f32 v14,v10,v10
qt 102,3,8704
s_add_u32 m0,s13,98304
qm 68,104,196
v_mul_f32 v12,0xbdd2d3e8,v10
qt 104,2,12288
qw 5
qm 72,108,124
v_mul_f32 v10,0xc0135761,v10
qt 106,2,12800
s_mov_b32 s14,s17
qm 76,108,196
v_mul_f32 v12,v12,v14
qt 108,3,12288
qw 5
qm 64,96,128
v_rcp_f32 v14,v14
qt 110,3,12800
ql 6,44,0
qm 68,96,200
v_mul_f32 v15,v11,v11
qt 96,2,16384
qw 5
qm 72,100,128
v_mul_f32 v13,0xbdd2d3e8,v11
qt 98,2,16896
ql 7,44,1024
qm 76,100,200
v_mul_f32 v11,0xc0135761,v11
qt 100,3,16384
qw 5
qm 64,104,132
v_mul_f32 v13,v13,v15
qt 102,3,16896
s_add_u32 m0,s13,106496
qm 68,104,204
v_rcp_f32 v15,v15
qt 104,2,20480
qw 5
qm 72,108,132
s_waitcnt vmcnt(6)
s_barrier
qt 106,2,20992
s_add_u32 s14,s17,589824
qm 76,108,204
qt 108,3,20480
qw 5
qm 64,96,136
qt 110,3,20992
ql 6,44,0
qm 68,96,208
qt 96,2,24576
qw 5
qm 72,100,136
qt 98,2,25088
ql 7,44,1024
qm 76,100,208
qt 100,3,24576
qw 5
qm 64,104,140
qt 102,3,25088
s_add_u32 m0,s13,49152
qm 68,104,212
qt 104,2,28672
qw 5
qm 72,108,140
qt 106,2,29184
s_add_u32 s14,s17,1179648
qm 76,108,212
qt 108,3,28672
qw 5
qm 64,96,144
qt 110,3,29184
ql 6,44,0
qm 68,96,216
qt 96,2,32768
qw 5
qm 72,100,144
qt 98,2,33280
ql 7,44,1024
qm 76,100,216
qt 100,3,32768
qw 5
qm 64,104,148
qt 102,3,33280
s_add_u32 m0,s13,57344
qm 68,104,220
qt 104,2,36864
qw 5
qm 72,108,148
qt 106,2,37376
s_add_u32 s14,s17,1769472
qm 76,108,220
qt 108,3,36864
qw 5
qm 64,96,152
qt 110,3,37376
ql 6,44,0
qm 68,96,224
qt 96,2,40960
qw 5
qm 72,100,152
qt 98,2,41472
ql 7,44,1024
qm 76,100,224
qt 100,3,40960
qw 5
qm 64,104,156
qt 102,3,41472
s_add_u32 m0,s13,65536
qm 68,104,228
qt 104,2,45056
qw 5
qm 72,108,156
s_waitcnt vmcnt(6)
s_barrier
qt 106,2,45568
s_add_u32 s14,s17,2359296
qm 76,108,228
qt 108,3,45056
qw 5
qm 64,96,160
qt 110,3,45568
ql 6,44,0
qm 68,96,232
qt 96,4,16384
qw 5
qm 72,100,160
qt 98,4,16896
ql 7,44,1024
qm 76,100,232
qt 100,5,16384
qw 5
qm 64,104,164
qt 102,5,16896
s_add_u32 m0,s13,73728
qm 68,104,236
qt 104,4,20480
qw 5
qm 72,108,164
qt 106,4,20992
s_add_u32 s14,s17,2949120
qm 76,108,236
qt 108,5,20480
qw 5
qm 64,96,168
qt 110,5,20992
ql 6,44,0
qm 68,96,240
qt 96,4,24576
qw 5
qm 72,100,168
qt 98,4,25088
ql 7,44,1024
qm 76,100,240
qt 100,5,24576
qw 5
qm 64,104,172
qt 102,5,25088
s_mov_b32 m0,s13
qm 68,104,244
qt 104,4,28672
qw 5
qm 72,108,172
qt 106,4,29184
s_add_u32 s14,s17,3538944
qm 76,108,244
qt 108,5,28672
qw 5
qm 64,96,176
qt 110,5,29184
ql 6,44,0
qm 68,96,248
qt 96,4,32768
qw 5
qm 72,100,176
qt 98,4,33280
ql 7,44,1024
qm 76,100,248
qt 100,5,32768
qw 5
qm 64,104,180
qt 102,5,33280
s_add_u32 m0,s13,81920
qm 68,104,252
qt 104,4,36864
qw 5
qm 72,108,180
s_waitcnt vmcnt(6)
s_barrier
qt 106,4,37376
s_add_u32 s14,s17,4128768
qm 76,108,252
qt 108,5,36864
qw 5
qz 80,96,112
qt 110,5,37376
ql 6,44,0
qz 84,96,184
v_mul_f32 v16,v64,v64
qt 96,4,40960
qw 5
qz 88,100,112
v_fma_f32 v16,v16,v12,v10
qt 98,4,41472
ql 7,44,1024
qz 92,100,184
v_mul_f32 v16,v16,v64
qt 100,5,40960
qw 5
qm 80,104,116
v_exp_f32 v16,v16
qt 102,5,41472
s_add_u32 m0,s13,90112
qm 84,104,188
v_fma_f32 v16,v16,v14,v14
qt 104,4,45056
qw 5
qm 88,108,116
v_rcp_f32 v16,v16
qt 106,4,45568
s_add_u32 s14,s17,4718592
qm 92,108,188
v_mul_f32 v64,v64,v16
qt 108,5,45056
qw 5
qm 80,96,120
v_mul_f32 v16,v65,v65
qt 110,5,45568
ql 6,44,0
qm 84,96,192
v_fma_f32 v16,v16,v12,v10
qt 96,2,49152
qw 5
qm 88,100,120
v_mul_f32 v16,v16,v65
qt 98,2,49664
ql 7,44,1024
qm 92,100,192
v_exp_f32 v16,v16
qt 100,3,49152
qw 5
qm 80,104,124
v_fma_f32 v16,v16,v14,v14
qt 102,3,49664
s_add_u32 m0,s13,8192
qm 84,104,196
v_rcp_f32 v16,v16
qt 104,2,53248
qw 5
qm 88,108,124
v_mul_f32 v65,v65,v16
qt 106,2,53760
s_add_u32 s14,s17,128
qm 92,108,196
v_mul_f32 v16,v66,v66
qt 108,3,53248
qw 5
qm 80,96,128
v_fma_f32 v16,v16,v12,v10
qt 110,3,53760
ql 6,40,0
qm 84,96,200
v_mul_f32 v16,v16,v66
qt 96,2,57344
qw 5
qm 88,100,128
v_exp_f32 v16,v16
qt 98,2,57856
ql 7,40,1024
qm 92,100,200
v_fma_f32 v16,v16,v14,v14
qt 100,3,57344
qw 5
qm 80,104,132
v_rcp_f32 v16,v16
qt 102,3,57856
s_add_u32 m0,s13,98304
qm 84,104,204
v_mul_f32 v66,v66,v16
qt 104,2,61440
qw 5
qm 88,108,132
s_waitcnt vmcnt(6)
s_barrier
v_mul_f32 v16,v67,v67
qt 106,2,61952
s_add_u32 s14,s17,589952
qm 92,108,204
v_fma_f32 v16,v16,v12,v10
qt 108,3,61440
qw 5
qm 80,96,136
v_mul_f32 v16,v16,v67
qt 110,3,61952
ql 6,40,0
qm 84,96,208
v_exp_f32 v16,v16
qt 96,4,0
qw 5
qm 88,100,136
v_fma_f32 v16,v16,v14,v14
qt 98,4,512
ql 7,40,1024
qm 92,100,208
v_rcp_f32 v16,v16
qt 100,5,0
qw 5
qm 80,104,140
v_mul_f32 v67,v67,v16
qt 102,5,512
s_add_u32 m0,s13,106496
qm 84,104,212
v_mul_f32 v16,v72,v72
qt 104,4,4096
qw 5
qm 88,108,140
v_fma_f32 v16,v16,v12,v10
qt 106,4,4608
s_add_u32 s14,s17,1179776
qm 92,108,212
v_mul_f32 v16,v16,v72
qt 108,5,4096
qw 5
qm 80,96,144
v_exp_f32 v16,v16
qt 110,5,4608
ql 6,40,0
qm 84,96,216
v_fma_f32 v16,v16,v14,v14
qt 96,4,8192
qw 5
qm 88,100,144
v_rcp_f32 v16,v16
qt 98,4,8704
ql 7,40,1024
qm 92,100,216
v_mul_f32 v72,v72,v16
qt 100,5,8192
qw 5
qm 80,104,148
v_mul_f32 v16,v73,v73
qt 102,5,8704
s_add_u32 m0,s13,16384
qm 84,104,220
v_fma_f32 v16,v16,v12,v10
qt 104,4,12288
qw 5
qm 88,108,148
v_mul_f32 v16,v16,v73
qt 106,4,12800
s_add_u32 s14,s17,1769600
qm 92,108,220
v_exp_f32 v16,v16
qt 108,5,12288
qw 5
qm 80,96,152
v_fma_f32 v16,v16,v14,v14
qt 110,5,12800
ql 6,40,0
qm 84,96,224
v_rcp_f32 v16,v16
qt 96,2,0
qw 5
qm 88,100,152
v_mul_f32 v73,v73,v16
qt 98,2,512
ql 7,40,1024
qm 92,100,224
v_mul_f32 v16,v74,v74
qt 100,3,0
qw 5
qm 80,104,156
v_fma_f32 v16,v16,v12,v10
qt 102,3,512
s_add_u32 m0,s13,24576
qm 84,104,228
v_mul_f32 v16,v16,v74
qt 104,2,4096
qw 5
qm 88,108,156
s_waitcnt vmcnt(6)
s_barrier
v_exp_f32 v16,v16
qt 106,2,4608
s_add_u32 s14,s17,2359424
qm 92,108,228
v_fma_f32 v16,v16,v14,v14
qt 108,3,4096
qw 5
qm 80,96,160
v_rcp_f32 v16,v16
qt 110,3,4608
ql 6,40,0
qm 84,96,232
v_mul_f32 v74,v74,v16
qt 96,4,16384
qw 5
qm 88,100,160
v_mul_f32 v16,v75,v75
qt 98,4,16896
ql 7,40,1024
qm 92,100,232
v_fma_f32 v16,v16,v12,v10
qt 100,5,16384
qw 5
qm 80,104,164
v_mul_f32 v16,v16,v75
qt 102,5,16896
s_add_u32 m0,s13,32768
qm 84,104,236
v_exp_f32 v16,v16
qt 104,4,20480
qw 5
qm 88,108,164
v_fma_f32 v16,v16,v14,v14
qt 106,4,20992
s_add_u32 s14,s17,2949248
qm 92,108,236
v_rcp_f32 v16,v16
qt 108,5,20480
qw 5
qm 80,96,168
v_mul_f32 v75,v75,v16
qt 110,5,20992
ql 6,40,0
qm 84,96,240
v_mul_f32 v16,v68,v68
qt 96,4,24576
qw 5
qm 88,100,168
v_fma_f32 v16,v16,v13,v11
qt 98,4,25088
ql 7,40,1024
qm 92,100,240
v_mul_f32 v16,v16,v68
qt 100,5,24576
qw 5
qm 80,104,172
v_exp_f32 v16,v16
qt 102,5,25088
s_add_u32 m0,s13,40960
qm 84,104,244
v_fma_f32 v16,v16,v15,v15
qt 104,4,28672
qw 5
qm 88,108,172
v_rcp_f32 v16,v16
qt 106,4,29184
s_add_u32 s14,s17,3539072
qm 92,108,244
v_mul_f32 v68,v68,v16
qt 108,5,28672
qw 5
qm 80,96,176
v_mul_f32 v16,v69,v69
qt 110,5,29184
ql 6,40,0
qm 84,96,248
v_fma_f32 v16,v16,v13,v11
qt 96,2,8192
qw 5
qm 88,100,176
v_mul_f32 v16,v16,v69
qt 98,2,8704
ql 7,40,1024
qm 92,100,248
v_exp_f32 v16,v16
qt 100,3,8192
qw 5
qm 80,104,180
v_fma_f32 v16,v16,v15,v15
qt 102,3,8704
s_add_u32 m0,s13,81920
qm 84,104,252
v_rcp_f32 v16,v16
qt 104,2,12288
qw 5
qm 88,108,180
s_waitcnt vmcnt(6)
s_barrier
v_mul_f32 v69,v69,v16
qt 106,2,12800
s_add_u32 s14,s17,4128896
qm 92,108,252
qt 108,3,12288
qw 5
qz 48,96,112
v_mul_f32 v16,v70,v70
qt 110,3,12800
ql 6,40,0
qz 52,96,184
v_fma_f32 v16,v16,v13,v11
qt 96,4,32768
qw 5
qz 56,100,112
v_mul_f32 v16,v16,v70
qt 98,4,33280
ql 7,40,1024
qz 60,100,184
v_exp_f32 v16,v16
qt 100,5,32768
qw 5
qm 48,104,116
v_fma_f32 v16,v16,v15,v15
qt 102,5,33280
s_add_u32 m0,s13,90112
qm 52,104,188
v_rcp_f32 v16,v16
qt 104,4,36864
qw 5
qm 56,108,116
v_mul_f32 v70,v70,v16
qt 106,4,37376
s_add_u32 s14,s17,4718720
qm 60,108,188
v_mul_f32 v16,v71,v71
qt 108,5,36864
qw 5
qm 48,96,120
v_fma_f32 v16,v16,v13,v11
qt 110,5,37376
ql 6,40,0
qm 52,96,192
v_mul_f32 v16,v16,v71
qt 96,4,40960
qw 5
qm 56,100,120
v_exp_f32 v16,v16
qt 98,4,41472
ql 7,40,1024
qm 60,100,192
v_fma_f32 v16,v16,v15,v15
qt 100,5,40960
qw 5
qm 48,104,124
v_rcp_f32 v16,v16
qt 102,5,41472
s_add_u32 m0,s13,49152
qm 52,104,196
v_mul_f32 v71,v71,v16
qt 104,4,45056
qw 5
qm 56,108,124
v_mul_f32 v16,v76,v76
qt 106,4,45568
s_add_u32 s14,s17,128
qm 60,108,196
v_fma_f32 v16,v16,v13,v11
qt 108,5,45056
qw 5
qm 48,96,128
v_mul_f32 v16,v16,v76
qt 110,5,45568
ql 6,44,0
qm 52,96,200
v_exp_f32 v16,v16
qt 96,2,16384
qw 5
qm 56,100,128
v_fma_f32 v16,v16,v15,v15
qt 98,2,16896
ql 7,44,1024
qm 60,100,200
v_rcp_f32 v16,v16
qt 100,3,16384
qw 5
qm 48,104,132
v_mul_f32 v76,v76,v16
qt 102,3,16896
s_add_u32 m0,s13,98304
qm 52,104,204
v_mul_f32 v16,v77,v77
qt 104,2,20480
qw 5
qm 56,108,132
s_waitcnt vmcnt(6)
s_barrier
v_fma_f32 v16,v16,v13,v11
qt 106,2,20992
s_add_u32 s14,s17,589952
qm 60,108,204
v_mul_f32 v16,v16,v77
qt 108,3,20480
qw 5
qm 48,96,136
v_exp_f32 v16,v16
qt 110,3,20992
ql 6,44,0
qm 52,96,208
v_fma_f32 v16,v16,v15,v15
qt 96,2,24576
qw 5
qm 56,100,136
v_rcp_f32 v16,v16
qt 98,2,25088
ql 7,44,1024
qm 60,100,208
v_mul_f32 v77,v77,v16
qt 100,3,24576
qw 5
qm 48,104,140
v_mul_f32 v16,v78,v78
qt 102,3,25088
s_add_u32 m0,s13,106496
qm 52,104,212
v_fma_f32 v16,v16,v13,v11
qt 104,2,28672
qw 5
qm 56,108,140
v_mul_f32 v16,v16,v78
qt 106,2,29184
s_add_u32 s14,s17,1179776
qm 60,108,212
v_exp_f32 v16,v16
qt 108,3,28672
qw 5
qm 48,96,144
v_fma_f32 v16,v16,v15,v15
qt 110,3,29184
ql 6,44,0
qm 52,96,216
v_rcp_f32 v16,v16
qt 96,2,32768
qw 5
qm 56,100,144
v_mul_f32 v78,v78,v16
qt 98,2,33280
ql 7,44,1024
qm 60,100,216
v_mul_f32 v16,v79,v79
qt 100,3,32768
qw 5
qm 48,104,148
v_fma_f32 v16,v16,v13,v11
qt 102,3,33280
s_add_u32 m0,s13,57344
qm 52,104,220
v_mul_f32 v16,v16,v79
qt 104,2,36864
qw 5
qm 56,108,148
v_exp_f32 v16,v16
qt 106,2,37376
s_add_u32 s14,s17,1769600
qm 60,108,220
v_fma_f32 v16,v16,v15,v15
qt 108,3,36864
qw 5
qm 48,96,152
v_rcp_f32 v16,v16
qt 110,3,37376
ql 6,44,0
qm 52,96,224
v_mul_f32 v79,v79,v16
qt 96,2,40960
qw 5
qm 56,100,152
v_mul_f32 v64,v64,v80
qt 98,2,41472
ql 7,44,1024
qm 60,100,224
v_mul_f32 v65,v65,v81
qt 100,3,40960
qw 5
qm 48,104,156
v_cvt_pk_bf16_f32 v64,v64,v65
qt 102,3,41472
s_add_u32 m0,s13,65536
qm 52,104,228
v_mul_f32 v66,v66,v82
qt 104,2,45056
qw 5
qm 56,108,156
s_waitcnt vmcnt(6)
s_barrier
v_mul_f32 v67,v67,v83
qt 106,2,45568
s_add_u32 s14,s17,2359424
qm 60,108,228
v_cvt_pk_bf16_f32 v65,v66,v67
qt 108,3,45056
qw 5
qm 48,96,160
v_mul_f32 v72,v72,v88
qt 110,3,45568
ql 6,44,0
qm 52,96,232
v_mul_f32 v73,v73,v89
qt 96,4,16384
qw 5
qm 56,100,160
v_cvt_pk_bf16_f32 v66,v72,v73
qt 98,4,16896
ql 7,44,1024
qm 60,100,232
v_mul_f32 v74,v74,v90
qt 100,5,16384
qw 5
qm 48,104,164
v_mul_f32 v75,v75,v91
qt 102,5,16896
s_add_u32 m0,s13,73728
qm 52,104,236
v_cvt_pk_bf16_f32 v67,v74,v75
qt 104,4,20480
qw 5
qm 56,108,164
ds_write_b128 v18,v[64:67] offset:49152
v_mul_f32 v68,v68,v84
qt 106,4,20992
s_add_u32 s14,s17,2949248
qm 60,108,236
s_add_u32 s15,s76,0
buffer_store_dwordx4 v[64:67],v18,s[88:91],s15 offen offset:0 sc1
v_mul_f32 v69,v69,v85
qt 108,5,20480
qw 6
qm 48,96,168
v_cvt_pk_bf16_f32 v68,v68,v69
qt 110,5,20992
ql 6,44,0
qm 52,96,240
v_mul_f32 v70,v70,v86
qt 96,4,24576
qw 6
qm 56,100,168
v_mul_f32 v71,v71,v87
qt 98,4,25088
ql 7,44,1024
qm 60,100,240
v_cvt_pk_bf16_f32 v69,v70,v71
qt 100,5,24576
qw 5
qm 48,104,172
v_mul_f32 v76,v76,v92
qt 102,5,25088
s_mov_b32 m0,s13
qm 52,104,244
v_mul_f32 v77,v77,v93
qt 104,4,28672
qw 5
qm 56,108,172
v_cvt_pk_bf16_f32 v70,v76,v77
qt 106,4,29184
s_add_u32 s14,s17,3539072
qm 60,108,244
v_mul_f32 v78,v78,v94
qt 108,5,28672
qw 5
qm 48,96,176
v_mul_f32 v79,v79,v95
qt 110,5,29184
ql 6,44,0
qm 52,96,248
v_cvt_pk_bf16_f32 v71,v78,v79
qt 96,2,49152
qw 5
qm 56,100,176
ds_write_b128 v18,v[68:71] offset:50176
qt 98,2,49664
ql 7,44,1024
qm 60,100,248
s_add_u32 s15,s76,0
buffer_store_dwordx4 v[68:71],v18,s[88:91],s15 offen offset:1024 sc1
qt 100,3,49152
qw 6
qm 48,104,180
qt 102,3,49664
s_add_u32 m0,s13,81920
qm 52,104,252
qt 104,2,53248
qw 6
qm 56,108,180
s_waitcnt vmcnt(8) lgkmcnt(4)
s_barrier
qt 106,2,53760
s_add_u32 s14,s17,4128896
qm 60,108,252
qt 108,3,53248
qw 5
qz 80,96,112
qt 110,3,53760
ql 6,44,0
qz 84,96,184
v_mul_f32 v16,v48,v48
qt 96,4,32768
qw 5
qz 88,100,112
v_fma_f32 v16,v16,v12,v10
qt 98,4,33280
ql 7,44,1024
qz 92,100,184
v_mul_f32 v16,v16,v48
qt 100,5,32768
qw 5
qm 80,104,116
v_exp_f32 v16,v16
qt 102,5,33280
s_add_u32 m0,s13,90112
qm 84,104,188
v_fma_f32 v16,v16,v14,v14
qt 104,4,36864
qw 5
qm 88,108,116
v_rcp_f32 v16,v16
qt 106,4,37376
s_add_u32 s14,s17,4718720
qm 92,108,188
v_mul_f32 v48,v48,v16
qt 108,5,36864
qw 5
qm 80,96,120
v_mul_f32 v16,v49,v49
qt 110,5,37376
ql 6,44,0
qm 84,96,192
v_fma_f32 v16,v16,v12,v10
qt 96,4,40960
qw 5
qm 88,100,120
v_mul_f32 v16,v16,v49
qt 98,4,41472
ql 7,44,1024
qm 92,100,192
v_exp_f32 v16,v16
qt 100,5,40960
qw 5
qm 80,104,124
v_fma_f32 v16,v16,v14,v14
qt 102,5,41472
s_add_u32 m0,s13,8192
qm 84,104,196
v_rcp_f32 v16,v16
qt 104,4,45056
qw 5
qm 88,108,124
v_mul_f32 v49,v49,v16
qt 106,4,45568
s_add_u32 s14,s17,256
qm 92,108,196
v_mul_f32 v16,v50,v50
qt 108,5,45056
qw 5
qm 80,96,128
v_fma_f32 v16,v16,v12,v10
qt 110,5,45568
ql 6,40,0
qm 84,96,200
v_mul_f32 v16,v16,v50
qt 96,2,57344
qw 5
qm 88,100,128
v_exp_f32 v16,v16
qt 98,2,57856
ql 7,40,1024
qm 92,100,200
v_fma_f32 v16,v16,v14,v14
qt 100,3,57344
qw 5
qm 80,104,132
v_rcp_f32 v16,v16
qt 102,3,57856
s_add_u32 m0,s13,98304
qm 84,104,204
v_mul_f32 v50,v50,v16
qt 104,2,61440
qw 5
qm 88,108,132
s_waitcnt vmcnt(7)
s_barrier
v_mul_f32 v16,v51,v51
qt 106,2,61952
s_add_u32 s14,s17,590080
qm 92,108,204
v_fma_f32 v16,v16,v12,v10
qt 108,3,61440
qw 5
qm 80,96,136
v_mul_f32 v16,v16,v51
qt 110,3,61952
ql 6,40,0
qm 84,96,208
v_exp_f32 v16,v16
qt 96,4,0
qw 5
qm 88,100,136
v_fma_f32 v16,v16,v14,v14
qt 98,4,512
ql 7,40,1024
qm 92,100,208
v_rcp_f32 v16,v16
qt 100,5,0
qw 5
qm 80,104,140
v_mul_f32 v51,v51,v16
qt 102,5,512
s_add_u32 m0,s13,106496
qm 84,104,212
v_mul_f32 v16,v56,v56
qt 104,4,4096
qw 5
qm 88,108,140
v_fma_f32 v16,v16,v12,v10
qt 106,4,4608
s_add_u32 s14,s17,1179904
qm 92,108,212
v_mul_f32 v16,v16,v56
qt 108,5,4096
qw 5
qm 80,96,144
v_exp_f32 v16,v16
qt 110,5,4608
ql 6,40,0
qm 84,96,216
v_fma_f32 v16,v16,v14,v14
qt 96,4,8192
qw 5
qm 88,100,144
v_rcp_f32 v16,v16
qt 98,4,8704
ql 7,40,1024
qm 92,100,216
v_mul_f32 v56,v56,v16
qt 100,5,8192
qw 5
qm 80,104,148
v_mul_f32 v16,v57,v57
qt 102,5,8704
s_add_u32 m0,s13,16384
qm 84,104,220
v_fma_f32 v16,v16,v12,v10
qt 104,4,12288
qw 5
qm 88,108,148
v_mul_f32 v16,v16,v57
qt 106,4,12800
s_add_u32 s14,s17,1769728
qm 92,108,220
v_exp_f32 v16,v16
qt 108,5,12288
qw 5
qm 80,96,152
v_fma_f32 v16,v16,v14,v14
qt 110,5,12800
ql 6,40,0
qm 84,96,224
v_rcp_f32 v16,v16
qt 96,2,0
qw 5
qm 88,100,152
v_mul_f32 v57,v57,v16
qt 98,2,512
ql 7,40,1024
qm 92,100,224
v_mul_f32 v16,v58,v58
qt 100,3,0
qw 5
qm 80,104,156
v_fma_f32 v16,v16,v12,v10
qt 102,3,512
s_add_u32 m0,s13,24576
qm 84,104,228
v_mul_f32 v16,v16,v58
qt 104,2,4096
qw 5
qm 88,108,156
s_waitcnt vmcnt(6)
s_barrier
v_exp_f32 v16,v16
qt 106,2,4608
s_add_u32 s14,s17,2359552
qm 92,108,228
v_fma_f32 v16,v16,v14,v14
qt 108,3,4096
qw 5
qm 80,96,160
v_rcp_f32 v16,v16
qt 110,3,4608
ql 6,40,0
qm 84,96,232
v_mul_f32 v58,v58,v16
qt 96,4,16384
qw 5
qm 88,100,160
v_mul_f32 v16,v59,v59
qt 98,4,16896
ql 7,40,1024
qm 92,100,232
v_fma_f32 v16,v16,v12,v10
qt 100,5,16384
qw 5
qm 80,104,164
v_mul_f32 v16,v16,v59
qt 102,5,16896
s_add_u32 m0,s13,32768
qm 84,104,236
v_exp_f32 v16,v16
qt 104,4,20480
qw 5
qm 88,108,164
v_fma_f32 v16,v16,v14,v14
qt 106,4,20992
s_add_u32 s14,s17,2949376
qm 92,108,236
v_rcp_f32 v16,v16
qt 108,5,20480
qw 5
qm 80,96,168
v_mul_f32 v59,v59,v16
qt 110,5,20992
ql 6,40,0
qm 84,96,240
v_mul_f32 v16,v52,v52
qt 96,4,24576
qw 5
qm 88,100,168
v_fma_f32 v16,v16,v13,v11
qt 98,4,25088
ql 7,40,1024
qm 92,100,240
v_mul_f32 v16,v16,v52
qt 100,5,24576
qw 5
qm 80,104,172
v_exp_f32 v16,v16
qt 102,5,25088
s_add_u32 m0,s13,40960
qm 84,104,244
v_fma_f32 v16,v16,v15,v15
qt 104,4,28672
qw 5
qm 88,108,172
v_rcp_f32 v16,v16
qt 106,4,29184
s_add_u32 s14,s17,3539200
qm 92,108,244
v_mul_f32 v52,v52,v16
qt 108,5,28672
qw 5
qm 80,96,176
v_mul_f32 v16,v53,v53
qt 110,5,29184
ql 6,40,0
qm 84,96,248
v_fma_f32 v16,v16,v13,v11
qt 96,2,8192
qw 5
qm 88,100,176
v_mul_f32 v16,v16,v53
qt 98,2,8704
ql 7,40,1024
qm 92,100,248
v_exp_f32 v16,v16
qt 100,3,8192
qw 5
qm 80,104,180
v_fma_f32 v16,v16,v15,v15
qt 102,3,8704
s_add_u32 m0,s13,81920
qm 84,104,252
v_rcp_f32 v16,v16
qt 104,2,12288
qw 5
qm 88,108,180
s_waitcnt vmcnt(6)
s_barrier
v_mul_f32 v53,v53,v16
qt 106,2,12800
s_add_u32 s14,s17,4129024
qm 92,108,252
qt 108,3,12288
qw 5
qz 64,96,112
v_mul_f32 v16,v54,v54
qt 110,3,12800
ql 6,40,0
qz 68,96,184
v_fma_f32 v16,v16,v13,v11
qt 96,4,32768
qw 5
qz 72,100,112
v_mul_f32 v16,v16,v54
qt 98,4,33280
ql 7,40,1024
qz 76,100,184
v_exp_f32 v16,v16
qt 100,5,32768
qw 5
qm 64,104,116
v_fma_f32 v16,v16,v15,v15
qt 102,5,33280
s_add_u32 m0,s13,90112
qm 68,104,188
v_rcp_f32 v16,v16
qt 104,4,36864
qw 5
qm 72,108,116
v_mul_f32 v54,v54,v16
qt 106,4,37376
s_add_u32 s14,s17,4718848
qm 76,108,188
v_mul_f32 v16,v55,v55
qt 108,5,36864
qw 5
qm 64,96,120
v_fma_f32 v16,v16,v13,v11
qt 110,5,37376
ql 6,40,0
qm 68,96,192
v_mul_f32 v16,v16,v55
qt 96,4,40960
qw 5
qm 72,100,120
v_exp_f32 v16,v16
qt 98,4,41472
ql 7,40,1024
qm 76,100,192
v_fma_f32 v16,v16,v15,v15
qt 100,5,40960
qw 5
qm 64,104,124
v_rcp_f32 v16,v16
qt 102,5,41472
s_add_u32 m0,s13,49152
qm 68,104,196
v_mul_f32 v55,v55,v16
qt 104,4,45056
qw 5
qm 72,108,124
v_mul_f32 v16,v60,v60
qt 106,4,45568
s_add_u32 s14,s17,256
qm 76,108,196
v_fma_f32 v16,v16,v13,v11
qt 108,5,45056
qw 5
qm 64,96,128
v_mul_f32 v16,v16,v60
qt 110,5,45568
ql 6,44,0
qm 68,96,200
v_exp_f32 v16,v16
qt 96,2,16384
qw 5
qm 72,100,128
v_fma_f32 v16,v16,v15,v15
qt 98,2,16896
ql 7,44,1024
qm 76,100,200
v_rcp_f32 v16,v16
qt 100,3,16384
qw 5
qm 64,104,132
v_mul_f32 v60,v60,v16
qt 102,3,16896
s_add_u32 m0,s13,98304
qm 68,104,204
v_mul_f32 v16,v61,v61
qt 104,2,20480
qw 5
qm 72,108,132
s_waitcnt vmcnt(6)
s_barrier
v_fma_f32 v16,v16,v13,v11
qt 106,2,20992
s_add_u32 s14,s17,590080
qm 76,108,204
v_mul_f32 v16,v16,v61
qt 108,3,20480
qw 5
qm 64,96,136
v_exp_f32 v16,v16
qt 110,3,20992
ql 6,44,0
qm 68,96,208
v_fma_f32 v16,v16,v15,v15
qt 96,2,24576
qw 5
qm 72,100,136
v_rcp_f32 v16,v16
qt 98,2,25088
ql 7,44,1024
qm 76,100,208
v_mul_f32 v61,v61,v16
qt 100,3,24576
qw 5
qm 64,104,140
v_mul_f32 v16,v62,v62
qt 102,3,25088
s_add_u32 m0,s13,57344
qm 68,104,212
v_fma_f32 v16,v16,v13,v11
qt 104,2,28672
qw 5
qm 72,108,140
v_mul_f32 v16,v16,v62
qt 106,2,29184
s_add_u32 s14,s17,1179904
qm 76,108,212
v_exp_f32 v16,v16
qt 108,3,28672
qw 5
qm 64,96,144
v_fma_f32 v16,v16,v15,v15
qt 110,3,29184
ql 6,44,0
qm 68,96,216
v_rcp_f32 v16,v16
qt 96,2,32768
qw 5
qm 72,100,144
v_mul_f32 v62,v62,v16
qt 98,2,33280
ql 7,44,1024
qm 76,100,216
v_mul_f32 v16,v63,v63
qt 100,3,32768
qw 5
qm 64,104,148
v_fma_f32 v16,v16,v13,v11
qt 102,3,33280
s_add_u32 m0,s13,65536
qm 68,104,220
v_mul_f32 v16,v16,v63
qt 104,2,36864
qw 5
qm 72,108,148
v_exp_f32 v16,v16
qt 106,2,37376
s_add_u32 s14,s17,1769728
qm 76,108,220
v_fma_f32 v16,v16,v15,v15
qt 108,3,36864
qw 5
qm 64,96,152
v_rcp_f32 v16,v16
qt 110,3,37376
ql 6,44,0
qm 68,96,224
v_mul_f32 v63,v63,v16
qt 96,2,40960
qw 5
qm 72,100,152
v_mul_f32 v48,v48,v80
qt 98,2,41472
ql 7,44,1024
qm 76,100,224
v_mul_f32 v49,v49,v81
qt 100,3,40960
qw 5
qm 64,104,156
v_cvt_pk_bf16_f32 v48,v48,v49
qt 102,3,41472
s_add_u32 m0,s13,73728
qm 68,104,228
v_mul_f32 v50,v50,v82
qt 104,2,45056
qw 5
qm 72,108,156
s_waitcnt vmcnt(6)
s_barrier
v_mul_f32 v51,v51,v83
qt 106,2,45568
s_add_u32 s14,s17,2359552
qm 76,108,228
v_cvt_pk_bf16_f32 v49,v50,v51
qt 108,3,45056
qw 5
qm 64,96,160
v_mul_f32 v56,v56,v88
qt 110,3,45568
ql 6,44,0
qm 68,96,232
v_mul_f32 v57,v57,v89
qt 96,4,16384
qw 5
qm 72,100,160
v_cvt_pk_bf16_f32 v50,v56,v57
qt 98,4,16896
ql 7,44,1024
qm 76,100,232
v_mul_f32 v58,v58,v90
qt 100,5,16384
qw 5
qm 64,104,164
v_mul_f32 v59,v59,v91
qt 102,5,16896
s_mov_b32 m0,s13
qm 68,104,236
v_cvt_pk_bf16_f32 v51,v58,v59
qt 104,4,20480
qw 5
qm 72,108,164
ds_write_b128 v19,v[48:51] offset:8192
v_mul_f32 v52,v52,v84
qt 106,4,20992
s_add_u32 s14,s17,2949376
qm 76,108,236
s_add_u32 s15,s76,16384
buffer_store_dwordx4 v[48:51],v18,s[88:91],s15 offen offset:0 sc1
v_mul_f32 v53,v53,v85
qt 108,5,20480
qw 6
qm 64,96,168
v_cvt_pk_bf16_f32 v52,v52,v53
qt 110,5,20992
ql 6,44,0
qm 68,96,240
v_mul_f32 v54,v54,v86
qt 96,4,24576
qw 6
qm 72,100,168
v_mul_f32 v55,v55,v87
qt 98,4,25088
ql 7,44,1024
qm 76,100,240
v_cvt_pk_bf16_f32 v53,v54,v55
qt 100,5,24576
qw 5
qm 64,104,172
v_mul_f32 v60,v60,v92
qt 102,5,25088
s_add_u32 m0,s13,8192
qm 68,104,244
v_mul_f32 v61,v61,v93
qt 104,4,28672
qw 5
qm 72,108,172
v_cvt_pk_bf16_f32 v54,v60,v61
qt 106,4,29184
s_add_u32 s14,s17,3539200
qm 76,108,244
v_mul_f32 v62,v62,v94
qt 108,5,28672
qw 5
qm 64,96,176
v_mul_f32 v63,v63,v95
qt 110,5,29184
ql 6,44,0
qm 68,96,248
v_cvt_pk_bf16_f32 v55,v62,v63
qt 96,2,49152
qw 5
qm 72,100,176
ds_write_b128 v19,v[52:55] offset:9216
qt 98,2,49664
ql 7,44,1024
qm 76,100,248
s_add_u32 s15,s76,16384
buffer_store_dwordx4 v[52:55],v18,s[88:91],s15 offen offset:1024 sc1
qt 100,3,49152
qw 6
qm 64,104,180
qt 102,3,49664
s_add_u32 m0,s13,81920
qm 68,104,252
qt 104,2,53248
qw 6
qm 72,108,180
s_waitcnt vmcnt(8) lgkmcnt(4)
s_barrier
qt 106,2,53760
s_add_u32 s14,s17,4129024
qm 76,108,252
qt 108,3,53248
qw 5
qz 80,96,112
qt 110,3,53760
ql 6,44,0
qz 84,96,184
v_mul_f32 v16,v64,v64
qt 96,4,32768
qw 5
qz 88,100,112
v_fma_f32 v16,v16,v12,v10
qt 98,4,33280
ql 7,44,1024
qz 92,100,184
v_mul_f32 v16,v16,v64
qt 100,5,32768
qw 5
qm 80,104,116
v_exp_f32 v16,v16
qt 102,5,33280
s_add_u32 m0,s13,90112
qm 84,104,188
v_fma_f32 v16,v16,v14,v14
qt 104,4,36864
qw 5
qm 88,108,116
v_rcp_f32 v16,v16
qt 106,4,37376
s_add_u32 s14,s17,4718848
qm 92,108,188
v_mul_f32 v64,v64,v16
qt 108,5,36864
qw 5
qm 80,96,120
v_mul_f32 v16,v65,v65
qt 110,5,37376
ql 6,44,0
qm 84,96,192
v_fma_f32 v16,v16,v12,v10
qt 96,2,57344
qw 5
qm 88,100,120
v_mul_f32 v16,v16,v65
qt 98,2,57856
ql 7,44,1024
qm 92,100,192
v_exp_f32 v16,v16
qt 100,3,57344
qw 5
qm 80,104,124
v_fma_f32 v16,v16,v14,v14
qt 102,3,57856
s_add_u32 m0,s13,16384
qm 84,104,196
v_rcp_f32 v16,v16
qt 104,2,61440
qw 5
qm 88,108,124
v_mul_f32 v65,v65,v16
qt 106,2,61952
s_add_u32 s14,s17,384
qm 92,108,196
v_mul_f32 v16,v66,v66
qt 108,3,61440
qw 5
qm 80,96,128
v_fma_f32 v16,v16,v12,v10
qt 110,3,61952
ql 6,40,0
qm 84,96,200
v_mul_f32 v16,v16,v66
qt 96,4,0
qw 5
qm 88,100,128
v_exp_f32 v16,v16
qt 98,4,512
ql 7,40,1024
qm 92,100,200
v_fma_f32 v16,v16,v14,v14
qt 100,5,0
qw 5
qm 80,104,132
v_rcp_f32 v16,v16
qt 102,5,512
s_add_u32 m0,s13,98304
qm 84,104,204
v_mul_f32 v66,v66,v16
qt 104,4,4096
qw 5
qm 88,108,132
s_waitcnt vmcnt(7)
s_barrier
v_mul_f32 v16,v67,v67
qt 106,4,4608
s_add_u32 s14,s17,590208
qm 92,108,204
v_fma_f32 v16,v16,v12,v10
qt 108,5,4096
qw 5
qm 80,96,136
v_mul_f32 v16,v16,v67
qt 110,5,4608
ql 6,40,0
qm 84,96,208
v_exp_f32 v16,v16
qt 96,4,8192
qw 5
qm 88,100,136
v_fma_f32 v16,v16,v14,v14
qt 98,4,8704
ql 7,40,1024
qm 92,100,208
v_rcp_f32 v16,v16
qt 100,5,8192
qw 5
qm 80,104,140
v_mul_f32 v67,v67,v16
qt 102,5,8704
s_add_u32 m0,s13,24576
qm 84,104,212
v_mul_f32 v16,v72,v72
qt 104,4,12288
qw 5
qm 88,108,140
v_fma_f32 v16,v16,v12,v10
qt 106,4,12800
s_add_u32 s14,s17,1180032
qm 92,108,212
v_mul_f32 v16,v16,v72
qt 108,5,12288
qw 5
qm 80,96,144
v_exp_f32 v16,v16
qt 110,5,12800
ql 6,40,0
qm 84,96,216
v_fma_f32 v16,v16,v14,v14
qt 96,2,0
qw 5
qm 88,100,144
v_rcp_f32 v16,v16
qt 98,2,512
ql 7,40,1024
qm 92,100,216
v_mul_f32 v72,v72,v16
qt 100,3,0
qw 5
qm 80,104,148
v_mul_f32 v16,v73,v73
qt 102,3,512
s_add_u32 m0,s13,32768
qm 84,104,220
v_fma_f32 v16,v16,v12,v10
qt 104,2,4096
qw 5
qm 88,108,148
v_mul_f32 v16,v16,v73
qt 106,2,4608
s_add_u32 s14,s17,1769856
qm 92,108,220
v_exp_f32 v16,v16
qt 108,3,4096
qw 5
qm 80,96,152
v_fma_f32 v16,v16,v14,v14
qt 110,3,4608
ql 6,40,0
qm 84,96,224
v_rcp_f32 v16,v16
qt 96,2,8192
qw 5
qm 88,100,152
v_mul_f32 v73,v73,v16
qt 98,2,8704
ql 7,40,1024
qm 92,100,224
v_mul_f32 v16,v74,v74
qt 100,3,8192
qw 5
qm 80,104,156
v_fma_f32 v16,v16,v12,v10
qt 102,3,8704
s_add_u32 m0,s13,40960
qm 84,104,228
v_mul_f32 v16,v16,v74
qt 104,2,12288
qw 5
qm 88,108,156
s_waitcnt vmcnt(6)
s_barrier
v_exp_f32 v16,v16
qt 106,2,12800
s_add_u32 s14,s17,2359680
qm 92,108,228
v_fma_f32 v16,v16,v14,v14
qt 108,3,12288
qw 5
qm 80,96,160
v_rcp_f32 v16,v16
qt 110,3,12800
ql 6,40,0
qm 84,96,232
v_mul_f32 v74,v74,v16
qt 96,4,16384
qw 5
qm 88,100,160
v_mul_f32 v16,v75,v75
qt 98,4,16896
ql 7,40,1024
qm 92,100,232
v_fma_f32 v16,v16,v12,v10
qt 100,5,16384
qw 5
qm 80,104,164
v_mul_f32 v16,v16,v75
qt 102,5,16896
s_add_u32 m0,s13,49152
qm 84,104,236
v_exp_f32 v16,v16
qt 104,4,20480
qw 5
qm 88,108,164
v_fma_f32 v16,v16,v14,v14
qt 106,4,20992
s_add_u32 s14,s17,2949504
qm 92,108,236
v_rcp_f32 v16,v16
qt 108,5,20480
qw 5
qm 80,96,168
v_mul_f32 v75,v75,v16
qt 110,5,20992
ql 6,40,0
qm 84,96,240
v_mul_f32 v16,v68,v68
qt 96,4,24576
qw 5
qm 88,100,168
v_fma_f32 v16,v16,v13,v11
qt 98,4,25088
ql 7,40,1024
qm 92,100,240
v_mul_f32 v16,v16,v68
qt 100,5,24576
qw 5
qm 80,104,172
v_exp_f32 v16,v16
qt 102,5,25088
s_add_u32 m0,s13,57344
qm 84,104,244
v_fma_f32 v16,v16,v15,v15
qt 104,4,28672
qw 5
qm 88,108,172
v_rcp_f32 v16,v16
qt 106,4,29184
s_add_u32 s14,s17,3539328
qm 92,108,244
v_mul_f32 v68,v68,v16
qt 108,5,28672
qw 5
qm 80,96,176
v_mul_f32 v16,v69,v69
qt 110,5,29184
ql 6,40,0
qm 84,96,248
v_fma_f32 v16,v16,v13,v11
qt 96,2,16384
qw 5
qm 88,100,176
v_mul_f32 v16,v16,v69
qt 98,2,16896
ql 7,40,1024
qm 92,100,248
v_exp_f32 v16,v16
qt 100,3,16384
qw 5
qm 80,104,180
v_fma_f32 v16,v16,v15,v15
qt 102,3,16896
s_add_u32 m0,s13,81920
qm 84,104,252
v_rcp_f32 v16,v16
qt 104,2,20480
qw 5
qm 88,108,180
s_waitcnt vmcnt(6)
s_barrier
v_mul_f32 v69,v69,v16
qt 106,2,20992
s_add_u32 s14,s17,4129152
qm 92,108,252
qt 108,3,20480
qw 5
qz 48,96,112
v_mul_f32 v16,v70,v70
qt 110,3,20992
ql 6,40,0
qz 52,96,184
v_fma_f32 v16,v16,v13,v11
v_bfe_u32 v44,v0,2,2
qt 96,4,32768
qw 5
qz 56,100,112
v_mul_f32 v16,v16,v70
qt 98,4,33280
ql 7,40,1024
qz 60,100,184
v_exp_f32 v16,v16
qt 100,5,32768
qw 5
qm 48,104,116
v_fma_f32 v16,v16,v15,v15
qt 102,5,33280
s_add_u32 m0,s13,90112
qm 52,104,188
v_rcp_f32 v16,v16
s_and_b32 s33,s8,3
s_lshl_b32 s33,s33,2
s_add_u32 s33,s33,s7
s_lshl_b32 s33,s33,3
s_lshl_b32 s33,s33,2
s_add_u32 s33,s33,122880
v_lshl_add_u32 v45,v44,2,s33
qt 104,4,36864
qw 5
qm 56,108,116
v_mul_f32 v70,v70,v16
qt 106,4,37376
s_add_u32 s14,s17,4718976
qm 60,108,188
v_mul_f32 v16,v71,v71
qt 108,5,36864
qw 5
qm 48,96,120
v_fma_f32 v16,v16,v13,v11
qt 110,5,37376
ql 6,40,0
qm 52,96,192
v_mul_f32 v16,v16,v71
qt 96,2,24576
qw 5
qm 56,100,120
v_exp_f32 v16,v16
ds_read_b32 v40,v45 offset:0
qt 98,2,25088
ql 7,40,1024
qm 60,100,192
v_fma_f32 v16,v16,v15,v15
qt 100,3,24576
qw 6
qm 48,104,124
v_rcp_f32 v16,v16
qt 102,3,25088
s_add_u32 m0,s13,65536
qm 52,104,196
v_mul_f32 v71,v71,v16
qt 104,2,28672
qw 6
qm 56,108,124
v_mul_f32 v16,v76,v76
ds_read_b32 v41,v45 offset:512
qt 106,2,29184
s_add_u32 s14,s17,384
qm 60,108,196
v_fma_f32 v16,v16,v13,v11
qt 108,3,28672
qw 6
qm 48,96,128
v_mul_f32 v16,v16,v76
qt 110,3,29184
ql 6,44,0
qm 52,96,200
v_exp_f32 v16,v16
qt 96,2,32768
qw 6
qm 56,100,128
v_fma_f32 v16,v16,v15,v15
qt 98,2,33280
ql 7,44,1024
qm 60,100,200
v_rcp_f32 v16,v16
qt 100,3,32768
qw 5
qm 48,104,132
v_mul_f32 v76,v76,v16
qt 102,3,33280
s_add_u32 m0,s13,98304
qm 52,104,204
v_mul_f32 v16,v77,v77
qt 104,2,36864
qw 5
qm 56,108,132
s_waitcnt vmcnt(6)
s_barrier
v_fma_f32 v16,v16,v13,v11
qt 106,2,37376
s_add_u32 s14,s17,590208
qm 60,108,204
v_mul_f32 v16,v16,v77
qt 108,3,36864
qw 5
qm 48,96,136
v_exp_f32 v16,v16
qt 110,3,37376
ql 6,44,0
qm 52,96,208
v_fma_f32 v16,v16,v15,v15
qt 96,2,40960
qw 5
qm 56,100,136
v_rcp_f32 v16,v16
qt 98,2,41472
ql 7,44,1024
qm 60,100,208
v_mul_f32 v77,v77,v16
qt 100,3,40960
qw 5
qm 48,104,140
v_mul_f32 v16,v78,v78
qt 102,3,41472
s_add_u32 m0,s13,73728
qm 52,104,212
v_fma_f32 v16,v16,v13,v11
qt 104,2,45056
qw 5
qm 56,108,140
v_mul_f32 v16,v16,v78
qt 106,2,45568
s_add_u32 s14,s17,1180032
qm 60,108,212
v_exp_f32 v16,v16
qt 108,3,45056
qw 5
qm 48,96,144
v_fma_f32 v16,v16,v15,v15
qt 110,3,45568
ql 6,44,0
qm 52,96,216
v_rcp_f32 v16,v16
qt 96,2,49152
qw 5
qm 56,100,144
v_mul_f32 v78,v78,v16
qt 98,2,49664
ql 7,44,1024
qm 60,100,216
v_mul_f32 v16,v79,v79
qt 100,3,49152
qw 5
qm 48,104,148
v_fma_f32 v16,v16,v13,v11
qt 102,3,49664
s_mov_b32 m0,s13
qm 52,104,220
v_mul_f32 v16,v16,v79
qt 104,2,53248
qw 5
qm 56,108,148
v_exp_f32 v16,v16
qt 106,2,53760
s_add_u32 s14,s17,1769856
qm 60,108,220
v_fma_f32 v16,v16,v15,v15
qt 108,3,53248
qw 5
qm 48,96,152
v_rcp_f32 v16,v16
qt 110,3,53760
ql 6,44,0
qm 52,96,224
v_mul_f32 v79,v79,v16
qt 96,2,57344
qw 5
qm 56,100,152
v_mul_f32 v64,v64,v80
qt 98,2,57856
ql 7,44,1024
qm 60,100,224
v_mul_f32 v65,v65,v81
qt 100,3,57344
qw 5
qm 48,104,156
v_cvt_pk_bf16_f32 v64,v64,v65
qt 102,3,57856
s_add_u32 m0,s13,8192
qm 52,104,228
v_mul_f32 v66,v66,v82
qt 104,2,61440
qw 5
qm 56,108,156
s_waitcnt vmcnt(6)
s_barrier
v_mul_f32 v67,v67,v83
qt 106,2,61952
s_add_u32 s14,s17,2359680
qm 60,108,228
v_cvt_pk_bf16_f32 v65,v66,v67
qt 108,3,61440
qw 5
qm 48,96,160
v_mul_f32 v72,v72,v88
qt 110,3,61952
ql 6,44,0
qm 52,96,232
v_mul_f32 v73,v73,v89
qt 96,4,16384
qw 5
qm 56,100,160
v_cvt_pk_bf16_f32 v66,v72,v73
qt 98,4,16896
ql 7,44,1024
qm 60,100,232
v_mul_f32 v74,v74,v90
qt 100,5,16384
qw 5
qm 48,104,164
v_mul_f32 v75,v75,v91
qt 102,5,16896
s_add_u32 m0,s13,16384
qm 52,104,236
v_cvt_pk_bf16_f32 v67,v74,v75
qt 104,4,20480
qw 5
qm 56,108,164
ds_write_b128 v18,v[64:67] offset:32768
v_mul_f32 v68,v68,v84
qt 106,4,20992
s_add_u32 s14,s17,2949504
qm 60,108,236
s_add_u32 s15,s76,32768
buffer_store_dwordx4 v[64:67],v18,s[88:91],s15 offen offset:0 sc1
v_mul_f32 v69,v69,v85
qt 108,5,20480
qw 6
qm 48,96,168
v_cvt_pk_bf16_f32 v68,v68,v69
qt 110,5,20992
ql 6,44,0
qm 52,96,240
v_mul_f32 v70,v70,v86
qt 96,4,24576
qw 6
qm 56,100,168
v_mul_f32 v71,v71,v87
qt 98,4,25088
ql 7,44,1024
qm 60,100,240
v_cvt_pk_bf16_f32 v69,v70,v71
qt 100,5,24576
qw 5
qm 48,104,172
v_mul_f32 v76,v76,v92
qt 102,5,25088
s_add_u32 m0,s13,24576
qm 52,104,244
v_mul_f32 v77,v77,v93
qt 104,4,28672
qw 5
qm 56,108,172
v_cvt_pk_bf16_f32 v70,v76,v77
qt 106,4,29184
s_add_u32 s14,s17,3539328
qm 60,108,244
)ASM"
R"ASM(v_mul_f32 v78,v78,v94
qt 108,5,28672
qw 5
qm 48,96,176
v_mul_f32 v79,v79,v95
qt 110,5,29184
ql 6,44,0
qm 52,96,248
v_cvt_pk_bf16_f32 v71,v78,v79
qt 96,4,0
qw 5
qm 56,100,176
ds_write_b128 v18,v[68:71] offset:33792
qt 98,4,512
ql 7,44,1024
qm 60,100,248
s_add_u32 s15,s76,32768
buffer_store_dwordx4 v[68:71],v18,s[88:91],s15 offen offset:1024 sc1
qt 100,5,0
qw 6
qm 48,104,180
qt 102,5,512
s_add_u32 m0,s13,81920
qm 52,104,252
qt 104,4,4096
qw 6
qm 56,108,180
s_waitcnt vmcnt(8) lgkmcnt(4)
s_barrier
qt 106,4,4608
s_add_u32 s14,s17,4129152
qm 60,108,252
qt 108,5,4096
qw 5
qz 80,96,112
qt 110,5,4608
ql 6,44,0
qz 84,96,184
v_mul_f32 v16,v48,v48
qt 96,4,32768
qw 5
qz 88,100,112
v_fma_f32 v16,v16,v12,v10
qt 98,4,33280
ql 7,44,1024
qz 92,100,184
v_mul_f32 v16,v16,v48
qt 100,5,32768
qw 5
qm 80,104,116
v_exp_f32 v16,v16
qt 102,5,33280
s_add_u32 m0,s13,90112
qm 84,104,188
v_fma_f32 v16,v16,v14,v14
qt 104,4,36864
qw 5
qm 88,108,116
v_rcp_f32 v16,v16
qt 106,4,37376
s_add_u32 s14,s17,4718976
qm 92,108,188
v_mul_f32 v48,v48,v16
qt 108,5,36864
qw 5
qm 80,96,120
v_mul_f32 v16,v49,v49
qt 110,5,37376
ql 6,44,0
qm 84,96,192
v_fma_f32 v16,v16,v12,v10
qt 96,4,8192
qw 5
qm 88,100,120
v_mul_f32 v16,v16,v49
qt 98,4,8704
ql 7,44,1024
qm 92,100,192
v_exp_f32 v16,v16
qt 100,5,8192
qw 5
qm 80,104,124
v_fma_f32 v16,v16,v14,v14
qt 102,5,8704
s_add_u32 m0,s13,32768
qm 84,104,196
v_rcp_f32 v16,v16
qt 104,4,12288
qw 5
qm 88,108,124
v_mul_f32 v49,v49,v16
qt 106,4,12800
s_add_u32 s14,s17,512
qm 92,108,196
v_mul_f32 v16,v50,v50
qt 108,5,12288
qw 5
qm 80,96,128
v_fma_f32 v16,v16,v12,v10
qt 110,5,12800
ql 6,40,0
qm 84,96,200
v_mul_f32 v16,v16,v50
qt 96,2,0
qw 5
qm 88,100,128
v_exp_f32 v16,v16
qt 98,2,512
ql 7,40,1024
qm 92,100,200
v_fma_f32 v16,v16,v14,v14
qt 100,3,0
qw 5
qm 80,104,132
v_rcp_f32 v16,v16
qt 102,3,512
s_add_u32 m0,s13,98304
qm 84,104,204
v_mul_f32 v50,v50,v16
qt 104,2,4096
qw 5
qm 88,108,132
s_waitcnt vmcnt(7)
s_barrier
v_mul_f32 v16,v51,v51
qt 106,2,4608
s_add_u32 s14,s17,590336
qm 92,108,204
v_fma_f32 v16,v16,v12,v10
qt 108,3,4096
qw 5
qm 80,96,136
v_mul_f32 v16,v16,v51
qt 110,3,4608
ql 6,40,0
qm 84,96,208
v_exp_f32 v16,v16
qt 96,2,8192
qw 5
qm 88,100,136
v_fma_f32 v16,v16,v14,v14
qt 98,2,8704
ql 7,40,1024
qm 92,100,208
v_rcp_f32 v16,v16
qt 100,3,8192
qw 5
qm 80,104,140
v_mul_f32 v51,v51,v16
qt 102,3,8704
s_add_u32 m0,s13,40960
qm 84,104,212
v_mul_f32 v16,v56,v56
qt 104,2,12288
qw 5
qm 88,108,140
v_fma_f32 v16,v16,v12,v10
qt 106,2,12800
s_add_u32 s14,s17,1180160
qm 92,108,212
v_mul_f32 v16,v16,v56
qt 108,3,12288
qw 5
qm 80,96,144
v_exp_f32 v16,v16
qt 110,3,12800
ql 6,40,0
qm 84,96,216
v_fma_f32 v16,v16,v14,v14
qt 96,2,16384
qw 5
qm 88,100,144
v_rcp_f32 v16,v16
qt 98,2,16896
ql 7,40,1024
qm 92,100,216
v_mul_f32 v56,v56,v16
qt 100,3,16384
qw 5
qm 80,104,148
v_mul_f32 v16,v57,v57
qt 102,3,16896
s_add_u32 m0,s13,49152
qm 84,104,220
v_fma_f32 v16,v16,v12,v10
qt 104,2,20480
qw 5
qm 88,108,148
v_mul_f32 v16,v16,v57
qt 106,2,20992
s_add_u32 s14,s17,1769984
qm 92,108,220
v_exp_f32 v16,v16
qt 108,3,20480
qw 5
qm 80,96,152
v_fma_f32 v16,v16,v14,v14
qt 110,3,20992
ql 6,40,0
qm 84,96,224
v_rcp_f32 v16,v16
qt 96,2,24576
qw 5
qm 88,100,152
v_mul_f32 v57,v57,v16
qt 98,2,25088
ql 7,40,1024
qm 92,100,224
v_mul_f32 v16,v58,v58
qt 100,3,24576
qw 5
qm 80,104,156
v_fma_f32 v16,v16,v12,v10
qt 102,3,25088
s_add_u32 m0,s13,57344
qm 84,104,228
v_mul_f32 v16,v16,v58
qt 104,2,28672
qw 5
qm 88,108,156
s_waitcnt vmcnt(6)
s_barrier
v_exp_f32 v16,v16
qt 106,2,29184
s_add_u32 s14,s17,2359808
qm 92,108,228
v_fma_f32 v16,v16,v14,v14
qt 108,3,28672
qw 5
qm 80,96,160
v_rcp_f32 v16,v16
qt 110,3,29184
ql 6,40,0
qm 84,96,232
v_mul_f32 v58,v58,v16
qt 96,4,16384
qw 5
qm 88,100,160
v_mul_f32 v16,v59,v59
qt 98,4,16896
ql 7,40,1024
qm 92,100,232
v_fma_f32 v16,v16,v12,v10
qt 100,5,16384
qw 5
qm 80,104,164
v_mul_f32 v16,v16,v59
qt 102,5,16896
s_add_u32 m0,s13,65536
qm 84,104,236
v_exp_f32 v16,v16
qt 104,4,20480
qw 5
qm 88,108,164
v_fma_f32 v16,v16,v14,v14
qt 106,4,20992
s_add_u32 s14,s17,2949632
qm 92,108,236
v_rcp_f32 v16,v16
qt 108,5,20480
qw 5
qm 80,96,168
v_mul_f32 v59,v59,v16
qt 110,5,20992
ql 6,40,0
qm 84,96,240
v_mul_f32 v16,v52,v52
qt 96,4,24576
qw 5
qm 88,100,168
v_fma_f32 v16,v16,v13,v11
qt 98,4,25088
ql 7,40,1024
qm 92,100,240
v_mul_f32 v16,v16,v52
qt 100,5,24576
qw 5
qm 80,104,172
v_exp_f32 v16,v16
qt 102,5,25088
s_add_u32 m0,s13,73728
qm 84,104,244
v_fma_f32 v16,v16,v15,v15
qt 104,4,28672
qw 5
qm 88,108,172
v_rcp_f32 v16,v16
qt 106,4,29184
s_add_u32 s14,s17,3539456
qm 92,108,244
v_mul_f32 v52,v52,v16
qt 108,5,28672
qw 5
qm 80,96,176
v_mul_f32 v16,v53,v53
qt 110,5,29184
ql 6,40,0
qm 84,96,248
v_fma_f32 v16,v16,v13,v11
qt 96,2,32768
qw 5
qm 88,100,176
v_mul_f32 v16,v16,v53
qt 98,2,33280
ql 7,40,1024
qm 92,100,248
v_exp_f32 v16,v16
qt 100,3,32768
qw 5
qm 80,104,180
v_fma_f32 v16,v16,v15,v15
qt 102,3,33280
s_add_u32 m0,s13,81920
qm 84,104,252
v_rcp_f32 v16,v16
qt 104,2,36864
qw 5
qm 88,108,180
s_waitcnt vmcnt(6)
s_barrier
v_mul_f32 v53,v53,v16
qt 106,2,37376
s_add_u32 s14,s17,4129280
qm 92,108,252
qt 108,3,36864
qw 5
qz 64,96,112
v_mul_f32 v16,v54,v54
qt 110,3,37376
ql 6,40,0
qz 68,96,184
v_fma_f32 v16,v16,v13,v11
qt 96,4,32768
qw 5
qz 72,100,112
v_mul_f32 v16,v16,v54
v_mov_b32 v38,s16
v_and_b32 v37,48,v0
qt 98,4,33280
ql 7,40,1024
qz 76,100,184
v_exp_f32 v16,v16
v_lshlrev_b32 v37,24,v37
qt 100,5,32768
qw 5
qm 64,104,116
v_fma_f32 v16,v16,v15,v15
v_lshl_or_b32 v37,v0,7,v37
qt 102,5,33280
s_mov_b32 m0,s13
qm 68,104,188
v_rcp_f32 v16,v16
s_mul_i32 s72,s10,2048
qt 104,4,36864
qw 5
qm 72,108,116
v_mul_f32 v54,v54,v16
s_add_u32 s72,s72,8192
qt 106,4,37376
s_add_u32 s14,s17,4719104
qm 76,108,188
v_mul_f32 v16,v55,v55
s_and_b32 s30,s8,3
qt 108,5,36864
qw 5
qm 64,96,120
v_fma_f32 v16,v16,v13,v11
s_lshl_b32 s30,s30,4
qt 110,5,37376
ql 6,40,0
qm 68,96,192
v_mul_f32 v16,v16,v55
s_and_b32 s31,s8,4
qt 96,2,40960
qw 5
qm 72,100,120
v_exp_f32 v16,v16
s_add_u32 s30,s30,s31
qt 98,2,41472
ql 7,40,1024
qm 76,100,192
v_fma_f32 v16,v16,v15,v15
s_add_u32 s68,s30,s72
qt 100,3,40960
qw 5
qm 64,104,124
v_rcp_f32 v16,v16
s_lshl_b32 s31,s8,8
qt 102,3,41472
s_add_u32 m0,s13,8192
qm 68,104,196
v_mul_f32 v55,v55,v16
s_add_u32 s78,s68,s31
qt 104,2,45056
qw 5
qm 72,108,124
v_mul_f32 v16,v60,v60
s_add_u32 s78,s78,8
qt 106,2,45568
s_add_u32 s14,s17,512
qm 76,108,196
v_fma_f32 v16,v16,v13,v11
s_add_u32 s68,s68,0
s_cmp_eq_u32 s6,4
s_cselect_b32 s68,s68,0x7ff00000
qt 108,3,45056
qw 5
qm 64,96,128
v_mul_f32 v16,v16,v60
s_add_u32 s72,s72,s31
qt 110,3,45568
ql 6,44,0
qm 68,96,200
v_exp_f32 v16,v16
s_add_u32 s72,s72,0
qt 96,2,49152
qw 5
qm 72,100,128
v_fma_f32 v16,v16,v15,v15
s_add_u32 s74,s16,8
qt 98,2,49664
ql 7,44,1024
qm 76,100,200
v_rcp_f32 v16,v16
v_bfe_u32 v20,v0,3,2
qt 100,3,49152
qw 5
qm 64,104,132
v_mul_f32 v60,v60,v16
v_lshrrev_b32 v21,1,v0
qt 102,3,49664
s_add_u32 m0,s13,16384
qm 68,104,204
v_mul_f32 v16,v61,v61
v_and_or_b32 v20,v21,16,v20
qt 104,2,53248
qw 5
qm 72,108,132
s_waitcnt vmcnt(6)
s_barrier
v_fma_f32 v16,v16,v13,v11
v_mul_u32_u24 v20,1152,v20
qt 106,2,53760
s_add_u32 s14,s17,590336
qm 76,108,204
v_mul_f32 v16,v16,v61
v_lshrrev_b32 v21,3,v0
qt 108,3,53248
qw 5
qm 64,96,136
v_exp_f32 v16,v16
v_and_b32 v21,2,v21
qt 110,3,53760
ql 6,44,0
qm 68,96,208
v_fma_f32 v16,v16,v15,v15
v_xor_b32 v21,v21,v0
qt 96,2,57344
qw 5
qm 72,100,136
v_rcp_f32 v16,v16
v_and_b32 v21,7,v21
qt 98,2,57856
ql 7,44,1024
qm 76,100,208
v_mul_f32 v61,v61,v16
v_lshl_add_u32 v20,v21,4,v20
qt 100,3,57344
s_add_u32 m0,s13,24576
s_add_u32 s14,s17,1180160
qw 5
qm 64,104,140
v_mul_f32 v16,v62,v62
v_xor_b32 v21,64,v20
qt 102,3,57856
ql 6,44,0
qm 68,104,212
v_fma_f32 v16,v16,v13,v11
v_add_u32 v21,3584,v21
v_and_b32 v42,15,v0
qt 104,2,61440
qw 5
qm 72,108,140
v_mul_f32 v16,v16,v62
v_lshrrev_b32 v43,5,v0
qt 106,2,61952
ql 7,44,1024
qm 76,108,212
v_exp_f32 v16,v16
v_bfe_u32 v25,v0,4,1
qt 108,3,61440
s_add_u32 m0,s13,32768
s_add_u32 s14,s17,1769984
qw 5
qm 64,96,144
v_fma_f32 v16,v16,v15,v15
v_lshlrev_b32 v25,3,v25
qt 110,3,61952
ql 6,44,0
qm 68,96,216
v_rcp_f32 v16,v16
v_add_u32 v25,0,v25
qt 96,4,0
qw 5
qm 72,100,144
v_mul_f32 v62,v62,v16
v_lshl_add_u32 v24,v42,7,v25
qt 98,4,512
ql 7,44,1024
qm 76,100,216
v_mul_f32 v16,v63,v63
v_lshl_add_u32 v22,v42,5,v25
qt 100,5,0
qw 5
qm 64,104,148
v_fma_f32 v16,v16,v13,v11
v_lshrrev_b32 v42,1,v42
qt 102,5,512
qm 68,104,220
v_mul_f32 v16,v16,v63
v_xor_b32 v25,v42,v43
qt 104,4,4096
qw 5
qm 72,108,148
v_exp_f32 v16,v16
v_lshl_add_u32 v24,v25,4,v24
qt 106,4,4608
qm 76,108,220
v_fma_f32 v16,v16,v15,v15
v_lshrrev_b32 v42,2,v42
qt 108,5,4096
qw 5
qm 64,96,152
v_rcp_f32 v16,v16
v_xor_b32 v42,v42,v43
qt 110,5,4608
qm 68,96,224
v_mul_f32 v63,v63,v16
v_lshl_add_u32 v22,v42,4,v22
qt 96,4,8192
qw 5
qm 72,100,152
v_mul_f32 v48,v48,v80
v_add_u32 v23,0x10000,v22
v_xor_b32 v25,32,v24
qt 98,4,8704
qm 76,100,224
v_mul_f32 v49,v49,v81
v_xor_b32 v26,64,v24
qt 100,5,8192
qw 5
qm 64,104,156
v_cvt_pk_bf16_f32 v48,v48,v49
v_xor_b32 v27,0x60,v24
qt 102,5,8704
s_add_u32 m0,s13,40960
qm 68,104,228
v_mul_f32 v50,v50,v82
v_lshlrev_b32 v36,3,v0
qt 104,4,12288
qw 5
qm 72,108,156
s_waitcnt vmcnt(0)
s_barrier
v_mul_f32 v51,v51,v83
v_add_u32 v36,s13,v36
qt 106,4,12800
s_add_u32 s14,s17,2359808
qm 76,108,228
v_cvt_pk_bf16_f32 v49,v50,v51
v_add_u32 v36,147456,v36
qt 108,5,12288
qw 5
qm 64,96,160
v_mul_f32 v56,v56,v88
v_lshrrev_b32 v8,3,v0
qt 110,5,12800
ql 6,44,0
qm 68,96,232
v_mul_f32 v57,v57,v89
v_mul_u32_u24 v8,1152,v8
qt 96,4,16384
qw 5
qm 72,100,160
v_cvt_pk_bf16_f32 v50,v56,v57
v_lshrrev_b32 v9,4,v0
qt 98,4,16896
ql 7,44,1024
qm 76,100,232
v_mul_f32 v58,v58,v90
v_xor_b32 v9,v9,v0
qt 100,5,16384
qw 5
qm 64,104,164
v_mul_f32 v59,v59,v91
v_and_b32 v9,7,v9
qt 102,5,16896
s_add_u32 m0,s13,49152
qm 68,104,236
v_cvt_pk_bf16_f32 v51,v58,v59
v_lshl_add_u32 v8,v9,4,v8
qt 104,4,20480
qw 5
qm 72,108,164
ds_write_b128 v18,v[48:51] offset:8192
v_mul_f32 v52,v52,v84
v_xor_b32 v9,64,v8
qt 106,4,20992
s_add_u32 s14,s17,2949632
qm 76,108,236
s_add_u32 s15,s76,49152
buffer_store_dwordx4 v[48:51],v18,s[88:91],s15 offen offset:0 sc1
v_mul_f32 v53,v53,v85
v_add_u32 v9,8192,v9
qt 108,5,20480
qw 6
qm 64,96,168
v_cvt_pk_bf16_f32 v52,v52,v53
s_lshr_b32 s30,s7,1
s_lshl_b32 s30,s30,5
qt 110,5,20992
ql 6,44,0
qm 68,96,240
v_mul_f32 v54,v54,v86
s_and_b32 s31,s7,1
qt 96,2,0
qw 6
qm 72,100,168
v_mul_f32 v55,v55,v87
s_lshl_b32 s32,s31,3
qt 98,2,512
ql 7,44,1024
qm 76,100,240
v_cvt_pk_bf16_f32 v53,v54,v55
s_add_u32 s30,s30,s32
qt 100,3,0
qw 5
qm 64,104,172
v_mul_f32 v60,v60,v92
s_mul_i32 s32,s8,576
qt 102,3,512
s_add_u32 m0,s13,57344
qm 68,104,244
v_mul_f32 v61,v61,v93
s_add_u32 s30,s30,s32
qt 104,2,4096
qw 5
qm 72,108,172
v_cvt_pk_bf16_f32 v54,v60,v61
s_mul_i32 s19,s30,1152
qt 106,2,4608
s_add_u32 s14,s17,3539456
qm 76,108,244
v_mul_f32 v62,v62,v94
s_mul_i32 s31,s31,36864
qt 108,3,4096
qw 5
qm 64,96,176
v_mul_f32 v63,v63,v95
s_and_b32 s30,s8,6
qt 110,3,4608
ql 6,44,0
qm 68,96,248
v_cvt_pk_bf16_f32 v55,v62,v63
s_lshl_b32 s30,s30,4
qt 96,2,8192
qw 5
qm 72,100,176
ds_write_b128 v18,v[52:55] offset:9216
s_add_u32 s18,s31,s30
qt 98,2,8704
ql 7,44,1024
qm 76,100,248
s_add_u32 s15,s76,49152
buffer_store_dwordx4 v[52:55],v18,s[88:91],s15 offen offset:1024 sc1
s_add_u32 s18,s18,1024
qt 100,3,8192
qw 6
qm 64,104,180
s_lshl_b32 s30,s8,7
qt 102,3,8704
s_add_u32 m0,s13,81920
qm 68,104,252
s_add_u32 s28,s31,s30
qt 104,2,12288
qw 6
qm 72,108,180
qw 4
s_barrier
qt 106,2,12800
s_add_u32 s14,s17,4129280
qm 76,108,252
qt 108,3,12288
qw 5
qz 80,96,112
qt 110,3,12800
ql 6,44,0
qz 84,96,184
v_mul_f32 v16,v64,v64
qt 96,2,16384
qw 5
qz 88,100,112
v_fma_f32 v16,v16,v12,v10
qt 98,2,16896
ql 7,44,1024
qz 92,100,184
v_mul_f32 v16,v16,v64
qt 100,3,16384
qw 5
qm 80,104,116
v_exp_f32 v16,v16
qt 102,3,16896
s_add_u32 m0,s13,65536
qm 84,104,188
v_fma_f32 v16,v16,v14,v14
qt 104,2,20480
qw 5
qm 88,108,116
v_rcp_f32 v16,v16
qt 106,2,20992
s_add_u32 s14,s17,4719104
qm 92,108,188
v_mul_f32 v64,v64,v16
qt 108,3,20480
qw 5
qm 80,96,120
v_mul_f32 v16,v65,v65
qt 110,3,20992
ql 6,44,0
qm 84,96,192
v_fma_f32 v16,v16,v12,v10
qr 112,18,49152
qt 96,2,24576
qw 6
qm 88,100,120
v_mul_f32 v16,v16,v65
qt 98,2,25088
ql 7,44,1024
qm 92,100,192
v_exp_f32 v16,v16
qr 184,18,50176
qt 100,3,24576
qw 7
qm 80,104,124
v_fma_f32 v16,v16,v14,v14
qt 102,3,25088
s_add_u32 m0,s13,73728
qm 84,104,196
v_rcp_f32 v16,v16
qr 116,18,57344
qt 104,2,28672
qw 8
qm 88,108,124
v_mul_f32 v65,v65,v16
qt 106,2,29184
s_mov_b32 s14,s19
qm 92,108,196
v_mul_f32 v16,v66,v66
qr 188,18,58368
qt 108,3,28672
qw 8
qm 80,96,128
v_fma_f32 v16,v16,v12,v10
qt 110,3,29184
ql 20,48,0
qm 84,96,200
v_mul_f32 v16,v16,v66
qr 120,19,8192
qt 96,2,32768
qw 8
qm 88,100,128
v_exp_f32 v16,v16
qt 98,2,33280
ql 21,48,1024
qm 92,100,200
v_fma_f32 v16,v16,v14,v14
qr 192,19,9216
qt 100,3,32768
qw 8
qm 80,104,132
v_rcp_f32 v16,v16
qt 102,3,33280
s_mov_b32 m0,s13
qm 84,104,204
v_mul_f32 v66,v66,v16
qr 124,18,24576
qt 104,2,36864
qw 8
qm 88,108,132
s_waitcnt vmcnt(7) lgkmcnt(1)
s_barrier
v_mul_f32 v16,v67,v67
qt 106,2,37376
s_add_u32 s14,s19,128
qm 92,108,204
v_fma_f32 v16,v16,v12,v10
qr 196,18,25600
qt 108,3,36864
qm 80,96,136
v_mul_f32 v16,v16,v67
v_lshrrev_b32 v6,1,v0
v_add_u32 v28,0x10000,v24
qt 110,3,37376
ql 20,48,0
qm 84,96,208
v_exp_f32 v16,v16
v_add_u32 v29,0x10000,v25
qt 32,36,0
qt 96,2,40960
qm 88,100,136
v_fma_f32 v16,v16,v14,v14
v_mul_u32_u24 v6,1152,v6
qt 98,2,41472
ql 21,48,1024
qm 92,100,208
v_rcp_f32 v16,v16
v_add_u32 v30,0x10000,v26
qt 34,36,512
s_add_u32 s15,s77,0
buffer_load_dwordx4 v[128:131],v18,s[88:91],s15 offen offset:0 sc1
qt 100,3,40960
qw 8
qm 80,104,140
v_mul_f32 v67,v67,v16
v_lshrrev_b32 v7,4,v0
v_add_u32 v31,0x10000,v27
qt 102,3,41472
s_add_u32 m0,s13,8192
qm 84,104,212
v_mul_f32 v16,v72,v72
qt 42,36,1024
qt 104,2,45056
qw 8
qm 88,108,140
v_fma_f32 v16,v16,v12,v10
v_xor_b32 v7,v7,v0
qt 106,2,45568
s_add_u32 s14,s19,512
qm 92,108,212
v_mul_f32 v16,v16,v72
qt 44,36,1536
qt 108,3,45056
qw 8
qm 80,96,144
v_exp_f32 v16,v16
qt 110,3,45568
ql 20,48,0
qm 84,96,216
v_fma_f32 v16,v16,v14,v14
v_and_b32 v7,1,v7
qt 96,2,49152
qw 7
qm 88,100,144
v_rcp_f32 v16,v16
qt 98,2,49664
ql 21,48,1024
qm 92,100,216
v_mul_f32 v72,v72,v16
v_lshl_add_u32 v6,v7,4,v6
s_add_u32 s15,s77,0
buffer_load_dwordx4 v[200:203],v18,s[88:91],s15 offen offset:1024 sc1
qt 100,3,49152
qw 6
qm 80,104,148
v_mul_f32 v16,v73,v73
qt 102,3,49664
s_add_u32 m0,s13,16384
qm 84,104,220
v_fma_f32 v16,v16,v12,v10
v_add_u32 v7,72704,v6
qt 104,2,53248
qw 5
qm 88,108,148
v_mul_f32 v16,v16,v73
qt 106,2,53760
s_add_u32 s14,s19,73728
qm 92,108,220
v_exp_f32 v16,v16
qt 108,3,53248
qw 5
qm 80,96,152
v_fma_f32 v16,v16,v14,v14
qt 110,3,53760
ql 20,48,0
qm 84,96,224
v_rcp_f32 v16,v16
qt 96,2,57344
qw 5
qm 88,100,152
v_mul_f32 v73,v73,v16
s_mul_i32 s30,s10,1310720
qt 98,2,57856
ql 21,48,1024
qm 92,100,224
v_mul_f32 v16,v74,v74
s_lshl_b32 s31,s8,14
qt 100,3,57344
qw 5
qm 80,104,156
v_fma_f32 v16,v16,v12,v10
s_add_u32 s15,s77,8192
buffer_load_dwordx4 v[132:135],v18,s[88:91],s15 offen offset:0 sc1
qt 102,3,57856
s_add_u32 m0,s13,24576
qm 84,104,228
v_mul_f32 v16,v16,v74
s_add_u32 s73,s30,s31
qt 104,2,61440
qw 5
qm 88,108,156
s_waitcnt vmcnt(9)
s_barrier
v_exp_f32 v16,v16
qt 106,2,61952
s_add_u32 s14,s19,73856
qm 92,108,228
v_fma_f32 v16,v16,v14,v14
s_add_u32 s73,s73,49152
qt 108,3,61440
qw 5
qm 80,96,160
v_rcp_f32 v16,v16
qt 110,3,61952
ql 20,48,0
qm 84,96,232
v_mul_f32 v74,v74,v16
s_lshl_b32 s31,s8,11
qt 96,4,16384
qw 5
qm 88,100,160
v_mul_f32 v16,v75,v75
s_add_u32 s69,s30,s31
qt 98,4,16896
ql 21,48,1024
qm 92,100,232
v_fma_f32 v16,v16,v12,v10
qt 100,5,16384
qw 5
qm 80,104,164
v_mul_f32 v16,v16,v75
s_lshl_b32 s31,s7,8
qt 102,5,16896
s_add_u32 m0,s13,32768
qm 84,104,236
v_exp_f32 v16,v16
s_add_u32 s15,s77,8192
buffer_load_dwordx4 v[204:207],v18,s[88:91],s15 offen offset:1024 sc1
qt 104,4,20480
qw 5
qm 88,108,164
v_fma_f32 v16,v16,v14,v14
s_add_u32 s69,s69,s31
qt 106,4,20992
s_add_u32 s14,s19,74240
qm 92,108,236
v_rcp_f32 v16,v16
qt 108,5,20480
qw 5
qm 80,96,168
v_mul_f32 v75,v75,v16
s_add_u32 s69,s69,131072
qt 110,5,20992
ql 20,48,0
qm 84,96,240
v_mul_f32 v16,v68,v68
s_and_b32 s75,s8,4
qt 96,4,0
qw 5
qm 88,100,168
v_fma_f32 v16,v16,v13,v11
qt 98,4,512
ql 21,48,1024
qm 92,100,240
v_mul_f32 v16,v16,v68
s_lshl_b32 s75,s75,4
qt 100,5,0
qw 5
qm 80,104,172
v_exp_f32 v16,v16
qt 102,5,512
s_add_u32 m0,s13,40960
qm 84,104,244
v_fma_f32 v16,v16,v15,v15
s_and_b32 s33,s8,3
s_lshl_b32 s33,s33,2
s_add_u32 s33,s33,s7
s_lshl_b32 s33,s33,3
s_lshl_b32 s30,s10,7
s_add_u32 s30,s30,s33
qt 104,4,4096
qw 5
qm 88,108,172
v_rcp_f32 v16,v16
s_add_u32 s15,s77,16384
buffer_load_dwordx4 v[136:139],v18,s[88:91],s15 offen offset:0 sc1
qt 106,4,4608
s_mov_b32 s14,s18
qm 92,108,244
v_mul_f32 v68,v68,v16
s_add_u32 s30,s30,0
qt 108,5,4096
qw 5
qm 80,96,176
v_mul_f32 v16,v69,v69
s_mul_i32 s30,s30,1152
qt 110,5,4608
ql 6,24,0
qm 84,96,248
v_fma_f32 v16,v16,v13,v11
qt 96,4,8192
qw 5
qm 88,100,176
v_mul_f32 v16,v16,v69
s_add_u32 s70,s30,s75
qt 98,4,8704
ql 7,24,1024
qm 92,100,248
v_exp_f32 v16,v16
qt 100,5,8192
qw 5
qm 80,104,180
v_fma_f32 v16,v16,v15,v15
s_lshl_b32 s71,s70,1
qt 102,5,8704
s_add_u32 m0,s13,81920
qm 84,104,252
v_rcp_f32 v16,v16
qt 104,4,12288
qw 5
qm 88,108,180
s_waitcnt vmcnt(9)
s_barrier
v_mul_f32 v69,v69,v16
qt 106,4,12800
s_add_u32 s14,s18,294912
qm 92,108,252
s_add_u32 s15,s77,16384
buffer_load_dwordx4 v[208:211],v18,s[88:91],s15 offen offset:1024 sc1
qt 108,5,12288
qw 5
qz 144,96,112
v_mul_f32 v16,v70,v70
v_mul_f32 v17,v71,v71
v_fma_f32 v16,v16,v13,v11
qt 110,5,12800
ql 6,24,0
qz 216,96,184
v_fma_f32 v17,v17,v13,v11
v_mul_f32 v16,v16,v70
qt 96,2,0
qw 5
qz 148,100,112
v_mul_f32 v17,v17,v71
v_exp_f32 v16,v16
qt 98,2,512
ql 7,24,1024
qz 220,100,184
v_exp_f32 v17,v17
v_fma_f32 v16,v16,v15,v15
v_fma_f32 v17,v17,v15,v15
qt 100,3,0
qw 5
qm 144,104,116
v_rcp_f32 v16,v16
v_rcp_f32 v17,v17
qt 102,3,512
s_add_u32 m0,s13,49152
qm 216,104,188
v_mul_f32 v70,v70,v16
v_mul_f32 v71,v71,v17
qt 104,2,4096
qw 5
qm 148,108,116
v_mul_f32 v16,v76,v76
v_mul_f32 v17,v77,v77
v_fma_f32 v16,v16,v13,v11
qt 106,2,4608
s_add_u32 s14,s19,147456
qm 220,108,188
v_fma_f32 v17,v17,v13,v11
v_mul_f32 v16,v16,v76
qt 108,3,4096
qw 5
qz 152,96,112
v_mul_f32 v17,v17,v77
v_exp_f32 v16,v16
s_add_u32 s15,s77,24576
buffer_load_dwordx4 v[140:143],v18,s[88:91],s15 offen offset:0 sc1
qt 110,3,4608
ql 20,48,0
qz 224,96,184
v_exp_f32 v17,v17
v_fma_f32 v16,v16,v15,v15
v_fma_f32 v17,v17,v15,v15
qt 96,2,8192
qw 5
qz 156,100,112
v_rcp_f32 v16,v16
v_rcp_f32 v17,v17
qt 98,2,8704
ql 21,48,1024
qz 228,100,184
v_mul_f32 v76,v76,v16
v_mul_f32 v77,v77,v17
qt 100,3,8192
qw 5
qm 152,104,116
v_mul_f32 v16,v78,v78
v_mul_f32 v17,v79,v79
v_fma_f32 v16,v16,v13,v11
qt 102,3,8704
s_add_u32 m0,s13,57344
qm 224,104,188
v_fma_f32 v17,v17,v13,v11
v_mul_f32 v16,v16,v78
qt 104,2,12288
qw 5
qm 156,108,116
v_mul_f32 v17,v17,v79
v_exp_f32 v16,v16
qt 106,2,12800
s_add_u32 s14,s19,147584
qm 228,108,188
v_exp_f32 v17,v17
v_fma_f32 v16,v16,v15,v15
v_fma_f32 v17,v17,v15,v15
qt 108,3,12288
qw 5
qz 176,96,112
v_rcp_f32 v16,v16
v_rcp_f32 v17,v17
qt 110,3,12800
ql 20,48,0
qz 248,96,184
v_mul_f32 v78,v78,v16
v_mul_f32 v79,v79,v17
qr 160,18,32768
s_add_u32 s15,s77,24576
buffer_load_dwordx4 v[212:215],v18,s[88:91],s15 offen offset:1024 sc1
qt 96,2,16384
qw 6
qz 180,100,112
v_mul_f32 v64,v64,v80
v_mul_f32 v65,v65,v81
v_cvt_pk_bf16_f32 v64,v64,v65
qt 98,2,16896
ql 21,48,1024
qz 252,100,184
v_mul_f32 v66,v66,v82
v_mul_f32 v67,v67,v83
qr 232,18,33792
qt 100,3,16384
qw 7
qm 176,104,116
v_cvt_pk_bf16_f32 v65,v66,v67
v_mul_f32 v72,v72,v88
qt 102,3,16896
s_add_u32 m0,s13,65536
qm 248,104,188
v_mul_f32 v73,v73,v89
v_cvt_pk_bf16_f32 v66,v72,v73
v_mul_f32 v74,v74,v90
qr 164,18,40960
qt 104,2,20480
qw 8
qm 180,108,116
s_waitcnt vmcnt(9) lgkmcnt(1)
s_barrier
v_mul_f32 v75,v75,v91
v_cvt_pk_bf16_f32 v67,v74,v75
qt 106,2,20992
s_add_u32 s14,s19,147968
qm 252,108,188
s_add_u32 s15,s76,65536
buffer_store_dwordx4 v[64:67],v18,s[88:91],s15 offen offset:0 sc1
ds_write_b128 v18,v[64:67] offset:49152
qr 236,18,41984
qt 108,3,20480
qm 144,96,120
v_mul_f32 v68,v68,v84
v_mul_f32 v69,v69,v85
v_cvt_pk_bf16_f32 v68,v68,v69
qt 110,3,20992
ql 20,48,0
qm 216,96,192
v_mul_f32 v70,v70,v86
v_mul_f32 v71,v71,v87
qt 96,2,24576
qm 148,100,120
v_cvt_pk_bf16_f32 v69,v70,v71
v_mul_f32 v76,v76,v92
s_add_u32 s15,s77,65536
buffer_load_dwordx4 v[48:51],v18,s[88:91],s15 offen offset:0 sc1
qt 98,2,25088
ql 21,48,1024
qm 220,100,192
v_mul_f32 v77,v77,v93
v_cvt_pk_bf16_f32 v70,v76,v77
v_mul_f32 v78,v78,v94
qt 100,3,24576
qw 7
qm 144,104,124
v_mul_f32 v79,v79,v95
v_cvt_pk_bf16_f32 v71,v78,v79
qt 102,3,25088
s_add_u32 m0,s13,73728
qm 216,104,196
s_add_u32 s15,s76,65536
buffer_store_dwordx4 v[68:71],v18,s[88:91],s15 offen offset:1024 sc1
ds_write_b128 v18,v[68:71] offset:50176
qt 104,2,28672
qw 6
qm 148,108,124
qt 106,2,29184
s_add_u32 s14,s19,221184
qm 220,108,196
qt 108,3,28672
qw 6
qm 152,96,120
qt 110,3,29184
ql 20,48,0
qm 224,96,192
qt 96,2,32768
qw 6
qm 156,100,120
qt 98,2,33280
ql 21,48,1024
qm 228,100,192
s_add_u32 s15,s77,65536
buffer_load_dwordx4 v[56:59],v18,s[88:91],s15 offen offset:1024 sc1
qt 100,3,32768
qw 5
qm 152,104,124
qt 102,3,33280
s_mov_b32 m0,s13
qm 224,104,196
qt 104,2,36864
qw 5
qm 156,108,124
qt 106,2,37376
s_add_u32 s14,s19,221312
qm 228,108,196
qt 108,3,36864
qw 5
qm 176,96,120
qt 110,3,37376
ql 20,48,0
qm 248,96,192
qr 168,18,8192
qd 96,22,40960
qw 6
qm 180,100,120
qd 98,22,45056
ql 21,48,1024
qm 252,100,192
qr 240,18,9216
qd 100,22,41472
qw 7
qm 176,104,124
s_add_u32 s15,s77,73728
buffer_load_dwordx4 v[52:55],v18,s[88:91],s15 offen offset:0 sc1
qd 102,22,45568
s_add_u32 m0,s13,8192
qm 248,104,196
qr 172,18,16384
qd 104,22,41984
qw 8
qm 180,108,124
s_waitcnt vmcnt(11) lgkmcnt(1)
s_barrier
qd 106,22,46080
s_add_u32 s14,s19,221696
qm 252,108,196
qr 244,18,17408
qd 108,22,42496
qm 144,96,32
qd 110,22,46592
ql 20,48,0
qm 216,96,42
qd 96,23,16384
qm 148,100,32
qd 98,23,20480
ql 21,48,1024
qm 220,100,42
qd 100,23,16896
s_add_u32 m0,s13,16384
s_add_u32 s14,s19,295424
qw 6
qm 152,104,32
qd 102,23,20992
ql 20,48,0
qm 224,104,42
qw 4
qm 156,108,32
ql 21,48,1024
qm 228,108,42
s_add_u32 m0,s13,24576
s_add_u32 s14,s19,294912
qw 2
qm 176,96,32
ql 20,48,0
qm 248,96,42
qr 120,18,49152
qt 96,2,49152
qw 2
qm 180,100,32
qt 98,2,49664
ql 21,48,1024
qm 252,100,42
qr 192,18,50176
qt 100,3,49152
qt 102,3,49664
qr 124,18,57344
qt 104,2,53248
qt 106,2,53760
qr 196,18,58368
qt 108,3,53248
qw 8
qm 144,96,160
qt 110,3,53760
qm 216,96,232
qt 96,2,57344
qw 7
qm 148,100,160
qt 98,2,57856
qm 220,100,232
qt 100,3,57344
qw 6
qm 144,104,164
qt 102,3,57856
s_add_u32 m0,s13,81920
qm 216,104,236
qt 104,2,61440
qw 5
qm 148,108,164
s_waitcnt vmcnt(0)
s_barrier
qt 106,2,61952
s_add_u32 s14,s19,295040
qm 220,108,236
qt 108,3,61440
qw 5
qm 152,96,160
v_and_b32 v1,0xfffffc3f,v18
qt 110,3,61952
ql 20,48,0
qm 224,96,232
qt 96,4,0
qw 5
qm 156,100,160
qt 98,4,512
ql 21,48,1024
qm 228,100,232
s_add_u32 s15,s77,73728
buffer_load_dwordx4 v[60:63],v18,s[88:91],s15 offen offset:1024 sc1
qt 100,5,0
qw 5
qm 152,104,164
v_lshrrev_b32 v16,4,v0
qt 102,5,512
s_add_u32 m0,s13,32768
qm 224,104,236
qt 104,4,4096
qw 5
qm 156,108,164
qt 106,4,4608
s_add_u32 s14,s19,368640
qm 228,108,236
qt 108,5,4096
qw 5
qm 176,96,160
qt 110,5,4608
ql 20,48,0
qm 248,96,232
v_lshl_add_u32 v1,v16,6,v1
qt 96,4,8192
qw 5
qm 180,100,160
qt 98,4,8704
ql 21,48,1024
qm 252,100,232
qt 100,5,8192
qw 5
qm 176,104,164
s_add_u32 s15,s77,81920
buffer_load_dwordx4 v[80:83],v18,s[88:91],s15 offen offset:0 sc1
qt 102,5,8704
s_add_u32 m0,s13,40960
qm 248,104,236
qt 104,4,12288
qw 5
qm 180,108,164
v_and_b32 v16,12,v0
qt 106,4,12800
s_add_u32 s14,s19,368768
qm 252,108,236
qt 108,5,12288
qw 5
qm 144,96,168
qt 110,5,12800
ql 20,48,0
qm 216,96,240
qt 96,2,0
qw 5
qm 148,100,168
qt 98,2,512
ql 21,48,1024
qm 220,100,240
v_lshl_add_u32 v1,v16,6,v1
qt 100,3,0
qw 5
qm 144,104,172
qt 102,3,512
s_add_u32 m0,s13,49152
qm 216,104,244
s_add_u32 s15,s77,81920
buffer_load_dwordx4 v[88:91],v18,s[88:91],s15 offen offset:1024 sc1
qt 104,2,4096
qw 5
qm 148,108,172
s_barrier
qt 106,2,4608
s_add_u32 s14,s19,442368
qm 220,108,244
qt 108,3,4096
qw 5
qm 152,96,168
qt 110,3,4608
ql 20,48,0
qm 224,96,240
qt 96,2,8192
qw 5
qm 156,100,168
qt 98,2,8704
ql 21,48,1024
qm 228,100,240
qt 100,3,8192
qw 5
qm 152,104,172
qt 102,3,8704
s_add_u32 m0,s13,57344
qm 224,104,244
qt 104,2,12288
qw 5
qm 156,108,172
s_add_u32 s15,s77,90112
buffer_load_dwordx4 v[84:87],v18,s[88:91],s15 offen offset:0 sc1
qt 106,2,12800
s_add_u32 s14,s19,442496
qm 228,108,244
qt 108,3,12288
qw 5
qm 176,96,168
qt 110,3,12800
ql 20,48,0
qm 248,96,240
qt 96,2,16384
qw 5
qm 180,100,168
qt 98,2,16896
ql 21,48,1024
qm 252,100,240
qt 100,3,16384
qw 5
qm 176,104,172
qt 102,3,16896
s_add_u32 m0,s13,65536
qm 248,104,244
qt 104,2,20480
qw 5
qm 180,108,172
qt 106,2,20992
s_mov_b32 s14,s28
qm 252,108,244
s_add_u32 s15,s77,90112
buffer_load_dwordx4 v[92:95],v18,s[88:91],s15 offen offset:1024 sc1
qt 108,3,20480
qw 5
qm 176,96,120
qt 110,3,20992
ql 8,24,0
qm 248,96,192
qt 96,2,24576
qw 5
qm 180,100,120
qt 98,2,25088
ql 9,24,1024
qm 252,100,192
qt 100,3,24576
qw 5
qm 176,104,124
qt 102,3,25088
s_add_u32 m0,s13,73728
qm 248,104,196
qt 104,2,28672
qw 5
qm 180,108,124
s_waitcnt vmcnt(9)
s_barrier
qt 106,2,29184
s_add_u32 s14,s28,73728
qm 252,108,196
qt 108,3,28672
qw 5
qm 144,96,120
s_add_u32 s15,s77,32768
buffer_load_dwordx4 v[64:67],v18,s[88:91],s15 offen offset:0 sc1
qt 110,3,29184
ql 8,24,0
qm 216,96,192
qt 96,4,16384
qw 5
qm 148,100,120
qt 98,4,16896
ql 9,24,1024
qm 220,100,192
v_cvt_pk_bf16_f32 v176,v176,v177
qt 100,5,16384
qw 5
qm 144,104,124
v_cvt_pk_bf16_f32 v177,v178,v179
qt 102,5,16896
s_mov_b32 m0,s13
qm 216,104,196
v_cvt_pk_bf16_f32 v178,v180,v181
qt 104,4,20480
qw 5
qm 148,108,124
v_cvt_pk_bf16_f32 v179,v182,v183
qt 106,4,20992
s_add_u32 s14,s28,18432
qm 220,108,196
s_add_u32 s15,s77,32768
buffer_load_dwordx4 v[72:75],v18,s[88:91],s15 offen offset:1024 sc1
qt 108,5,20480
qw 5
qm 152,96,120
v_cvt_pk_bf16_f32 v248,v248,v249
qt 110,5,20992
ql 8,24,0
qm 224,96,192
v_cvt_pk_bf16_f32 v249,v250,v251
qt 96,2,32768
qw 5
qm 156,100,120
v_cvt_pk_bf16_f32 v250,v252,v253
qt 98,2,33280
ql 9,24,1024
qm 228,100,192
v_cvt_pk_bf16_f32 v251,v254,v255
qt 100,3,32768
qw 5
qm 152,104,124
qt 102,3,33280
s_add_u32 m0,s13,8192
qm 224,104,196
qt 104,2,36864
qw 5
qm 156,108,124
s_add_u32 s15,s77,40960
buffer_load_dwordx4 v[68:71],v18,s[88:91],s15 offen offset:0 sc1
qt 106,2,37376
s_add_u32 s14,s19,516096
qm 228,108,196
qt 108,3,36864
qw 5
qm 144,96,128
qt 110,3,37376
ql 20,48,0
qm 216,96,200
qt 96,2,40960
qw 5
qm 148,100,128
qt 98,2,41472
ql 21,48,1024
qm 220,100,200
qt 100,3,40960
qw 5
qm 144,104,132
qt 102,3,41472
s_add_u32 m0,s13,81920
qm 216,104,204
s_add_u32 s15,s77,40960
buffer_load_dwordx4 v[76:79],v18,s[88:91],s15 offen offset:1024 sc1
qt 104,2,45056
qw 5
qm 148,108,132
s_waitcnt vmcnt(10)
s_barrier
qt 106,2,45568
s_add_u32 s14,s19,589824
qm 220,108,204
qt 108,3,45056
qw 5
qm 152,96,128
v_and_b32 v15,48,v0
qt 110,3,45568
ql 20,48,0
qm 224,96,200
qt 96,2,49152
qw 5
qm 156,100,128
v_lshlrev_b32 v10,10,v15
qt 98,2,49664
ql 21,48,1024
qm 228,100,200
qt 100,3,49152
qw 5
qm 152,104,132
s_add_u32 s15,s77,49152
buffer_load_dwordx4 v[120:123],v18,s[88:91],s15 offen offset:0 sc1
qt 102,3,49664
s_add_u32 m0,s13,16384
qm 224,104,204
v_and_b32 v16,16,v0
qt 104,2,53248
qw 5
qm 156,108,132
qt 106,2,53760
s_add_u32 s14,s28,92160
qm 228,108,204
qt 108,3,53248
qw 5
qm 144,96,136
v_lshrrev_b32 v17,5,v0
qt 110,3,53760
ql 8,24,0
qm 216,96,208
qt 96,2,57344
qw 5
qm 148,100,136
v_lshl_add_u32 v12,v17,6,v16
qt 98,2,57856
ql 9,24,1024
qm 220,100,208
s_add_u32 s15,s77,49152
buffer_load_dwordx4 v[192:195],v18,s[88:91],s15 offen offset:1024 sc1
qt 100,3,57344
qw 5
qm 144,104,140
qt 102,3,57856
s_add_u32 m0,s13,24576
qm 216,104,212
v_and_b32 v16,3,v0
qt 104,2,61440
qw 5
qm 148,108,140
qt 106,2,61952
s_add_u32 s14,s19,516224
qm 220,108,212
qt 108,3,61440
qw 5
qm 152,96,136
v_lshl_add_u32 v12,v16,2,v12
qt 110,3,61952
ql 20,48,0
qm 224,96,208
qd 96,28,0
qw 5
qm 156,100,136
s_add_u32 s15,s77,57344
buffer_load_dwordx4 v[124:127],v18,s[88:91],s15 offen offset:0 sc1
v_lshl_add_u32 v10,v16,6,v10
qd 98,28,4096
ql 21,48,1024
qm 228,100,208
qd 100,29,0
qw 5
qm 152,104,140
qd 102,29,4096
s_add_u32 m0,s13,32768
qm 224,104,212
v_bfe_u32 v11,v0,2,2
qd 104,30,0
qw 5
qm 156,108,140
s_waitcnt vmcnt(10)
s_barrier
qd 106,30,4096
s_add_u32 s14,s19,589952
qm 228,108,212
qd 108,31,0
qw 5
qm 144,96,48
v_lshl_add_u32 v10,v11,4,v10
qd 110,31,4096
ql 20,48,0
qm 216,96,56
s_add_u32 s15,s77,57344
buffer_load_dwordx4 v[196:199],v18,s[88:91],s15 offen offset:1024 sc1
qd 96,28,8192
qw 5
qm 144,100,52
v_lshlrev_b32 v12,1,v12
qd 98,28,12288
ql 21,48,1024
qm 216,100,60
qd 100,29,8192
qw 5
qm 144,104,80
qd 102,29,12288
s_add_u32 m0,s13,40960
qm 216,104,88
v_mul_u32_u24 v11,1152,v11
qd 104,30,8192
qw 5
qm 144,108,84
qd 106,30,12288
s_add_u32 s14,s19,256
qm 216,108,92
qd 108,31,8192
qw 5
qm 152,96,48
v_add_u32 v11,v11,v12
qd 110,31,12288
ql 20,48,0
qm 224,96,56
qd 96,24,0
qw 5
qm 152,100,52
v_lshlrev_b32 v13,1,v11
qd 98,24,4096
ql 21,48,1024
qm 224,100,60
qd 100,25,0
qw 5
qm 152,104,80
qd 102,25,4096
s_add_u32 m0,s13,49152
qm 224,104,88
s_add_u32 s15,s73,524288
buffer_store_dwordx4 v[176:179],v1,s[88:91],s15 offen offset:0 sc1
v_lshl_or_b32 v14,v17,30,v13
qd 104,26,0
qw 5
qm 152,108,84
qd 106,26,4096
s_add_u32 s14,s19,384
qm 224,108,92
qd 108,27,0
qw 5
qm 148,96,48
s_and_b32 s30,s8,3
s_lshl_b32 s30,s30,4
s_mov_b64 s[32:33],0xffff
s_lshl_b64 s[32:33],s[32:33],s30
s_bitcmp1_b32 s8,2
s_cselect_b64 s[40:41],0,s[32:33]
s_cselect_b64 s[42:43],s[32:33],0
qd 110,27,4096
ql 20,48,0
qm 220,96,56
qt 96,2,8192
qw 5
qm 148,100,52
s_mov_b64 s[52:53],0
qt 98,2,8704
ql 21,48,1024
qm 220,100,60
qt 100,3,8192
qw 5
qm 148,104,80
qt 102,3,8704
s_add_u32 m0,s13,57344
qm 220,104,88
s_add_u32 s15,s73,524288
buffer_store_dwordx4 v[248:251],v1,s[88:91],s15 offen offset:1024 sc1
s_mov_b64 s[54:55],0
qt 104,2,12288
qw 5
qm 148,108,84
s_waitcnt vmcnt(9)
s_barrier
qt 106,2,12800
s_add_u32 s14,s18,147456
qm 220,108,92
qt 108,3,12288
qw 5
qm 144,96,64
s_mov_b64 s[56:57],0
qt 110,3,12800
ql 6,24,0
qm 216,96,72
qt 96,4,16384
qw 5
qm 148,100,64
s_mov_b64 s[58:59],0
qt 98,4,16896
ql 7,24,1024
qm 220,100,72
qt 100,5,16384
qw 5
qm 144,104,68
qt 102,5,16896
s_add_u32 m0,s13,65536
qm 216,104,76
s_mov_b32 s76,0
qt 104,4,20480
qw 5
qm 148,108,68
qt 106,4,20992
s_add_u32 s14,s19,368896
qm 220,108,76
qt 108,5,20480
qw 5
qm 144,96,120
qt 110,5,20992
ql 20,48,0
qm 216,96,192
qd 96,24,16384
qw 5
qm 148,100,120
qd 98,24,20480
ql 21,48,1024
qm 220,100,192
qd 100,25,16384
qw 5
qm 144,104,124
qd 102,25,20480
s_add_u32 m0,s13,73728
s_waitcnt vmcnt(11)
qm 216,104,196
qd 104,26,16384
qw 5
qm 148,108,124
qd 106,26,20480
s_add_u32 s14,s19,369024
qm 220,108,196
qd 108,27,16384
qw 5
qm 156,96,48
qd 110,27,20480
ql 20,48,0
qm 228,96,56
qt 96,2,24576
qw 5
qm 156,100,52
qt 98,2,25088
ql 21,48,1024
qm 228,100,60
v_cvt_pk_bf16_f32 v144,v144,v145
qt 100,3,24576
qw 5
qm 156,104,80
v_cvt_pk_bf16_f32 v145,v146,v147
qt 102,3,25088
s_add_u32 m0,s13,81920
qm 228,104,88
v_cvt_pk_bf16_f32 v146,v148,v149
qt 104,2,28672
qw 5
qm 156,108,84
s_waitcnt vmcnt(7)
s_barrier
v_cvt_pk_bf16_f32 v147,v150,v151
qt 106,2,29184
s_add_u32 s14,s19,442624
qm 228,108,92
qt 108,3,28672
qw 5
qm 152,96,64
v_cvt_pk_bf16_f32 v216,v216,v217
qt 110,3,29184
ql 20,48,0
qm 224,96,72
v_cvt_pk_bf16_f32 v217,v218,v219
qt 96,2,32768
qw 5
qm 156,100,64
v_cvt_pk_bf16_f32 v218,v220,v221
qt 98,2,33280
ql 21,48,1024
qm 228,100,72
v_cvt_pk_bf16_f32 v219,v222,v223
qt 100,3,32768
qw 5
qm 152,104,68
s_add_u32 s15,s73,0
buffer_store_dwordx4 v[144:147],v1,s[88:91],s15 offen offset:0 sc1
qt 102,3,33280
s_mov_b32 m0,s13
qm 224,104,76
qt 104,2,36864
qw 5
qm 156,108,68
qt 106,2,37376
s_add_u32 s14,s19,442752
qm 228,108,76
qt 108,3,36864
qw 5
qm 152,96,120
qt 110,3,37376
ql 20,48,0
qm 224,96,192
qt 96,2,40960
qw 5
qm 156,100,120
qt 98,2,41472
ql 21,48,1024
qm 228,100,192
qt 100,3,40960
qw 5
qm 152,104,124
s_add_u32 s15,s73,0
buffer_store_dwordx4 v[216:219],v1,s[88:91],s15 offen offset:1024 sc1
qt 102,3,41472
s_add_u32 m0,s13,8192
qm 224,104,196
qt 104,2,45056
qw 5
qm 156,108,124
qt 106,2,45568
s_add_u32 s14,s28,147456
qm 228,108,196
qt 108,3,45056
qw 5
qz 160,96,112
qt 110,3,45568
ql 8,24,0
qz 232,96,184
qt 96,2,49152
qw 5
qz 164,100,112
qt 98,2,49664
ql 9,24,1024
qz 236,100,184
v_cvt_pk_bf16_f32 v152,v152,v153
qt 100,3,49152
qw 5
qm 160,104,116
v_cvt_pk_bf16_f32 v153,v154,v155
qt 102,3,49664
s_add_u32 m0,s13,16384
qm 232,104,188
v_cvt_pk_bf16_f32 v154,v156,v157
v_and_b32 v16,1,v0
qt 104,2,53248
qw 5
qm 164,108,116
s_waitcnt vmcnt(8)
s_barrier
v_cvt_pk_bf16_f32 v155,v158,v159
qt 106,2,53760
s_add_u32 s14,s28,221184
qm 236,108,188
v_lshlrev_b32 v16,4,v16
qt 108,3,53248
qw 5
qz 168,96,112
v_cvt_pk_bf16_f32 v224,v224,v225
qt 110,3,53760
ql 8,24,0
qz 240,96,184
v_cvt_pk_bf16_f32 v225,v226,v227
v_mov_b32 v17,0x3f80
qd 96,22,57344
qw 5
qz 172,100,112
v_cvt_pk_bf16_f32 v226,v228,v229
qd 98,22,61440
ql 9,24,1024
qz 244,100,184
v_cvt_pk_bf16_f32 v227,v230,v231
v_lshlrev_b32 v16,v16,v17
qd 100,22,57856
qw 5
qm 168,104,116
s_add_u32 s15,s73,131072
buffer_store_dwordx4 v[152:155],v1,s[88:91],s15 offen offset:0 sc1
qd 102,22,61952
s_add_u32 m0,s13,24576
qm 240,104,188
v_bfe_u32 v17,v0,1,3
qd 104,22,58368
qw 5
qm 172,108,116
qd 106,22,62464
s_add_u32 s14,s28,165888
qm 244,108,188
v_cmp_eq_u32 vcc,0,v17
s_nop 1
v_cndmask_b32 v144,0,v16,vcc
qd 108,22,58880
qw 5
qm 160,96,32
qd 110,22,62976
ql 8,24,0
qm 232,96,42
v_cmp_eq_u32 vcc,1,v17
s_nop 1
v_cndmask_b32 v145,0,v16,vcc
qt 96,4,0
qw 5
qm 164,100,32
qt 98,4,512
ql 9,24,1024
qm 236,100,42
v_cmp_eq_u32 vcc,2,v17
s_nop 1
v_cndmask_b32 v146,0,v16,vcc
qt 100,5,0
qw 5
qm 168,104,32
s_add_u32 s15,s73,131072
buffer_store_dwordx4 v[224:227],v1,s[88:91],s15 offen offset:1024 sc1
qt 102,5,512
s_add_u32 m0,s13,32768
qm 240,104,42
v_cmp_eq_u32 vcc,3,v17
s_nop 1
v_cndmask_b32 v147,0,v16,vcc
qt 104,4,4096
qw 5
qm 172,108,32
qt 106,4,4608
s_add_u32 s14,s28,239616
qm 244,108,42
v_cmp_eq_u32 vcc,4,v17
s_nop 1
v_cndmask_b32 v148,0,v16,vcc
qt 108,5,4096
qw 5
qm 160,96,128
qt 110,5,4608
ql 8,24,0
qm 232,96,200
v_cmp_eq_u32 vcc,5,v17
s_nop 1
v_cndmask_b32 v149,0,v16,vcc
qt 96,4,8192
qw 5
qm 164,100,128
qt 98,4,8704
ql 9,24,1024
qm 236,100,200
v_cmp_eq_u32 vcc,6,v17
s_nop 1
v_cndmask_b32 v150,0,v16,vcc
qt 100,5,8192
qw 5
qm 160,104,132
qt 102,5,8704
s_add_u32 m0,s13,40960
qm 232,104,204
v_cmp_eq_u32 vcc,7,v17
s_nop 1
v_cndmask_b32 v151,0,v16,vcc
qt 104,4,12288
qw 5
qm 164,108,132
s_waitcnt vmcnt(8)
s_barrier
qt 106,4,12800
s_add_u32 s14,s19,516352
qm 236,108,204
qt 108,5,12288
qw 5
qm 168,96,128
qt 110,5,12800
ql 20,48,0
qm 240,96,200
qt 96,4,16384
qw 5
qm 172,100,128
qt 98,4,16896
ql 21,48,1024
qm 244,100,200
qt 100,5,16384
s_add_u32 m0,s13,49152
s_add_u32 s14,s19,516480
qw 5
qm 168,104,132
qt 102,5,16896
ql 20,48,0
qm 240,104,204
qt 104,4,20480
qw 5
qm 172,108,132
qt 106,4,20992
ql 21,48,1024
qm 244,108,204
qt 108,5,20480
s_add_u32 m0,s13,57344
s_add_u32 s14,s19,590080
qw 5
qm 160,96,136
qt 110,5,20992
ql 20,48,0
qm 232,96,208
qt 96,2,0
qw 5
qm 164,100,136
qt 98,2,512
ql 21,48,1024
qm 236,100,208
qt 100,3,0
qw 5
qm 160,104,140
qt 102,3,512
qm 232,104,212
qt 104,2,4096
qw 5
qm 164,108,140
qt 106,2,4608
qm 236,108,212
qt 108,3,4096
qw 5
qm 168,96,136
qt 110,3,4608
qm 240,96,208
qd 96,24,8192
qw 5
qm 172,100,136
qd 98,24,12288
qm 244,100,208
qd 100,25,8192
qw 5
qm 168,104,140
qd 102,25,12288
s_add_u32 m0,s13,81920
qm 240,104,212
qd 104,26,8192
qw 5
qm 172,108,140
s_waitcnt vmcnt(0)
s_barrier
qd 106,26,12288
s_add_u32 s14,s19,590208
qm 244,108,212
qd 108,27,8192
qw 5
qm 160,96,48
buffer_load_dwordx2 v[216:217],v11,s[36:39],s70 offen offset:256
qd 110,27,12288
ql 20,48,0
qm 232,96,56
qr 136,19,8192
qd 96,24,16384
qw 6
qm 160,100,52
qd 98,24,20480
ql 21,48,1024
qm 232,100,60
qr 208,19,9216
qd 100,25,16384
qw 7
qm 160,104,80
qd 102,25,20480
s_add_u32 m0,s13,65536
qm 232,104,88
qr 140,18,24576
qd 104,26,16384
qw 8
qm 160,108,84
qd 106,26,20480
s_add_u32 s14,s19,73984
qm 232,108,92
qr 212,18,25600
qd 108,27,16384
qw 8
qm 168,96,48
qd 110,27,20480
ql 20,48,0
qm 240,96,56
buffer_load_dwordx2 v[218:219],v12,s[80:83],s75 offen offset:256
qd 96,24,24576
qw 7
qm 168,100,52
qd 98,24,28672
ql 21,48,1024
qm 240,100,60
qd 100,25,24576
qw 6
qm 168,104,80
qd 102,25,28672
s_add_u32 m0,s13,73728
qm 240,104,88
qd 104,26,24576
qw 5
qm 168,108,84
qd 106,26,28672
s_add_u32 s14,s19,147712
qm 240,108,92
qd 108,27,24576
qw 5
qm 164,96,48
qd 110,27,28672
ql 20,48,0
qm 236,96,56
qd 96,24,32768
qw 5
qm 164,100,52
buffer_load_dwordx2 v[220:221],v11,s[36:39],s70 offen offset:896
qd 98,24,36864
ql 21,48,1024
qm 236,100,60
qd 100,25,32768
qw 5
qm 164,104,80
qd 102,25,36864
s_mov_b32 m0,s13
qm 236,104,88
qd 104,26,32768
qw 5
qm 164,108,84
s_barrier
qd 106,26,36864
s_add_u32 s14,s19,221440
qm 236,108,92
qd 108,27,32768
qw 5
qm 172,96,48
qd 110,27,36864
ql 20,48,0
qm 244,96,56
qt 96,2,40960
qw 5
qm 172,100,52
qt 98,2,41472
ql 21,48,1024
qm 244,100,60
buffer_load_dwordx2 v[222:223],v12,s[80:83],s75 offen offset:896
qt 100,3,40960
qw 5
qm 172,104,80
qt 102,3,41472
s_add_u32 m0,s13,8192
qm 244,104,88
qt 104,2,45056
qw 5
qm 172,108,84
qt 106,2,45568
s_add_u32 s14,s19,295168
qm 244,108,92
qt 108,3,45056
qw 5
qm 160,96,64
qt 110,3,45568
ql 20,48,0
qm 232,96,72
qr 48,18,32768
qt 96,2,49152
qw 6
qm 164,100,64
qt 98,2,49664
ql 21,48,1024
qm 236,100,72
qr 56,18,33792
qt 100,3,49152
qw 7
qm 160,104,68
qt 102,3,49664
s_add_u32 m0,s13,16384
qm 232,104,76
qr 52,18,40960
qt 104,2,53248
qw 8
qm 164,108,68
qt 106,2,53760
s_add_u32 s14,s19,74112
qm 236,108,76
qr 60,18,41984
qt 108,3,53248
qw 8
qm 168,96,64
qt 110,3,53760
ql 20,48,0
qm 240,96,72
qt 96,2,57344
qw 7
qm 172,100,64
qt 98,2,57856
ql 21,48,1024
qm 244,100,72
qt 100,3,57344
qw 6
qm 168,104,68
qt 102,3,57856
s_add_u32 m0,s13,24576
qm 240,104,76
qt 104,2,61440
qw 5
qm 172,108,68
s_waitcnt vmcnt(7)
s_barrier
qt 106,2,61952
s_add_u32 s14,s19,147840
qm 244,108,76
qt 108,3,61440
qw 5
qm 160,96,120
qt 110,3,61952
ql 20,48,0
qm 232,96,192
qr 80,18,8192
qt 96,4,16384
qw 6
qm 164,100,120
qt 98,4,16896
ql 21,48,1024
qm 236,100,192
qr 88,18,9216
qt 100,5,16384
qw 7
qm 160,104,124
qt 102,5,16896
s_add_u32 m0,s13,32768
qm 232,104,196
qr 84,18,16384
qt 104,4,20480
qw 8
qm 164,108,124
qt 106,4,20992
s_add_u32 s14,s19,221568
qm 236,108,196
qr 92,18,17408
qt 108,5,20480
qw 8
qm 168,96,120
qt 110,5,20992
ql 20,48,0
qm 240,96,192
qt 96,4,0
qw 7
qm 172,100,120
qt 98,4,512
ql 21,48,1024
qm 244,100,192
qt 100,5,0
qw 6
qm 168,104,124
qt 102,5,512
s_add_u32 m0,s13,40960
qm 240,104,196
qt 104,4,4096
qw 5
qm 172,108,124
qt 106,4,4608
s_add_u32 s14,s19,295296
qm 244,108,196
qt 108,5,4096
qw 5
qm 160,96,136
qt 110,5,4608
ql 20,48,0
qm 232,96,208
qr 64,18,49152
qt 96,4,8192
qw 6
qm 164,100,136
qt 98,4,8704
ql 21,48,1024
qm 236,100,208
qr 72,18,50176
qt 100,5,8192
qw 7
qm 160,104,140
qt 102,5,8704
qm 232,104,212
qr 68,18,57344
qt 104,4,12288
qw 8
qm 164,108,140
s_waitcnt vmcnt(6) lgkmcnt(1)
s_barrier
qt 106,4,12800
qm 236,108,212
qr 76,18,58368
qt 108,5,12288
qm 160,96,48
qt 110,5,12800
qm 232,96,56
qt 96,2,0
qm 164,100,48
qt 98,2,512
qm 236,100,56
qt 100,3,0
qw 6
qm 160,104,52
qt 102,3,512
qm 232,104,60
qt 104,2,4096
qw 5
qm 164,108,52
qt 106,2,4608
qm 236,108,60
qt 108,3,4096
qw 5
qm 160,96,80
qt 110,3,4608
qm 232,96,88
qt 96,2,8192
qw 5
qm 164,100,80
qt 98,2,8704
qm 236,100,88
qt 100,3,8192
qw 5
qm 160,104,84
qt 102,3,8704
qm 232,104,92
qt 104,2,12288
qw 5
qm 164,108,84
qt 106,2,12800
qm 236,108,92
qt 108,3,12288
qw 5
qm 160,96,64
qt 110,3,12800
qm 232,96,72
qt 96,2,16384
qw 5
qm 164,100,64
qt 98,2,16896
qm 236,100,72
qt 100,3,16384
qw 5
qm 160,104,68
qt 102,3,16896
qm 232,104,76
qt 104,2,20480
qw 5
qm 164,108,68
s_waitcnt vmcnt(0)
s_barrier
qt 106,2,20992
qm 236,108,76
qt 108,3,20480
qw 5
qm 168,96,136
qt 110,3,20992
qm 240,96,208
qt 96,2,24576
qw 5
qm 172,100,136
qt 98,2,25088
qm 244,100,208
v_cvt_pk_bf16_f32 v160,v160,v161
qt 100,3,24576
qw 5
qm 168,104,140
v_cvt_pk_bf16_f32 v161,v162,v163
qt 102,3,25088
qm 240,104,212
v_cvt_pk_bf16_f32 v162,v164,v165
qt 104,2,28672
qw 5
qm 172,108,140
v_cvt_pk_bf16_f32 v163,v166,v167
qt 106,2,29184
qm 244,108,212
qt 108,3,28672
qw 5
qm 168,96,48
v_cvt_pk_bf16_f32 v232,v232,v233
qt 110,3,29184
qm 240,96,56
v_cvt_pk_bf16_f32 v233,v234,v235
qt 96,2,32768
qw 5
qm 172,100,48
v_cvt_pk_bf16_f32 v234,v236,v237
qt 98,2,33280
qm 244,100,56
v_cvt_pk_bf16_f32 v235,v238,v239
qt 100,3,32768
qw 5
qm 168,104,52
s_add_u32 s15,s73,262144
buffer_store_dwordx4 v[160:163],v1,s[88:91],s15 offen offset:0 sc1
qt 102,3,33280
qm 240,104,60
qt 104,2,36864
qw 5
qm 172,108,52
qt 106,2,37376
qm 244,108,60
qt 108,3,36864
qw 5
qm 168,96,80
qt 110,3,37376
qm 240,96,88
qt 96,2,40960
qw 5
qm 172,100,80
qt 98,2,41472
qm 244,100,88
qt 100,3,40960
qw 5
qm 168,104,84
s_add_u32 s15,s73,262144
buffer_store_dwordx4 v[232:235],v1,s[88:91],s15 offen offset:1024 sc1
qt 102,3,41472
qm 240,104,92
qt 104,2,45056
qw 5
qm 172,108,84
s_barrier
qt 106,2,45568
qm 244,108,92
qt 108,3,45056
qw 5
qm 168,96,64
qt 110,3,45568
qm 240,96,72
qw 4
qm 172,100,64
qm 244,100,72
qw 2
qm 168,104,68
qm 240,104,76
qw 0
qm 172,108,68
qm 244,108,76
s_nop 3
v_cvt_pk_bf16_f32 v168,v168,v169
v_cvt_pk_bf16_f32 v169,v170,v171
s_nop 0
v_cvt_pk_bf16_f32 v170,v172,v173
v_cvt_pk_bf16_f32 v171,v174,v175
s_add_u32 s15,s73,393216
buffer_store_dwordx4 v[168:171],v1,s[88:91],s15 offen offset:0 sc1
v_cvt_pk_bf16_f32 v240,v240,v241
v_cvt_pk_bf16_f32 v241,v242,v243
v_cvt_pk_bf16_f32 v242,v244,v245
v_cvt_pk_bf16_f32 v243,v246,v247
s_add_u32 s15,s73,393216
buffer_store_dwordx4 v[240:243],v1,s[88:91],s15 offen offset:1024 sc1
v_lshlrev_b32 v16,16,v218
v_lshlrev_b32 v176,16,v216
v_mul_f32 v16,v16,v40
v_add_f32 v17,v176,v41
v_fma_f32 v176,v17,v16,v176
v_and_b32 v16,0xffff0000,v218
v_and_b32 v177,0xffff0000,v216
v_mul_f32 v16,v16,v40
v_add_f32 v17,v177,v41
v_fma_f32 v177,v17,v16,v177
v_lshlrev_b32 v16,16,v219
v_lshlrev_b32 v178,16,v217
v_mul_f32 v16,v16,v40
v_add_f32 v17,v178,v41
v_fma_f32 v178,v17,v16,v178
v_and_b32 v16,0xffff0000,v219
v_and_b32 v179,0xffff0000,v217
v_mul_f32 v16,v16,v40
v_add_f32 v17,v179,v41
v_fma_f32 v179,v17,v16,v179
v_lshlrev_b32 v16,16,v222
v_lshlrev_b32 v180,16,v220
v_mul_f32 v16,v16,v40
v_add_f32 v17,v180,v41
v_fma_f32 v180,v17,v16,v180
v_and_b32 v16,0xffff0000,v222
v_and_b32 v181,0xffff0000,v220
v_mul_f32 v16,v16,v40
v_add_f32 v17,v181,v41
v_fma_f32 v181,v17,v16,v181
v_lshlrev_b32 v16,16,v223
v_lshlrev_b32 v182,16,v221
v_mul_f32 v16,v16,v40
v_add_f32 v17,v182,v41
v_fma_f32 v182,v17,v16,v182
v_and_b32 v16,0xffff0000,v223
v_and_b32 v183,0xffff0000,v221
v_mul_f32 v16,v16,v40
v_add_f32 v17,v183,v41
v_fma_f32 v183,v17,v16,v183
buffer_load_dwordx4 v[252:255],v15,s[96:99],s72 offen sc1
s_waitcnt vmcnt(0)
s_barrier
.L_tX10:
v_sub_u32 v252,s74,v252
v_sub_u32 v253,s74,v253
v_sub_u32 v254,s74,v254
v_sub_u32 v255,s74,v255
v_cndmask_b32 v252,v252,0,s[40:41]
v_cndmask_b32 v253,v253,0,s[42:43]
v_cmp_ge_u32 vcc,0,v252
s_nop 1
s_andn2_b64 exec,vcc,s[52:53]
s_cbranch_scc0 .L_tX10r10
s_add_u32 s31,s69,262144
buffer_load_dwordx4 v[152:155],v10,s[88:91],s31 offen sc1
s_add_u32 s31,s69,393216
buffer_load_dwordx4 v[224:227],v10,s[88:91],s31 offen sc1
.L_tX10r10:
s_mov_b64 exec,-1
s_or_b64 s[52:53],s[52:53],vcc
v_cmp_ge_u32 vcc,0,v253
s_nop 1
s_andn2_b64 exec,vcc,s[54:55]
s_cbranch_scc0 .L_tX10r11
s_add_u32 s31,s69,327680
buffer_load_dwordx4 v[156:159],v10,s[88:91],s31 offen sc1
s_add_u32 s31,s69,458752
buffer_load_dwordx4 v[228:231],v10,s[88:91],s31 offen sc1
.L_tX10r11:
s_mov_b64 exec,-1
s_or_b64 s[54:55],s[54:55],vcc
v_cmp_ge_u32 vcc,0,v254
s_nop 1
s_andn2_b64 exec,vcc,s[56:57]
s_cbranch_scc0 .L_tX10r12
s_add_u32 s31,s69,917504
buffer_load_dwordx4 v[112:115],v10,s[88:91],s31 offen sc1
s_add_u32 s31,s69,1048576
buffer_load_dwordx4 v[184:187],v10,s[88:91],s31 offen sc1
.L_tX10r12:
s_mov_b64 exec,-1
s_or_b64 s[56:57],s[56:57],vcc
v_cmp_ge_u32 vcc,0,v255
s_nop 1
s_andn2_b64 exec,vcc,s[58:59]
s_cbranch_scc0 .L_tX10r13
s_add_u32 s31,s69,983040
buffer_load_dwordx4 v[116:119],v10,s[88:91],s31 offen sc1
s_add_u32 s31,s69,1114112
buffer_load_dwordx4 v[188:191],v10,s[88:91],s31 offen sc1
.L_tX10r13:
s_mov_b64 exec,-1
s_or_b64 s[58:59],s[58:59],vcc
.L_tX10c:
s_and_b64 s[32:33],s[52:53],s[54:55]
s_and_b64 s[32:33],s[32:33],s[56:57]
s_and_b64 s[32:33],s[32:33],s[58:59]
s_cmp_eq_u64 s[32:33],-1
s_cbranch_scc1 .L_tX10x
s_add_u32 s76,s76,1
s_cmp_gt_u32 s76,1000000
s_cbranch_scc1 .L_tX10x
s_sleep 4
buffer_load_dwordx4 v[252:255],v15,s[96:99],s72 offen sc1
s_waitcnt vmcnt(0)
s_branch .L_tX10
.L_tX10x:
s_waitcnt vmcnt(0)
qm 176,144,152
qm 180,144,112
qm 176,144,156
qm 180,144,116
qm 176,148,224
qm 180,148,184
qm 176,148,228
qm 180,148,188
s_nop 6
buffer_store_dwordx4 v[176:179],v13,s[92:95],s71 offen offset:512 sc1
buffer_store_dwordx4 v[180:183],v13,s[92:95],s71 offen offset:1792 sc1
s_endpgm
.org .L_code_X1+40960
.L_code_X11:
qw 0
s_barrier
qr 112,24,24576
qr 184,24,0
qr 96,20,0
qr 100,20,2048
qr 116,25,28672
qr 188,25,4096
qr 104,21,0
qr 108,21,2048
qr 120,24,32768
qr 192,24,8192
qw 7
qz 48,112,96
qr 124,25,36864
qr 196,25,12288
ds_write_b128 v31,v[208:211] offset:24576
qz 52,184,96
qr 200,24,16384
ds_write_b128 v32,v[212:215] offset:24576
qr 96,20,8192
qw 12
qz 56,112,100
qr 128,24,40960
qr 204,25,20480
s_sub_u32 s15,s17,0
buffer_load_dword v1,v6,s[44:47],s15 offen
qz 60,184,100
ds_write_b128 v31,v[216:219] offset:32768
qr 100,20,10240
qw 13
qm 48,116,104
qr 132,25,45056
ds_write_b128 v32,v[220:223] offset:32768
qm 52,188,104
qr 104,21,8192
qw 15
qm 56,116,108
qr 136,24,49152
ds_write_b128 v31,v[224:227] offset:40960
qm 60,188,108
ds_write_b128 v32,v[228:231] offset:40960
qr 108,21,10240
qw 11
qm 48,120,96
qw 9
s_barrier
qr 140,25,53248
qm 52,192,96
ds_write_b128 v31,v[232:235] offset:49152
qr 96,20,16384
qw 10
qm 56,120,100
qr 144,24,57344
buffer_load_dword v1,v8,s[32:35],s53 offen
qm 60,192,100
ds_write_b128 v32,v[236:239] offset:49152
qr 100,20,18432
qw 10
qm 48,124,104
qr 148,25,61440
qm 52,196,104
ds_write_b128 v31,v[240:243] offset:57344
qr 104,21,16384
qw 9
qm 56,124,108
qr 152,26,8192
qm 60,196,108
ds_write_b128 v32,v[244:247] offset:57344
qr 108,21,18432
qw 9
qm 48,128,96
qr 156,27,12288
qm 52,200,96
ds_write_b128 v38,v[248:251] offset:8192
qr 96,20,24576
qw 9
qm 56,128,100
qr 160,26,16384
buffer_load_dword v1,v8,s[80:83],0 offen
qm 60,200,100
ds_write_b128 v39,v[252:255] offset:8192
qr 100,20,26624
qw 9
qm 48,132,104
qr 164,27,20480
qm 52,204,104
qr 104,21,24576
qw 8
qm 56,132,108
qw 1
s_barrier
qr 168,26,24576
qr 208,24,24576
qm 60,204,108
qr 212,25,28672
qr 108,21,26624
qm 48,136,96
qr 172,27,28672
v_add_f32 v16,v16,v17
qw 3
qm 52,208,96
qr 216,24,32768
v_add_f32 v17,v28,v29
qr 96,20,32768
qm 56,136,100
qr 176,26,32768
qr 220,25,36864
v_add_f32_dpp v16,v16,v16 quad_perm:[1,0,3,2] row_mask:0xf bank_mask:0xf
qm 60,208,100
v_add_f32 v33,v33,v35
qr 100,20,34816
qm 48,140,104
qr 180,27,36864
qr 224,24,40960
v_add_f32_dpp v17,v17,v17 quad_perm:[1,0,3,2] row_mask:0xf bank_mask:0xf
qw 9
qm 52,212,104
qr 228,25,45056
v_add_f32_dpp v16,v16,v16 quad_perm:[2,3,0,1] row_mask:0xf bank_mask:0xf
qr 104,21,32768
qw 10
qm 56,140,108
v_add_f32 v34,v34,v36
qm 60,212,108
qr 232,24,49152
v_add_f32_dpp v17,v17,v17 quad_perm:[2,3,0,1] row_mask:0xf bank_mask:0xf
qr 108,21,34816
qw 9
qm 48,144,96
qr 236,25,53248
qm 52,216,96
v_add_f32_dpp v16,v16,v16 row_half_mirror row_mask:0xf bank_mask:0xf
qr 96,20,40960
qw 8
qm 56,144,100
qr 240,24,57344
v_lshrrev_b32 v44,3,v0
qm 60,216,100
qr 244,25,61440
v_add_f32_dpp v17,v17,v17 row_half_mirror row_mask:0xf bank_mask:0xf
qr 100,20,43008
qw 7
qm 48,148,104
v_mul_f32 v16,0x3ae38e39,v16
qm 52,220,104
qr 248,26,8192
v_fma_f32 v16,-v33,v33,v16
qr 104,21,40960
qw 7
qm 56,148,108
qr 252,27,12288
v_max_f32 v16,0,v16
qm 60,220,108
v_mul_f32 v17,0x3ae38e39,v17
qr 108,21,43008
qw 7
qm 48,152,96
v_fma_f32 v17,-v34,v34,v17
qm 52,224,96
qr 96,20,49152
qw 5
qm 56,152,100
v_max_f32 v17,0,v17
s_mov_b32 s53,s10
qm 60,224,100
v_cvt_pk_bf16_f32 v42,v16,v17
qr 100,20,51200
qw 4
qm 48,156,104
v_lshlrev_b32 v16,16,v42
s_lshl_b32 s54,s53,2
qm 52,228,104
v_and_b32 v17,0xffff0000,v42
qr 104,21,49152
qw 3
qm 56,156,108
s_barrier
v_add_f32 v16,0x3727c5ac,v16
s_add_u32 s54,s54,s7
qm 60,228,108
v_add_f32 v17,0x3727c5ac,v17
qr 108,21,51200
qw 3
qm 48,160,96
v_cvt_pk_bf16_f32 v42,v16,v17
s_mul_i32 s54,s54,6912
qm 52,232,96
v_lshlrev_b32 v16,16,v42
qr 96,20,57344
qw 3
qm 56,160,100
s_mul_i32 s52,s8,663552
qm 60,232,100
v_and_b32 v17,0xffff0000,v42
qr 100,20,59392
qw 3
qm 48,164,104
v_sqrt_f32 v16,v16
qm 52,236,104
v_sqrt_f32 v17,v17
s_add_u32 s54,s54,s52
qr 104,21,57344
qw 3
qm 56,164,108
v_and_b32 v43,4,v44
qm 60,236,108
v_cvt_pk_bf16_f32 v42,v16,v17
s_add_u32 s54,s54,221184
qr 108,21,59392
qw 3
qm 48,168,96
v_lshlrev_b32 v16,16,v42
qm 52,240,96
v_and_b32 v17,0xffff0000,v42
v_min_u32 v8,53,v0
qr 96,22,0
qw 3
qm 56,168,100
v_readfirstlane_b32 s3,v13
s_mul_i32 s16,s3,0x9E3779B1
s_add_u32 s16,s16,0x7F4A7C15
v_rcp_f32 v16,v16
qm 60,240,100
v_lshlrev_b32 v8,7,v8
qr 100,22,2048
qw 3
qm 48,172,104
v_rcp_f32 v17,v17
qm 52,244,104
v_add3_u32 v44,v43,v44,s29
qr 104,23,0
qw 3
qm 56,172,108
v_lshlrev_b32 v43,2,v44
qm 60,244,108
v_add_u32 v43,122880,v43
qr 108,23,2048
qw 3
qm 48,176,96
ds_write_b32 v43,v16
qm 52,248,96
ds_write_b32 v43,v17 offset:64
qt 96,4,8192
qw 5
qm 56,176,100
ds_write_b32 v43,v35 offset:512
qt 98,4,8704
qm 60,248,100
ds_write_b32 v43,v36 offset:576
qt 100,5,8192
qw 8
qm 48,180,104
qt 102,5,8704
qm 52,252,104
qt 104,4,12288
qw 9
qm 56,180,108
qw 3
s_barrier
qt 106,4,12800
qm 60,252,108
qt 108,5,12288
qz 64,96,112
v_lshrrev_b32 v42,4,v0
v_and_b32 v43,15,v0
v_bfe_u32 v45,v0,2,2
qt 110,5,12800
s_setprio 0
qz 68,96,184
v_lshl_add_u32 v36,s7,2,v42
v_lshlrev_b32 v42,5,v36
v_lshl_add_u32 v45,s7,2,v45
qt 96,2,0
qw 5
qz 72,100,112
v_lshlrev_b32 v37,3,v43
v_and_b32 v14,3,v0
qt 98,2,512
qz 76,100,184
v_lshl_add_u32 v43,v36,9,v37
v_add_u32 v42,122880,v42
v_lshl_add_u32 v45,v45,3,v14
qt 100,3,0
s_add_u32 s15,s17,128
buffer_load_dword v1,v6,s[40:43],s15 offen
qw 5
qm 64,104,116
v_add_u32 v37,124928,v37
v_add_u32 v43,114688,v43
v_lshlrev_b32 v45,2,v45
s_lshl_b32 s30,s10,3
s_add_u32 s30,s30,s8
s_mul_i32 s30,s30,196608
s_add_u32 s76,s30,21020672
s_add_u32 s77,s30,21118976
s_lshl_b32 s30,s7,13
s_add_u32 s79,s76,s30
qt 102,3,512
qm 68,104,188
qr 28,42,0
v_add_u32 v45,122880,v45
qt 104,2,4096
qw 6
qm 72,108,116
qr 32,42,16
qd 36,37,0
ds_read_b32 v10,v45
qt 106,2,4608
qm 76,108,188
s_nop 0
s_nop 0
ds_read_b32 v11,v45 offset:16
qt 108,3,4096
qw 10
qm 64,96,120
s_nop 0
qt 110,3,4608
qm 68,96,192
qw 8
v_mul_f32 v48,v48,v28
v_mul_f32 v56,v56,v28
qt 96,2,8192
qm 72,100,120
v_mov_b32 v38,v48
qt 98,2,8704
qm 76,100,192
v_mov_b32 v39,v56
v_cvt_pk_bf16_f32 v48,v48,v56
qt 100,3,8192
qw 6
qm 64,104,124
v_mul_f32 v49,v49,v29
v_mul_f32 v57,v57,v29
v_mul_f32 v14,v10,v10
qt 102,3,8704
qm 68,104,196
v_fma_f32 v40,v36,v38,v49
v_mul_f32 v12,0xbdd2d3e8,v10
qt 104,2,12288
qw 5
qm 72,108,124
v_fma_f32 v41,v36,v39,v57
v_fma_f32 v40,-v37,v39,v40
v_mul_f32 v10,0xc0135761,v10
qt 106,2,12800
qm 76,108,196
v_fma_f32 v41,v37,v38,v41
v_cvt_pk_bf16_f32 v49,v49,v57
v_mul_f32 v12,v12,v14
qt 108,3,12288
qw 5
qm 64,96,128
v_mul_f32 v50,v50,v30
v_rcp_f32 v14,v14
qt 110,3,12800
qm 68,96,200
v_mul_f32 v58,v58,v30
v_fma_f32 v38,v36,v40,v50
v_mul_f32 v15,v11,v11
qt 96,2,16384
qw 5
qm 72,100,128
v_fma_f32 v39,v36,v41,v58
v_fma_f32 v38,-v37,v41,v38
v_mul_f32 v13,0xbdd2d3e8,v11
qt 98,2,16896
qm 76,100,200
v_fma_f32 v39,v37,v40,v39
v_mul_f32 v11,0xc0135761,v11
qt 100,3,16384
qw 5
qm 64,104,132
v_cvt_pk_bf16_f32 v50,v50,v58
v_mul_f32 v51,v51,v31
v_mul_f32 v13,v13,v15
qt 102,3,16896
qm 68,104,204
v_mul_f32 v59,v59,v31
v_rcp_f32 v15,v15
qt 104,2,20480
qw 5
qm 72,108,132
s_barrier
v_fma_f32 v40,v36,v38,v51
v_fma_f32 v41,v36,v39,v59
qt 106,2,20992
qm 76,108,204
v_fma_f32 v40,-v37,v39,v40
v_fma_f32 v41,v37,v38,v41
qt 108,3,20480
qw 5
qm 64,96,136
v_cvt_pk_bf16_f32 v51,v51,v59
qt 110,3,20992
qm 68,96,208
ds_write_b128 v19,v[48:51]
v_mul_f32 v52,v52,v32
qt 96,2,24576
qw 6
qm 72,100,136
v_mul_f32 v60,v60,v32
v_fma_f32 v38,v36,v40,v52
qt 98,2,25088
qm 76,100,208
v_fma_f32 v39,v36,v41,v60
qt 100,3,24576
qw 6
qm 64,104,140
v_fma_f32 v38,-v37,v41,v38
v_fma_f32 v39,v37,v40,v39
qt 102,3,25088
qm 68,104,212
v_cvt_pk_bf16_f32 v52,v52,v60
v_mul_f32 v53,v53,v33
qt 104,2,28672
qw 6
qm 72,108,140
v_mul_f32 v61,v61,v33
qt 106,2,29184
qm 76,108,212
v_fma_f32 v40,v36,v38,v53
v_fma_f32 v41,v36,v39,v61
qt 108,3,28672
qw 5
qm 64,96,144
v_fma_f32 v40,-v37,v39,v40
qt 110,3,29184
qm 68,96,216
v_fma_f32 v41,v37,v38,v41
v_cvt_pk_bf16_f32 v53,v53,v61
qt 96,2,32768
qw 5
qm 72,100,144
v_mul_f32 v54,v54,v34
v_mul_f32 v62,v62,v34
qt 98,2,33280
qm 76,100,216
v_fma_f32 v38,v36,v40,v54
qt 100,3,32768
qw 5
qm 64,104,148
v_fma_f32 v39,v36,v41,v62
v_fma_f32 v38,-v37,v41,v38
qt 102,3,33280
qm 68,104,220
v_fma_f32 v39,v37,v40,v39
v_cvt_pk_bf16_f32 v54,v54,v62
qt 104,2,36864
qw 5
qm 72,108,148
v_mul_f32 v55,v55,v35
qt 106,2,37376
qm 76,108,220
v_mul_f32 v63,v63,v35
v_fma_f32 v40,v36,v38,v55
qt 108,3,36864
qw 5
qm 64,96,152
v_fma_f32 v41,v36,v39,v63
v_fma_f32 v40,-v37,v39,v40
qt 110,3,37376
qm 68,96,224
v_fma_f32 v41,v37,v38,v41
qt 96,2,40960
qw 5
qm 72,100,152
v_cvt_pk_bf16_f32 v55,v55,v63
ds_write_b128 v19,v[52:55] offset:1024
qt 98,2,41472
qm 76,100,224
ds_write_b64 v43,v[40:41] offset:0
qt 100,3,40960
qw 7
qm 64,104,156
qt 102,3,41472
qm 68,104,228
qt 104,2,45056
qw 7
qm 72,108,156
qw 3
s_barrier
qt 106,2,45568
qm 76,108,228
v_lshrrev_b32 v43,4,v0
v_and_b32 v60,15,v0
qt 108,3,45056
qm 64,96,160
v_lshl_add_u32 v60,s7,4,v60
qt 110,3,45568
qm 68,96,232
v_lshlrev_b32 v43,11,v43
v_lshl_add_u32 v43,v60,3,v43
qt 96,4,16384
qw 5
qm 72,100,160
v_add_u32 v43,114688,v43
qt 98,4,16896
qm 76,100,232
v_lshlrev_b32 v60,4,v60
v_add_u32 v42,123904,v60
qt 100,5,16384
qw 5
qm 64,104,164
qd 36,42,0
qt 102,5,16896
qm 68,104,236
qd 28,43,0
qd 30,43,512
qt 104,4,20480
qw 8
qm 72,108,164
qd 32,43,1024
qt 106,4,20992
qm 76,108,236
qd 34,43,1536
s_mov_b32 s30,0
qt 108,5,20480
qw 10
qm 64,96,168
s_mov_b32 s31,-1
qt 110,5,20992
qm 68,96,240
s_mov_b32 s32,0xffff0000
s_mov_b32 s33,0xffff0000
qt 96,4,24576
qw 9
qm 72,100,168
v_mov_b32 v57,s16
qt 98,4,25088
qm 76,100,240
v_mov_b32 v59,s16
v_mul_f32 v42,0x41000000,v36
qt 100,5,24576
qw 6
qm 64,104,172
v_mul_f32 v44,0x42000000,v36
qt 102,5,25088
qm 68,104,244
v_mul_f32 v39,0x41000000,v37
v_mul_f32 v41,0x42000000,v37
qt 104,4,28672
qw 5
qm 72,108,172
v_exp_f32 v42,v42
qt 106,4,29184
qm 76,108,244
v_exp_f32 v44,v44
v_fract_f32 v39,v39
qt 108,5,28672
qw 5
qm 64,96,176
v_fract_f32 v41,v41
qt 110,5,29184
qm 68,96,248
v_cos_f32 v38,v39
v_cos_f32 v40,v41
qt 96,4,32768
qw 5
qm 72,100,176
v_sin_f32 v39,v39
qt 98,4,33280
qm 76,100,248
v_sin_f32 v41,v41
v_mul_f32 v38,v38,v42
qt 100,5,32768
qw 5
qm 64,104,180
v_mul_f32 v40,v40,v44
qt 102,5,33280
qm 68,104,252
v_mul_f32 v39,v39,v42
qt 104,4,36864
qw 5
qm 72,108,180
s_barrier
v_mul_f32 v41,v41,v44
v_fma_f32 v44,v38,v28,v30
qt 106,4,37376
qm 76,108,252
v_fma_f32 v45,v38,v29,v31
qt 108,5,36864
qw 5
qz 80,96,112
v_fma_f32 v44,-v39,v29,v44
v_fma_f32 v45,v39,v28,v45
qt 110,5,37376
qz 84,96,184
v_fma_f32 v48,v38,v44,v32
v_mul_f32 v16,v64,v64
qt 96,4,40960
qw 5
qz 88,100,112
v_fma_f32 v49,v38,v45,v33
v_fma_f32 v48,-v39,v45,v48
v_fma_f32 v16,v16,v12,v10
qt 98,4,41472
qz 92,100,184
v_fma_f32 v49,v39,v44,v49
v_mul_f32 v16,v16,v64
qt 100,5,40960
s_add_u32 s15,s17,256
buffer_load_dword v1,v6,s[40:43],s15 offen
qw 5
qm 80,104,116
v_fma_f32 v44,v38,v48,v34
v_fma_f32 v45,v38,v49,v35
v_exp_f32 v16,v16
qt 102,5,41472
qm 84,104,188
v_fma_f32 v44,-v39,v49,v44
v_fma_f32 v16,v16,v14,v14
qt 104,4,45056
qw 5
qm 88,108,116
v_fma_f32 v45,v39,v48,v45
v_mov_b32 v48,v44
v_rcp_f32 v16,v16
qt 106,4,45568
qm 92,108,188
v_mov_b32 v49,v45
v_mul_f32 v64,v64,v16
qt 108,5,45056
qw 5
qm 80,96,120
v_permlane16_swap_b32 v44,v48
v_permlane16_swap_b32 v45,v49
v_mul_f32 v16,v65,v65
qt 110,5,45568
qm 84,96,192
v_fma_f32 v50,v40,v44,v48
v_fma_f32 v16,v16,v12,v10
qt 96,2,49152
qw 5
qm 88,100,120
v_fma_f32 v51,v40,v45,v49
v_fma_f32 v50,-v41,v45,v50
v_mul_f32 v16,v16,v65
qt 98,2,49664
qm 92,100,192
v_fma_f32 v51,v41,v44,v51
v_exp_f32 v16,v16
qt 100,3,49152
qw 5
qm 80,104,124
v_mov_b32 v48,v50
v_mov_b32 v49,v51
v_fma_f32 v16,v16,v14,v14
qt 102,3,49664
qm 84,104,196
v_permlane32_swap_b32 v50,v48
v_rcp_f32 v16,v16
qt 104,2,53248
qw 5
qm 88,108,124
v_permlane32_swap_b32 v51,v49
v_cndmask_b32 v50,0,v50,s[30:31]
v_mul_f32 v65,v65,v16
qt 106,2,53760
qm 92,108,196
v_cndmask_b32 v51,0,v51,s[30:31]
v_mul_f32 v16,v66,v66
qt 108,3,53248
qw 5
qm 80,96,128
v_fma_f32 v48,v40,v50,v44
v_fma_f32 v49,v40,v51,v45
v_fma_f32 v16,v16,v12,v10
qt 110,3,53760
qm 84,96,200
v_fma_f32 v48,-v41,v51,v48
v_mul_f32 v16,v16,v66
qt 96,2,57344
qw 5
qm 88,100,128
v_fma_f32 v49,v41,v50,v49
v_exp_f32 v16,v16
qt 98,2,57856
qm 92,100,200
v_cndmask_b32 v50,v50,v48,s[32:33]
v_cndmask_b32 v51,v51,v49,s[32:33]
v_fma_f32 v16,v16,v14,v14
qt 100,3,57344
qw 5
qm 80,104,132
ds_write_b64 v43,v[50:51] offset:0
v_rcp_f32 v16,v16
qt 102,3,57856
qm 84,104,204
v_fma_f32 v48,v38,v50,v28
v_fma_f32 v49,v38,v51,v29
v_mul_f32 v66,v66,v16
qt 104,2,61440
qw 6
qm 88,108,132
qw 2
s_barrier
v_fma_f32 v48,-v39,v51,v48
v_mul_f32 v16,v67,v67
qt 106,2,61952
qm 92,108,204
v_fma_f32 v49,v39,v50,v49
ds_write_b64 v43,v[48:49] offset:512
v_fma_f32 v16,v16,v12,v10
qt 108,3,61440
qm 80,96,136
v_fma_f32 v44,v38,v48,v30
v_mul_f32 v16,v16,v67
qt 110,3,61952
qm 84,96,208
v_fma_f32 v45,v38,v49,v31
v_fma_f32 v44,-v39,v49,v44
v_exp_f32 v16,v16
qt 96,4,0
qw 6
qm 88,100,136
v_fma_f32 v45,v39,v48,v45
v_fma_f32 v16,v16,v14,v14
qt 98,4,512
qm 92,100,208
ds_write_b64 v43,v[44:45] offset:1024
v_fma_f32 v52,v38,v44,v32
v_rcp_f32 v16,v16
qt 100,5,0
qw 7
qm 80,104,140
v_fma_f32 v53,v38,v45,v33
v_mul_f32 v67,v67,v16
qt 102,5,512
qm 84,104,212
v_fma_f32 v52,-v39,v45,v52
v_fma_f32 v53,v39,v44,v53
v_mul_f32 v16,v72,v72
qt 104,4,4096
qw 6
qm 88,108,140
ds_write_b64 v43,v[52:53] offset:1536
v_fma_f32 v16,v16,v12,v10
qt 106,4,4608
qm 92,108,212
v_fma_f32 v56,v38,v52,v34
v_fma_f32 v58,v38,v53,v35
v_mul_f32 v16,v16,v72
qt 108,5,4096
qw 7
qm 80,96,144
v_fma_f32 v56,-v39,v53,v56
v_exp_f32 v16,v16
qt 110,5,4608
qm 84,96,216
v_fma_f32 v58,v39,v52,v58
v_cmp_gt_u32 vcc,48,v0
v_fma_f32 v16,v16,v14,v14
qt 96,4,8192
qw 6
qm 88,100,144
v_mov_b32 v42,0x7ff00000
v_rcp_f32 v16,v16
qt 98,4,8704
qm 92,100,216
s_mul_i32 s30,s10,9216
s_lshl_b32 s31,s12,8
v_mul_f32 v72,v72,v16
qt 100,5,8192
qw 5
qm 80,104,148
s_add_u32 s30,s30,s31
v_mul_f32 v16,v73,v73
qt 102,5,8704
qm 84,104,220
v_cndmask_b32 v60,v60,v42,vcc
s_cmp_eq_u32 s7,0
s_cselect_b32 s30,s30,0x7ff00000
v_fma_f32 v16,v16,v12,v10
qt 104,4,12288
qw 5
qm 88,108,148
buffer_store_dwordx2 v[56:57],v60,s[84:87],s30 offen sc1
v_mul_f32 v16,v16,v73
qt 106,4,12800
qm 92,108,220
buffer_store_dwordx2 v[58:59],v60,s[84:87],s30 offen offset:8 sc1
v_exp_f32 v16,v16
qt 108,5,12288
qw 5
qm 80,96,152
v_fma_f32 v16,v16,v14,v14
qt 110,5,12800
qm 84,96,224
v_rcp_f32 v16,v16
qt 96,2,0
qw 5
qm 88,100,152
v_mul_f32 v73,v73,v16
qt 98,2,512
qm 92,100,224
v_mul_f32 v16,v74,v74
qt 100,3,0
qw 5
qm 80,104,156
v_fma_f32 v16,v16,v12,v10
qt 102,3,512
qm 84,104,228
v_mul_f32 v16,v16,v74
qt 104,2,4096
qw 5
qm 88,108,156
s_barrier
v_exp_f32 v16,v16
qt 106,2,4608
qm 92,108,228
v_fma_f32 v16,v16,v14,v14
qt 108,3,4096
qw 5
qm 80,96,160
v_rcp_f32 v16,v16
qt 110,3,4608
qm 84,96,232
v_mul_f32 v74,v74,v16
qt 96,4,16384
qw 5
qm 88,100,160
v_mul_f32 v16,v75,v75
qt 98,4,16896
qm 92,100,232
v_fma_f32 v16,v16,v12,v10
qt 100,5,16384
qw 5
qm 80,104,164
v_mul_f32 v16,v16,v75
qt 102,5,16896
qm 84,104,236
v_exp_f32 v16,v16
qt 104,4,20480
qw 5
qm 88,108,164
v_fma_f32 v16,v16,v14,v14
qt 106,4,20992
qm 92,108,236
v_rcp_f32 v16,v16
qt 108,5,20480
qw 5
qm 80,96,168
v_mul_f32 v75,v75,v16
qt 110,5,20992
qm 84,96,240
v_mul_f32 v16,v68,v68
qt 96,4,24576
qw 5
qm 88,100,168
v_fma_f32 v16,v16,v13,v11
qt 98,4,25088
qm 92,100,240
v_mul_f32 v16,v16,v68
qt 100,5,24576
qw 5
qm 80,104,172
v_exp_f32 v16,v16
qt 102,5,25088
qm 84,104,244
v_fma_f32 v16,v16,v15,v15
qt 104,4,28672
qw 5
qm 88,108,172
v_rcp_f32 v16,v16
qt 106,4,29184
qm 92,108,244
v_mul_f32 v68,v68,v16
qt 108,5,28672
qw 5
qm 80,96,176
v_mul_f32 v16,v69,v69
qt 110,5,29184
)ASM"
R"ASM(qm 84,96,248
v_fma_f32 v16,v16,v13,v11
qt 96,2,8192
qw 5
qm 88,100,176
v_mul_f32 v16,v16,v69
qt 98,2,8704
qm 92,100,248
v_exp_f32 v16,v16
qt 100,3,8192
qw 5
qm 80,104,180
v_fma_f32 v16,v16,v15,v15
qt 102,3,8704
qm 84,104,252
v_rcp_f32 v16,v16
qt 104,2,12288
qw 5
qm 88,108,180
s_barrier
v_mul_f32 v69,v69,v16
qt 106,2,12800
qm 92,108,252
qt 108,3,12288
qw 5
qz 48,96,112
v_mul_f32 v16,v70,v70
qt 110,3,12800
qz 52,96,184
v_fma_f32 v16,v16,v13,v11
qt 96,4,32768
qw 5
qz 56,100,112
v_mul_f32 v16,v16,v70
qt 98,4,33280
qz 60,100,184
v_exp_f32 v16,v16
qt 100,5,32768
qw 5
qm 48,104,116
v_fma_f32 v16,v16,v15,v15
qt 102,5,33280
qm 52,104,188
v_rcp_f32 v16,v16
qt 104,4,36864
qw 5
qm 56,108,116
v_mul_f32 v70,v70,v16
qt 106,4,37376
qm 60,108,188
v_mul_f32 v16,v71,v71
qt 108,5,36864
qw 5
qm 48,96,120
v_fma_f32 v16,v16,v13,v11
qt 110,5,37376
qm 52,96,192
v_mul_f32 v16,v16,v71
qt 96,4,40960
qw 5
qm 56,100,120
v_exp_f32 v16,v16
qt 98,4,41472
qm 60,100,192
v_fma_f32 v16,v16,v15,v15
qt 100,5,40960
qw 5
qm 48,104,124
v_rcp_f32 v16,v16
qt 102,5,41472
qm 52,104,196
v_mul_f32 v71,v71,v16
qt 104,4,45056
qw 5
qm 56,108,124
v_mul_f32 v16,v76,v76
qt 106,4,45568
qm 60,108,196
v_fma_f32 v16,v16,v13,v11
qt 108,5,45056
qw 5
qm 48,96,128
v_mul_f32 v16,v16,v76
qt 110,5,45568
qm 52,96,200
v_exp_f32 v16,v16
qt 96,2,16384
qw 5
qm 56,100,128
v_fma_f32 v16,v16,v15,v15
qt 98,2,16896
qm 60,100,200
v_rcp_f32 v16,v16
qt 100,3,16384
qw 5
qm 48,104,132
v_mul_f32 v76,v76,v16
qt 102,3,16896
qm 52,104,204
v_mul_f32 v16,v77,v77
qt 104,2,20480
qw 5
qm 56,108,132
s_barrier
v_fma_f32 v16,v16,v13,v11
qt 106,2,20992
qm 60,108,204
v_mul_f32 v16,v16,v77
qt 108,3,20480
qw 5
qm 48,96,136
v_exp_f32 v16,v16
qt 110,3,20992
qm 52,96,208
v_fma_f32 v16,v16,v15,v15
qt 96,2,24576
qw 5
qm 56,100,136
v_rcp_f32 v16,v16
qt 98,2,25088
qm 60,100,208
v_mul_f32 v77,v77,v16
qt 100,3,24576
qw 5
qm 48,104,140
v_mul_f32 v16,v78,v78
qt 102,3,25088
qm 52,104,212
v_fma_f32 v16,v16,v13,v11
qt 104,2,28672
qw 5
qm 56,108,140
v_mul_f32 v16,v16,v78
qt 106,2,29184
qm 60,108,212
v_exp_f32 v16,v16
qt 108,3,28672
qw 5
qm 48,96,144
v_fma_f32 v16,v16,v15,v15
qt 110,3,29184
qm 52,96,216
v_rcp_f32 v16,v16
qt 96,2,32768
qw 5
qm 56,100,144
v_mul_f32 v78,v78,v16
qt 98,2,33280
qm 60,100,216
v_mul_f32 v16,v79,v79
qt 100,3,32768
qw 5
qm 48,104,148
v_fma_f32 v16,v16,v13,v11
qt 102,3,33280
qm 52,104,220
v_mul_f32 v16,v16,v79
qt 104,2,36864
qw 5
qm 56,108,148
v_exp_f32 v16,v16
qt 106,2,37376
qm 60,108,220
v_fma_f32 v16,v16,v15,v15
qt 108,3,36864
qw 5
qm 48,96,152
v_rcp_f32 v16,v16
qt 110,3,37376
qm 52,96,224
v_mul_f32 v79,v79,v16
qt 96,2,40960
qw 5
qm 56,100,152
v_mul_f32 v64,v64,v80
qt 98,2,41472
qm 60,100,224
v_mul_f32 v65,v65,v81
qt 100,3,40960
qw 5
qm 48,104,156
v_cvt_pk_bf16_f32 v64,v64,v65
qt 102,3,41472
qm 52,104,228
v_mul_f32 v66,v66,v82
qt 104,2,45056
qw 5
qm 56,108,156
s_barrier
v_mul_f32 v67,v67,v83
qt 106,2,45568
qm 60,108,228
v_cvt_pk_bf16_f32 v65,v66,v67
qt 108,3,45056
qw 5
qm 48,96,160
v_mul_f32 v72,v72,v88
qt 110,3,45568
qm 52,96,232
v_mul_f32 v73,v73,v89
qt 96,4,16384
qw 5
qm 56,100,160
v_cvt_pk_bf16_f32 v66,v72,v73
qt 98,4,16896
qm 60,100,232
v_mul_f32 v74,v74,v90
qt 100,5,16384
qw 5
qm 48,104,164
v_mul_f32 v75,v75,v91
qt 102,5,16896
qm 52,104,236
v_cvt_pk_bf16_f32 v67,v74,v75
qt 104,4,20480
qw 5
qm 56,108,164
ds_write_b128 v18,v[64:67] offset:57344
v_mul_f32 v68,v68,v84
qt 106,4,20992
qm 60,108,236
s_add_u32 s15,s76,8192
buffer_store_dwordx4 v[64:67],v18,s[88:91],s15 offen offset:0 sc1
v_mul_f32 v69,v69,v85
qt 108,5,20480
qw 6
qm 48,96,168
v_cvt_pk_bf16_f32 v68,v68,v69
qt 110,5,20992
qm 52,96,240
v_mul_f32 v70,v70,v86
qt 96,4,24576
qw 6
qm 56,100,168
v_mul_f32 v71,v71,v87
qt 98,4,25088
qm 60,100,240
v_cvt_pk_bf16_f32 v69,v70,v71
qt 100,5,24576
qw 5
qm 48,104,172
v_mul_f32 v76,v76,v92
qt 102,5,25088
qm 52,104,244
v_mul_f32 v77,v77,v93
qt 104,4,28672
qw 5
qm 56,108,172
v_cvt_pk_bf16_f32 v70,v76,v77
qt 106,4,29184
qm 60,108,244
v_mul_f32 v78,v78,v94
qt 108,5,28672
qw 5
qm 48,96,176
v_mul_f32 v79,v79,v95
qt 110,5,29184
qm 52,96,248
v_cvt_pk_bf16_f32 v71,v78,v79
qt 96,2,49152
qw 5
qm 56,100,176
ds_write_b128 v18,v[68:71] offset:58368
qt 98,2,49664
qm 60,100,248
s_add_u32 s15,s76,8192
buffer_store_dwordx4 v[68:71],v18,s[88:91],s15 offen offset:1024 sc1
qt 100,3,49152
qw 6
qm 48,104,180
qt 102,3,49664
qm 52,104,252
qt 104,2,53248
qw 6
qm 56,108,180
qw 4
s_barrier
qt 106,2,53760
qm 60,108,252
qt 108,3,53248
qw 5
qz 80,96,112
qt 110,3,53760
qz 84,96,184
v_mul_f32 v16,v48,v48
qt 96,4,32768
qw 5
qz 88,100,112
v_fma_f32 v16,v16,v12,v10
v_lshlrev_b32 v44,4,v0
qt 98,4,33280
qz 92,100,184
v_mul_f32 v16,v16,v48
s_lshl_b32 s30,s7,2
qt 100,5,32768
qw 5
qm 80,104,116
v_exp_f32 v16,v16
s_sub_i32 s30,s10,s30
qt 102,5,33280
qm 84,104,188
v_fma_f32 v16,v16,v14,v14
qt 104,4,36864
qw 5
qm 88,108,116
v_rcp_f32 v16,v16
s_lshl_b32 s31,s12,8
qt 106,4,37376
qm 92,108,188
v_mul_f32 v48,v48,v16
s_sub_i32 s30,s30,1
qt 108,5,36864
qw 5
qm 80,96,120
v_mul_f32 v16,v49,v49
qt 110,5,37376
qm 84,96,192
v_fma_f32 v16,v16,v12,v10
s_mul_i32 s68,s30,9216
qt 96,4,40960
qw 5
qm 88,100,120
v_mul_f32 v16,v16,v49
s_add_u32 s68,s68,s31
qt 98,4,41472
qm 92,100,192
v_exp_f32 v16,v16
s_cmp_ge_i32 s30,0
s_cselect_b32 s68,s68,0x7ff00000
s_cselect_b32 s72,s16,0
qt 100,5,40960
qw 5
qm 80,104,124
v_fma_f32 v16,v16,v14,v14
qt 102,5,41472
qm 84,104,196
v_rcp_f32 v16,v16
s_sub_i32 s30,s30,1
qt 104,4,45056
qw 5
qm 88,108,124
v_mul_f32 v49,v49,v16
s_mul_i32 s69,s30,9216
qt 106,4,45568
qm 92,108,196
v_mul_f32 v16,v50,v50
qt 108,5,45056
qw 5
qm 80,96,128
v_fma_f32 v16,v16,v12,v10
s_add_u32 s69,s69,s31
qt 110,5,45568
qm 84,96,200
v_mul_f32 v16,v16,v50
s_cmp_ge_i32 s30,0
s_cselect_b32 s69,s69,0x7ff00000
s_cselect_b32 s73,s16,0
qt 96,2,57344
qw 5
qm 88,100,128
v_exp_f32 v16,v16
s_sub_i32 s30,s30,1
qt 98,2,57856
qm 92,100,200
v_fma_f32 v16,v16,v14,v14
qt 100,3,57344
qw 5
qm 80,104,132
v_rcp_f32 v16,v16
s_mul_i32 s70,s30,9216
qt 102,3,57856
qm 84,104,204
v_mul_f32 v50,v50,v16
s_add_u32 s70,s70,s31
qt 104,2,61440
qw 5
qm 88,108,132
s_barrier
v_mul_f32 v16,v51,v51
qt 106,2,61952
qm 92,108,204
v_fma_f32 v16,v16,v12,v10
s_cmp_ge_i32 s30,0
s_cselect_b32 s70,s70,0x7ff00000
s_cselect_b32 s74,s16,0
qt 108,3,61440
qw 5
qm 80,96,136
v_mul_f32 v16,v16,v51
s_sub_i32 s30,s30,1
qt 110,3,61952
qm 84,96,208
v_exp_f32 v16,v16
s_mul_i32 s71,s30,9216
qt 96,4,0
qw 5
qm 88,100,136
v_fma_f32 v16,v16,v14,v14
qt 98,4,512
qm 92,100,208
v_rcp_f32 v16,v16
s_add_u32 s71,s71,s31
qt 100,5,0
qw 5
qm 80,104,140
v_mul_f32 v51,v51,v16
s_cmp_ge_i32 s30,0
s_cselect_b32 s71,s71,0x7ff00000
s_cselect_b32 s75,s16,0
qt 102,5,512
qm 84,104,212
v_mul_f32 v16,v56,v56
qt 104,4,4096
qw 5
qm 88,108,140
v_fma_f32 v16,v16,v12,v10
buffer_load_dwordx4 v[28:31],v44,s[84:87],s68 offen sc1
qt 106,4,4608
qm 92,108,212
v_mul_f32 v16,v16,v56
qt 108,5,4096
qw 5
qm 80,96,144
v_exp_f32 v16,v16
buffer_load_dwordx4 v[32:35],v44,s[84:87],s69 offen sc1
qt 110,5,4608
qm 84,96,216
v_fma_f32 v16,v16,v14,v14
qt 96,4,8192
qw 5
qm 88,100,144
v_rcp_f32 v16,v16
buffer_load_dwordx4 v[36:39],v44,s[84:87],s70 offen sc1
qt 98,4,8704
qm 92,100,216
v_mul_f32 v56,v56,v16
qt 100,5,8192
qw 5
qm 80,104,148
v_mul_f32 v16,v57,v57
buffer_load_dwordx4 v[40:43],v44,s[84:87],s71 offen sc1
qt 102,5,8704
qm 84,104,220
v_fma_f32 v16,v16,v12,v10
qt 104,4,12288
qw 5
qm 88,108,148
v_mul_f32 v16,v16,v57
qt 106,4,12800
qm 92,108,220
v_exp_f32 v16,v16
qt 108,5,12288
qw 5
qm 80,96,152
v_fma_f32 v16,v16,v14,v14
v_add_u32 v26,123904,v44
qd 20,26,0
qt 110,5,12800
qm 84,96,224
v_rcp_f32 v16,v16
qt 96,2,0
qw 6
qm 88,100,152
v_mul_f32 v57,v57,v16
qt 98,2,512
qm 92,100,224
v_mul_f32 v16,v58,v58
qt 100,3,0
qw 6
qm 80,104,156
v_fma_f32 v16,v16,v12,v10
qt 102,3,512
qm 84,104,228
v_mul_f32 v16,v16,v58
qt 104,2,4096
qw 5
qm 88,108,156
s_barrier
v_exp_f32 v16,v16
s_lshl_b32 s30,s7,9
v_cvt_f32_u32 v17,s30
qt 106,2,4608
qm 92,108,228
v_fma_f32 v16,v16,v14,v14
v_mul_f32 v26,0x43000000,v20
qt 108,3,4096
qw 5
qm 80,96,160
v_rcp_f32 v16,v16
v_mul_f32 v27,v17,v20
qt 110,3,4608
qm 84,96,232
v_mul_f32 v58,v58,v16
v_mul_f32 v23,0x43000000,v21
qt 96,4,16384
qw 5
qm 88,100,160
v_mul_f32 v16,v59,v59
qt 98,4,16896
qm 92,100,232
v_fma_f32 v16,v16,v12,v10
v_mul_f32 v25,v17,v21
qt 100,5,16384
qw 5
qm 80,104,164
v_mul_f32 v16,v16,v59
v_exp_f32 v26,v26
qt 102,5,16896
qm 84,104,236
v_exp_f32 v16,v16
v_exp_f32 v27,v27
qt 104,4,20480
qw 5
qm 88,108,164
v_fma_f32 v16,v16,v14,v14
qt 106,4,20992
qm 92,108,236
v_rcp_f32 v16,v16
v_fract_f32 v23,v23
qt 108,5,20480
qw 5
qm 80,96,168
v_mul_f32 v59,v59,v16
v_fract_f32 v25,v25
qt 110,5,20992
qm 84,96,240
v_mul_f32 v16,v52,v52
v_cos_f32 v22,v23
qt 96,4,24576
qw 5
qm 88,100,168
v_fma_f32 v16,v16,v13,v11
v_cos_f32 v24,v25
qt 98,4,25088
qm 92,100,240
v_mul_f32 v16,v16,v52
qt 100,5,24576
qw 5
qm 80,104,172
v_exp_f32 v16,v16
v_sin_f32 v23,v23
qt 102,5,25088
qm 84,104,244
v_fma_f32 v16,v16,v15,v15
v_sin_f32 v25,v25
qt 104,4,28672
qw 5
qm 88,108,172
v_rcp_f32 v16,v16
v_mul_f32 v22,v22,v26
qt 106,4,29184
qm 92,108,244
v_mul_f32 v52,v52,v16
qt 108,5,28672
qw 5
qm 80,96,176
v_mul_f32 v16,v53,v53
v_mul_f32 v24,v24,v27
qt 110,5,29184
qm 84,96,248
v_fma_f32 v16,v16,v13,v11
v_mul_f32 v23,v23,v26
qt 96,2,8192
qw 5
qm 88,100,176
v_mul_f32 v16,v16,v53
v_mul_f32 v25,v25,v27
qt 98,2,8704
qm 92,100,248
v_exp_f32 v16,v16
qt 100,3,8192
qw 5
qm 80,104,180
v_fma_f32 v16,v16,v15,v15
qt 102,3,8704
qm 84,104,252
v_rcp_f32 v16,v16
qt 104,2,12288
qw 5
qm 88,108,180
s_barrier
v_mul_f32 v53,v53,v16
qt 106,2,12800
qm 92,108,252
qt 108,3,12288
s_mov_b32 s34,0
s_mov_b32 s32,0xffff
s_mov_b32 s33,0
s_waitcnt vmcnt(0)
.L_gX1:
s_waitcnt vmcnt(0)
v_xor_b32 v26,s72,v29
v_xor_b32 v27,s72,v31
v_or3_b32 v17,v26,v27,0
v_xor_b32 v26,s73,v33
v_xor_b32 v27,s73,v35
v_or3_b32 v17,v26,v27,v17
v_xor_b32 v26,s74,v37
v_xor_b32 v27,s74,v39
v_or3_b32 v17,v26,v27,v17
v_xor_b32 v26,s75,v41
v_xor_b32 v27,s75,v43
v_or3_b32 v17,v26,v27,v17
v_cmp_ne_u32 vcc,0,v17
s_nop 1
s_and_b64 s[30:31],vcc,s[32:33]
s_cbranch_scc0 .L_gdX1
s_add_u32 s34,s34,1
s_cmp_gt_u32 s34,1000000
s_cbranch_scc1 .L_gdX1
s_sleep 4
buffer_load_dwordx4 v[28:31],v44,s[84:87],s68 offen sc1
buffer_load_dwordx4 v[32:35],v44,s[84:87],s69 offen sc1
buffer_load_dwordx4 v[36:39],v44,s[84:87],s70 offen sc1
buffer_load_dwordx4 v[40:43],v44,s[84:87],s71 offen sc1
s_branch .L_gX1
.L_gdX1:
qw 5
qz 64,96,112
v_mul_f32 v16,v54,v54
v_fma_f32 v36,v22,v40,v36
v_fma_f32 v38,v22,v42,v38
qt 110,3,12800
qz 68,96,184
v_fma_f32 v16,v16,v13,v11
v_fma_f32 v36,-v23,v42,v36
qt 96,4,32768
qw 5
qz 72,100,112
v_mul_f32 v16,v16,v54
v_fma_f32 v38,v23,v40,v38
qt 98,4,33280
qz 76,100,184
v_exp_f32 v16,v16
v_fma_f32 v32,v22,v36,v32
qt 100,5,32768
qw 5
qm 64,104,116
v_fma_f32 v16,v16,v15,v15
v_fma_f32 v34,v22,v38,v34
qt 102,5,33280
qm 68,104,188
v_rcp_f32 v16,v16
v_fma_f32 v32,-v23,v38,v32
qt 104,4,36864
qw 5
qm 72,108,116
v_mul_f32 v54,v54,v16
v_fma_f32 v34,v23,v36,v34
qt 106,4,37376
qm 76,108,188
v_mul_f32 v16,v55,v55
v_fma_f32 v28,v22,v32,v28
qt 108,5,36864
qw 5
qm 64,96,120
v_fma_f32 v16,v16,v13,v11
v_fma_f32 v30,v22,v34,v30
qt 110,5,37376
qm 68,96,192
v_mul_f32 v16,v16,v55
v_fma_f32 v28,-v23,v34,v28
v_fma_f32 v30,v23,v32,v30
qt 96,4,40960
qw 5
qm 72,100,120
v_exp_f32 v16,v16
v_mul_f32 v20,v24,v28
qt 98,4,41472
qm 76,100,192
v_fma_f32 v16,v16,v15,v15
v_mul_f32 v21,v24,v30
qt 100,5,40960
qw 5
qm 64,104,124
v_rcp_f32 v16,v16
v_fma_f32 v20,-v25,v30,v20
qt 102,5,41472
qm 68,104,196
v_mul_f32 v55,v55,v16
v_fma_f32 v21,v25,v28,v21
qt 104,4,45056
qw 5
qm 72,108,124
v_mul_f32 v16,v60,v60
v_lshlrev_b32 v26,3,v0
qt 106,4,45568
qm 76,108,196
v_fma_f32 v16,v16,v13,v11
v_lshl_add_u32 v26,s7,9,v26
qt 108,5,45056
qw 5
qm 64,96,128
v_mul_f32 v16,v16,v60
v_add_u32 v26,125440,v26
qt 110,5,45568
qm 68,96,200
v_exp_f32 v16,v16
ds_write_b64 v26,v[20:21]
qt 96,2,16384
qw 6
qm 72,100,128
v_fma_f32 v16,v16,v15,v15
qt 98,2,16896
qm 76,100,200
v_rcp_f32 v16,v16
qt 100,3,16384
qw 6
qm 64,104,132
v_mul_f32 v60,v60,v16
qt 102,3,16896
qm 68,104,204
v_mul_f32 v16,v61,v61
qt 104,2,20480
qw 6
qm 72,108,132
qw 5
s_barrier
v_fma_f32 v16,v16,v13,v11
qt 106,2,20992
qm 76,108,204
v_mul_f32 v16,v16,v61
qt 108,3,20480
qw 5
qm 64,96,136
v_exp_f32 v16,v16
qt 110,3,20992
qm 68,96,208
v_fma_f32 v16,v16,v15,v15
qt 96,2,24576
qw 5
qm 72,100,136
v_rcp_f32 v16,v16
qt 98,2,25088
qm 76,100,208
v_mul_f32 v61,v61,v16
qt 100,3,24576
qw 5
qm 64,104,140
v_mul_f32 v16,v62,v62
qt 102,3,25088
qm 68,104,212
v_fma_f32 v16,v16,v13,v11
qt 104,2,28672
qw 5
qm 72,108,140
v_mul_f32 v16,v16,v62
qt 106,2,29184
qm 76,108,212
v_exp_f32 v16,v16
qt 108,3,28672
qw 5
qm 64,96,144
v_fma_f32 v16,v16,v15,v15
qt 110,3,29184
qm 68,96,216
v_rcp_f32 v16,v16
qt 96,2,32768
qw 5
qm 72,100,144
v_mul_f32 v62,v62,v16
qt 98,2,33280
qm 76,100,216
v_mul_f32 v16,v63,v63
qt 100,3,32768
qw 5
qm 64,104,148
v_fma_f32 v16,v16,v13,v11
qt 102,3,33280
qm 68,104,220
v_mul_f32 v16,v16,v63
qt 104,2,36864
qw 5
qm 72,108,148
v_exp_f32 v16,v16
qt 106,2,37376
qm 76,108,220
v_fma_f32 v16,v16,v15,v15
qt 108,3,36864
qw 5
qm 64,96,152
v_rcp_f32 v16,v16
qt 110,3,37376
qm 68,96,224
v_mul_f32 v63,v63,v16
qt 96,2,40960
qw 5
qm 72,100,152
v_mul_f32 v48,v48,v80
qt 98,2,41472
qm 76,100,224
v_mul_f32 v49,v49,v81
qt 100,3,40960
qw 5
qm 64,104,156
v_cvt_pk_bf16_f32 v48,v48,v49
qt 102,3,41472
qm 68,104,228
v_mul_f32 v50,v50,v82
qt 104,2,45056
qw 5
qm 72,108,156
s_barrier
v_mul_f32 v51,v51,v83
qt 106,2,45568
qm 76,108,228
v_cvt_pk_bf16_f32 v49,v50,v51
qt 108,3,45056
qw 5
qm 64,96,160
v_mul_f32 v56,v56,v88
v_and_b32 v44,15,v0
v_lshrrev_b32 v45,4,v0
qt 110,3,45568
qm 68,96,232
v_mul_f32 v57,v57,v89
v_lshl_add_u32 v17,v44,2,v45
qt 96,4,16384
qw 5
qm 72,100,160
v_cvt_pk_bf16_f32 v50,v56,v57
v_lshl_add_u32 v45,s7,2,v45
qt 98,4,16896
qm 76,100,232
v_mul_f32 v58,v58,v90
v_lshlrev_b32 v28,4,v44
qt 100,5,16384
qw 5
qm 64,104,164
v_mul_f32 v59,v59,v91
v_add_u32 v28,123904,v28
qt 102,5,16896
qm 68,104,236
v_cvt_pk_bf16_f32 v51,v58,v59
v_lshlrev_b32 v44,3,v44
v_lshl_add_u32 v42,v45,9,v44
qt 104,4,20480
qw 5
qm 72,108,164
ds_write_b128 v18,v[48:51] offset:24576
v_mul_f32 v52,v52,v84
v_add_u32 v42,114688,v42
qt 106,4,20992
qm 76,108,236
s_add_u32 s15,s76,24576
buffer_store_dwordx4 v[48:51],v18,s[88:91],s15 offen offset:0 sc1
v_mul_f32 v53,v53,v85
v_add_u32 v44,124928,v44
qt 108,5,20480
qw 6
qm 64,96,168
v_cvt_pk_bf16_f32 v52,v52,v53
qr 28,28,0
qt 110,5,20992
qm 68,96,240
v_mul_f32 v54,v54,v86
qd 32,44,0
qt 96,4,24576
qw 8
qm 72,100,168
v_mul_f32 v55,v55,v87
qd 34,44,512
qd 36,44,1024
qt 98,4,25088
buffer_load_dword v1,v8,s[48:51],s54 offen
qm 76,100,240
v_cvt_pk_bf16_f32 v53,v54,v55
qd 38,44,1536
qt 100,5,24576
qw 10
qm 64,104,172
v_mul_f32 v60,v60,v92
qd 40,44,2048
qt 102,5,25088
qm 68,104,244
v_mul_f32 v61,v61,v93
qd 42,42,0
qt 104,4,28672
qw 11
qm 72,108,172
v_cvt_pk_bf16_f32 v54,v60,v61
qr 20,19,0
qt 106,4,29184
qm 76,108,244
v_mul_f32 v62,v62,v94
qr 24,19,1024
v_lshlrev_b32 v17,3,v17
qt 108,5,28672
qw 10
qm 64,96,176
v_mul_f32 v63,v63,v95
v_add_u32 v17,s13,v17
qt 110,5,29184
qm 68,96,248
v_cvt_pk_bf16_f32 v55,v62,v63
v_add_u32 v17,147456,v17
qt 96,2,49152
qw 8
qm 72,100,176
ds_write_b128 v18,v[52:55] offset:25600
v_lshlrev_b32 v45,3,v45
qt 98,2,49664
qm 76,100,248
s_add_u32 s15,s76,24576
buffer_store_dwordx4 v[52:55],v18,s[88:91],s15 offen offset:1024 sc1
v_cvt_f32_u32 v45,v45
qt 100,3,49152
qw 7
qm 64,104,180
qt 102,3,49664
qm 68,104,252
qt 104,2,53248
qw 6
qm 72,108,180
qw 4
s_barrier
qt 106,2,53760
qm 76,108,252
qt 108,3,53248
v_add_f32 v34,v34,v36
v_add_f32 v35,v35,v37
v_add_f32 v38,v38,v40
v_add_f32 v39,v39,v41
v_add_f32 v34,v34,v38
v_add_f32 v35,v35,v39
v_mul_f32 v44,v45,v28
v_mul_f32 v37,v45,v29
v_exp_f32 v44,v44
v_fract_f32 v37,v37
v_cos_f32 v36,v37
v_sin_f32 v37,v37
v_mul_f32 v36,v36,v44
v_mul_f32 v37,v37,v44
v_fma_f32 v38,v36,v34,v42
v_fma_f32 v39,v36,v35,v43
v_fma_f32 v38,-v37,v35,v38
v_fma_f32 v39,v37,v34,v39
v_lshlrev_b32 v34,16,v20
v_and_b32 v35,0xffff0000,v20
v_fma_f32 v40,v32,v38,v34
v_fma_f32 v41,v32,v39,v35
v_fma_f32 v40,-v33,v39,v40
v_fma_f32 v41,v33,v38,v41
v_mul_f32 v36,v30,v40
v_mul_f32 v42,v30,v41
v_fma_f32 v36,-v31,v41,v36
v_fma_f32 v42,v31,v40,v42
v_lshlrev_b32 v34,16,v21
v_and_b32 v35,0xffff0000,v21
v_fma_f32 v38,v32,v40,v34
v_fma_f32 v39,v32,v41,v35
v_fma_f32 v38,-v33,v41,v38
v_fma_f32 v39,v33,v40,v39
v_mul_f32 v37,v30,v38
v_mul_f32 v43,v30,v39
v_fma_f32 v37,-v31,v39,v37
v_fma_f32 v43,v31,v38,v43
v_cvt_pk_bf16_f32 v20,v36,v37
v_cvt_pk_bf16_f32 v28,-v42,-v43
v_lshlrev_b32 v34,16,v22
v_and_b32 v35,0xffff0000,v22
v_fma_f32 v40,v32,v38,v34
v_fma_f32 v41,v32,v39,v35
v_fma_f32 v40,-v33,v39,v40
v_fma_f32 v41,v33,v38,v41
v_mul_f32 v36,v30,v40
v_mul_f32 v42,v30,v41
v_fma_f32 v36,-v31,v41,v36
v_fma_f32 v42,v31,v40,v42
v_lshlrev_b32 v34,16,v23
v_and_b32 v35,0xffff0000,v23
v_fma_f32 v38,v32,v40,v34
v_fma_f32 v39,v32,v41,v35
v_fma_f32 v38,-v33,v41,v38
v_fma_f32 v39,v33,v40,v39
v_mul_f32 v37,v30,v38
v_mul_f32 v43,v30,v39
v_fma_f32 v37,-v31,v39,v37
v_fma_f32 v43,v31,v38,v43
v_cvt_pk_bf16_f32 v21,v36,v37
v_cvt_pk_bf16_f32 v29,-v42,-v43
ds_write_b64 v17,v[20:21] offset:0
ds_write_b64 v17,v[28:29] offset:512
v_lshlrev_b32 v34,16,v24
v_and_b32 v35,0xffff0000,v24
v_fma_f32 v40,v32,v38,v34
v_fma_f32 v41,v32,v39,v35
v_fma_f32 v40,-v33,v39,v40
v_fma_f32 v41,v33,v38,v41
v_mul_f32 v36,v30,v40
v_mul_f32 v42,v30,v41
v_fma_f32 v36,-v31,v41,v36
v_fma_f32 v42,v31,v40,v42
v_lshlrev_b32 v34,16,v25
v_and_b32 v35,0xffff0000,v25
v_fma_f32 v38,v32,v40,v34
v_fma_f32 v39,v32,v41,v35
v_fma_f32 v38,-v33,v41,v38
v_fma_f32 v39,v33,v40,v39
v_mul_f32 v37,v30,v38
v_mul_f32 v43,v30,v39
v_fma_f32 v37,-v31,v39,v37
v_fma_f32 v43,v31,v38,v43
v_cvt_pk_bf16_f32 v24,v36,v37
v_cvt_pk_bf16_f32 v28,-v42,-v43
v_lshlrev_b32 v34,16,v26
v_and_b32 v35,0xffff0000,v26
v_fma_f32 v40,v32,v38,v34
v_fma_f32 v41,v32,v39,v35
v_fma_f32 v40,-v33,v39,v40
v_fma_f32 v41,v33,v38,v41
v_mul_f32 v36,v30,v40
v_mul_f32 v42,v30,v41
v_fma_f32 v36,-v31,v41,v36
v_fma_f32 v42,v31,v40,v42
v_lshlrev_b32 v34,16,v27
v_and_b32 v35,0xffff0000,v27
v_fma_f32 v38,v32,v40,v34
v_fma_f32 v39,v32,v41,v35
v_fma_f32 v38,-v33,v41,v38
v_fma_f32 v39,v33,v40,v39
v_mul_f32 v37,v30,v38
v_mul_f32 v43,v30,v39
v_fma_f32 v37,-v31,v39,v37
v_fma_f32 v43,v31,v38,v43
v_cvt_pk_bf16_f32 v25,v36,v37
v_cvt_pk_bf16_f32 v29,-v42,-v43
ds_write_b64 v17,v[24:25] offset:1024
ds_write_b64 v17,v[28:29] offset:1536
qw 9
qz 80,96,112
qt 110,3,53760
qz 84,96,184
v_mul_f32 v16,v64,v64
qt 96,4,32768
qw 9
qz 88,100,112
v_fma_f32 v16,v16,v12,v10
v_lshlrev_b32 v36,3,v0
v_add_u32 v36,s13,v36
qt 98,4,33280
qz 92,100,184
v_mul_f32 v16,v16,v64
v_add_u32 v36,147456,v36
qt 100,5,32768
qw 9
qm 80,104,116
v_exp_f32 v16,v16
qt 28,36,0
qt 102,5,33280
qm 84,104,188
v_fma_f32 v16,v16,v14,v14
qt 30,36,512
qt 104,4,36864
qw 7
qm 88,108,116
v_rcp_f32 v16,v16
qt 32,36,1024
qt 106,4,37376
qm 92,108,188
v_mul_f32 v64,v64,v16
qt 34,36,1536
qt 108,5,36864
qw 9
qm 80,96,120
v_mul_f32 v16,v65,v65
qt 110,5,37376
qm 84,96,192
v_fma_f32 v16,v16,v12,v10
qt 96,2,57344
qw 8
qm 88,100,120
v_mul_f32 v16,v16,v65
qt 98,2,57856
qm 92,100,192
v_exp_f32 v16,v16
qt 100,3,57344
qw 6
qm 80,104,124
v_fma_f32 v16,v16,v14,v14
qt 102,3,57856
qm 84,104,196
v_rcp_f32 v16,v16
qt 104,2,61440
qw 5
qm 88,108,124
v_mul_f32 v65,v65,v16
qt 106,2,61952
qm 92,108,196
v_mul_f32 v16,v66,v66
s_add_u32 s15,s76,81920
buffer_store_dwordx4 v[28:31],v18,s[88:91],s15 offen offset:0 sc1
qt 108,3,61440
qw 5
qm 80,96,128
v_fma_f32 v16,v16,v12,v10
s_add_u32 s15,s76,81920
buffer_store_dwordx4 v[32:35],v18,s[88:91],s15 offen offset:1024 sc1
qt 110,3,61952
qm 84,96,200
v_mul_f32 v16,v16,v66
qt 96,4,0
qw 5
qm 88,100,128
v_exp_f32 v16,v16
qt 98,4,512
qm 92,100,200
v_fma_f32 v16,v16,v14,v14
qt 100,5,0
qw 5
qm 80,104,132
v_rcp_f32 v16,v16
qt 102,5,512
qm 84,104,204
v_mul_f32 v66,v66,v16
qt 104,4,4096
qw 5
qm 88,108,132
s_barrier
v_mul_f32 v16,v67,v67
qt 106,4,4608
qm 92,108,204
v_fma_f32 v16,v16,v12,v10
qt 108,5,4096
qw 5
qm 80,96,136
v_mul_f32 v16,v16,v67
qt 110,5,4608
qm 84,96,208
v_exp_f32 v16,v16
qt 96,4,8192
qw 5
qm 88,100,136
v_fma_f32 v16,v16,v14,v14
qt 98,4,8704
qm 92,100,208
v_rcp_f32 v16,v16
qt 100,5,8192
qw 5
qm 80,104,140
v_mul_f32 v67,v67,v16
qt 102,5,8704
qm 84,104,212
v_mul_f32 v16,v72,v72
qt 104,4,12288
qw 5
qm 88,108,140
v_fma_f32 v16,v16,v12,v10
qt 106,4,12800
qm 92,108,212
v_mul_f32 v16,v16,v72
qt 108,5,12288
qw 5
qm 80,96,144
v_exp_f32 v16,v16
qt 110,5,12800
qm 84,96,216
v_fma_f32 v16,v16,v14,v14
qt 96,2,0
qw 5
qm 88,100,144
v_rcp_f32 v16,v16
qt 98,2,512
qm 92,100,216
v_mul_f32 v72,v72,v16
qt 100,3,0
qw 5
qm 80,104,148
v_mul_f32 v16,v73,v73
qt 102,3,512
qm 84,104,220
v_fma_f32 v16,v16,v12,v10
qt 104,2,4096
qw 5
qm 88,108,148
v_mul_f32 v16,v16,v73
qt 106,2,4608
qm 92,108,220
v_exp_f32 v16,v16
qt 108,3,4096
qw 5
qm 80,96,152
v_fma_f32 v16,v16,v14,v14
qt 110,3,4608
qm 84,96,224
v_rcp_f32 v16,v16
qt 96,2,8192
qw 5
qm 88,100,152
v_mul_f32 v73,v73,v16
qt 98,2,8704
qm 92,100,224
v_mul_f32 v16,v74,v74
qt 100,3,8192
qw 5
qm 80,104,156
v_fma_f32 v16,v16,v12,v10
qt 102,3,8704
qm 84,104,228
v_mul_f32 v16,v16,v74
qt 104,2,12288
qw 5
qm 88,108,156
s_barrier
v_exp_f32 v16,v16
qt 106,2,12800
qm 92,108,228
v_fma_f32 v16,v16,v14,v14
qt 108,3,12288
qw 5
qm 80,96,160
v_rcp_f32 v16,v16
qt 110,3,12800
qm 84,96,232
v_mul_f32 v74,v74,v16
qt 96,4,16384
qw 5
qm 88,100,160
v_mul_f32 v16,v75,v75
qt 98,4,16896
qm 92,100,232
v_fma_f32 v16,v16,v12,v10
qt 100,5,16384
qw 5
qm 80,104,164
v_mul_f32 v16,v16,v75
qt 102,5,16896
qm 84,104,236
v_exp_f32 v16,v16
qt 104,4,20480
qw 5
qm 88,108,164
v_fma_f32 v16,v16,v14,v14
qt 106,4,20992
qm 92,108,236
v_rcp_f32 v16,v16
qt 108,5,20480
qw 5
qm 80,96,168
v_mul_f32 v75,v75,v16
qt 110,5,20992
qm 84,96,240
v_mul_f32 v16,v68,v68
qt 96,4,24576
qw 5
qm 88,100,168
v_fma_f32 v16,v16,v13,v11
qt 98,4,25088
qm 92,100,240
v_mul_f32 v16,v16,v68
qt 100,5,24576
qw 5
qm 80,104,172
v_exp_f32 v16,v16
qt 102,5,25088
qm 84,104,244
v_fma_f32 v16,v16,v15,v15
qt 104,4,28672
qw 5
qm 88,108,172
v_rcp_f32 v16,v16
qt 106,4,29184
qm 92,108,244
v_mul_f32 v68,v68,v16
qt 108,5,28672
qw 5
qm 80,96,176
v_mul_f32 v16,v69,v69
qt 110,5,29184
qm 84,96,248
v_fma_f32 v16,v16,v13,v11
qt 96,2,16384
qw 5
qm 88,100,176
v_mul_f32 v16,v16,v69
qt 98,2,16896
qm 92,100,248
v_exp_f32 v16,v16
qt 100,3,16384
qw 5
qm 80,104,180
v_fma_f32 v16,v16,v15,v15
qt 102,3,16896
qm 84,104,252
v_rcp_f32 v16,v16
qt 104,2,20480
qw 5
qm 88,108,180
s_barrier
v_mul_f32 v69,v69,v16
qt 106,2,20992
qm 92,108,252
qt 108,3,20480
qw 5
qz 48,96,112
v_mul_f32 v16,v70,v70
qt 110,3,20992
qz 52,96,184
v_fma_f32 v16,v16,v13,v11
v_bfe_u32 v44,v0,2,2
qt 96,4,32768
qw 5
qz 56,100,112
v_mul_f32 v16,v16,v70
qt 98,4,33280
qz 60,100,184
v_exp_f32 v16,v16
qt 100,5,32768
qw 5
qm 48,104,116
v_fma_f32 v16,v16,v15,v15
qt 102,5,33280
qm 52,104,188
v_rcp_f32 v16,v16
s_and_b32 s33,s8,3
s_lshl_b32 s33,s33,2
s_add_u32 s33,s33,s7
s_lshl_b32 s33,s33,3
s_lshl_b32 s33,s33,2
s_add_u32 s33,s33,122880
v_lshl_add_u32 v45,v44,2,s33
qt 104,4,36864
qw 5
qm 56,108,116
v_mul_f32 v70,v70,v16
qt 106,4,37376
qm 60,108,188
v_mul_f32 v16,v71,v71
qt 108,5,36864
qw 5
qm 48,96,120
v_fma_f32 v16,v16,v13,v11
qt 110,5,37376
qm 52,96,192
v_mul_f32 v16,v16,v71
qt 96,2,24576
qw 5
qm 56,100,120
v_exp_f32 v16,v16
ds_read_b32 v40,v45 offset:0
qt 98,2,25088
qm 60,100,192
v_fma_f32 v16,v16,v15,v15
qt 100,3,24576
qw 6
qm 48,104,124
v_rcp_f32 v16,v16
qt 102,3,25088
qm 52,104,196
v_mul_f32 v71,v71,v16
qt 104,2,28672
qw 6
qm 56,108,124
v_mul_f32 v16,v76,v76
ds_read_b32 v41,v45 offset:512
qt 106,2,29184
qm 60,108,196
v_fma_f32 v16,v16,v13,v11
qt 108,3,28672
qw 6
qm 48,96,128
v_mul_f32 v16,v16,v76
qt 110,3,29184
qm 52,96,200
v_exp_f32 v16,v16
qt 96,2,32768
qw 6
qm 56,100,128
v_fma_f32 v16,v16,v15,v15
qt 98,2,33280
qm 60,100,200
v_rcp_f32 v16,v16
qt 100,3,32768
qw 5
qm 48,104,132
v_mul_f32 v76,v76,v16
qt 102,3,33280
qm 52,104,204
v_mul_f32 v16,v77,v77
qt 104,2,36864
qw 5
qm 56,108,132
s_barrier
v_fma_f32 v16,v16,v13,v11
qt 106,2,37376
qm 60,108,204
v_mul_f32 v16,v16,v77
qt 108,3,36864
qw 5
qm 48,96,136
v_exp_f32 v16,v16
qt 110,3,37376
qm 52,96,208
v_fma_f32 v16,v16,v15,v15
qt 96,2,40960
qw 5
qm 56,100,136
v_rcp_f32 v16,v16
qt 98,2,41472
qm 60,100,208
v_mul_f32 v77,v77,v16
qt 100,3,40960
qw 5
qm 48,104,140
v_mul_f32 v16,v78,v78
qt 102,3,41472
qm 52,104,212
v_fma_f32 v16,v16,v13,v11
qt 104,2,45056
qw 5
qm 56,108,140
v_mul_f32 v16,v16,v78
qt 106,2,45568
qm 60,108,212
v_exp_f32 v16,v16
qt 108,3,45056
qw 5
qm 48,96,144
v_fma_f32 v16,v16,v15,v15
qt 110,3,45568
qm 52,96,216
v_rcp_f32 v16,v16
qt 96,2,49152
qw 5
qm 56,100,144
v_mul_f32 v78,v78,v16
qt 98,2,49664
qm 60,100,216
v_mul_f32 v16,v79,v79
qt 100,3,49152
qw 5
qm 48,104,148
v_fma_f32 v16,v16,v13,v11
qt 102,3,49664
qm 52,104,220
v_mul_f32 v16,v16,v79
qt 104,2,53248
qw 5
qm 56,108,148
v_exp_f32 v16,v16
qt 106,2,53760
qm 60,108,220
v_fma_f32 v16,v16,v15,v15
qt 108,3,53248
qw 5
qm 48,96,152
v_rcp_f32 v16,v16
qt 110,3,53760
qm 52,96,224
v_mul_f32 v79,v79,v16
qt 96,2,57344
qw 5
qm 56,100,152
v_mul_f32 v64,v64,v80
qt 98,2,57856
qm 60,100,224
v_mul_f32 v65,v65,v81
qt 100,3,57344
qw 5
qm 48,104,156
v_cvt_pk_bf16_f32 v64,v64,v65
qt 102,3,57856
qm 52,104,228
v_mul_f32 v66,v66,v82
qt 104,2,61440
qw 5
qm 56,108,156
s_barrier
v_mul_f32 v67,v67,v83
qt 106,2,61952
qm 60,108,228
v_cvt_pk_bf16_f32 v65,v66,v67
qt 108,3,61440
qw 5
qm 48,96,160
v_mul_f32 v72,v72,v88
qt 110,3,61952
qm 52,96,232
v_mul_f32 v73,v73,v89
qt 96,4,16384
qw 5
qm 56,100,160
v_cvt_pk_bf16_f32 v66,v72,v73
qt 98,4,16896
qm 60,100,232
v_mul_f32 v74,v74,v90
qt 100,5,16384
qw 5
qm 48,104,164
v_mul_f32 v75,v75,v91
qt 102,5,16896
qm 52,104,236
v_cvt_pk_bf16_f32 v67,v74,v75
qt 104,4,20480
qw 5
qm 56,108,164
ds_write_b128 v18,v[64:67] offset:40960
v_mul_f32 v68,v68,v84
qt 106,4,20992
qm 60,108,236
s_add_u32 s15,s76,40960
buffer_store_dwordx4 v[64:67],v18,s[88:91],s15 offen offset:0 sc1
v_mul_f32 v69,v69,v85
qt 108,5,20480
qw 6
qm 48,96,168
v_cvt_pk_bf16_f32 v68,v68,v69
qt 110,5,20992
qm 52,96,240
v_mul_f32 v70,v70,v86
qt 96,4,24576
qw 6
qm 56,100,168
v_mul_f32 v71,v71,v87
qt 98,4,25088
qm 60,100,240
v_cvt_pk_bf16_f32 v69,v70,v71
qt 100,5,24576
qw 5
qm 48,104,172
v_mul_f32 v76,v76,v92
qt 102,5,25088
qm 52,104,244
v_mul_f32 v77,v77,v93
qt 104,4,28672
qw 5
qm 56,108,172
v_cvt_pk_bf16_f32 v70,v76,v77
qt 106,4,29184
qm 60,108,244
v_mul_f32 v78,v78,v94
qt 108,5,28672
qw 5
qm 48,96,176
v_mul_f32 v79,v79,v95
qt 110,5,29184
qm 52,96,248
v_cvt_pk_bf16_f32 v71,v78,v79
qt 96,4,0
qw 5
qm 56,100,176
ds_write_b128 v18,v[68:71] offset:41984
qt 98,4,512
qm 60,100,248
s_add_u32 s15,s76,40960
buffer_store_dwordx4 v[68:71],v18,s[88:91],s15 offen offset:1024 sc1
qt 100,5,0
qw 6
qm 48,104,180
qt 102,5,512
qm 52,104,252
qt 104,4,4096
qw 6
qm 56,108,180
qw 4
s_barrier
qt 106,4,4608
qm 60,108,252
qt 108,5,4096
qw 5
qz 80,96,112
qt 110,5,4608
qz 84,96,184
v_mul_f32 v16,v48,v48
qt 96,4,32768
qw 5
qz 88,100,112
v_fma_f32 v16,v16,v12,v10
qt 98,4,33280
qz 92,100,184
v_mul_f32 v16,v16,v48
qt 100,5,32768
qw 5
qm 80,104,116
v_exp_f32 v16,v16
qt 102,5,33280
qm 84,104,188
v_fma_f32 v16,v16,v14,v14
qt 104,4,36864
qw 5
qm 88,108,116
v_rcp_f32 v16,v16
qt 106,4,37376
qm 92,108,188
v_mul_f32 v48,v48,v16
qt 108,5,36864
qw 5
qm 80,96,120
v_mul_f32 v16,v49,v49
qt 110,5,37376
qm 84,96,192
v_fma_f32 v16,v16,v12,v10
qt 96,4,8192
qw 5
qm 88,100,120
v_mul_f32 v16,v16,v49
qt 98,4,8704
qm 92,100,192
v_exp_f32 v16,v16
qt 100,5,8192
qw 5
qm 80,104,124
v_fma_f32 v16,v16,v14,v14
qt 102,5,8704
qm 84,104,196
v_rcp_f32 v16,v16
qt 104,4,12288
qw 5
qm 88,108,124
v_mul_f32 v49,v49,v16
qt 106,4,12800
qm 92,108,196
v_mul_f32 v16,v50,v50
qt 108,5,12288
qw 5
qm 80,96,128
v_fma_f32 v16,v16,v12,v10
qt 110,5,12800
qm 84,96,200
v_mul_f32 v16,v16,v50
qt 96,2,0
qw 5
qm 88,100,128
v_exp_f32 v16,v16
qt 98,2,512
qm 92,100,200
v_fma_f32 v16,v16,v14,v14
qt 100,3,0
qw 5
qm 80,104,132
v_rcp_f32 v16,v16
qt 102,3,512
qm 84,104,204
v_mul_f32 v50,v50,v16
qt 104,2,4096
qw 5
qm 88,108,132
s_barrier
v_mul_f32 v16,v51,v51
qt 106,2,4608
qm 92,108,204
v_fma_f32 v16,v16,v12,v10
qt 108,3,4096
qw 5
qm 80,96,136
v_mul_f32 v16,v16,v51
qt 110,3,4608
qm 84,96,208
v_exp_f32 v16,v16
qt 96,2,8192
qw 5
qm 88,100,136
v_fma_f32 v16,v16,v14,v14
qt 98,2,8704
qm 92,100,208
v_rcp_f32 v16,v16
qt 100,3,8192
qw 5
qm 80,104,140
v_mul_f32 v51,v51,v16
qt 102,3,8704
qm 84,104,212
v_mul_f32 v16,v56,v56
qt 104,2,12288
qw 5
qm 88,108,140
v_fma_f32 v16,v16,v12,v10
qt 106,2,12800
qm 92,108,212
v_mul_f32 v16,v16,v56
qt 108,3,12288
qw 5
qm 80,96,144
v_exp_f32 v16,v16
qt 110,3,12800
qm 84,96,216
v_fma_f32 v16,v16,v14,v14
qt 96,2,16384
qw 5
qm 88,100,144
v_rcp_f32 v16,v16
qt 98,2,16896
qm 92,100,216
v_mul_f32 v56,v56,v16
qt 100,3,16384
qw 5
qm 80,104,148
v_mul_f32 v16,v57,v57
qt 102,3,16896
qm 84,104,220
v_fma_f32 v16,v16,v12,v10
qt 104,2,20480
qw 5
qm 88,108,148
v_mul_f32 v16,v16,v57
qt 106,2,20992
qm 92,108,220
v_exp_f32 v16,v16
qt 108,3,20480
qw 5
qm 80,96,152
v_fma_f32 v16,v16,v14,v14
qt 110,3,20992
qm 84,96,224
v_rcp_f32 v16,v16
qt 96,2,24576
qw 5
qm 88,100,152
v_mul_f32 v57,v57,v16
qt 98,2,25088
qm 92,100,224
v_mul_f32 v16,v58,v58
qt 100,3,24576
qw 5
qm 80,104,156
v_fma_f32 v16,v16,v12,v10
qt 102,3,25088
qm 84,104,228
v_mul_f32 v16,v16,v58
qt 104,2,28672
qw 5
qm 88,108,156
s_barrier
v_exp_f32 v16,v16
qt 106,2,29184
qm 92,108,228
v_fma_f32 v16,v16,v14,v14
qt 108,3,28672
qw 5
qm 80,96,160
v_rcp_f32 v16,v16
qt 110,3,29184
qm 84,96,232
v_mul_f32 v58,v58,v16
qt 96,4,16384
qw 5
qm 88,100,160
v_mul_f32 v16,v59,v59
qt 98,4,16896
qm 92,100,232
v_fma_f32 v16,v16,v12,v10
qt 100,5,16384
qw 5
qm 80,104,164
v_mul_f32 v16,v16,v59
qt 102,5,16896
qm 84,104,236
v_exp_f32 v16,v16
qt 104,4,20480
qw 5
qm 88,108,164
v_fma_f32 v16,v16,v14,v14
qt 106,4,20992
qm 92,108,236
v_rcp_f32 v16,v16
qt 108,5,20480
qw 5
qm 80,96,168
v_mul_f32 v59,v59,v16
qt 110,5,20992
qm 84,96,240
v_mul_f32 v16,v52,v52
qt 96,4,24576
qw 5
qm 88,100,168
v_fma_f32 v16,v16,v13,v11
qt 98,4,25088
qm 92,100,240
v_mul_f32 v16,v16,v52
qt 100,5,24576
qw 5
qm 80,104,172
v_exp_f32 v16,v16
qt 102,5,25088
qm 84,104,244
v_fma_f32 v16,v16,v15,v15
qt 104,4,28672
qw 5
qm 88,108,172
v_rcp_f32 v16,v16
qt 106,4,29184
qm 92,108,244
v_mul_f32 v52,v52,v16
qt 108,5,28672
qw 5
qm 80,96,176
v_mul_f32 v16,v53,v53
qt 110,5,29184
qm 84,96,248
v_fma_f32 v16,v16,v13,v11
qt 96,2,32768
qw 5
qm 88,100,176
v_mul_f32 v16,v16,v53
qt 98,2,33280
qm 92,100,248
v_exp_f32 v16,v16
qt 100,3,32768
qw 5
qm 80,104,180
v_fma_f32 v16,v16,v15,v15
qt 102,3,33280
qm 84,104,252
v_rcp_f32 v16,v16
qt 104,2,36864
qw 5
qm 88,108,180
s_barrier
v_mul_f32 v53,v53,v16
qt 106,2,37376
qm 92,108,252
qt 108,3,36864
qw 5
qz 64,96,112
v_mul_f32 v16,v54,v54
qt 110,3,37376
qz 68,96,184
v_fma_f32 v16,v16,v13,v11
qt 96,4,32768
qw 5
qz 72,100,112
v_mul_f32 v16,v16,v54
v_mov_b32 v38,s16
qt 98,4,33280
qz 76,100,184
v_exp_f32 v16,v16
v_and_b32 v37,48,v0
qt 100,5,32768
qw 5
qm 64,104,116
v_fma_f32 v16,v16,v15,v15
qt 102,5,33280
qm 68,104,188
v_rcp_f32 v16,v16
v_lshlrev_b32 v37,24,v37
qt 104,4,36864
qw 5
qm 72,108,116
v_mul_f32 v54,v54,v16
qt 106,4,37376
qm 76,108,188
v_mul_f32 v16,v55,v55
v_lshl_or_b32 v37,v0,7,v37
qt 108,5,36864
qw 5
qm 64,96,120
v_fma_f32 v16,v16,v13,v11
s_mul_i32 s72,s10,2048
qt 110,5,37376
qm 68,96,192
v_mul_f32 v16,v16,v55
qt 96,2,40960
qw 5
qm 72,100,120
v_exp_f32 v16,v16
s_add_u32 s72,s72,8192
qt 98,2,41472
qm 76,100,192
v_fma_f32 v16,v16,v15,v15
qt 100,3,40960
qw 5
qm 64,104,124
v_rcp_f32 v16,v16
s_and_b32 s30,s8,3
qt 102,3,41472
qm 68,104,196
v_mul_f32 v55,v55,v16
qt 104,2,45056
qw 5
qm 72,108,124
v_mul_f32 v16,v60,v60
s_lshl_b32 s30,s30,4
qt 106,2,45568
qm 76,108,196
v_fma_f32 v16,v16,v13,v11
s_and_b32 s31,s8,4
qt 108,3,45056
qw 5
qm 64,96,128
v_mul_f32 v16,v16,v60
qt 110,3,45568
qm 68,96,200
v_exp_f32 v16,v16
s_add_u32 s30,s30,s31
qt 96,2,49152
qw 5
qm 72,100,128
v_fma_f32 v16,v16,v15,v15
qt 98,2,49664
qm 76,100,200
v_rcp_f32 v16,v16
s_add_u32 s68,s30,s72
qt 100,3,49152
qw 5
qm 64,104,132
v_mul_f32 v60,v60,v16
qt 102,3,49664
qm 68,104,204
v_mul_f32 v16,v61,v61
s_lshl_b32 s31,s8,8
qt 104,2,53248
qw 5
qm 72,108,132
s_barrier
v_fma_f32 v16,v16,v13,v11
s_add_u32 s78,s68,s31
qt 106,2,53760
qm 76,108,204
v_mul_f32 v16,v16,v61
qt 108,3,53248
qw 5
qm 64,96,136
v_exp_f32 v16,v16
s_add_u32 s78,s78,8
qt 110,3,53760
qm 68,96,208
v_fma_f32 v16,v16,v15,v15
qt 96,2,57344
qw 5
qm 72,100,136
v_rcp_f32 v16,v16
s_add_u32 s68,s68,0
qt 98,2,57856
qm 76,100,208
v_mul_f32 v61,v61,v16
qt 100,3,57344
qw 5
qm 64,104,140
v_mul_f32 v16,v62,v62
s_cmp_eq_u32 s6,4
s_cselect_b32 s68,s68,0x7ff00000
qt 102,3,57856
qm 68,104,212
v_fma_f32 v16,v16,v13,v11
s_add_u32 s72,s72,s31
qt 104,2,61440
qw 5
qm 72,108,140
v_mul_f32 v16,v16,v62
qt 106,2,61952
qm 76,108,212
v_exp_f32 v16,v16
s_add_u32 s72,s72,0
qt 108,3,61440
qw 5
qm 64,96,144
v_fma_f32 v16,v16,v15,v15
qt 110,3,61952
qm 68,96,216
v_rcp_f32 v16,v16
s_add_u32 s74,s16,8
qt 96,4,0
qw 5
qm 72,100,144
v_mul_f32 v62,v62,v16
qt 98,4,512
qm 76,100,216
v_mul_f32 v16,v63,v63
v_and_b32 v42,15,v0
qt 100,5,0
qw 5
qm 64,104,148
v_fma_f32 v16,v16,v13,v11
v_lshrrev_b32 v43,5,v0
qt 102,5,512
qm 68,104,220
v_mul_f32 v16,v16,v63
qt 104,4,4096
qw 5
qm 72,108,148
v_exp_f32 v16,v16
v_bfe_u32 v25,v0,4,1
qt 106,4,4608
qm 76,108,220
v_fma_f32 v16,v16,v15,v15
qt 108,5,4096
qw 5
qm 64,96,152
v_rcp_f32 v16,v16
v_lshlrev_b32 v25,3,v25
qt 110,5,4608
qm 68,96,224
v_mul_f32 v63,v63,v16
qt 96,4,8192
qw 5
qm 72,100,152
v_mul_f32 v48,v48,v80
v_add_u32 v25,2048,v25
qt 98,4,8704
qm 76,100,224
v_mul_f32 v49,v49,v81
v_lshl_add_u32 v24,v42,7,v25
qt 100,5,8192
qw 5
qm 64,104,156
v_cvt_pk_bf16_f32 v48,v48,v49
qt 102,5,8704
qm 68,104,228
v_mul_f32 v50,v50,v82
v_lshl_add_u32 v22,v42,5,v25
qt 104,4,12288
qw 5
qm 72,108,156
s_waitcnt vmcnt(0)
s_barrier
v_mul_f32 v51,v51,v83
v_add_u32 v38,1,v38
buffer_store_dword v38,v37,s[96:99],s68 offen sc1
qt 106,4,12800
qm 76,108,228
v_cvt_pk_bf16_f32 v49,v50,v51
v_lshrrev_b32 v42,1,v42
qt 108,5,12288
qw 5
qm 64,96,160
v_mul_f32 v56,v56,v88
qt 110,5,12800
qm 68,96,232
v_mul_f32 v57,v57,v89
v_xor_b32 v25,v42,v43
qt 96,4,16384
qw 5
qm 72,100,160
v_cvt_pk_bf16_f32 v50,v56,v57
v_lshl_add_u32 v24,v25,4,v24
qt 98,4,16896
qm 76,100,232
v_mul_f32 v58,v58,v90
qt 100,5,16384
qw 5
qm 64,104,164
v_mul_f32 v59,v59,v91
v_lshrrev_b32 v42,2,v42
qt 102,5,16896
qm 68,104,236
v_cvt_pk_bf16_f32 v51,v58,v59
qt 104,4,20480
qw 5
qm 72,108,164
ds_write_b128 v18,v[48:51] offset:16384
v_mul_f32 v52,v52,v84
v_xor_b32 v42,v42,v43
qt 106,4,20992
qm 76,108,236
s_add_u32 s15,s76,57344
buffer_store_dwordx4 v[48:51],v18,s[88:91],s15 offen offset:0 sc1
v_mul_f32 v53,v53,v85
qt 108,5,20480
qw 6
qm 64,96,168
v_cvt_pk_bf16_f32 v52,v52,v53
v_lshl_add_u32 v22,v42,4,v22
qt 110,5,20992
qm 68,96,240
v_mul_f32 v54,v54,v86
v_add_u32 v23,0x10000,v22
qt 96,2,0
qw 6
qm 72,100,168
v_mul_f32 v55,v55,v87
qt 98,2,512
qm 76,100,240
v_cvt_pk_bf16_f32 v53,v54,v55
v_xor_b32 v25,32,v24
qt 100,3,0
qw 5
qm 64,104,172
v_mul_f32 v60,v60,v92
qt 102,3,512
qm 68,104,244
v_mul_f32 v61,v61,v93
v_xor_b32 v26,64,v24
qt 104,2,4096
qw 5
qm 72,108,172
v_cvt_pk_bf16_f32 v54,v60,v61
qt 106,2,4608
qm 76,108,244
v_mul_f32 v62,v62,v94
v_xor_b32 v27,0x60,v24
qt 108,3,4096
qw 5
qm 64,96,176
v_mul_f32 v63,v63,v95
v_lshlrev_b32 v36,3,v0
qt 110,3,4608
qm 68,96,248
v_cvt_pk_bf16_f32 v55,v62,v63
qt 96,2,8192
qw 5
qm 72,100,176
ds_write_b128 v18,v[52:55] offset:17408
v_add_u32 v36,s13,v36
qt 98,2,8704
qm 76,100,248
s_add_u32 s15,s76,57344
buffer_store_dwordx4 v[52:55],v18,s[88:91],s15 offen offset:1024 sc1
qt 100,3,8192
qw 6
qm 64,104,180
buffer_load_dword v39,off,s[96:99],s78 sc1
v_add_u32 v36,147456,v36
qt 102,3,8704
qm 68,104,252
qt 104,2,12288
qw 6
qm 72,108,180
qw 4
s_barrier
qt 106,2,12800
qm 76,108,252
qt 108,3,12288
qw 5
qz 80,96,112
qt 110,3,12800
qz 84,96,184
v_mul_f32 v16,v64,v64
qt 96,2,16384
qw 5
qz 88,100,112
v_fma_f32 v16,v16,v12,v10
qt 98,2,16896
qz 92,100,184
v_mul_f32 v16,v16,v64
qt 100,3,16384
qw 5
qm 80,104,116
v_exp_f32 v16,v16
qt 102,3,16896
qm 84,104,188
v_fma_f32 v16,v16,v14,v14
qt 104,2,20480
qw 5
qm 88,108,116
v_rcp_f32 v16,v16
qt 106,2,20992
qm 92,108,188
v_mul_f32 v64,v64,v16
qt 108,3,20480
qw 5
qm 80,96,120
v_mul_f32 v16,v65,v65
qt 110,3,20992
qm 84,96,192
v_fma_f32 v16,v16,v12,v10
qr 112,18,49152
qt 96,2,24576
qw 6
qm 88,100,120
v_mul_f32 v16,v16,v65
qt 98,2,25088
qm 92,100,192
v_exp_f32 v16,v16
qr 184,18,50176
qt 100,3,24576
qw 7
qm 80,104,124
v_fma_f32 v16,v16,v14,v14
qt 102,3,25088
qm 84,104,196
v_rcp_f32 v16,v16
qr 116,18,57344
qt 104,2,28672
qw 8
qm 88,108,124
v_mul_f32 v65,v65,v16
qt 106,2,29184
qm 92,108,196
v_mul_f32 v16,v66,v66
qr 188,18,58368
qt 108,3,28672
qw 8
qm 80,96,128
v_fma_f32 v16,v16,v12,v10
qt 110,3,29184
qm 84,96,200
v_mul_f32 v16,v16,v66
qr 120,19,8192
qt 96,2,32768
qw 8
qm 88,100,128
v_exp_f32 v16,v16
qt 98,2,33280
qm 92,100,200
v_fma_f32 v16,v16,v14,v14
qr 192,19,9216
qt 100,3,32768
qw 8
qm 80,104,132
v_rcp_f32 v16,v16
qt 102,3,33280
qm 84,104,204
v_mul_f32 v66,v66,v16
qr 124,18,24576
qt 104,2,36864
qw 8
qm 88,108,132
s_waitcnt vmcnt(0)
s_mov_b32 s35,0
.L_wX11:
v_readfirstlane_b32 s34,v39
s_sub_u32 s34,s74,s34
s_cmp_le_u32 s34,7
s_cbranch_scc1 .L_wX11d
s_add_u32 s35,s35,1
s_cmp_gt_u32 s35,1000000
s_cbranch_scc1 .L_wX11d
s_sleep 2
buffer_load_dword v39,off,s[96:99],s78 sc1
s_waitcnt vmcnt(0)
s_branch .L_wX11
.L_wX11d:
qw 1
s_barrier
v_mul_f32 v16,v67,v67
qt 106,2,37376
qm 92,108,204
v_fma_f32 v16,v16,v12,v10
qr 196,18,25600
qt 108,3,36864
qm 80,96,136
v_mul_f32 v16,v16,v67
v_add_u32 v28,0x10000,v24
qt 110,3,37376
qm 84,96,208
v_exp_f32 v16,v16
v_add_u32 v29,0x10000,v25
qt 32,36,0
qt 96,2,40960
qm 88,100,136
v_fma_f32 v16,v16,v14,v14
qt 98,2,41472
qm 92,100,208
v_rcp_f32 v16,v16
v_add_u32 v30,0x10000,v26
qt 34,36,512
s_add_u32 s15,s77,0
buffer_load_dwordx4 v[128:131],v18,s[88:91],s15 offen offset:0 sc1
qt 100,3,40960
qw 8
qm 80,104,140
v_mul_f32 v67,v67,v16
v_add_u32 v31,0x10000,v27
qt 102,3,41472
qm 84,104,212
v_mul_f32 v16,v72,v72
qt 42,36,1024
qt 104,2,45056
qw 8
qm 88,108,140
v_fma_f32 v16,v16,v12,v10
qt 106,2,45568
qm 92,108,212
v_mul_f32 v16,v16,v72
qt 44,36,1536
qt 108,3,45056
qw 8
qm 80,96,144
v_exp_f32 v16,v16
qt 110,3,45568
qm 84,96,216
v_fma_f32 v16,v16,v14,v14
qt 96,2,49152
qw 7
qm 88,100,144
v_rcp_f32 v16,v16
qt 98,2,49664
qm 92,100,216
v_mul_f32 v72,v72,v16
s_add_u32 s15,s77,0
buffer_load_dwordx4 v[200:203],v18,s[88:91],s15 offen offset:1024 sc1
qt 100,3,49152
qw 6
qm 80,104,148
v_mul_f32 v16,v73,v73
qt 102,3,49664
qm 84,104,220
v_fma_f32 v16,v16,v12,v10
qt 104,2,53248
qw 5
qm 88,108,148
v_mul_f32 v16,v16,v73
qt 106,2,53760
qm 92,108,220
v_exp_f32 v16,v16
qt 108,3,53248
qw 5
qm 80,96,152
v_fma_f32 v16,v16,v14,v14
qt 110,3,53760
qm 84,96,224
v_rcp_f32 v16,v16
qt 96,2,57344
qw 5
qm 88,100,152
v_mul_f32 v73,v73,v16
s_mul_i32 s30,s10,1310720
qt 98,2,57856
qm 92,100,224
v_mul_f32 v16,v74,v74
s_lshl_b32 s31,s8,14
qt 100,3,57344
qw 5
qm 80,104,156
v_fma_f32 v16,v16,v12,v10
s_add_u32 s15,s77,8192
buffer_load_dwordx4 v[132:135],v18,s[88:91],s15 offen offset:0 sc1
qt 102,3,57856
qm 84,104,228
v_mul_f32 v16,v16,v74
s_add_u32 s73,s30,s31
qt 104,2,61440
qw 5
qm 88,108,156
s_barrier
v_exp_f32 v16,v16
qt 106,2,61952
qm 92,108,228
v_fma_f32 v16,v16,v14,v14
s_add_u32 s73,s73,57344
qt 108,3,61440
qw 5
qm 80,96,160
v_rcp_f32 v16,v16
qt 110,3,61952
qm 84,96,232
v_mul_f32 v74,v74,v16
s_lshl_b32 s31,s8,11
qt 96,4,16384
qw 5
qm 88,100,160
v_mul_f32 v16,v75,v75
s_add_u32 s69,s30,s31
qt 98,4,16896
qm 92,100,232
v_fma_f32 v16,v16,v12,v10
qt 100,5,16384
qw 5
qm 80,104,164
v_mul_f32 v16,v16,v75
s_lshl_b32 s31,s7,8
qt 102,5,16896
qm 84,104,236
v_exp_f32 v16,v16
s_add_u32 s15,s77,8192
buffer_load_dwordx4 v[204:207],v18,s[88:91],s15 offen offset:1024 sc1
qt 104,4,20480
qw 5
qm 88,108,164
v_fma_f32 v16,v16,v14,v14
s_add_u32 s69,s69,s31
qt 106,4,20992
qm 92,108,236
v_rcp_f32 v16,v16
qt 108,5,20480
qw 5
qm 80,96,168
v_mul_f32 v75,v75,v16
s_add_u32 s69,s69,131072
qt 110,5,20992
qm 84,96,240
v_mul_f32 v16,v68,v68
s_and_b32 s75,s8,4
qt 96,4,0
qw 5
qm 88,100,168
v_fma_f32 v16,v16,v13,v11
qt 98,4,512
qm 92,100,240
v_mul_f32 v16,v16,v68
s_lshl_b32 s75,s75,4
qt 100,5,0
qw 5
qm 80,104,172
v_exp_f32 v16,v16
qt 102,5,512
qm 84,104,244
v_fma_f32 v16,v16,v15,v15
s_and_b32 s33,s8,3
s_lshl_b32 s33,s33,2
s_add_u32 s33,s33,s7
s_lshl_b32 s33,s33,3
s_lshl_b32 s30,s10,7
s_add_u32 s30,s30,s33
qt 104,4,4096
qw 5
qm 88,108,172
v_rcp_f32 v16,v16
s_add_u32 s15,s77,16384
buffer_load_dwordx4 v[136:139],v18,s[88:91],s15 offen offset:0 sc1
qt 106,4,4608
qm 92,108,244
v_mul_f32 v68,v68,v16
s_add_u32 s30,s30,0
qt 108,5,4096
qw 5
qm 80,96,176
v_mul_f32 v16,v69,v69
s_mul_i32 s30,s30,1152
qt 110,5,4608
qm 84,96,248
v_fma_f32 v16,v16,v13,v11
qt 96,4,8192
qw 5
qm 88,100,176
v_mul_f32 v16,v16,v69
s_add_u32 s70,s30,s75
qt 98,4,8704
qm 92,100,248
v_exp_f32 v16,v16
qt 100,5,8192
qw 5
qm 80,104,180
v_fma_f32 v16,v16,v15,v15
s_lshl_b32 s71,s70,1
qt 102,5,8704
qm 84,104,252
v_rcp_f32 v16,v16
qt 104,4,12288
qw 5
qm 88,108,180
s_barrier
v_mul_f32 v69,v69,v16
qt 106,4,12800
qm 92,108,252
s_add_u32 s15,s77,16384
buffer_load_dwordx4 v[208:211],v18,s[88:91],s15 offen offset:1024 sc1
qt 108,5,12288
qw 5
qz 144,96,112
v_mul_f32 v16,v70,v70
v_mul_f32 v17,v71,v71
v_fma_f32 v16,v16,v13,v11
qt 110,5,12800
qz 216,96,184
v_fma_f32 v17,v17,v13,v11
v_mul_f32 v16,v16,v70
qt 96,2,0
qw 5
qz 148,100,112
v_mul_f32 v17,v17,v71
v_exp_f32 v16,v16
qt 98,2,512
qz 220,100,184
v_exp_f32 v17,v17
v_fma_f32 v16,v16,v15,v15
v_fma_f32 v17,v17,v15,v15
qt 100,3,0
qw 5
qm 144,104,116
v_rcp_f32 v16,v16
v_rcp_f32 v17,v17
qt 102,3,512
qm 216,104,188
v_mul_f32 v70,v70,v16
v_mul_f32 v71,v71,v17
qt 104,2,4096
qw 5
qm 148,108,116
v_mul_f32 v16,v76,v76
v_mul_f32 v17,v77,v77
v_fma_f32 v16,v16,v13,v11
qt 106,2,4608
qm 220,108,188
v_fma_f32 v17,v17,v13,v11
v_mul_f32 v16,v16,v76
qt 108,3,4096
qw 5
qz 152,96,112
v_mul_f32 v17,v17,v77
v_exp_f32 v16,v16
s_add_u32 s15,s77,24576
buffer_load_dwordx4 v[140:143],v18,s[88:91],s15 offen offset:0 sc1
qt 110,3,4608
qz 224,96,184
v_exp_f32 v17,v17
v_fma_f32 v16,v16,v15,v15
v_fma_f32 v17,v17,v15,v15
qt 96,2,8192
qw 5
qz 156,100,112
v_rcp_f32 v16,v16
v_rcp_f32 v17,v17
qt 98,2,8704
qz 228,100,184
v_mul_f32 v76,v76,v16
v_mul_f32 v77,v77,v17
qt 100,3,8192
qw 5
qm 152,104,116
v_mul_f32 v16,v78,v78
v_mul_f32 v17,v79,v79
v_fma_f32 v16,v16,v13,v11
qt 102,3,8704
qm 224,104,188
v_fma_f32 v17,v17,v13,v11
v_mul_f32 v16,v16,v78
qt 104,2,12288
qw 5
qm 156,108,116
v_mul_f32 v17,v17,v79
v_exp_f32 v16,v16
qt 106,2,12800
qm 228,108,188
v_exp_f32 v17,v17
v_fma_f32 v16,v16,v15,v15
v_fma_f32 v17,v17,v15,v15
qt 108,3,12288
qw 5
qz 176,96,112
v_rcp_f32 v16,v16
v_rcp_f32 v17,v17
qt 110,3,12800
qz 248,96,184
v_mul_f32 v78,v78,v16
v_mul_f32 v79,v79,v17
qr 160,18,32768
s_add_u32 s15,s77,24576
buffer_load_dwordx4 v[212:215],v18,s[88:91],s15 offen offset:1024 sc1
qt 96,2,16384
qw 6
qz 180,100,112
v_mul_f32 v64,v64,v80
v_mul_f32 v65,v65,v81
v_cvt_pk_bf16_f32 v64,v64,v65
qt 98,2,16896
qz 252,100,184
v_mul_f32 v66,v66,v82
v_mul_f32 v67,v67,v83
qr 232,18,33792
qt 100,3,16384
qw 7
qm 176,104,116
v_cvt_pk_bf16_f32 v65,v66,v67
v_mul_f32 v72,v72,v88
qt 102,3,16896
qm 248,104,188
v_mul_f32 v73,v73,v89
v_cvt_pk_bf16_f32 v66,v72,v73
v_mul_f32 v74,v74,v90
qr 164,18,40960
qt 104,2,20480
qw 8
qm 180,108,116
qw 1
s_barrier
v_mul_f32 v75,v75,v91
v_cvt_pk_bf16_f32 v67,v74,v75
qt 106,2,20992
qm 252,108,188
s_add_u32 s15,s76,73728
buffer_store_dwordx4 v[64:67],v18,s[88:91],s15 offen offset:0 sc1
ds_write_b128 v18,v[64:67] offset:57344
qr 236,18,41984
qt 108,3,20480
qm 144,96,120
v_mul_f32 v68,v68,v84
v_mul_f32 v69,v69,v85
v_cvt_pk_bf16_f32 v68,v68,v69
qt 110,3,20992
qm 216,96,192
v_mul_f32 v70,v70,v86
v_mul_f32 v71,v71,v87
qt 96,2,24576
qm 148,100,120
v_cvt_pk_bf16_f32 v69,v70,v71
v_mul_f32 v76,v76,v92
s_add_u32 s15,s77,65536
buffer_load_dwordx4 v[48:51],v18,s[88:91],s15 offen offset:0 sc1
qt 98,2,25088
qm 220,100,192
v_mul_f32 v77,v77,v93
v_cvt_pk_bf16_f32 v70,v76,v77
v_mul_f32 v78,v78,v94
qt 100,3,24576
qw 7
qm 144,104,124
v_mul_f32 v79,v79,v95
v_cvt_pk_bf16_f32 v71,v78,v79
qt 102,3,25088
qm 216,104,196
s_add_u32 s15,s76,73728
buffer_store_dwordx4 v[68:71],v18,s[88:91],s15 offen offset:1024 sc1
ds_write_b128 v18,v[68:71] offset:58368
qt 104,2,28672
qw 6
qm 148,108,124
qt 106,2,29184
qm 220,108,196
qt 108,3,28672
qw 6
qm 152,96,120
qt 110,3,29184
qm 224,96,192
qt 96,2,32768
qw 6
qm 156,100,120
qt 98,2,33280
qm 228,100,192
s_add_u32 s15,s77,65536
buffer_load_dwordx4 v[56:59],v18,s[88:91],s15 offen offset:1024 sc1
qt 100,3,32768
qw 5
qm 152,104,124
qt 102,3,33280
qm 224,104,196
qt 104,2,36864
qw 5
qm 156,108,124
qt 106,2,37376
qm 228,108,196
qt 108,3,36864
qw 5
qm 176,96,120
qt 110,3,37376
qm 248,96,192
qr 168,18,8192
qd 96,22,40960
qw 6
qm 180,100,120
qd 98,22,45056
qm 252,100,192
qr 240,18,9216
qd 100,22,41472
qw 7
qm 176,104,124
s_add_u32 s15,s77,73728
buffer_load_dwordx4 v[52:55],v18,s[88:91],s15 offen offset:0 sc1
qd 102,22,45568
qm 248,104,196
qr 172,18,16384
qd 104,22,41984
qw 8
qm 180,108,124
qw 1
s_barrier
qd 106,22,46080
qm 252,108,196
qr 244,18,17408
qd 108,22,42496
qm 144,96,32
qd 110,22,46592
qm 216,96,42
qd 96,23,16384
qm 148,100,32
qd 98,23,20480
qm 220,100,42
qd 100,23,16896
qw 6
qm 152,104,32
qd 102,23,20992
qm 224,104,42
qw 4
qm 156,108,32
qm 228,108,42
qw 2
qm 176,96,32
qm 248,96,42
qr 120,18,49152
qt 96,2,49152
qw 2
qm 180,100,32
qt 98,2,49664
qm 252,100,42
qr 192,18,50176
qt 100,3,49152
qt 102,3,49664
qr 124,18,57344
qt 104,2,53248
qt 106,2,53760
qr 196,18,58368
qt 108,3,53248
qw 8
qm 144,96,160
qt 110,3,53760
qm 216,96,232
qt 96,2,57344
qw 7
qm 148,100,160
qt 98,2,57856
qm 220,100,232
qt 100,3,57344
qw 6
qm 144,104,164
qt 102,3,57856
qm 216,104,236
qt 104,2,61440
qw 5
qm 148,108,164
s_waitcnt vmcnt(0)
s_barrier
v_add_u32 v38,1,v38
buffer_store_dword v38,v37,s[96:99],s68 offen sc1
qt 106,2,61952
qm 220,108,236
qt 108,3,61440
qw 5
qm 152,96,160
v_and_b32 v1,0xfffffc3f,v18
qt 110,3,61952
qm 224,96,232
qt 96,4,0
qw 5
qm 156,100,160
qt 98,4,512
qm 228,100,232
s_add_u32 s15,s77,73728
buffer_load_dwordx4 v[60:63],v18,s[88:91],s15 offen offset:1024 sc1
qt 100,5,0
qw 5
qm 152,104,164
v_lshrrev_b32 v16,4,v0
qt 102,5,512
qm 224,104,236
qt 104,4,4096
qw 5
qm 156,108,164
qt 106,4,4608
qm 228,108,236
qt 108,5,4096
qw 5
qm 176,96,160
qt 110,5,4608
qm 248,96,232
v_lshl_add_u32 v1,v16,6,v1
qt 96,4,8192
qw 5
qm 180,100,160
qt 98,4,8704
qm 252,100,232
qt 100,5,8192
qw 5
qm 176,104,164
s_add_u32 s15,s77,81920
buffer_load_dwordx4 v[80:83],v18,s[88:91],s15 offen offset:0 sc1
qt 102,5,8704
qm 248,104,236
qt 104,4,12288
qw 5
qm 180,108,164
v_and_b32 v16,12,v0
qt 106,4,12800
qm 252,108,236
qt 108,5,12288
qw 5
qm 144,96,168
qt 110,5,12800
qm 216,96,240
qt 96,2,0
qw 5
qm 148,100,168
qt 98,2,512
qm 220,100,240
v_lshl_add_u32 v1,v16,6,v1
qt 100,3,0
qw 5
qm 144,104,172
buffer_load_dword v39,off,s[96:99],s78 sc1
qt 102,3,512
qm 216,104,244
s_add_u32 s15,s77,81920
buffer_load_dwordx4 v[88:91],v18,s[88:91],s15 offen offset:1024 sc1
qt 104,2,4096
qw 5
qm 148,108,172
s_barrier
qt 106,2,4608
qm 220,108,244
qt 108,3,4096
qw 5
qm 152,96,168
qt 110,3,4608
qm 224,96,240
qt 96,2,8192
qw 5
qm 156,100,168
qt 98,2,8704
qm 228,100,240
qt 100,3,8192
qw 5
qm 152,104,172
qt 102,3,8704
qm 224,104,244
qt 104,2,12288
qw 5
qm 156,108,172
s_add_u32 s15,s77,90112
buffer_load_dwordx4 v[84:87],v18,s[88:91],s15 offen offset:0 sc1
qt 106,2,12800
qm 228,108,244
qt 108,3,12288
qw 5
qm 176,96,168
qt 110,3,12800
qm 248,96,240
qt 96,2,16384
qw 5
qm 180,100,168
qt 98,2,16896
qm 252,100,240
qt 100,3,16384
qw 5
qm 176,104,172
qt 102,3,16896
qm 248,104,244
qt 104,2,20480
qw 5
qm 180,108,172
qt 106,2,20992
qm 252,108,244
s_add_u32 s15,s77,90112
buffer_load_dwordx4 v[92:95],v18,s[88:91],s15 offen offset:1024 sc1
qt 108,3,20480
qw 5
qm 176,96,120
qt 110,3,20992
qm 248,96,192
qt 96,2,24576
qw 5
qm 180,100,120
qt 98,2,25088
qm 252,100,192
qt 100,3,24576
qw 5
qm 176,104,124
qt 102,3,25088
qm 248,104,196
qt 104,2,28672
qw 5
qm 180,108,124
s_waitcnt vmcnt(3)
s_mov_b32 s35,0
.L_wX12:
v_readfirstlane_b32 s34,v39
s_sub_u32 s34,s74,s34
s_cmp_le_u32 s34,6
s_cbranch_scc1 .L_wX12d
s_add_u32 s35,s35,1
s_cmp_gt_u32 s35,1000000
s_cbranch_scc1 .L_wX12d
s_sleep 2
buffer_load_dword v39,off,s[96:99],s78 sc1
s_waitcnt vmcnt(0)
s_branch .L_wX12
.L_wX12d:
s_barrier
qt 106,2,29184
qm 252,108,196
qt 108,3,28672
qw 5
qm 144,96,120
s_add_u32 s15,s77,32768
buffer_load_dwordx4 v[64:67],v18,s[88:91],s15 offen offset:0 sc1
qt 110,3,29184
qm 216,96,192
qt 96,4,16384
qw 5
qm 148,100,120
qt 98,4,16896
qm 220,100,192
v_cvt_pk_bf16_f32 v176,v176,v177
qt 100,5,16384
qw 5
qm 144,104,124
v_cvt_pk_bf16_f32 v177,v178,v179
qt 102,5,16896
qm 216,104,196
v_cvt_pk_bf16_f32 v178,v180,v181
qt 104,4,20480
qw 5
qm 148,108,124
v_cvt_pk_bf16_f32 v179,v182,v183
qt 106,4,20992
qm 220,108,196
s_add_u32 s15,s77,32768
buffer_load_dwordx4 v[72:75],v18,s[88:91],s15 offen offset:1024 sc1
qt 108,5,20480
qw 5
qm 152,96,120
v_cvt_pk_bf16_f32 v248,v248,v249
qt 110,5,20992
qm 224,96,192
v_cvt_pk_bf16_f32 v249,v250,v251
qt 96,2,32768
qw 5
qm 156,100,120
v_cvt_pk_bf16_f32 v250,v252,v253
qt 98,2,33280
qm 228,100,192
v_cvt_pk_bf16_f32 v251,v254,v255
qt 100,3,32768
qw 5
qm 152,104,124
qt 102,3,33280
qm 224,104,196
qt 104,2,36864
qw 5
qm 156,108,124
s_add_u32 s15,s77,40960
buffer_load_dwordx4 v[68:71],v18,s[88:91],s15 offen offset:0 sc1
qt 106,2,37376
qm 228,108,196
qt 108,3,36864
qw 5
qm 144,96,128
qt 110,3,37376
qm 216,96,200
qt 96,2,40960
qw 5
qm 148,100,128
qt 98,2,41472
qm 220,100,200
qt 100,3,40960
qw 5
qm 144,104,132
qt 102,3,41472
qm 216,104,204
s_add_u32 s15,s77,40960
buffer_load_dwordx4 v[76:79],v18,s[88:91],s15 offen offset:1024 sc1
qt 104,2,45056
qw 5
qm 148,108,132
s_barrier
qt 106,2,45568
qm 220,108,204
qt 108,3,45056
qw 5
qm 152,96,128
v_and_b32 v15,48,v0
qt 110,3,45568
qm 224,96,200
qt 96,2,49152
qw 5
qm 156,100,128
v_lshlrev_b32 v10,10,v15
qt 98,2,49664
qm 228,100,200
qt 100,3,49152
qw 5
qm 152,104,132
s_add_u32 s15,s77,49152
buffer_load_dwordx4 v[120:123],v18,s[88:91],s15 offen offset:0 sc1
qt 102,3,49664
qm 224,104,204
v_and_b32 v16,16,v0
qt 104,2,53248
qw 5
qm 156,108,132
qt 106,2,53760
qm 228,108,204
qt 108,3,53248
qw 5
qm 144,96,136
v_lshrrev_b32 v17,5,v0
qt 110,3,53760
qm 216,96,208
qt 96,2,57344
qw 5
qm 148,100,136
v_lshl_add_u32 v12,v17,6,v16
qt 98,2,57856
qm 220,100,208
s_add_u32 s15,s77,49152
buffer_load_dwordx4 v[192:195],v18,s[88:91],s15 offen offset:1024 sc1
qt 100,3,57344
qw 5
qm 144,104,140
qt 102,3,57856
qm 216,104,212
v_and_b32 v16,3,v0
qt 104,2,61440
qw 5
qm 148,108,140
qt 106,2,61952
qm 220,108,212
qt 108,3,61440
qw 5
qm 152,96,136
v_lshl_add_u32 v12,v16,2,v12
qt 110,3,61952
qm 224,96,208
qd 96,28,0
qw 5
qm 156,100,136
s_add_u32 s15,s77,57344
buffer_load_dwordx4 v[124:127],v18,s[88:91],s15 offen offset:0 sc1
v_lshl_add_u32 v10,v16,6,v10
qd 98,28,4096
qm 228,100,208
qd 100,29,0
qw 5
qm 152,104,140
qd 102,29,4096
qm 224,104,212
v_bfe_u32 v11,v0,2,2
qd 104,30,0
qw 5
qm 156,108,140
s_barrier
qd 106,30,4096
qm 228,108,212
qd 108,31,0
qw 5
qm 144,96,48
v_lshl_add_u32 v10,v11,4,v10
qd 110,31,4096
qm 216,96,56
s_add_u32 s15,s77,57344
buffer_load_dwordx4 v[196:199],v18,s[88:91],s15 offen offset:1024 sc1
qd 96,28,8192
qw 5
qm 144,100,52
v_lshlrev_b32 v12,1,v12
qd 98,28,12288
qm 216,100,60
qd 100,29,8192
qw 5
qm 144,104,80
qd 102,29,12288
s_waitcnt vmcnt(10)
qm 216,104,88
v_mul_u32_u24 v11,1152,v11
qd 104,30,8192
s_waitcnt vmcnt(9) lgkmcnt(5)
qm 144,108,84
qd 106,30,12288
s_waitcnt vmcnt(8)
qm 216,108,92
qd 108,31,8192
qw 5
qm 152,96,48
v_add_u32 v11,v11,v12
qd 110,31,12288
qm 224,96,56
s_add_u32 s15,s73,524288
buffer_store_dwordx4 v[176:179],v1,s[88:91],s15 offen offset:0 sc1
qd 96,24,0
qw 5
qm 152,100,52
v_lshlrev_b32 v13,1,v11
qd 98,24,4096
qm 224,100,60
qd 100,25,0
qw 5
qm 152,104,80
qd 102,25,4096
qm 224,104,88
v_lshl_or_b32 v14,v17,30,v13
qd 104,26,0
qw 5
qm 152,108,84
qd 106,26,4096
qm 224,108,92
qd 108,27,0
qw 5
qm 148,96,48
s_and_b32 s30,s8,3
s_lshl_b32 s30,s30,4
s_mov_b64 s[32:33],0xffff
s_lshl_b64 s[32:33],s[32:33],s30
s_bitcmp1_b32 s8,2
s_cselect_b64 s[40:41],0,s[32:33]
s_cselect_b64 s[42:43],s[32:33],0
qd 110,27,4096
qm 220,96,56
s_add_u32 s15,s73,524288
buffer_store_dwordx4 v[248:251],v1,s[88:91],s15 offen offset:1024 sc1
qt 96,2,8192
qw 5
qm 148,100,52
s_mov_b64 s[52:53],0
qt 98,2,8704
qm 220,100,60
qt 100,3,8192
qw 5
qm 148,104,80
qt 102,3,8704
qm 220,104,88
s_mov_b64 s[54:55],0
qt 104,2,12288
qw 5
qm 148,108,84
s_barrier
qt 106,2,12800
qm 220,108,92
qt 108,3,12288
s_waitcnt vmcnt(9) lgkmcnt(5)
qm 144,96,64
s_mov_b64 s[56:57],0
qt 110,3,12800
s_waitcnt vmcnt(8)
qm 216,96,72
qt 96,4,16384
qw 5
qm 148,100,64
s_mov_b64 s[58:59],0
qt 98,4,16896
qm 220,100,72
qt 100,5,16384
s_waitcnt vmcnt(7) lgkmcnt(5)
qm 144,104,68
qt 102,5,16896
s_waitcnt vmcnt(6)
qm 216,104,76
s_mov_b32 s76,0
qt 104,4,20480
qw 5
qm 148,108,68
qt 106,4,20992
qm 220,108,76
qt 108,5,20480
s_waitcnt vmcnt(5) lgkmcnt(5)
qm 144,96,120
qt 110,5,20992
s_waitcnt vmcnt(4)
qm 216,96,192
qd 96,24,16384
qw 5
qm 148,100,120
qd 98,24,20480
qm 220,100,192
qd 100,25,16384
s_waitcnt vmcnt(3) lgkmcnt(5)
qm 144,104,124
qd 102,25,20480
s_waitcnt vmcnt(2)
qm 216,104,196
qd 104,26,16384
qw 5
qm 148,108,124
qd 106,26,20480
qm 220,108,196
qd 108,27,16384
qw 5
qm 156,96,48
qd 110,27,20480
qm 228,96,56
qt 96,2,24576
qw 5
qm 156,100,52
qt 98,2,25088
qm 228,100,60
v_cvt_pk_bf16_f32 v144,v144,v145
qt 100,3,24576
qw 5
qm 156,104,80
v_cvt_pk_bf16_f32 v145,v146,v147
qt 102,3,25088
qm 228,104,88
v_cvt_pk_bf16_f32 v146,v148,v149
qt 104,2,28672
qw 5
qm 156,108,84
s_barrier
v_cvt_pk_bf16_f32 v147,v150,v151
qt 106,2,29184
qm 228,108,92
qt 108,3,28672
qw 5
qm 152,96,64
v_cvt_pk_bf16_f32 v216,v216,v217
s_add_u32 s15,s73,0
buffer_store_dwordx4 v[144:147],v1,s[88:91],s15 offen offset:0 sc1
qt 110,3,29184
qm 224,96,72
v_cvt_pk_bf16_f32 v217,v218,v219
qt 96,2,32768
qw 5
qm 156,100,64
v_cvt_pk_bf16_f32 v218,v220,v221
qt 98,2,33280
qm 228,100,72
v_cvt_pk_bf16_f32 v219,v222,v223
qt 100,3,32768
qw 5
qm 152,104,68
qt 102,3,33280
qm 224,104,76
qt 104,2,36864
qw 5
qm 156,108,68
qt 106,2,37376
qm 228,108,76
qt 108,3,36864
qw 5
qm 152,96,120
s_add_u32 s15,s73,0
buffer_store_dwordx4 v[216:219],v1,s[88:91],s15 offen offset:1024 sc1
qt 110,3,37376
qm 224,96,192
qt 96,2,40960
qw 5
qm 156,100,120
qt 98,2,41472
qm 228,100,192
qt 100,3,40960
qw 5
qm 152,104,124
qt 102,3,41472
qm 224,104,196
qt 104,2,45056
qw 5
qm 156,108,124
qt 106,2,45568
qm 228,108,196
qt 108,3,45056
qw 5
qz 160,96,112
qt 110,3,45568
qz 232,96,184
qt 96,2,49152
qw 5
qz 164,100,112
qt 98,2,49664
qz 236,100,184
v_cvt_pk_bf16_f32 v152,v152,v153
qt 100,3,49152
qw 5
qm 160,104,116
v_cvt_pk_bf16_f32 v153,v154,v155
qt 102,3,49664
qm 232,104,188
v_cvt_pk_bf16_f32 v154,v156,v157
v_and_b32 v16,1,v0
qt 104,2,53248
qw 5
qm 164,108,116
s_barrier
v_cvt_pk_bf16_f32 v155,v158,v159
qt 106,2,53760
qm 236,108,188
v_lshlrev_b32 v16,4,v16
qt 108,3,53248
qw 5
qz 168,96,112
v_cvt_pk_bf16_f32 v224,v224,v225
s_add_u32 s15,s73,131072
buffer_store_dwordx4 v[152:155],v1,s[88:91],s15 offen offset:0 sc1
qt 110,3,53760
qz 240,96,184
v_cvt_pk_bf16_f32 v225,v226,v227
v_mov_b32 v17,0x3f80
qd 96,22,57344
qw 5
qz 172,100,112
v_cvt_pk_bf16_f32 v226,v228,v229
qd 98,22,61440
qz 244,100,184
v_cvt_pk_bf16_f32 v227,v230,v231
v_lshlrev_b32 v16,v16,v17
qd 100,22,57856
qw 5
qm 168,104,116
qd 102,22,61952
qm 240,104,188
v_bfe_u32 v17,v0,1,3
qd 104,22,58368
qw 5
qm 172,108,116
qd 106,22,62464
qm 244,108,188
v_cmp_eq_u32 vcc,0,v17
s_nop 1
v_cndmask_b32 v144,0,v16,vcc
qd 108,22,58880
qw 5
qm 160,96,32
s_add_u32 s15,s73,131072
buffer_store_dwordx4 v[224:227],v1,s[88:91],s15 offen offset:1024 sc1
qd 110,22,62976
qm 232,96,42
v_cmp_eq_u32 vcc,1,v17
s_nop 1
v_cndmask_b32 v145,0,v16,vcc
qt 96,4,0
qw 5
qm 164,100,32
qt 98,4,512
qm 236,100,42
v_cmp_eq_u32 vcc,2,v17
s_nop 1
v_cndmask_b32 v146,0,v16,vcc
qt 100,5,0
qw 5
qm 168,104,32
qt 102,5,512
qm 240,104,42
v_cmp_eq_u32 vcc,3,v17
s_nop 1
v_cndmask_b32 v147,0,v16,vcc
qt 104,4,4096
qw 5
qm 172,108,32
qt 106,4,4608
qm 244,108,42
v_cmp_eq_u32 vcc,4,v17
s_nop 1
v_cndmask_b32 v148,0,v16,vcc
qt 108,5,4096
qw 5
qm 160,96,128
qt 110,5,4608
qm 232,96,200
v_cmp_eq_u32 vcc,5,v17
s_nop 1
v_cndmask_b32 v149,0,v16,vcc
qt 96,4,8192
qw 5
qm 164,100,128
qt 98,4,8704
qm 236,100,200
v_cmp_eq_u32 vcc,6,v17
s_nop 1
v_cndmask_b32 v150,0,v16,vcc
qt 100,5,8192
qw 5
qm 160,104,132
qt 102,5,8704
qm 232,104,204
v_cmp_eq_u32 vcc,7,v17
s_nop 1
v_cndmask_b32 v151,0,v16,vcc
qt 104,4,12288
qw 5
qm 164,108,132
s_barrier
qt 106,4,12800
qm 236,108,204
qt 108,5,12288
qw 5
qm 168,96,128
qt 110,5,12800
qm 240,96,200
qt 96,4,16384
qw 5
qm 172,100,128
qt 98,4,16896
qm 244,100,200
qt 100,5,16384
qw 5
qm 168,104,132
qt 102,5,16896
qm 240,104,204
qt 104,4,20480
qw 5
qm 172,108,132
qt 106,4,20992
qm 244,108,204
qt 108,5,20480
qw 5
qm 160,96,136
qt 110,5,20992
qm 232,96,208
qt 96,2,0
qw 5
qm 164,100,136
qt 98,2,512
qm 236,100,208
qt 100,3,0
qw 5
qm 160,104,140
qt 102,3,512
qm 232,104,212
qt 104,2,4096
qw 5
qm 164,108,140
qt 106,2,4608
qm 236,108,212
qt 108,3,4096
qw 5
qm 168,96,136
qt 110,3,4608
qm 240,96,208
qd 96,24,8192
qw 5
qm 172,100,136
qd 98,24,12288
qm 244,100,208
qd 100,25,8192
qw 5
qm 168,104,140
qd 102,25,12288
qm 240,104,212
qd 104,26,8192
qw 5
qm 172,108,140
s_waitcnt vmcnt(0)
s_barrier
v_add_u32 v38,1,v38
buffer_store_dword v38,v37,s[96:99],s68 offen sc1
qd 106,26,12288
qm 244,108,212
qd 108,27,8192
qw 5
qm 160,96,48
buffer_load_dwordx2 v[216:217],v11,s[36:39],s70 offen offset:512
qd 110,27,12288
qm 232,96,56
qr 136,19,8192
qd 96,24,16384
qw 6
qm 160,100,52
qd 98,24,20480
qm 232,100,60
qr 208,19,9216
qd 100,25,16384
qw 7
qm 160,104,80
qd 102,25,20480
qm 232,104,88
qr 140,18,24576
qd 104,26,16384
qw 8
qm 160,108,84
buffer_load_dwordx2 v[218:219],v12,s[80:83],s75 offen offset:512
qd 106,26,20480
qm 232,108,92
qr 212,18,25600
qd 108,27,16384
qw 8
qm 168,96,48
qd 110,27,20480
qm 240,96,56
qd 96,24,24576
qw 7
qm 168,100,52
qd 98,24,28672
qm 240,100,60
qd 100,25,24576
qw 6
qm 168,104,80
buffer_load_dwordx2 v[220:221],v11,s[36:39],s70 offen offset:0
qd 102,25,28672
qm 240,104,88
qd 104,26,24576
qw 5
qm 168,108,84
qd 106,26,28672
qm 240,108,92
qd 108,27,24576
qw 5
qm 164,96,48
buffer_load_dwordx4 v[252:255],v15,s[96:99],s72 offen sc1
qd 110,27,28672
qm 236,96,56
qd 96,24,32768
qw 5
qm 164,100,52
buffer_load_dwordx2 v[222:223],v12,s[80:83],s75 offen offset:0
qd 98,24,36864
qm 236,100,60
qd 100,25,32768
qw 5
qm 164,104,80
qd 102,25,36864
qm 236,104,88
qd 104,26,32768
qw 5
qm 164,108,84
s_barrier
qd 106,26,36864
qm 236,108,92
qd 108,27,32768
qw 5
qm 172,96,48
buffer_load_dwordx2 v[42:43],v11,s[36:39],s70 offen offset:640
qd 110,27,36864
qm 244,96,56
qt 96,2,40960
qw 5
qm 172,100,52
qt 98,2,41472
qm 244,100,60
qt 100,3,40960
qw 5
qm 172,104,80
qt 102,3,41472
qm 244,104,88
qt 104,2,45056
qw 5
qm 172,108,84
buffer_load_dwordx2 v[44:45],v12,s[80:83],s75 offen offset:640
qt 106,2,45568
qm 244,108,92
qt 108,3,45056
qw 5
qm 160,96,64
qt 110,3,45568
qm 232,96,72
qr 48,18,32768
qt 96,2,49152
qw 6
qm 164,100,64
qt 98,2,49664
qm 236,100,72
qr 56,18,33792
qt 100,3,49152
qw 7
qm 160,104,68
qt 102,3,49664
qm 232,104,76
qr 52,18,40960
qt 104,2,53248
qw 8
qm 164,108,68
qt 106,2,53760
qm 236,108,76
qr 60,18,41984
qt 108,3,53248
v_mov_b32 v16,0x7ff00000
s_waitcnt vmcnt(3)
v_sub_u32 v252,s74,v252
v_sub_u32 v253,s74,v253
v_sub_u32 v254,s74,v254
v_sub_u32 v255,s74,v255
v_cndmask_b32 v252,v252,0,s[40:41]
v_cndmask_b32 v253,v253,0,s[42:43]
qw 8
qm 168,96,64
qt 110,3,53760
v_cmp_ge_u32 s[52:53],5,v252
v_cmp_ge_u32 s[54:55],5,v253
v_cmp_ge_u32 s[56:57],5,v254
v_cmp_ge_u32 s[58:59],5,v255
qm 240,96,72
qt 96,2,57344
v_cndmask_b32 v32,v16,v10,s[52:53]
v_cndmask_b32 v33,v16,v10,s[54:55]
v_cndmask_b32 v34,v16,v10,s[56:57]
v_cndmask_b32 v35,v16,v10,s[58:59]
qw 7
qm 172,100,64
s_add_u32 s31,s69,524288
buffer_load_dwordx4 v[152:155],v32,s[88:91],s31 offen sc1
qt 98,2,57856
qm 244,100,72
qt 100,3,57344
qw 6
qm 168,104,68
qt 102,3,57856
qm 240,104,76
qt 104,2,61440
qw 5
qm 172,108,68
s_barrier
s_add_u32 s31,s69,0
buffer_load_dwordx4 v[112:115],v32,s[88:91],s31 offen sc1
qt 106,2,61952
qm 244,108,76
qt 108,3,61440
qw 5
qm 160,96,120
qt 110,3,61952
qm 232,96,192
qr 80,18,8192
qt 96,4,16384
qw 6
qm 164,100,120
s_add_u32 s31,s69,131072
buffer_load_dwordx4 v[184:187],v32,s[88:91],s31 offen sc1
qt 98,4,16896
qm 236,100,192
qr 88,18,9216
qt 100,5,16384
qw 7
qm 160,104,124
qt 102,5,16896
qm 232,104,196
qr 84,18,16384
qt 104,4,20480
qw 8
qm 164,108,124
s_add_u32 s31,s69,589824
buffer_load_dwordx4 v[156:159],v33,s[88:91],s31 offen sc1
qt 106,4,20992
)ASM"
R"ASM(qm 236,108,196
qr 92,18,17408
qt 108,5,20480
qw 8
qm 168,96,120
qt 110,5,20992
qm 240,96,192
qt 96,4,0
qw 7
qm 172,100,120
s_add_u32 s31,s69,65536
buffer_load_dwordx4 v[116:119],v33,s[88:91],s31 offen sc1
qt 98,4,512
qm 244,100,192
qt 100,5,0
qw 6
qm 168,104,124
qt 102,5,512
qm 240,104,196
qt 104,4,4096
qw 5
qm 172,108,124
s_add_u32 s31,s69,196608
buffer_load_dwordx4 v[188:191],v33,s[88:91],s31 offen sc1
qt 106,4,4608
qm 244,108,196
qt 108,5,4096
qw 5
qm 160,96,136
qt 110,5,4608
qm 232,96,208
qr 64,18,49152
qt 96,4,8192
qw 6
qm 164,100,136
s_add_u32 s31,s69,1179648
buffer_load_dwordx4 v[224:227],v34,s[88:91],s31 offen sc1
qt 98,4,8704
qm 236,100,208
qr 72,18,50176
qt 100,5,8192
qw 7
qm 160,104,140
qt 102,5,8704
qm 232,104,212
qr 68,18,57344
qt 104,4,12288
qw 8
qm 164,108,140
qw 1
s_barrier
s_add_u32 s31,s69,655360
buffer_load_dwordx4 v[128:131],v34,s[88:91],s31 offen sc1
qt 106,4,12800
qm 236,108,212
qr 76,18,58368
qt 108,5,12288
qm 160,96,48
qt 110,5,12800
qm 232,96,56
qt 96,2,0
qm 164,100,48
s_add_u32 s31,s69,786432
buffer_load_dwordx4 v[200:203],v34,s[88:91],s31 offen sc1
qt 98,2,512
qm 236,100,56
qt 100,3,0
qw 6
qm 160,104,52
qt 102,3,512
qm 232,104,60
qt 104,2,4096
qw 5
qm 164,108,52
s_add_u32 s31,s69,1245184
buffer_load_dwordx4 v[228:231],v35,s[88:91],s31 offen sc1
qt 106,2,4608
qm 236,108,60
qt 108,3,4096
qw 5
qm 160,96,80
qt 110,3,4608
qm 232,96,88
qt 96,2,8192
qw 5
qm 164,100,80
s_add_u32 s31,s69,720896
buffer_load_dwordx4 v[132:135],v35,s[88:91],s31 offen sc1
qt 98,2,8704
qm 236,100,88
qt 100,3,8192
qw 5
qm 160,104,84
qt 102,3,8704
qm 232,104,92
qt 104,2,12288
qw 5
qm 164,108,84
s_add_u32 s31,s69,851968
buffer_load_dwordx4 v[204:207],v35,s[88:91],s31 offen sc1
qt 106,2,12800
qm 236,108,92
qt 108,3,12288
qw 5
qm 160,96,64
qt 110,3,12800
qm 232,96,72
qt 96,2,16384
qw 5
qm 164,100,64
qt 98,2,16896
qm 236,100,72
qt 100,3,16384
qw 5
qm 160,104,68
qt 102,3,16896
qm 232,104,76
qt 104,2,20480
qw 5
qm 164,108,68
s_barrier
qt 106,2,20992
qm 236,108,76
qt 108,3,20480
qw 5
qm 168,96,136
qt 110,3,20992
qm 240,96,208
qt 96,2,24576
qw 5
qm 172,100,136
qt 98,2,25088
qm 244,100,208
v_cvt_pk_bf16_f32 v160,v160,v161
qt 100,3,24576
qw 5
qm 168,104,140
v_cvt_pk_bf16_f32 v161,v162,v163
qt 102,3,25088
qm 240,104,212
v_cvt_pk_bf16_f32 v162,v164,v165
qt 104,2,28672
qw 5
qm 172,108,140
v_cvt_pk_bf16_f32 v163,v166,v167
qt 106,2,29184
qm 244,108,212
qt 108,3,28672
qw 5
qm 168,96,48
v_cvt_pk_bf16_f32 v232,v232,v233
s_add_u32 s15,s73,262144
buffer_store_dwordx4 v[160:163],v1,s[88:91],s15 offen offset:0 sc1
qt 110,3,29184
qm 240,96,56
v_cvt_pk_bf16_f32 v233,v234,v235
qt 96,2,32768
qw 5
qm 172,100,48
v_cvt_pk_bf16_f32 v234,v236,v237
qt 98,2,33280
qm 244,100,56
v_cvt_pk_bf16_f32 v235,v238,v239
qt 100,3,32768
qw 5
qm 168,104,52
qt 102,3,33280
qm 240,104,60
qt 104,2,36864
qw 5
qm 172,108,52
qt 106,2,37376
qm 244,108,60
qt 108,3,36864
qw 5
qm 168,96,80
s_add_u32 s15,s73,262144
buffer_store_dwordx4 v[232:235],v1,s[88:91],s15 offen offset:1024 sc1
qt 110,3,37376
qm 240,96,88
qt 96,2,40960
qw 5
qm 172,100,80
qt 98,2,41472
qm 244,100,88
qt 100,3,40960
qw 5
qm 168,104,84
qt 102,3,41472
qm 240,104,92
qt 104,2,45056
qw 5
qm 172,108,84
s_barrier
qt 106,2,45568
qm 244,108,92
qt 108,3,45056
qw 5
qm 168,96,64
qt 110,3,45568
qm 240,96,72
qw 4
qm 172,100,64
qm 244,100,72
qw 2
qm 168,104,68
qm 240,104,76
qw 0
qm 172,108,68
qm 244,108,76
s_nop 3
v_cvt_pk_bf16_f32 v168,v168,v169
v_cvt_pk_bf16_f32 v169,v170,v171
s_nop 0
v_cvt_pk_bf16_f32 v170,v172,v173
v_cvt_pk_bf16_f32 v171,v174,v175
s_add_u32 s15,s73,393216
buffer_store_dwordx4 v[168:171],v1,s[88:91],s15 offen offset:0 sc1
v_cvt_pk_bf16_f32 v240,v240,v241
v_cvt_pk_bf16_f32 v241,v242,v243
v_cvt_pk_bf16_f32 v242,v244,v245
v_cvt_pk_bf16_f32 v243,v246,v247
s_add_u32 s15,s73,393216
buffer_store_dwordx4 v[240:243],v1,s[88:91],s15 offen offset:1024 sc1
v_lshlrev_b32 v16,16,v218
v_lshlrev_b32 v176,16,v216
v_mul_f32 v16,v16,v40
v_add_f32 v17,v176,v41
v_fma_f32 v176,v17,v16,v176
v_and_b32 v16,0xffff0000,v218
v_and_b32 v177,0xffff0000,v216
v_mul_f32 v16,v16,v40
v_add_f32 v17,v177,v41
v_fma_f32 v177,v17,v16,v177
v_lshlrev_b32 v16,16,v219
v_lshlrev_b32 v178,16,v217
v_mul_f32 v16,v16,v40
v_add_f32 v17,v178,v41
v_fma_f32 v178,v17,v16,v178
v_and_b32 v16,0xffff0000,v219
v_and_b32 v179,0xffff0000,v217
v_mul_f32 v16,v16,v40
v_add_f32 v17,v179,v41
v_fma_f32 v179,v17,v16,v179
s_waitcnt vmcnt(18)
v_lshlrev_b32 v16,16,v222
v_lshlrev_b32 v180,16,v220
v_mul_f32 v16,v16,v40
v_add_f32 v17,v180,v41
v_fma_f32 v180,v17,v16,v180
v_and_b32 v16,0xffff0000,v222
v_and_b32 v181,0xffff0000,v220
v_mul_f32 v16,v16,v40
v_add_f32 v17,v181,v41
v_fma_f32 v181,v17,v16,v181
v_lshlrev_b32 v16,16,v223
v_lshlrev_b32 v182,16,v221
v_mul_f32 v16,v16,v40
v_add_f32 v17,v182,v41
v_fma_f32 v182,v17,v16,v182
v_and_b32 v16,0xffff0000,v223
v_and_b32 v183,0xffff0000,v221
v_mul_f32 v16,v16,v40
v_add_f32 v17,v183,v41
v_fma_f32 v183,v17,v16,v183
s_waitcnt vmcnt(16)
v_lshlrev_b32 v16,16,v44
v_lshlrev_b32 v248,16,v42
v_mul_f32 v16,v16,v40
v_add_f32 v17,v248,v41
v_fma_f32 v248,v17,v16,v248
v_and_b32 v16,0xffff0000,v44
v_and_b32 v249,0xffff0000,v42
v_mul_f32 v16,v16,v40
v_add_f32 v17,v249,v41
v_fma_f32 v249,v17,v16,v249
v_lshlrev_b32 v16,16,v45
v_lshlrev_b32 v250,16,v43
v_mul_f32 v16,v16,v40
v_add_f32 v17,v250,v41
v_fma_f32 v250,v17,v16,v250
v_and_b32 v16,0xffff0000,v45
v_and_b32 v251,0xffff0000,v43
v_mul_f32 v16,v16,v40
v_add_f32 v17,v251,v41
v_fma_f32 v251,v17,v16,v251
buffer_load_dwordx4 v[252:255],v15,s[96:99],s72 offen sc1
v_add_u32 v38,5,v38
s_setprio 3
s_waitcnt vmcnt(0)
s_barrier
buffer_store_dword v38,v37,s[96:99],s68 offen sc1
s_setprio 0
.L_tX11:
v_sub_u32 v252,s74,v252
v_sub_u32 v253,s74,v253
v_sub_u32 v254,s74,v254
v_sub_u32 v255,s74,v255
v_cndmask_b32 v252,v252,0,s[40:41]
v_cndmask_b32 v253,v253,0,s[42:43]
v_cmp_ge_u32 vcc,5,v252
s_nop 1
s_andn2_b64 exec,vcc,s[52:53]
s_cbranch_scc0 .L_tX11r10
s_add_u32 s31,s69,524288
buffer_load_dwordx4 v[152:155],v10,s[88:91],s31 offen sc1
s_add_u32 s31,s69,0
buffer_load_dwordx4 v[112:115],v10,s[88:91],s31 offen sc1
s_add_u32 s31,s69,131072
buffer_load_dwordx4 v[184:187],v10,s[88:91],s31 offen sc1
.L_tX11r10:
s_mov_b64 exec,-1
s_or_b64 s[52:53],s[52:53],vcc
v_cmp_ge_u32 vcc,5,v253
s_nop 1
s_andn2_b64 exec,vcc,s[54:55]
s_cbranch_scc0 .L_tX11r11
s_add_u32 s31,s69,589824
buffer_load_dwordx4 v[156:159],v10,s[88:91],s31 offen sc1
s_add_u32 s31,s69,65536
buffer_load_dwordx4 v[116:119],v10,s[88:91],s31 offen sc1
s_add_u32 s31,s69,196608
buffer_load_dwordx4 v[188:191],v10,s[88:91],s31 offen sc1
.L_tX11r11:
s_mov_b64 exec,-1
s_or_b64 s[54:55],s[54:55],vcc
v_cmp_ge_u32 vcc,5,v254
s_nop 1
s_andn2_b64 exec,vcc,s[56:57]
s_cbranch_scc0 .L_tX11r12
s_add_u32 s31,s69,1179648
buffer_load_dwordx4 v[224:227],v10,s[88:91],s31 offen sc1
s_add_u32 s31,s69,655360
buffer_load_dwordx4 v[128:131],v10,s[88:91],s31 offen sc1
s_add_u32 s31,s69,786432
buffer_load_dwordx4 v[200:203],v10,s[88:91],s31 offen sc1
.L_tX11r12:
s_mov_b64 exec,-1
s_or_b64 s[56:57],s[56:57],vcc
v_cmp_ge_u32 vcc,5,v255
s_nop 1
s_andn2_b64 exec,vcc,s[58:59]
s_cbranch_scc0 .L_tX11r13
s_add_u32 s31,s69,1245184
buffer_load_dwordx4 v[228:231],v10,s[88:91],s31 offen sc1
s_add_u32 s31,s69,720896
buffer_load_dwordx4 v[132:135],v10,s[88:91],s31 offen sc1
s_add_u32 s31,s69,851968
buffer_load_dwordx4 v[204:207],v10,s[88:91],s31 offen sc1
.L_tX11r13:
s_mov_b64 exec,-1
s_or_b64 s[58:59],s[58:59],vcc
.L_tX11c:
s_and_b64 s[32:33],s[52:53],s[54:55]
s_and_b64 s[32:33],s[32:33],s[56:57]
s_and_b64 s[32:33],s[32:33],s[58:59]
s_cmp_eq_u64 s[32:33],-1
s_cbranch_scc1 .L_tX11x
s_add_u32 s76,s76,1
s_cmp_gt_u32 s76,1000000
s_cbranch_scc1 .L_tX11x
s_sleep 4
buffer_load_dwordx4 v[252:255],v15,s[96:99],s72 offen sc1
s_waitcnt vmcnt(0)
s_branch .L_tX11
.L_tX11x:
s_waitcnt vmcnt(0)
qm 176,144,152
qm 180,144,112
qm 248,144,128
qm 176,144,156
qm 180,144,116
qm 248,144,132
qm 176,144,224
qm 180,148,184
qm 248,148,200
qm 176,144,228
qm 180,148,188
qm 248,148,204
s_nop 5
buffer_store_dwordx4 v[176:179],v14,s[92:95],s71 offen offset:1024 sc1
buffer_store_dwordx4 v[180:183],v13,s[92:95],s71 offen offset:0 sc1
buffer_store_dwordx4 v[248:251],v13,s[92:95],s71 offen offset:1280 sc1
s_endpgm
.org .L_code_Y+163840
.L_code_X0:
.L_code_X00:
s_waitcnt vmcnt(11) lgkmcnt(0)
s_barrier
qr 184,24,0
qt 96,2,0
qt 98,2,512
qt 100,3,0
qt 102,3,512
qr 188,25,4096
qt 104,2,4096
qt 106,2,4608
qt 108,3,4096
qt 110,3,4608
qr 192,24,8192
qw 8
qz 48,96,112
qr 196,25,12288
qz 52,96,184
qr 200,24,16384
qt 96,2,8192
qw 9
qz 56,100,112
qr 204,25,20480
qt 98,2,8704
qz 60,100,184
qt 100,3,8192
qw 9
qm 48,104,116
qt 102,3,8704
qm 52,104,188
qt 104,2,12288
s_add_u32 m0,s13,57344
qw 9
qm 56,108,116
qt 106,2,12800
qm 60,108,188
qt 108,3,12288
s_add_u32 s14,s17,4128768
qw 5
qm 48,96,120
s_waitcnt vmcnt(7)
s_barrier
qt 110,3,12800
qm 52,96,192
qt 96,2,16384
ql 6,40,0
qw 5
qm 56,100,120
qt 98,2,16896
qm 60,100,192
qt 100,3,16384
ql 7,40,1024
qw 5
qm 48,104,124
qt 102,3,16896
s_add_u32 m0,s13,65536
s_add_u32 s14,s17,4718592
qm 52,104,196
qt 104,2,20480
ql 6,40,0
qw 5
qm 56,108,124
qt 106,2,20992
qm 60,108,196
qt 108,3,20480
ql 7,40,1024
qw 5
qm 48,96,128
qt 110,3,20992
s_add_u32 m0,s13,73728
s_mov_b32 s14,s17
qm 52,96,200
qt 96,2,24576
ql 6,44,0
qw 5
qm 56,100,128
qt 98,2,25088
qm 60,100,200
qt 100,3,24576
ql 7,44,1024
qw 5
qm 48,104,132
qt 102,3,25088
qm 52,104,204
qt 104,2,28672
qw 5
qm 56,108,132
s_waitcnt vmcnt(7)
s_barrier
qr 208,24,24576
qt 106,2,29184
qm 60,108,204
qr 212,25,28672
qt 108,3,28672
s_mov_b32 m0,s13
s_add_u32 s14,s17,589824
qw 7
qm 48,96,136
qt 110,3,29184
ql 6,44,0
qw 4
qm 52,96,208
qr 216,24,32768
qt 96,2,32768
qm 56,100,136
qr 220,25,36864
qt 98,2,33280
ql 7,44,1024
qm 60,100,208
qt 100,3,32768
qw 8
qm 48,104,140
qr 224,24,40960
qt 102,3,33280
s_add_u32 m0,s13,8192
qw 9
qm 52,104,212
qr 228,25,45056
qt 104,2,36864
qw 9
qm 56,108,140
qt 106,2,37376
s_add_u32 s14,s17,1179648
qm 60,108,212
qr 232,24,49152
qt 108,3,36864
qw 8
qm 48,96,144
qr 236,25,53248
qt 110,3,37376
ql 6,44,0
qm 52,96,216
qt 96,2,40960
qw 8
qm 56,100,144
qr 240,24,57344
qt 98,2,41472
ql 7,44,1024
qm 60,100,216
qr 244,25,61440
qt 100,3,40960
qw 9
qm 48,104,148
qt 102,3,41472
s_add_u32 m0,s13,16384
qm 52,104,220
qr 248,26,8192
qt 104,2,45056
qw 8
qm 56,108,148
qr 252,27,12288
qt 106,2,45568
s_add_u32 s14,s17,1769472
qm 60,108,220
qt 108,3,45056
qw 8
qm 48,96,152
qt 110,3,45568
ql 6,44,0
qm 52,96,224
qt 96,2,49152
qw 7
qm 56,100,152
qt 98,2,49664
ql 7,44,1024
qm 60,100,224
qt 100,3,49152
qw 5
qm 48,104,156
qt 102,3,49664
s_add_u32 m0,s13,24576
qm 52,104,228
qt 104,2,53248
qw 5
qm 56,108,156
s_waitcnt vmcnt(6)
s_barrier
qt 106,2,53760
s_add_u32 s14,s17,2359296
qm 60,108,228
qt 108,3,53248
qw 5
qm 48,96,160
qt 110,3,53760
ql 6,44,0
qm 52,96,232
qt 96,2,57344
qw 5
qm 56,100,160
qt 98,2,57856
ql 7,44,1024
qm 60,100,232
qt 100,3,57344
qw 5
qm 48,104,164
qt 102,3,57856
s_add_u32 m0,s13,32768
qm 52,104,236
qt 104,2,61440
qw 5
qm 56,108,164
qt 106,2,61952
s_add_u32 s14,s17,2949120
qm 60,108,236
qt 108,3,61440
qw 5
qm 48,96,168
qt 110,3,61952
ql 6,44,0
qm 52,96,240
qt 96,4,0
qw 5
qm 56,100,168
v_readfirstlane_b32 s3,v13
s_mul_i32 s16,s3,0x9E3779B1
s_add_u32 s16,s16,0x7F4A7C15
qt 98,4,512
ql 7,44,1024
qm 60,100,240
qt 100,5,0
qw 5
qm 48,104,172
v_bfe_u32 v45,v0,2,2
qt 102,5,512
s_add_u32 m0,s13,40960
qm 52,104,244
v_lshl_add_u32 v45,s7,2,v45
qt 104,4,4096
qw 5
qm 56,108,172
v_and_b32 v14,3,v0
qt 106,4,4608
s_add_u32 s14,s17,3538944
qm 60,108,244
v_lshl_add_u32 v45,v45,3,v14
qt 108,5,4096
qw 5
qm 48,96,176
v_lshlrev_b32 v45,2,v45
qt 110,5,4608
ql 6,44,0
qm 52,96,248
v_add_u32 v45,122880,v45
qt 96,4,8192
qw 5
qm 56,100,176
qt 98,4,8704
ql 7,44,1024
qm 60,100,248
qt 100,5,8192
qw 5
qm 48,104,180
qt 102,5,8704
s_add_u32 m0,s13,81920
qm 52,104,252
qt 104,4,12288
qw 5
qm 56,108,180
s_waitcnt vmcnt(6)
s_barrier
ds_read_b32 v10,v45
ds_read_b32 v11,v45 offset:16
qt 106,4,12800
s_add_u32 s14,s17,4128768
qm 60,108,252
qt 108,5,12288
qw 7
qz 80,96,112
qw 3
v_mul_f32 v14,v10,v10
v_mul_f32 v12,0xbdd2d3e8,v10
v_mul_f32 v10,0xc0135761,v10
v_mul_f32 v12,v12,v14
v_rcp_f32 v14,v14
qw 2
v_mul_f32 v15,v11,v11
v_mul_f32 v13,0xbdd2d3e8,v11
v_mul_f32 v11,0xc0135761,v11
v_mul_f32 v13,v13,v15
v_rcp_f32 v15,v15
qt 110,5,12800
ql 6,44,0
qz 84,96,184
v_mul_f32 v16,v48,v48
qt 96,2,0
qz 88,100,112
v_fma_f32 v16,v16,v12,v10
qt 98,2,512
ql 7,44,1024
qz 92,100,184
v_mul_f32 v16,v16,v48
qt 100,3,0
qw 5
qm 80,104,116
v_exp_f32 v16,v16
s_lshl_b32 s30,s10,3
s_add_u32 s30,s30,s8
s_mul_i32 s30,s30,196608
s_add_u32 s76,s30,21020672
s_add_u32 s77,s30,21118976
s_lshl_b32 s30,s7,13
s_add_u32 s79,s76,s30
qt 102,3,512
s_add_u32 m0,s13,90112
qm 84,104,188
v_fma_f32 v16,v16,v14,v14
qt 104,2,4096
qw 5
qm 88,108,116
v_rcp_f32 v16,v16
qt 106,2,4608
s_add_u32 s14,s17,4718592
qm 92,108,188
v_mul_f32 v48,v48,v16
qt 108,3,4096
qw 5
qm 80,96,120
v_mul_f32 v16,v49,v49
qt 110,3,4608
ql 6,44,0
qm 84,96,192
v_fma_f32 v16,v16,v12,v10
qt 96,2,8192
qw 5
qm 88,100,120
v_mul_f32 v16,v16,v49
qt 98,2,8704
ql 7,44,1024
qm 92,100,192
v_exp_f32 v16,v16
qt 100,3,8192
qw 5
qm 80,104,124
v_fma_f32 v16,v16,v14,v14
qt 102,3,8704
s_add_u32 m0,s13,98304
qm 84,104,196
v_rcp_f32 v16,v16
qt 104,2,12288
qw 5
qm 88,108,124
v_mul_f32 v49,v49,v16
qt 106,2,12800
s_add_u32 s14,s17,128
qm 92,108,196
v_mul_f32 v16,v50,v50
qt 108,3,12288
qw 5
qm 80,96,128
v_fma_f32 v16,v16,v12,v10
qt 110,3,12800
ql 6,40,0
qm 84,96,200
v_mul_f32 v16,v16,v50
qt 96,2,16384
qw 5
qm 88,100,128
v_exp_f32 v16,v16
qt 98,2,16896
ql 7,40,1024
qm 92,100,200
v_fma_f32 v16,v16,v14,v14
qt 100,3,16384
qw 5
qm 80,104,132
v_rcp_f32 v16,v16
qt 102,3,16896
s_add_u32 m0,s13,106496
qm 84,104,204
v_mul_f32 v50,v50,v16
qt 104,2,20480
qw 5
qm 88,108,132
s_waitcnt vmcnt(6)
s_barrier
v_mul_f32 v16,v51,v51
qt 106,2,20992
s_add_u32 s14,s17,589952
qm 92,108,204
v_fma_f32 v16,v16,v12,v10
qt 108,3,20480
qw 5
qm 80,96,136
v_mul_f32 v16,v16,v51
qt 110,3,20992
ql 6,40,0
qm 84,96,208
v_exp_f32 v16,v16
qt 96,2,24576
qw 5
qm 88,100,136
v_fma_f32 v16,v16,v14,v14
qt 98,2,25088
ql 7,40,1024
qm 92,100,208
v_rcp_f32 v16,v16
qt 100,3,24576
qw 5
qm 80,104,140
v_mul_f32 v51,v51,v16
qt 102,3,25088
s_add_u32 m0,s13,114688
qm 84,104,212
v_mul_f32 v16,v56,v56
qt 104,2,28672
qw 5
qm 88,108,140
v_fma_f32 v16,v16,v12,v10
qt 106,2,29184
s_add_u32 s14,s17,1179776
qm 92,108,212
v_mul_f32 v16,v16,v56
qt 108,3,28672
qw 5
qm 80,96,144
v_exp_f32 v16,v16
qt 110,3,29184
ql 6,40,0
qm 84,96,216
v_fma_f32 v16,v16,v14,v14
qt 96,2,32768
qw 5
qm 88,100,144
v_rcp_f32 v16,v16
qt 98,2,33280
ql 7,40,1024
qm 92,100,216
v_mul_f32 v56,v56,v16
qt 100,3,32768
qw 5
qm 80,104,148
v_mul_f32 v16,v57,v57
qt 102,3,33280
s_add_u32 m0,s13,49152
qm 84,104,220
v_fma_f32 v16,v16,v12,v10
qt 104,2,36864
qw 5
qm 88,108,148
v_mul_f32 v16,v16,v57
qt 106,2,37376
s_add_u32 s14,s17,1769600
qm 92,108,220
v_exp_f32 v16,v16
qt 108,3,36864
qw 5
qm 80,96,152
v_fma_f32 v16,v16,v14,v14
qt 110,3,37376
ql 6,40,0
qm 84,96,224
v_rcp_f32 v16,v16
qt 96,2,40960
qw 5
qm 88,100,152
v_mul_f32 v57,v57,v16
qt 98,2,41472
ql 7,40,1024
qm 92,100,224
v_mul_f32 v16,v58,v58
qt 100,3,40960
qw 5
qm 80,104,156
v_fma_f32 v16,v16,v12,v10
qt 102,3,41472
s_add_u32 m0,s13,57344
qm 84,104,228
v_mul_f32 v16,v16,v58
qt 104,2,45056
qw 5
qm 88,108,156
s_waitcnt vmcnt(6)
s_barrier
v_exp_f32 v16,v16
qt 106,2,45568
s_add_u32 s14,s17,2359424
qm 92,108,228
v_fma_f32 v16,v16,v14,v14
qt 108,3,45056
qw 5
qm 80,96,160
v_rcp_f32 v16,v16
qt 110,3,45568
ql 6,40,0
qm 84,96,232
v_mul_f32 v58,v58,v16
qt 96,4,16384
qw 5
qm 88,100,160
v_mul_f32 v16,v59,v59
qt 98,4,16896
ql 7,40,1024
qm 92,100,232
v_fma_f32 v16,v16,v12,v10
qt 100,5,16384
qw 5
qm 80,104,164
v_mul_f32 v16,v16,v59
qt 102,5,16896
s_add_u32 m0,s13,65536
qm 84,104,236
v_exp_f32 v16,v16
qt 104,4,20480
qw 5
qm 88,108,164
v_fma_f32 v16,v16,v14,v14
qt 106,4,20992
s_add_u32 s14,s17,2949248
qm 92,108,236
v_rcp_f32 v16,v16
qt 108,5,20480
qw 5
qm 80,96,168
v_mul_f32 v59,v59,v16
qt 110,5,20992
ql 6,40,0
qm 84,96,240
v_mul_f32 v16,v52,v52
qt 96,4,24576
qw 5
qm 88,100,168
v_fma_f32 v16,v16,v13,v11
qt 98,4,25088
ql 7,40,1024
qm 92,100,240
v_mul_f32 v16,v16,v52
qt 100,5,24576
qw 5
qm 80,104,172
v_exp_f32 v16,v16
qt 102,5,25088
s_add_u32 m0,s13,73728
qm 84,104,244
v_fma_f32 v16,v16,v15,v15
qt 104,4,28672
qw 5
qm 88,108,172
v_rcp_f32 v16,v16
qt 106,4,29184
s_add_u32 s14,s17,3539072
qm 92,108,244
v_mul_f32 v52,v52,v16
qt 108,5,28672
qw 5
qm 80,96,176
v_mul_f32 v16,v53,v53
qt 110,5,29184
ql 6,40,0
qm 84,96,248
v_fma_f32 v16,v16,v13,v11
qt 96,4,32768
qw 5
qm 88,100,176
v_mul_f32 v16,v16,v53
qt 98,4,33280
ql 7,40,1024
qm 92,100,248
v_exp_f32 v16,v16
qt 100,5,32768
qw 5
qm 80,104,180
v_fma_f32 v16,v16,v15,v15
qt 102,5,33280
s_add_u32 m0,s13,81920
qm 84,104,252
v_rcp_f32 v16,v16
qt 104,4,36864
qw 5
qm 88,108,180
s_waitcnt vmcnt(6)
s_barrier
v_mul_f32 v53,v53,v16
qt 106,4,37376
s_add_u32 s14,s17,4128896
qm 92,108,252
qt 108,5,36864
qw 5
qz 64,96,112
v_mul_f32 v16,v54,v54
qt 110,5,37376
ql 6,40,0
qz 68,96,184
v_fma_f32 v16,v16,v13,v11
qt 96,4,40960
qw 5
qz 72,100,112
v_mul_f32 v16,v16,v54
qt 98,4,41472
ql 7,40,1024
qz 76,100,184
v_exp_f32 v16,v16
qt 100,5,40960
qw 5
qm 64,104,116
v_fma_f32 v16,v16,v15,v15
qt 102,5,41472
s_add_u32 m0,s13,90112
qm 68,104,188
v_rcp_f32 v16,v16
qt 104,4,45056
qw 5
qm 72,108,116
v_mul_f32 v54,v54,v16
qt 106,4,45568
s_add_u32 s14,s17,4718720
qm 76,108,188
v_mul_f32 v16,v55,v55
qt 108,5,45056
qw 5
qm 64,96,120
v_fma_f32 v16,v16,v13,v11
qt 110,5,45568
ql 6,40,0
qm 68,96,192
v_mul_f32 v16,v16,v55
qt 96,4,49152
qw 5
qm 72,100,120
v_exp_f32 v16,v16
qt 98,4,49664
ql 7,40,1024
qm 76,100,192
v_fma_f32 v16,v16,v15,v15
qt 100,5,49152
qw 5
qm 64,104,124
v_rcp_f32 v16,v16
qt 102,5,49664
s_mov_b32 m0,s13
qm 68,104,196
v_mul_f32 v55,v55,v16
qt 104,4,53248
qw 5
qm 72,108,124
v_mul_f32 v16,v60,v60
qt 106,4,53760
s_add_u32 s14,s17,128
qm 76,108,196
v_fma_f32 v16,v16,v13,v11
qt 108,5,53248
qw 5
qm 64,96,128
v_mul_f32 v16,v16,v60
qt 110,5,53760
ql 6,44,0
qm 68,96,200
v_exp_f32 v16,v16
qt 96,2,49152
qw 5
qm 72,100,128
v_fma_f32 v16,v16,v15,v15
qt 98,2,49664
ql 7,44,1024
qm 76,100,200
v_rcp_f32 v16,v16
qt 100,3,49152
qw 5
qm 64,104,132
v_mul_f32 v60,v60,v16
qt 102,3,49664
s_add_u32 m0,s13,98304
qm 68,104,204
v_mul_f32 v16,v61,v61
qt 104,2,53248
qw 5
qm 72,108,132
s_waitcnt vmcnt(6)
s_barrier
v_fma_f32 v16,v16,v13,v11
qt 106,2,53760
s_add_u32 s14,s17,589952
qm 76,108,204
v_mul_f32 v16,v16,v61
qt 108,3,53248
qw 5
qm 64,96,136
v_exp_f32 v16,v16
qt 110,3,53760
ql 6,44,0
qm 68,96,208
v_fma_f32 v16,v16,v15,v15
qt 96,2,57344
qw 5
qm 72,100,136
v_rcp_f32 v16,v16
qt 98,2,57856
ql 7,44,1024
qm 76,100,208
v_mul_f32 v61,v61,v16
qt 100,3,57344
qw 5
qm 64,104,140
v_mul_f32 v16,v62,v62
qt 102,3,57856
s_add_u32 m0,s13,106496
qm 68,104,212
v_fma_f32 v16,v16,v13,v11
qt 104,2,61440
qw 5
qm 72,108,140
v_mul_f32 v16,v16,v62
qt 106,2,61952
s_add_u32 s14,s17,1179776
qm 76,108,212
v_exp_f32 v16,v16
qt 108,3,61440
qw 5
qm 64,96,144
v_fma_f32 v16,v16,v15,v15
qt 110,3,61952
ql 6,44,0
qm 68,96,216
v_rcp_f32 v16,v16
qt 96,4,0
qw 5
qm 72,100,144
v_mul_f32 v62,v62,v16
qt 98,4,512
ql 7,44,1024
qm 76,100,216
v_mul_f32 v16,v63,v63
qt 100,5,0
qw 5
qm 64,104,148
v_fma_f32 v16,v16,v13,v11
qt 102,5,512
s_add_u32 m0,s13,114688
qm 68,104,220
v_mul_f32 v16,v16,v63
qt 104,4,4096
qw 5
qm 72,108,148
v_exp_f32 v16,v16
qt 106,4,4608
s_add_u32 s14,s17,1769600
qm 76,108,220
v_fma_f32 v16,v16,v15,v15
qt 108,5,4096
qw 5
qm 64,96,152
v_rcp_f32 v16,v16
qt 110,5,4608
ql 6,44,0
qm 68,96,224
v_mul_f32 v63,v63,v16
qt 96,4,8192
qw 5
qm 72,100,152
v_mul_f32 v48,v48,v80
qt 98,4,8704
ql 7,44,1024
qm 76,100,224
v_mul_f32 v49,v49,v81
qt 100,5,8192
qw 5
qm 64,104,156
v_cvt_pk_bf16_f32 v48,v48,v49
qt 102,5,8704
s_add_u32 m0,s13,8192
qm 68,104,228
v_mul_f32 v50,v50,v82
qt 104,4,12288
qw 5
qm 72,108,156
s_waitcnt vmcnt(6)
s_barrier
v_mul_f32 v51,v51,v83
qt 106,4,12800
s_add_u32 s14,s17,2359424
qm 76,108,228
v_cvt_pk_bf16_f32 v49,v50,v51
qt 108,5,12288
qw 5
qm 64,96,160
v_mul_f32 v56,v56,v88
qt 110,5,12800
ql 6,44,0
qm 68,96,232
v_mul_f32 v57,v57,v89
qt 96,4,16384
qw 5
qm 72,100,160
v_cvt_pk_bf16_f32 v50,v56,v57
qt 98,4,16896
ql 7,44,1024
qm 76,100,232
v_mul_f32 v58,v58,v90
qt 100,5,16384
qw 5
qm 64,104,164
v_mul_f32 v59,v59,v91
qt 102,5,16896
s_add_u32 m0,s13,16384
qm 68,104,236
v_cvt_pk_bf16_f32 v51,v58,v59
qt 104,4,20480
qw 5
qm 72,108,164
ds_write_b128 v19,v[48:51]
v_mul_f32 v52,v52,v84
qt 106,4,20992
s_add_u32 s14,s17,2949248
qm 76,108,236
s_add_u32 s15,s76,0
buffer_store_dwordx4 v[48:51],v18,s[88:91],s15 offen offset:0 sc1
v_mul_f32 v53,v53,v85
qt 108,5,20480
qw 6
qm 64,96,168
v_cvt_pk_bf16_f32 v52,v52,v53
qt 110,5,20992
ql 6,44,0
qm 68,96,240
v_mul_f32 v54,v54,v86
qt 96,4,24576
qw 6
qm 72,100,168
v_mul_f32 v55,v55,v87
qt 98,4,25088
ql 7,44,1024
qm 76,100,240
v_cvt_pk_bf16_f32 v53,v54,v55
qt 100,5,24576
qw 5
qm 64,104,172
v_mul_f32 v60,v60,v92
qt 102,5,25088
s_add_u32 m0,s13,24576
qm 68,104,244
v_mul_f32 v61,v61,v93
qt 104,4,28672
qw 5
qm 72,108,172
v_cvt_pk_bf16_f32 v54,v60,v61
qt 106,4,29184
s_add_u32 s14,s17,3539072
qm 76,108,244
v_mul_f32 v62,v62,v94
qt 108,5,28672
qw 5
qm 64,96,176
v_mul_f32 v63,v63,v95
qt 110,5,29184
ql 6,44,0
qm 68,96,248
v_cvt_pk_bf16_f32 v55,v62,v63
qt 96,2,0
qw 5
qm 72,100,176
ds_write_b128 v19,v[52:55] offset:1024
qt 98,2,512
ql 7,44,1024
qm 76,100,248
s_add_u32 s15,s76,0
buffer_store_dwordx4 v[52:55],v18,s[88:91],s15 offen offset:1024 sc1
qt 100,3,0
qw 6
qm 64,104,180
qt 102,3,512
s_add_u32 m0,s13,81920
qm 68,104,252
qt 104,2,4096
qw 6
qm 72,108,180
s_waitcnt vmcnt(8) lgkmcnt(4)
s_barrier
qt 106,2,4608
s_add_u32 s14,s17,4128896
qm 76,108,252
qt 108,3,4096
qw 5
qz 80,96,112
qt 110,3,4608
ql 6,44,0
qz 84,96,184
v_mul_f32 v16,v64,v64
qt 96,4,32768
qw 5
qz 88,100,112
v_fma_f32 v16,v16,v12,v10
qt 98,4,33280
ql 7,44,1024
qz 92,100,184
v_mul_f32 v16,v16,v64
qt 100,5,32768
qw 5
qm 80,104,116
v_exp_f32 v16,v16
qt 102,5,33280
s_add_u32 m0,s13,90112
qm 84,104,188
v_fma_f32 v16,v16,v14,v14
qt 104,4,36864
qw 5
qm 88,108,116
v_rcp_f32 v16,v16
qt 106,4,37376
s_add_u32 s14,s17,4718720
qm 92,108,188
v_mul_f32 v64,v64,v16
qt 108,5,36864
qw 5
qm 80,96,120
v_mul_f32 v16,v65,v65
qt 110,5,37376
ql 6,44,0
qm 84,96,192
v_fma_f32 v16,v16,v12,v10
qt 96,4,40960
qw 5
qm 88,100,120
v_mul_f32 v16,v16,v65
qt 98,4,41472
ql 7,44,1024
qm 92,100,192
v_exp_f32 v16,v16
qt 100,5,40960
qw 5
qm 80,104,124
v_fma_f32 v16,v16,v14,v14
qt 102,5,41472
s_add_u32 m0,s13,32768
qm 84,104,196
v_rcp_f32 v16,v16
qt 104,4,45056
qw 5
qm 88,108,124
v_mul_f32 v65,v65,v16
qt 106,4,45568
s_add_u32 s14,s17,256
qm 92,108,196
v_mul_f32 v16,v66,v66
qt 108,5,45056
qw 5
qm 80,96,128
v_fma_f32 v16,v16,v12,v10
qt 110,5,45568
ql 6,40,0
qm 84,96,200
v_mul_f32 v16,v16,v66
qt 96,4,49152
qw 5
qm 88,100,128
v_exp_f32 v16,v16
qt 98,4,49664
ql 7,40,1024
qm 92,100,200
v_fma_f32 v16,v16,v14,v14
qt 100,5,49152
qw 5
qm 80,104,132
v_rcp_f32 v16,v16
qt 102,5,49664
s_add_u32 m0,s13,98304
qm 84,104,204
v_mul_f32 v66,v66,v16
qt 104,4,53248
qw 5
qm 88,108,132
s_waitcnt vmcnt(7)
s_barrier
v_mul_f32 v16,v67,v67
qt 106,4,53760
s_add_u32 s14,s17,590080
qm 92,108,204
v_fma_f32 v16,v16,v12,v10
qt 108,5,53248
qw 5
qm 80,96,136
v_mul_f32 v16,v16,v67
qt 110,5,53760
ql 6,40,0
qm 84,96,208
v_exp_f32 v16,v16
qt 96,2,8192
qw 5
qm 88,100,136
v_fma_f32 v16,v16,v14,v14
qt 98,2,8704
ql 7,40,1024
qm 92,100,208
v_rcp_f32 v16,v16
qt 100,3,8192
qw 5
qm 80,104,140
v_mul_f32 v67,v67,v16
qt 102,3,8704
s_add_u32 m0,s13,106496
qm 84,104,212
v_mul_f32 v16,v72,v72
qt 104,2,12288
qw 5
qm 88,108,140
v_fma_f32 v16,v16,v12,v10
qt 106,2,12800
s_add_u32 s14,s17,1179904
qm 92,108,212
v_mul_f32 v16,v16,v72
qt 108,3,12288
qw 5
qm 80,96,144
v_exp_f32 v16,v16
qt 110,3,12800
ql 6,40,0
qm 84,96,216
v_fma_f32 v16,v16,v14,v14
qt 96,2,16384
qw 5
qm 88,100,144
v_rcp_f32 v16,v16
qt 98,2,16896
ql 7,40,1024
qm 92,100,216
v_mul_f32 v72,v72,v16
qt 100,3,16384
qw 5
qm 80,104,148
v_mul_f32 v16,v73,v73
qt 102,3,16896
s_add_u32 m0,s13,40960
qm 84,104,220
v_fma_f32 v16,v16,v12,v10
qt 104,2,20480
qw 5
qm 88,108,148
v_mul_f32 v16,v16,v73
qt 106,2,20992
s_add_u32 s14,s17,1769728
qm 92,108,220
v_exp_f32 v16,v16
qt 108,3,20480
qw 5
qm 80,96,152
v_fma_f32 v16,v16,v14,v14
qt 110,3,20992
ql 6,40,0
qm 84,96,224
v_rcp_f32 v16,v16
qt 96,2,24576
qw 5
qm 88,100,152
v_mul_f32 v73,v73,v16
qt 98,2,25088
ql 7,40,1024
qm 92,100,224
v_mul_f32 v16,v74,v74
qt 100,3,24576
qw 5
qm 80,104,156
v_fma_f32 v16,v16,v12,v10
qt 102,3,25088
s_add_u32 m0,s13,114688
qm 84,104,228
v_mul_f32 v16,v16,v74
qt 104,2,28672
qw 5
qm 88,108,156
s_waitcnt vmcnt(6)
s_barrier
v_exp_f32 v16,v16
qt 106,2,29184
s_add_u32 s14,s17,2359552
qm 92,108,228
v_fma_f32 v16,v16,v14,v14
qt 108,3,28672
qw 5
qm 80,96,160
v_rcp_f32 v16,v16
qt 110,3,29184
ql 6,40,0
qm 84,96,232
v_mul_f32 v74,v74,v16
qt 96,4,16384
qw 5
qm 88,100,160
v_mul_f32 v16,v75,v75
qt 98,4,16896
ql 7,40,1024
qm 92,100,232
v_fma_f32 v16,v16,v12,v10
qt 100,5,16384
qw 5
qm 80,104,164
v_mul_f32 v16,v16,v75
qt 102,5,16896
s_add_u32 m0,s13,49152
qm 84,104,236
v_exp_f32 v16,v16
qt 104,4,20480
qw 5
qm 88,108,164
v_fma_f32 v16,v16,v14,v14
qt 106,4,20992
s_add_u32 s14,s17,2949376
qm 92,108,236
v_rcp_f32 v16,v16
qt 108,5,20480
qw 5
qm 80,96,168
v_mul_f32 v75,v75,v16
qt 110,5,20992
ql 6,40,0
qm 84,96,240
v_mul_f32 v16,v68,v68
qt 96,4,24576
qw 5
qm 88,100,168
v_fma_f32 v16,v16,v13,v11
qt 98,4,25088
ql 7,40,1024
qm 92,100,240
v_mul_f32 v16,v16,v68
qt 100,5,24576
qw 5
qm 80,104,172
v_exp_f32 v16,v16
qt 102,5,25088
s_add_u32 m0,s13,57344
qm 84,104,244
v_fma_f32 v16,v16,v15,v15
qt 104,4,28672
qw 5
qm 88,108,172
v_rcp_f32 v16,v16
qt 106,4,29184
s_add_u32 s14,s17,3539200
qm 92,108,244
v_mul_f32 v68,v68,v16
qt 108,5,28672
qw 5
qm 80,96,176
v_mul_f32 v16,v69,v69
qt 110,5,29184
ql 6,40,0
qm 84,96,248
v_fma_f32 v16,v16,v13,v11
qt 96,2,32768
qw 5
qm 88,100,176
v_mul_f32 v16,v16,v69
qt 98,2,33280
ql 7,40,1024
qm 92,100,248
v_exp_f32 v16,v16
qt 100,3,32768
qw 5
qm 80,104,180
v_fma_f32 v16,v16,v15,v15
qt 102,3,33280
s_add_u32 m0,s13,81920
qm 84,104,252
v_rcp_f32 v16,v16
qt 104,2,36864
qw 5
qm 88,108,180
s_waitcnt vmcnt(6)
s_barrier
v_mul_f32 v69,v69,v16
qt 106,2,37376
s_add_u32 s14,s17,4129024
qm 92,108,252
qt 108,3,36864
qw 5
qz 48,96,112
v_mul_f32 v16,v70,v70
qt 110,3,37376
ql 6,40,0
qz 52,96,184
v_fma_f32 v16,v16,v13,v11
qt 96,4,32768
qw 5
qz 56,100,112
v_mul_f32 v16,v16,v70
qt 98,4,33280
ql 7,40,1024
qz 60,100,184
v_exp_f32 v16,v16
qt 100,5,32768
qw 5
qm 48,104,116
v_fma_f32 v16,v16,v15,v15
qt 102,5,33280
s_add_u32 m0,s13,90112
qm 52,104,188
v_rcp_f32 v16,v16
qt 104,4,36864
qw 5
qm 56,108,116
v_mul_f32 v70,v70,v16
qt 106,4,37376
s_add_u32 s14,s17,4718848
qm 60,108,188
v_mul_f32 v16,v71,v71
qt 108,5,36864
qw 5
qm 48,96,120
v_fma_f32 v16,v16,v13,v11
qt 110,5,37376
ql 6,40,0
qm 52,96,192
v_mul_f32 v16,v16,v71
qt 96,4,40960
qw 5
qm 56,100,120
v_exp_f32 v16,v16
qt 98,4,41472
ql 7,40,1024
qm 60,100,192
v_fma_f32 v16,v16,v15,v15
qt 100,5,40960
qw 5
qm 48,104,124
v_rcp_f32 v16,v16
qt 102,5,41472
s_add_u32 m0,s13,65536
qm 52,104,196
v_mul_f32 v71,v71,v16
qt 104,4,45056
qw 5
qm 56,108,124
v_mul_f32 v16,v76,v76
qt 106,4,45568
s_add_u32 s14,s17,256
qm 60,108,196
v_fma_f32 v16,v16,v13,v11
qt 108,5,45056
qw 5
qm 48,96,128
v_mul_f32 v16,v16,v76
qt 110,5,45568
ql 6,44,0
qm 52,96,200
v_exp_f32 v16,v16
qt 96,2,40960
qw 5
qm 56,100,128
v_fma_f32 v16,v16,v15,v15
qt 98,2,41472
ql 7,44,1024
qm 60,100,200
v_rcp_f32 v16,v16
qt 100,3,40960
qw 5
qm 48,104,132
v_mul_f32 v76,v76,v16
qt 102,3,41472
s_add_u32 m0,s13,98304
qm 52,104,204
v_mul_f32 v16,v77,v77
qt 104,2,45056
qw 5
qm 56,108,132
s_waitcnt vmcnt(6)
s_barrier
v_fma_f32 v16,v16,v13,v11
qt 106,2,45568
s_add_u32 s14,s17,590080
qm 60,108,204
v_mul_f32 v16,v16,v77
qt 108,3,45056
qw 5
qm 48,96,136
v_exp_f32 v16,v16
qt 110,3,45568
ql 6,44,0
qm 52,96,208
v_fma_f32 v16,v16,v15,v15
qt 96,4,49152
qw 5
qm 56,100,136
v_rcp_f32 v16,v16
qt 98,4,49664
ql 7,44,1024
qm 60,100,208
v_mul_f32 v77,v77,v16
qt 100,5,49152
qw 5
qm 48,104,140
v_mul_f32 v16,v78,v78
qt 102,5,49664
s_add_u32 m0,s13,106496
qm 52,104,212
v_fma_f32 v16,v16,v13,v11
qt 104,4,53248
qw 5
qm 56,108,140
v_mul_f32 v16,v16,v78
qt 106,4,53760
s_add_u32 s14,s17,1179904
qm 60,108,212
v_exp_f32 v16,v16
qt 108,5,53248
qw 5
qm 48,96,144
v_fma_f32 v16,v16,v15,v15
qt 110,5,53760
ql 6,44,0
qm 52,96,216
v_rcp_f32 v16,v16
qt 96,2,49152
qw 5
qm 56,100,144
v_mul_f32 v78,v78,v16
qt 98,2,49664
ql 7,44,1024
qm 60,100,216
v_mul_f32 v16,v79,v79
qt 100,3,49152
qw 5
qm 48,104,148
v_fma_f32 v16,v16,v13,v11
qt 102,3,49664
s_add_u32 m0,s13,73728
qm 52,104,220
v_mul_f32 v16,v16,v79
qt 104,2,53248
qw 5
qm 56,108,148
v_exp_f32 v16,v16
qt 106,2,53760
s_add_u32 s14,s17,1769728
qm 60,108,220
v_fma_f32 v16,v16,v15,v15
qt 108,3,53248
qw 5
qm 48,96,152
v_rcp_f32 v16,v16
qt 110,3,53760
ql 6,44,0
qm 52,96,224
v_mul_f32 v79,v79,v16
qt 96,2,57344
qw 5
qm 56,100,152
v_mul_f32 v64,v64,v80
qt 98,2,57856
ql 7,44,1024
qm 60,100,224
v_mul_f32 v65,v65,v81
qt 100,3,57344
qw 5
qm 48,104,156
v_cvt_pk_bf16_f32 v64,v64,v65
qt 102,3,57856
s_add_u32 m0,s13,114688
qm 52,104,228
v_mul_f32 v66,v66,v82
qt 104,2,61440
qw 5
qm 56,108,156
s_waitcnt vmcnt(6)
s_barrier
v_mul_f32 v67,v67,v83
qt 106,2,61952
s_add_u32 s14,s17,2359552
qm 60,108,228
v_cvt_pk_bf16_f32 v65,v66,v67
qt 108,3,61440
qw 5
qm 48,96,160
v_mul_f32 v72,v72,v88
qt 110,3,61952
ql 6,44,0
qm 52,96,232
v_mul_f32 v73,v73,v89
qt 96,4,16384
qw 5
qm 56,100,160
v_cvt_pk_bf16_f32 v66,v72,v73
qt 98,4,16896
ql 7,44,1024
qm 60,100,232
v_mul_f32 v74,v74,v90
qt 100,5,16384
qw 5
qm 48,104,164
v_mul_f32 v75,v75,v91
qt 102,5,16896
s_mov_b32 m0,s13
qm 52,104,236
v_cvt_pk_bf16_f32 v67,v74,v75
qt 104,4,20480
qw 5
qm 56,108,164
ds_write_b128 v18,v[64:67] offset:49152
v_mul_f32 v68,v68,v84
qt 106,4,20992
s_add_u32 s14,s17,2949376
qm 60,108,236
s_add_u32 s15,s76,16384
buffer_store_dwordx4 v[64:67],v18,s[88:91],s15 offen offset:0 sc1
v_mul_f32 v69,v69,v85
qt 108,5,20480
qw 6
qm 48,96,168
v_cvt_pk_bf16_f32 v68,v68,v69
qt 110,5,20992
ql 6,44,0
qm 52,96,240
v_mul_f32 v70,v70,v86
qt 96,4,24576
qw 6
qm 56,100,168
v_mul_f32 v71,v71,v87
qt 98,4,25088
ql 7,44,1024
qm 60,100,240
v_cvt_pk_bf16_f32 v69,v70,v71
qt 100,5,24576
qw 5
qm 48,104,172
v_mul_f32 v76,v76,v92
qt 102,5,25088
s_add_u32 m0,s13,8192
qm 52,104,244
v_mul_f32 v77,v77,v93
qt 104,4,28672
qw 5
qm 56,108,172
v_cvt_pk_bf16_f32 v70,v76,v77
qt 106,4,29184
s_add_u32 s14,s17,3539200
qm 60,108,244
v_mul_f32 v78,v78,v94
qt 108,5,28672
qw 5
qm 48,96,176
v_mul_f32 v79,v79,v95
qt 110,5,29184
ql 6,44,0
qm 52,96,248
v_cvt_pk_bf16_f32 v71,v78,v79
qt 96,4,0
qw 5
qm 56,100,176
ds_write_b128 v18,v[68:71] offset:50176
qt 98,4,512
ql 7,44,1024
qm 60,100,248
s_add_u32 s15,s76,16384
buffer_store_dwordx4 v[68:71],v18,s[88:91],s15 offen offset:1024 sc1
qt 100,5,0
qw 6
qm 48,104,180
qt 102,5,512
s_add_u32 m0,s13,81920
qm 52,104,252
qt 104,4,4096
qw 6
qm 56,108,180
s_waitcnt vmcnt(8) lgkmcnt(4)
s_barrier
qt 106,4,4608
s_add_u32 s14,s17,4129024
qm 60,108,252
qt 108,5,4096
qw 5
qz 80,96,112
qt 110,5,4608
ql 6,44,0
qz 84,96,184
v_mul_f32 v16,v48,v48
qt 96,4,32768
qw 5
qz 88,100,112
v_fma_f32 v16,v16,v12,v10
qt 98,4,33280
ql 7,44,1024
qz 92,100,184
v_mul_f32 v16,v16,v48
qt 100,5,32768
qw 5
qm 80,104,116
v_exp_f32 v16,v16
qt 102,5,33280
s_add_u32 m0,s13,90112
qm 84,104,188
v_fma_f32 v16,v16,v14,v14
qt 104,4,36864
qw 5
qm 88,108,116
v_rcp_f32 v16,v16
qt 106,4,37376
s_add_u32 s14,s17,4718848
qm 92,108,188
v_mul_f32 v48,v48,v16
qt 108,5,36864
qw 5
qm 80,96,120
v_mul_f32 v16,v49,v49
qt 110,5,37376
ql 6,44,0
qm 84,96,192
v_fma_f32 v16,v16,v12,v10
qt 96,4,40960
qw 5
qm 88,100,120
v_mul_f32 v16,v16,v49
qt 98,4,41472
ql 7,44,1024
qm 92,100,192
v_exp_f32 v16,v16
qt 100,5,40960
qw 5
qm 80,104,124
v_fma_f32 v16,v16,v14,v14
qt 102,5,41472
s_add_u32 m0,s13,16384
qm 84,104,196
v_rcp_f32 v16,v16
qt 104,4,45056
qw 5
qm 88,108,124
v_mul_f32 v49,v49,v16
qt 106,4,45568
s_add_u32 s14,s17,384
qm 92,108,196
v_mul_f32 v16,v50,v50
qt 108,5,45056
qw 5
qm 80,96,128
v_fma_f32 v16,v16,v12,v10
qt 110,5,45568
ql 6,40,0
qm 84,96,200
v_mul_f32 v16,v16,v50
qt 96,4,8192
qw 5
qm 88,100,128
v_exp_f32 v16,v16
qt 98,4,8704
ql 7,40,1024
qm 92,100,200
v_fma_f32 v16,v16,v14,v14
qt 100,5,8192
qw 5
qm 80,104,132
v_rcp_f32 v16,v16
qt 102,5,8704
s_add_u32 m0,s13,98304
qm 84,104,204
v_mul_f32 v50,v50,v16
qt 104,4,12288
qw 5
qm 88,108,132
s_waitcnt vmcnt(7)
s_barrier
v_mul_f32 v16,v51,v51
qt 106,4,12800
s_add_u32 s14,s17,590208
qm 92,108,204
v_fma_f32 v16,v16,v12,v10
qt 108,5,12288
qw 5
qm 80,96,136
v_mul_f32 v16,v16,v51
qt 110,5,12800
ql 6,40,0
qm 84,96,208
v_exp_f32 v16,v16
qt 96,4,49152
qw 5
qm 88,100,136
v_fma_f32 v16,v16,v14,v14
qt 98,4,49664
ql 7,40,1024
qm 92,100,208
v_rcp_f32 v16,v16
qt 100,5,49152
qw 5
qm 80,104,140
v_mul_f32 v51,v51,v16
qt 102,5,49664
s_add_u32 m0,s13,106496
qm 84,104,212
v_mul_f32 v16,v56,v56
qt 104,4,53248
qw 5
qm 88,108,140
v_fma_f32 v16,v16,v12,v10
qt 106,4,53760
s_add_u32 s14,s17,1180032
qm 92,108,212
v_mul_f32 v16,v16,v56
qt 108,5,53248
qw 5
qm 80,96,144
v_exp_f32 v16,v16
qt 110,5,53760
ql 6,40,0
qm 84,96,216
v_fma_f32 v16,v16,v14,v14
qt 96,2,0
qw 5
qm 88,100,144
v_rcp_f32 v16,v16
qt 98,2,512
ql 7,40,1024
qm 92,100,216
v_mul_f32 v56,v56,v16
qt 100,3,0
qw 5
qm 80,104,148
v_mul_f32 v16,v57,v57
qt 102,3,512
s_add_u32 m0,s13,24576
qm 84,104,220
v_fma_f32 v16,v16,v12,v10
qt 104,2,4096
qw 5
qm 88,108,148
v_mul_f32 v16,v16,v57
qt 106,2,4608
s_add_u32 s14,s17,1769856
qm 92,108,220
v_exp_f32 v16,v16
qt 108,3,4096
qw 5
qm 80,96,152
v_fma_f32 v16,v16,v14,v14
qt 110,3,4608
ql 6,40,0
qm 84,96,224
v_rcp_f32 v16,v16
qt 96,2,8192
qw 5
qm 88,100,152
v_mul_f32 v57,v57,v16
qt 98,2,8704
ql 7,40,1024
qm 92,100,224
v_mul_f32 v16,v58,v58
qt 100,3,8192
qw 5
qm 80,104,156
v_fma_f32 v16,v16,v12,v10
qt 102,3,8704
s_add_u32 m0,s13,114688
qm 84,104,228
v_mul_f32 v16,v16,v58
qt 104,2,12288
qw 5
qm 88,108,156
s_waitcnt vmcnt(6)
s_barrier
v_exp_f32 v16,v16
qt 106,2,12800
s_add_u32 s14,s17,2359680
qm 92,108,228
v_fma_f32 v16,v16,v14,v14
qt 108,3,12288
qw 5
qm 80,96,160
v_rcp_f32 v16,v16
qt 110,3,12800
ql 6,40,0
qm 84,96,232
v_mul_f32 v58,v58,v16
qt 96,4,16384
qw 5
qm 88,100,160
v_mul_f32 v16,v59,v59
qt 98,4,16896
ql 7,40,1024
qm 92,100,232
v_fma_f32 v16,v16,v12,v10
qt 100,5,16384
qw 5
qm 80,104,164
v_mul_f32 v16,v16,v59
qt 102,5,16896
s_add_u32 m0,s13,32768
qm 84,104,236
v_exp_f32 v16,v16
qt 104,4,20480
qw 5
qm 88,108,164
v_fma_f32 v16,v16,v14,v14
qt 106,4,20992
s_add_u32 s14,s17,2949504
qm 92,108,236
v_rcp_f32 v16,v16
qt 108,5,20480
qw 5
qm 80,96,168
v_mul_f32 v59,v59,v16
qt 110,5,20992
ql 6,40,0
qm 84,96,240
v_mul_f32 v16,v52,v52
qt 96,4,24576
qw 5
qm 88,100,168
v_fma_f32 v16,v16,v13,v11
qt 98,4,25088
ql 7,40,1024
qm 92,100,240
v_mul_f32 v16,v16,v52
qt 100,5,24576
qw 5
qm 80,104,172
v_exp_f32 v16,v16
qt 102,5,25088
s_add_u32 m0,s13,40960
qm 84,104,244
v_fma_f32 v16,v16,v15,v15
qt 104,4,28672
qw 5
qm 88,108,172
v_rcp_f32 v16,v16
qt 106,4,29184
s_add_u32 s14,s17,3539328
qm 92,108,244
v_mul_f32 v52,v52,v16
qt 108,5,28672
qw 5
qm 80,96,176
v_mul_f32 v16,v53,v53
qt 110,5,29184
ql 6,40,0
qm 84,96,248
v_fma_f32 v16,v16,v13,v11
qt 96,2,16384
qw 5
qm 88,100,176
v_mul_f32 v16,v16,v53
qt 98,2,16896
ql 7,40,1024
qm 92,100,248
v_exp_f32 v16,v16
qt 100,3,16384
qw 5
qm 80,104,180
v_fma_f32 v16,v16,v15,v15
qt 102,3,16896
s_add_u32 m0,s13,81920
qm 84,104,252
v_rcp_f32 v16,v16
qt 104,2,20480
qw 5
qm 88,108,180
s_waitcnt vmcnt(6)
s_barrier
v_mul_f32 v53,v53,v16
qt 106,2,20992
s_add_u32 s14,s17,4129152
qm 92,108,252
qt 108,3,20480
qw 5
qz 64,96,112
v_mul_f32 v16,v54,v54
qt 110,3,20992
ql 6,40,0
qz 68,96,184
v_fma_f32 v16,v16,v13,v11
v_bfe_u32 v44,v0,2,2
qt 96,4,32768
qw 5
qz 72,100,112
v_mul_f32 v16,v16,v54
qt 98,4,33280
ql 7,40,1024
qz 76,100,184
v_exp_f32 v16,v16
qt 100,5,32768
qw 5
qm 64,104,116
v_fma_f32 v16,v16,v15,v15
qt 102,5,33280
s_add_u32 m0,s13,90112
qm 68,104,188
v_rcp_f32 v16,v16
s_and_b32 s33,s8,3
s_lshl_b32 s33,s33,2
s_add_u32 s33,s33,s7
s_lshl_b32 s33,s33,3
s_lshl_b32 s33,s33,2
s_add_u32 s33,s33,122880
v_lshl_add_u32 v45,v44,2,s33
qt 104,4,36864
qw 5
qm 72,108,116
v_mul_f32 v54,v54,v16
qt 106,4,37376
s_add_u32 s14,s17,4718976
qm 76,108,188
v_mul_f32 v16,v55,v55
qt 108,5,36864
qw 5
qm 64,96,120
v_fma_f32 v16,v16,v13,v11
qt 110,5,37376
ql 6,40,0
qm 68,96,192
v_mul_f32 v16,v16,v55
qt 96,4,40960
qw 5
qm 72,100,120
v_exp_f32 v16,v16
ds_read_b32 v40,v45 offset:0
qt 98,4,41472
ql 7,40,1024
qm 76,100,192
v_fma_f32 v16,v16,v15,v15
qt 100,5,40960
qw 6
qm 64,104,124
v_rcp_f32 v16,v16
qt 102,5,41472
s_add_u32 m0,s13,49152
qm 68,104,196
v_mul_f32 v55,v55,v16
qt 104,4,45056
qw 6
qm 72,108,124
v_mul_f32 v16,v60,v60
ds_read_b32 v41,v45 offset:512
qt 106,4,45568
s_add_u32 s14,s17,384
qm 76,108,196
v_fma_f32 v16,v16,v13,v11
qt 108,5,45056
qw 6
qm 64,96,128
v_mul_f32 v16,v16,v60
qt 110,5,45568
ql 6,44,0
qm 68,96,200
v_exp_f32 v16,v16
qt 96,2,24576
qw 6
qm 72,100,128
v_fma_f32 v16,v16,v15,v15
qt 98,2,25088
ql 7,44,1024
qm 76,100,200
v_rcp_f32 v16,v16
qt 100,3,24576
qw 5
qm 64,104,132
v_mul_f32 v60,v60,v16
qt 102,3,25088
s_add_u32 m0,s13,98304
qm 68,104,204
v_mul_f32 v16,v61,v61
qt 104,2,28672
qw 5
qm 72,108,132
s_waitcnt vmcnt(6)
s_barrier
v_fma_f32 v16,v16,v13,v11
qt 106,2,29184
s_add_u32 s14,s17,590208
qm 76,108,204
v_mul_f32 v16,v16,v61
qt 108,3,28672
qw 5
qm 64,96,136
v_exp_f32 v16,v16
qt 110,3,29184
ql 6,44,0
qm 68,96,208
v_fma_f32 v16,v16,v15,v15
qt 96,4,49152
qw 5
qm 72,100,136
v_rcp_f32 v16,v16
qt 98,4,49664
ql 7,44,1024
qm 76,100,208
v_mul_f32 v61,v61,v16
qt 100,5,49152
qw 5
qm 64,104,140
v_mul_f32 v16,v62,v62
qt 102,5,49664
s_add_u32 m0,s13,106496
qm 68,104,212
v_fma_f32 v16,v16,v13,v11
qt 104,4,53248
qw 5
qm 72,108,140
v_mul_f32 v16,v16,v62
qt 106,4,53760
s_add_u32 s14,s17,1180032
qm 76,108,212
v_exp_f32 v16,v16
qt 108,5,53248
qw 5
qm 64,96,144
v_fma_f32 v16,v16,v15,v15
qt 110,5,53760
ql 6,44,0
qm 68,96,216
v_rcp_f32 v16,v16
qt 96,2,32768
qw 5
qm 72,100,144
v_mul_f32 v62,v62,v16
qt 98,2,33280
ql 7,44,1024
qm 76,100,216
v_mul_f32 v16,v63,v63
qt 100,3,32768
qw 5
qm 64,104,148
v_fma_f32 v16,v16,v13,v11
qt 102,3,33280
s_add_u32 m0,s13,57344
qm 68,104,220
v_mul_f32 v16,v16,v63
qt 104,2,36864
qw 5
qm 72,108,148
v_exp_f32 v16,v16
qt 106,2,37376
s_add_u32 s14,s17,1769856
qm 76,108,220
v_fma_f32 v16,v16,v15,v15
qt 108,3,36864
qw 5
qm 64,96,152
v_rcp_f32 v16,v16
qt 110,3,37376
ql 6,44,0
qm 68,96,224
v_mul_f32 v63,v63,v16
qt 96,2,40960
qw 5
qm 72,100,152
v_mul_f32 v48,v48,v80
qt 98,2,41472
ql 7,44,1024
qm 76,100,224
v_mul_f32 v49,v49,v81
qt 100,3,40960
qw 5
qm 64,104,156
v_cvt_pk_bf16_f32 v48,v48,v49
qt 102,3,41472
s_add_u32 m0,s13,65536
qm 68,104,228
v_mul_f32 v50,v50,v82
qt 104,2,45056
qw 5
qm 72,108,156
s_waitcnt vmcnt(6)
s_barrier
v_mul_f32 v51,v51,v83
qt 106,2,45568
s_add_u32 s14,s17,2359680
qm 76,108,228
v_cvt_pk_bf16_f32 v49,v50,v51
qt 108,3,45056
qw 5
qm 64,96,160
v_mul_f32 v56,v56,v88
qt 110,3,45568
ql 6,44,0
qm 68,96,232
v_mul_f32 v57,v57,v89
qt 96,4,16384
qw 5
qm 72,100,160
v_cvt_pk_bf16_f32 v50,v56,v57
qt 98,4,16896
ql 7,44,1024
qm 76,100,232
v_mul_f32 v58,v58,v90
qt 100,5,16384
qw 5
qm 64,104,164
v_mul_f32 v59,v59,v91
qt 102,5,16896
s_add_u32 m0,s13,73728
qm 68,104,236
v_cvt_pk_bf16_f32 v51,v58,v59
qt 104,4,20480
qw 5
qm 72,108,164
ds_write_b128 v18,v[48:51] offset:32768
v_mul_f32 v52,v52,v84
qt 106,4,20992
s_add_u32 s14,s17,2949504
qm 76,108,236
s_add_u32 s15,s76,32768
buffer_store_dwordx4 v[48:51],v18,s[88:91],s15 offen offset:0 sc1
v_mul_f32 v53,v53,v85
qt 108,5,20480
qw 6
qm 64,96,168
v_cvt_pk_bf16_f32 v52,v52,v53
qt 110,5,20992
ql 6,44,0
qm 68,96,240
v_mul_f32 v54,v54,v86
qt 96,4,24576
qw 6
qm 72,100,168
v_mul_f32 v55,v55,v87
qt 98,4,25088
ql 7,44,1024
qm 76,100,240
v_cvt_pk_bf16_f32 v53,v54,v55
qt 100,5,24576
qw 5
qm 64,104,172
v_mul_f32 v60,v60,v92
qt 102,5,25088
s_mov_b32 m0,s13
qm 68,104,244
v_mul_f32 v61,v61,v93
qt 104,4,28672
qw 5
qm 72,108,172
v_cvt_pk_bf16_f32 v54,v60,v61
qt 106,4,29184
s_add_u32 s14,s17,3539328
qm 76,108,244
v_mul_f32 v62,v62,v94
qt 108,5,28672
qw 5
qm 64,96,176
v_mul_f32 v63,v63,v95
qt 110,5,29184
ql 6,44,0
qm 68,96,248
v_cvt_pk_bf16_f32 v55,v62,v63
qt 96,2,49152
qw 5
qm 72,100,176
ds_write_b128 v18,v[52:55] offset:33792
qt 98,2,49664
ql 7,44,1024
qm 76,100,248
s_add_u32 s15,s76,32768
buffer_store_dwordx4 v[52:55],v18,s[88:91],s15 offen offset:1024 sc1
qt 100,3,49152
qw 6
qm 64,104,180
qt 102,3,49664
s_add_u32 m0,s13,81920
qm 68,104,252
qt 104,2,53248
qw 6
qm 72,108,180
s_waitcnt vmcnt(8) lgkmcnt(4)
s_barrier
qt 106,2,53760
s_add_u32 s14,s17,4129152
qm 76,108,252
qt 108,3,53248
qw 5
qz 80,96,112
qt 110,3,53760
ql 6,44,0
qz 84,96,184
v_mul_f32 v16,v64,v64
qt 96,4,32768
qw 5
qz 88,100,112
v_fma_f32 v16,v16,v12,v10
qt 98,4,33280
ql 7,44,1024
qz 92,100,184
v_mul_f32 v16,v16,v64
qt 100,5,32768
qw 5
qm 80,104,116
v_exp_f32 v16,v16
qt 102,5,33280
s_add_u32 m0,s13,90112
qm 84,104,188
v_fma_f32 v16,v16,v14,v14
qt 104,4,36864
qw 5
qm 88,108,116
v_rcp_f32 v16,v16
qt 106,4,37376
s_add_u32 s14,s17,4718976
qm 92,108,188
v_mul_f32 v64,v64,v16
qt 108,5,36864
qw 5
qm 80,96,120
v_mul_f32 v16,v65,v65
qt 110,5,37376
ql 6,44,0
qm 84,96,192
v_fma_f32 v16,v16,v12,v10
qt 96,4,40960
qw 5
qm 88,100,120
v_mul_f32 v16,v16,v65
qt 98,4,41472
ql 7,44,1024
qm 92,100,192
v_exp_f32 v16,v16
qt 100,5,40960
qw 5
qm 80,104,124
v_fma_f32 v16,v16,v14,v14
qt 102,5,41472
s_add_u32 m0,s13,8192
qm 84,104,196
v_rcp_f32 v16,v16
qt 104,4,45056
qw 5
qm 88,108,124
v_mul_f32 v65,v65,v16
qt 106,4,45568
s_add_u32 s14,s17,512
qm 92,108,196
v_mul_f32 v16,v66,v66
qt 108,5,45056
qw 5
qm 80,96,128
v_fma_f32 v16,v16,v12,v10
qt 110,5,45568
ql 6,40,0
qm 84,96,200
v_mul_f32 v16,v16,v66
qt 96,2,57344
qw 5
qm 88,100,128
v_exp_f32 v16,v16
qt 98,2,57856
ql 7,40,1024
qm 92,100,200
v_fma_f32 v16,v16,v14,v14
qt 100,3,57344
qw 5
qm 80,104,132
v_rcp_f32 v16,v16
qt 102,3,57856
s_add_u32 m0,s13,98304
qm 84,104,204
v_mul_f32 v66,v66,v16
qt 104,2,61440
qw 5
qm 88,108,132
s_waitcnt vmcnt(7)
s_barrier
v_mul_f32 v16,v67,v67
qt 106,2,61952
s_add_u32 s14,s17,590336
qm 92,108,204
v_fma_f32 v16,v16,v12,v10
qt 108,3,61440
qw 5
qm 80,96,136
v_mul_f32 v16,v16,v67
qt 110,3,61952
ql 6,40,0
qm 84,96,208
v_exp_f32 v16,v16
qt 96,4,0
qw 5
qm 88,100,136
v_fma_f32 v16,v16,v14,v14
qt 98,4,512
ql 7,40,1024
qm 92,100,208
v_rcp_f32 v16,v16
qt 100,5,0
qw 5
qm 80,104,140
v_mul_f32 v67,v67,v16
qt 102,5,512
s_add_u32 m0,s13,106496
qm 84,104,212
v_mul_f32 v16,v72,v72
qt 104,4,4096
qw 5
qm 88,108,140
v_fma_f32 v16,v16,v12,v10
qt 106,4,4608
s_add_u32 s14,s17,1180160
qm 92,108,212
v_mul_f32 v16,v16,v72
qt 108,5,4096
qw 5
qm 80,96,144
v_exp_f32 v16,v16
qt 110,5,4608
ql 6,40,0
qm 84,96,216
v_fma_f32 v16,v16,v14,v14
qt 96,4,8192
qw 5
qm 88,100,144
v_rcp_f32 v16,v16
qt 98,4,8704
ql 7,40,1024
qm 92,100,216
v_mul_f32 v72,v72,v16
qt 100,5,8192
qw 5
qm 80,104,148
v_mul_f32 v16,v73,v73
qt 102,5,8704
s_add_u32 m0,s13,16384
qm 84,104,220
v_fma_f32 v16,v16,v12,v10
qt 104,4,12288
qw 5
qm 88,108,148
v_mul_f32 v16,v16,v73
qt 106,4,12800
s_add_u32 s14,s17,1769984
qm 92,108,220
v_exp_f32 v16,v16
qt 108,5,12288
qw 5
qm 80,96,152
v_fma_f32 v16,v16,v14,v14
qt 110,5,12800
ql 6,40,0
qm 84,96,224
v_rcp_f32 v16,v16
qt 96,2,0
qw 5
qm 88,100,152
v_mul_f32 v73,v73,v16
qt 98,2,512
ql 7,40,1024
qm 92,100,224
v_mul_f32 v16,v74,v74
qt 100,3,0
qw 5
qm 80,104,156
v_fma_f32 v16,v16,v12,v10
qt 102,3,512
s_add_u32 m0,s13,24576
qm 84,104,228
v_mul_f32 v16,v16,v74
qt 104,2,4096
qw 5
qm 88,108,156
s_waitcnt vmcnt(6)
s_barrier
v_exp_f32 v16,v16
qt 106,2,4608
s_add_u32 s14,s17,2359808
qm 92,108,228
v_fma_f32 v16,v16,v14,v14
qt 108,3,4096
qw 5
qm 80,96,160
v_rcp_f32 v16,v16
qt 110,3,4608
ql 6,40,0
qm 84,96,232
v_mul_f32 v74,v74,v16
qt 96,4,16384
qw 5
qm 88,100,160
v_mul_f32 v16,v75,v75
qt 98,4,16896
ql 7,40,1024
qm 92,100,232
v_fma_f32 v16,v16,v12,v10
qt 100,5,16384
qw 5
qm 80,104,164
v_mul_f32 v16,v16,v75
qt 102,5,16896
s_add_u32 m0,s13,32768
qm 84,104,236
v_exp_f32 v16,v16
qt 104,4,20480
qw 5
qm 88,108,164
v_fma_f32 v16,v16,v14,v14
qt 106,4,20992
s_add_u32 s14,s17,2949632
qm 92,108,236
v_rcp_f32 v16,v16
qt 108,5,20480
qw 5
qm 80,96,168
v_mul_f32 v75,v75,v16
qt 110,5,20992
ql 6,40,0
qm 84,96,240
v_mul_f32 v16,v68,v68
qt 96,4,24576
qw 5
qm 88,100,168
v_fma_f32 v16,v16,v13,v11
qt 98,4,25088
ql 7,40,1024
qm 92,100,240
v_mul_f32 v16,v16,v68
qt 100,5,24576
qw 5
qm 80,104,172
v_exp_f32 v16,v16
qt 102,5,25088
s_add_u32 m0,s13,40960
qm 84,104,244
v_fma_f32 v16,v16,v15,v15
qt 104,4,28672
qw 5
qm 88,108,172
v_rcp_f32 v16,v16
qt 106,4,29184
s_add_u32 s14,s17,3539456
qm 92,108,244
v_mul_f32 v68,v68,v16
qt 108,5,28672
qw 5
qm 80,96,176
v_mul_f32 v16,v69,v69
qt 110,5,29184
ql 6,40,0
qm 84,96,248
v_fma_f32 v16,v16,v13,v11
qt 96,2,8192
qw 5
qm 88,100,176
v_mul_f32 v16,v16,v69
qt 98,2,8704
ql 7,40,1024
qm 92,100,248
v_exp_f32 v16,v16
qt 100,3,8192
qw 5
qm 80,104,180
v_fma_f32 v16,v16,v15,v15
qt 102,3,8704
s_add_u32 m0,s13,81920
qm 84,104,252
v_rcp_f32 v16,v16
qt 104,2,12288
qw 5
qm 88,108,180
s_waitcnt vmcnt(6)
s_barrier
v_mul_f32 v69,v69,v16
qt 106,2,12800
s_add_u32 s14,s17,4129280
qm 92,108,252
qt 108,3,12288
qw 5
qz 48,96,112
v_mul_f32 v16,v70,v70
qt 110,3,12800
ql 6,40,0
qz 52,96,184
v_fma_f32 v16,v16,v13,v11
qt 96,4,32768
qw 5
qz 56,100,112
v_mul_f32 v16,v16,v70
v_mov_b32 v38,s16
v_and_b32 v37,48,v0
qt 98,4,33280
ql 7,40,1024
qz 60,100,184
v_exp_f32 v16,v16
v_lshlrev_b32 v37,24,v37
qt 100,5,32768
qw 5
qm 48,104,116
v_fma_f32 v16,v16,v15,v15
v_lshl_or_b32 v37,v0,7,v37
qt 102,5,33280
s_add_u32 m0,s13,90112
qm 52,104,188
v_rcp_f32 v16,v16
s_mul_i32 s72,s10,2048
qt 104,4,36864
qw 5
qm 56,108,116
v_mul_f32 v70,v70,v16
s_add_u32 s72,s72,8192
qt 106,4,37376
s_add_u32 s14,s17,4719104
qm 60,108,188
v_mul_f32 v16,v71,v71
s_and_b32 s30,s8,3
qt 108,5,36864
qw 5
qm 48,96,120
v_fma_f32 v16,v16,v13,v11
s_lshl_b32 s30,s30,4
qt 110,5,37376
ql 6,40,0
qm 52,96,192
v_mul_f32 v16,v16,v71
s_and_b32 s31,s8,4
qt 96,4,40960
qw 5
qm 56,100,120
v_exp_f32 v16,v16
s_add_u32 s30,s30,s31
qt 98,4,41472
ql 7,40,1024
qm 60,100,192
v_fma_f32 v16,v16,v15,v15
s_add_u32 s68,s30,s72
qt 100,5,40960
qw 5
qm 48,104,124
v_rcp_f32 v16,v16
s_lshl_b32 s31,s8,8
qt 102,5,41472
s_add_u32 m0,s13,49152
qm 52,104,196
v_mul_f32 v71,v71,v16
s_add_u32 s78,s68,s31
qt 104,4,45056
qw 5
qm 56,108,124
v_mul_f32 v16,v76,v76
s_add_u32 s78,s78,8
qt 106,4,45568
s_add_u32 s14,s17,512
qm 60,108,196
v_fma_f32 v16,v16,v13,v11
s_add_u32 s68,s68,0
s_cmp_eq_u32 s6,4
s_cselect_b32 s68,s68,0x7ff00000
qt 108,5,45056
qw 5
qm 48,96,128
v_mul_f32 v16,v16,v76
s_add_u32 s72,s72,s31
qt 110,5,45568
ql 6,44,0
qm 52,96,200
v_exp_f32 v16,v16
s_add_u32 s72,s72,0
qt 96,2,16384
qw 5
qm 56,100,128
v_fma_f32 v16,v16,v15,v15
s_add_u32 s74,s16,8
qt 98,2,16896
ql 7,44,1024
qm 60,100,200
v_rcp_f32 v16,v16
v_bfe_u32 v20,v0,3,2
qt 100,3,16384
qw 5
qm 48,104,132
v_mul_f32 v76,v76,v16
v_lshrrev_b32 v21,1,v0
qt 102,3,16896
s_add_u32 m0,s13,57344
qm 52,104,204
v_mul_f32 v16,v77,v77
v_and_or_b32 v20,v21,16,v20
qt 104,2,20480
qw 5
qm 56,108,132
s_waitcnt vmcnt(6)
s_barrier
v_fma_f32 v16,v16,v13,v11
v_mul_u32_u24 v20,1152,v20
qt 106,2,20992
s_add_u32 s14,s17,590336
qm 60,108,204
v_mul_f32 v16,v16,v77
v_lshrrev_b32 v21,3,v0
qt 108,3,20480
qw 5
qm 48,96,136
v_exp_f32 v16,v16
v_and_b32 v21,2,v21
qt 110,3,20992
ql 6,44,0
qm 52,96,208
v_fma_f32 v16,v16,v15,v15
v_xor_b32 v21,v21,v0
qt 96,2,24576
qw 5
qm 56,100,136
v_rcp_f32 v16,v16
v_and_b32 v21,7,v21
qt 98,2,25088
ql 7,44,1024
qm 60,100,208
v_mul_f32 v77,v77,v16
v_lshl_add_u32 v20,v21,4,v20
qt 100,3,24576
s_add_u32 m0,s13,65536
s_add_u32 s14,s17,1180160
qw 5
qm 48,104,140
v_mul_f32 v16,v78,v78
v_xor_b32 v21,64,v20
qt 102,3,25088
ql 6,44,0
qm 52,104,212
v_fma_f32 v16,v16,v13,v11
v_add_u32 v21,3584,v21
v_and_b32 v42,15,v0
qt 104,2,28672
qw 5
qm 56,108,140
v_mul_f32 v16,v16,v78
v_lshrrev_b32 v43,5,v0
qt 106,2,29184
ql 7,44,1024
qm 60,108,212
v_exp_f32 v16,v16
v_bfe_u32 v25,v0,4,1
qt 108,3,28672
s_add_u32 m0,s13,73728
s_add_u32 s14,s17,1769984
qw 5
qm 48,96,144
v_fma_f32 v16,v16,v15,v15
v_lshlrev_b32 v25,3,v25
qt 110,3,29184
ql 6,44,0
qm 52,96,216
v_rcp_f32 v16,v16
v_add_u32 v25,0,v25
qt 96,2,32768
qw 5
qm 56,100,144
v_mul_f32 v78,v78,v16
v_lshl_add_u32 v24,v42,7,v25
qt 98,2,33280
ql 7,44,1024
qm 60,100,216
v_mul_f32 v16,v79,v79
v_lshl_add_u32 v22,v42,5,v25
qt 100,3,32768
qw 5
qm 48,104,148
v_fma_f32 v16,v16,v13,v11
v_lshrrev_b32 v42,1,v42
qt 102,3,33280
qm 52,104,220
v_mul_f32 v16,v16,v79
v_xor_b32 v25,v42,v43
qt 104,2,36864
qw 5
qm 56,108,148
v_exp_f32 v16,v16
v_lshl_add_u32 v24,v25,4,v24
qt 106,2,37376
qm 60,108,220
v_fma_f32 v16,v16,v15,v15
v_lshrrev_b32 v42,2,v42
qt 108,3,36864
qw 5
qm 48,96,152
v_rcp_f32 v16,v16
v_xor_b32 v42,v42,v43
qt 110,3,37376
qm 52,96,224
v_mul_f32 v79,v79,v16
v_lshl_add_u32 v22,v42,4,v22
qt 96,2,40960
qw 5
qm 56,100,152
v_mul_f32 v64,v64,v80
v_add_u32 v23,0x10000,v22
v_xor_b32 v25,32,v24
qt 98,2,41472
qm 60,100,224
v_mul_f32 v65,v65,v81
v_xor_b32 v26,64,v24
qt 100,3,40960
qw 5
qm 48,104,156
v_cvt_pk_bf16_f32 v64,v64,v65
v_xor_b32 v27,0x60,v24
qt 102,3,41472
s_mov_b32 m0,s13
qm 52,104,228
v_mul_f32 v66,v66,v82
v_lshlrev_b32 v36,3,v0
qt 104,2,45056
qw 5
qm 56,108,156
s_waitcnt vmcnt(0)
s_barrier
v_mul_f32 v67,v67,v83
v_add_u32 v36,s13,v36
qt 106,2,45568
s_add_u32 s14,s17,2359808
qm 60,108,228
v_cvt_pk_bf16_f32 v65,v66,v67
v_add_u32 v36,147456,v36
qt 108,3,45056
qw 5
qm 48,96,160
v_mul_f32 v72,v72,v88
v_lshrrev_b32 v8,3,v0
qt 110,3,45568
ql 6,44,0
qm 52,96,232
v_mul_f32 v73,v73,v89
v_mul_u32_u24 v8,1152,v8
qt 96,4,16384
qw 5
qm 56,100,160
v_cvt_pk_bf16_f32 v66,v72,v73
v_lshrrev_b32 v9,4,v0
qt 98,4,16896
ql 7,44,1024
qm 60,100,232
v_mul_f32 v74,v74,v90
v_xor_b32 v9,v9,v0
qt 100,5,16384
qw 5
qm 48,104,164
v_mul_f32 v75,v75,v91
v_and_b32 v9,7,v9
qt 102,5,16896
s_add_u32 m0,s13,8192
qm 52,104,236
v_cvt_pk_bf16_f32 v67,v74,v75
v_lshl_add_u32 v8,v9,4,v8
qt 104,4,20480
qw 5
qm 56,108,164
ds_write_b128 v18,v[64:67] offset:16384
v_mul_f32 v68,v68,v84
v_xor_b32 v9,64,v8
qt 106,4,20992
s_add_u32 s14,s17,2949632
qm 60,108,236
s_add_u32 s15,s76,49152
buffer_store_dwordx4 v[64:67],v18,s[88:91],s15 offen offset:0 sc1
v_mul_f32 v69,v69,v85
v_add_u32 v9,8192,v9
qt 108,5,20480
qw 6
qm 48,96,168
v_cvt_pk_bf16_f32 v68,v68,v69
s_lshr_b32 s30,s7,1
s_lshl_b32 s30,s30,5
qt 110,5,20992
ql 6,44,0
qm 52,96,240
v_mul_f32 v70,v70,v86
s_and_b32 s31,s7,1
qt 96,4,24576
qw 6
qm 56,100,168
v_mul_f32 v71,v71,v87
s_lshl_b32 s32,s31,3
qt 98,4,25088
ql 7,44,1024
qm 60,100,240
v_cvt_pk_bf16_f32 v69,v70,v71
s_add_u32 s30,s30,s32
qt 100,5,24576
qw 5
qm 48,104,172
v_mul_f32 v76,v76,v92
s_mul_i32 s32,s8,576
qt 102,5,25088
s_add_u32 m0,s13,16384
qm 52,104,244
v_mul_f32 v77,v77,v93
s_add_u32 s30,s30,s32
qt 104,4,28672
qw 5
qm 56,108,172
v_cvt_pk_bf16_f32 v70,v76,v77
s_mul_i32 s19,s30,1152
qt 106,4,29184
s_add_u32 s14,s17,3539456
qm 60,108,244
v_mul_f32 v78,v78,v94
s_mul_i32 s31,s31,36864
qt 108,5,28672
qw 5
qm 48,96,176
v_mul_f32 v79,v79,v95
s_and_b32 s30,s8,6
qt 110,5,29184
ql 6,44,0
qm 52,96,248
v_cvt_pk_bf16_f32 v71,v78,v79
s_lshl_b32 s30,s30,4
qt 96,2,49152
qw 5
qm 56,100,176
ds_write_b128 v18,v[68:71] offset:17408
s_add_u32 s18,s31,s30
qt 98,2,49664
ql 7,44,1024
qm 60,100,248
s_add_u32 s15,s76,49152
buffer_store_dwordx4 v[68:71],v18,s[88:91],s15 offen offset:1024 sc1
s_add_u32 s18,s18,1024
qt 100,3,49152
qw 6
qm 48,104,180
s_lshl_b32 s30,s8,7
qt 102,3,49664
s_add_u32 m0,s13,81920
qm 52,104,252
s_add_u32 s28,s31,s30
qt 104,2,53248
qw 6
qm 56,108,180
qw 4
s_barrier
qt 106,2,53760
s_add_u32 s14,s17,4129280
qm 60,108,252
qt 108,3,53248
qw 5
qz 80,96,112
qt 110,3,53760
ql 6,44,0
qz 84,96,184
v_mul_f32 v16,v48,v48
qt 96,2,57344
qw 5
qz 88,100,112
v_fma_f32 v16,v16,v12,v10
qt 98,2,57856
ql 7,44,1024
qz 92,100,184
v_mul_f32 v16,v16,v48
qt 100,3,57344
qw 5
qm 80,104,116
v_exp_f32 v16,v16
qt 102,3,57856
s_add_u32 m0,s13,90112
qm 84,104,188
v_fma_f32 v16,v16,v14,v14
qt 104,2,61440
qw 5
qm 88,108,116
v_rcp_f32 v16,v16
qt 106,2,61952
s_add_u32 s14,s17,4719104
qm 92,108,188
v_mul_f32 v48,v48,v16
qt 108,3,61440
qw 5
qm 80,96,120
v_mul_f32 v16,v49,v49
qt 110,3,61952
ql 6,44,0
qm 84,96,192
v_fma_f32 v16,v16,v12,v10
qr 112,19,0
qt 96,4,0
qw 6
qm 88,100,120
v_mul_f32 v16,v16,v49
qt 98,4,512
ql 7,44,1024
qm 92,100,192
v_exp_f32 v16,v16
qr 184,19,1024
qt 100,5,0
qw 7
qm 80,104,124
v_fma_f32 v16,v16,v14,v14
qt 102,5,512
s_add_u32 m0,s13,24576
qm 84,104,196
v_rcp_f32 v16,v16
qr 116,19,8192
qt 104,4,4096
qw 8
qm 88,108,124
v_mul_f32 v49,v49,v16
qt 106,4,4608
s_mov_b32 s14,s19
qm 92,108,196
v_mul_f32 v16,v50,v50
qr 188,19,9216
qt 108,5,4096
qw 8
qm 80,96,128
v_fma_f32 v16,v16,v12,v10
qt 110,5,4608
ql 20,48,0
qm 84,96,200
v_mul_f32 v16,v16,v50
qr 120,18,49152
qt 96,4,8192
qw 8
qm 88,100,128
v_exp_f32 v16,v16
qt 98,4,8704
ql 21,48,1024
qm 92,100,200
v_fma_f32 v16,v16,v14,v14
qr 192,18,50176
qt 100,5,8192
qw 8
qm 80,104,132
v_rcp_f32 v16,v16
qt 102,5,8704
s_add_u32 m0,s13,32768
qm 84,104,204
v_mul_f32 v50,v50,v16
qr 124,18,57344
qt 104,4,12288
qw 8
qm 88,108,132
s_waitcnt vmcnt(7) lgkmcnt(1)
s_barrier
v_mul_f32 v16,v51,v51
qt 106,4,12800
s_add_u32 s14,s19,128
qm 92,108,204
v_fma_f32 v16,v16,v12,v10
qr 196,18,58368
qt 108,5,12288
qm 80,96,136
v_mul_f32 v16,v16,v51
v_lshrrev_b32 v6,1,v0
v_add_u32 v28,0x10000,v24
qt 110,5,12800
ql 20,48,0
qm 84,96,208
v_exp_f32 v16,v16
v_add_u32 v29,0x10000,v25
qt 96,2,0
qm 88,100,136
v_fma_f32 v16,v16,v14,v14
v_mul_u32_u24 v6,1152,v6
qt 98,2,512
ql 21,48,1024
qm 92,100,208
v_rcp_f32 v16,v16
v_add_u32 v30,0x10000,v26
qt 100,3,0
qw 6
qm 80,104,140
v_mul_f32 v51,v51,v16
v_lshrrev_b32 v7,4,v0
v_add_u32 v31,0x10000,v27
qt 102,3,512
s_add_u32 m0,s13,40960
qm 84,104,212
v_mul_f32 v16,v56,v56
qt 104,2,4096
qw 5
qm 88,108,140
v_fma_f32 v16,v16,v12,v10
v_xor_b32 v7,v7,v0
qt 106,2,4608
s_add_u32 s14,s19,512
qm 92,108,212
v_mul_f32 v16,v16,v56
qt 108,3,4096
qw 5
qm 80,96,144
v_exp_f32 v16,v16
qt 110,3,4608
ql 20,48,0
qm 84,96,216
v_fma_f32 v16,v16,v14,v14
v_and_b32 v7,1,v7
qt 96,2,8192
qw 5
qm 88,100,144
v_rcp_f32 v16,v16
qt 98,2,8704
ql 21,48,1024
qm 92,100,216
v_mul_f32 v56,v56,v16
v_lshl_add_u32 v6,v7,4,v6
qt 100,3,8192
qw 5
qm 80,104,148
v_mul_f32 v16,v57,v57
qt 102,3,8704
s_add_u32 m0,s13,49152
qm 84,104,220
v_fma_f32 v16,v16,v12,v10
v_add_u32 v7,72704,v6
qt 104,2,12288
qw 5
qm 88,108,148
v_mul_f32 v16,v16,v57
qt 106,2,12800
s_add_u32 s14,s19,73728
qm 92,108,220
v_exp_f32 v16,v16
qt 108,3,12288
qw 5
qm 80,96,152
v_fma_f32 v16,v16,v14,v14
qt 110,3,12800
ql 20,48,0
qm 84,96,224
v_rcp_f32 v16,v16
qt 96,2,16384
qw 5
qm 88,100,152
v_mul_f32 v57,v57,v16
s_mul_i32 s30,s10,1310720
qt 98,2,16896
ql 21,48,1024
qm 92,100,224
v_mul_f32 v16,v58,v58
s_lshl_b32 s31,s8,14
qt 100,3,16384
qw 5
qm 80,104,156
v_fma_f32 v16,v16,v12,v10
qt 102,3,16896
s_add_u32 m0,s13,57344
qm 84,104,228
v_mul_f32 v16,v16,v58
s_add_u32 s73,s30,s31
qt 104,2,20480
qw 5
qm 88,108,156
s_waitcnt vmcnt(6)
s_barrier
v_exp_f32 v16,v16
qt 106,2,20992
s_add_u32 s14,s19,73856
qm 92,108,228
v_fma_f32 v16,v16,v14,v14
s_add_u32 s73,s73,49152
qt 108,3,20480
qw 5
qm 80,96,160
v_rcp_f32 v16,v16
qt 110,3,20992
ql 20,48,0
qm 84,96,232
v_mul_f32 v58,v58,v16
s_lshl_b32 s31,s8,11
qt 96,4,16384
qw 5
qm 88,100,160
v_mul_f32 v16,v59,v59
s_add_u32 s69,s30,s31
qt 98,4,16896
ql 21,48,1024
qm 92,100,232
v_fma_f32 v16,v16,v12,v10
qt 100,5,16384
qw 5
qm 80,104,164
v_mul_f32 v16,v16,v59
s_lshl_b32 s31,s7,8
qt 102,5,16896
s_add_u32 m0,s13,65536
qm 84,104,236
v_exp_f32 v16,v16
qt 104,4,20480
qw 5
qm 88,108,164
v_fma_f32 v16,v16,v14,v14
s_add_u32 s69,s69,s31
qt 106,4,20992
s_add_u32 s14,s19,74240
qm 92,108,236
v_rcp_f32 v16,v16
qt 108,5,20480
qw 5
qm 80,96,168
v_mul_f32 v59,v59,v16
s_add_u32 s69,s69,131072
qt 110,5,20992
ql 20,48,0
qm 84,96,240
v_mul_f32 v16,v52,v52
s_and_b32 s75,s8,4
qt 96,4,24576
qw 5
qm 88,100,168
v_fma_f32 v16,v16,v13,v11
qt 98,4,25088
ql 21,48,1024
qm 92,100,240
v_mul_f32 v16,v16,v52
s_lshl_b32 s75,s75,4
qt 100,5,24576
qw 5
qm 80,104,172
v_exp_f32 v16,v16
qt 102,5,25088
s_add_u32 m0,s13,73728
qm 84,104,244
v_fma_f32 v16,v16,v15,v15
s_and_b32 s33,s8,3
s_lshl_b32 s33,s33,2
s_add_u32 s33,s33,s7
s_lshl_b32 s33,s33,3
s_lshl_b32 s30,s10,7
s_add_u32 s30,s30,s33
qt 104,4,28672
qw 5
qm 88,108,172
v_rcp_f32 v16,v16
qt 106,4,29184
s_add_u32 s14,s19,147456
qm 92,108,244
v_mul_f32 v52,v52,v16
s_add_u32 s30,s30,0
qt 108,5,28672
qw 5
qm 80,96,176
v_mul_f32 v16,v53,v53
s_mul_i32 s30,s30,1152
qt 110,5,29184
ql 20,48,0
qm 84,96,248
v_fma_f32 v16,v16,v13,v11
qt 96,2,24576
qw 5
qm 88,100,176
v_mul_f32 v16,v16,v53
s_add_u32 s70,s30,s75
qt 98,2,25088
ql 21,48,1024
qm 92,100,248
v_exp_f32 v16,v16
qt 100,3,24576
qw 5
qm 80,104,180
v_fma_f32 v16,v16,v15,v15
s_lshl_b32 s71,s70,1
qt 102,3,25088
s_add_u32 m0,s13,81920
qm 84,104,252
v_rcp_f32 v16,v16
qt 104,2,28672
qw 5
qm 88,108,180
s_waitcnt vmcnt(6)
s_barrier
v_mul_f32 v53,v53,v16
qt 106,2,29184
s_add_u32 s14,s19,147584
qm 92,108,252
qt 108,3,28672
qw 5
qz 144,96,112
v_mul_f32 v16,v54,v54
v_mul_f32 v17,v55,v55
v_fma_f32 v16,v16,v13,v11
qt 110,3,29184
ql 20,48,0
qz 216,96,184
v_fma_f32 v17,v17,v13,v11
v_mul_f32 v16,v16,v54
qt 96,2,32768
qw 5
qz 148,100,112
v_mul_f32 v17,v17,v55
v_exp_f32 v16,v16
qt 98,2,33280
ql 21,48,1024
qz 220,100,184
v_exp_f32 v17,v17
v_fma_f32 v16,v16,v15,v15
v_fma_f32 v17,v17,v15,v15
qt 100,3,32768
qw 5
qm 144,104,116
v_rcp_f32 v16,v16
v_rcp_f32 v17,v17
qt 102,3,33280
s_add_u32 m0,s13,90112
qm 216,104,188
v_mul_f32 v54,v54,v16
v_mul_f32 v55,v55,v17
qt 104,2,36864
qw 5
qm 148,108,116
v_mul_f32 v16,v60,v60
v_mul_f32 v17,v61,v61
v_fma_f32 v16,v16,v13,v11
qt 106,2,37376
s_add_u32 s14,s19,147968
qm 220,108,188
v_fma_f32 v17,v17,v13,v11
v_mul_f32 v16,v16,v60
qt 108,3,36864
qw 5
qz 152,96,112
v_mul_f32 v17,v17,v61
v_exp_f32 v16,v16
qt 110,3,37376
ql 20,48,0
qz 224,96,184
v_exp_f32 v17,v17
v_fma_f32 v16,v16,v15,v15
v_fma_f32 v17,v17,v15,v15
qt 96,2,40960
qw 5
qz 156,100,112
v_rcp_f32 v16,v16
v_rcp_f32 v17,v17
qt 98,2,41472
ql 21,48,1024
qz 228,100,184
v_mul_f32 v60,v60,v16
v_mul_f32 v61,v61,v17
qt 100,3,40960
qw 5
qm 152,104,116
v_mul_f32 v16,v62,v62
v_mul_f32 v17,v63,v63
v_fma_f32 v16,v16,v13,v11
qt 102,3,41472
s_mov_b32 m0,s13
qm 224,104,188
v_fma_f32 v17,v17,v13,v11
v_mul_f32 v16,v16,v62
qt 104,2,45056
qw 5
qm 156,108,116
v_mul_f32 v17,v17,v63
v_exp_f32 v16,v16
qt 106,2,45568
s_add_u32 s14,s19,221184
qm 228,108,188
v_exp_f32 v17,v17
v_fma_f32 v16,v16,v15,v15
v_fma_f32 v17,v17,v15,v15
qt 108,3,45056
qw 5
qz 176,96,112
v_rcp_f32 v16,v16
v_rcp_f32 v17,v17
qt 110,3,45568
ql 20,48,0
qz 248,96,184
v_mul_f32 v62,v62,v16
v_mul_f32 v63,v63,v17
qr 160,18,32768
qt 96,2,49152
qw 6
qz 180,100,112
v_mul_f32 v48,v48,v80
v_mul_f32 v49,v49,v81
v_cvt_pk_bf16_f32 v48,v48,v49
qt 98,2,49664
ql 21,48,1024
qz 252,100,184
v_mul_f32 v50,v50,v82
v_mul_f32 v51,v51,v83
qr 232,18,33792
qt 100,3,49152
qw 7
qm 176,104,116
v_cvt_pk_bf16_f32 v49,v50,v51
v_mul_f32 v56,v56,v88
qt 102,3,49664
s_add_u32 m0,s13,8192
qm 248,104,188
v_mul_f32 v57,v57,v89
v_cvt_pk_bf16_f32 v50,v56,v57
v_mul_f32 v58,v58,v90
qr 164,18,40960
qt 104,2,53248
qw 8
qm 180,108,116
s_waitcnt vmcnt(6) lgkmcnt(1)
s_barrier
v_mul_f32 v59,v59,v91
v_cvt_pk_bf16_f32 v51,v58,v59
qt 106,2,53760
s_add_u32 s14,s19,221312
qm 252,108,188
s_add_u32 s15,s76,65536
buffer_store_dwordx4 v[48:51],v18,s[88:91],s15 offen offset:0 sc1
ds_write_b128 v19,v[48:51]
qr 236,18,41984
qt 108,3,53248
qm 144,96,120
v_mul_f32 v52,v52,v84
v_mul_f32 v53,v53,v85
v_cvt_pk_bf16_f32 v52,v52,v53
qt 110,3,53760
ql 20,48,0
qm 216,96,192
v_mul_f32 v54,v54,v86
v_mul_f32 v55,v55,v87
qt 96,2,57344
qm 148,100,120
v_cvt_pk_bf16_f32 v53,v54,v55
v_mul_f32 v60,v60,v92
qt 98,2,57856
ql 21,48,1024
qm 220,100,192
v_mul_f32 v61,v61,v93
v_cvt_pk_bf16_f32 v54,v60,v61
v_mul_f32 v62,v62,v94
s_add_u32 s15,s77,0
buffer_load_dwordx4 v[128:131],v18,s[88:91],s15 offen offset:0 sc1
qt 100,3,57344
qw 7
qm 144,104,124
v_mul_f32 v63,v63,v95
v_cvt_pk_bf16_f32 v55,v62,v63
qt 102,3,57856
s_add_u32 m0,s13,16384
qm 216,104,196
s_add_u32 s15,s76,65536
buffer_store_dwordx4 v[52:55],v18,s[88:91],s15 offen offset:1024 sc1
ds_write_b128 v19,v[52:55] offset:1024
qt 104,2,61440
qw 6
qm 148,108,124
qt 106,2,61952
s_add_u32 s14,s19,221696
qm 220,108,196
qt 108,3,61440
qw 6
qm 152,96,120
s_add_u32 s15,s77,0
buffer_load_dwordx4 v[200:203],v18,s[88:91],s15 offen offset:1024 sc1
qt 110,3,61952
ql 20,48,0
qm 224,96,192
qt 96,4,0
qw 6
qm 156,100,120
qt 98,4,512
ql 21,48,1024
qm 228,100,192
qt 100,5,0
qw 5
qm 152,104,124
qt 102,5,512
s_add_u32 m0,s13,24576
qm 224,104,196
qt 104,4,4096
qw 5
qm 156,108,124
s_add_u32 s15,s77,8192
buffer_load_dwordx4 v[132:135],v18,s[88:91],s15 offen offset:0 sc1
qt 106,4,4608
)ASM"
R"ASM(s_add_u32 s14,s19,295424
qm 228,108,196
qt 108,5,4096
qw 5
qm 176,96,120
qt 110,5,4608
ql 20,48,0
qm 248,96,192
qr 168,18,16384
qt 96,4,8192
qw 6
qm 180,100,120
qt 98,4,8704
ql 21,48,1024
qm 252,100,192
qr 240,18,17408
qt 100,5,8192
qw 7
qm 176,104,124
s_add_u32 s15,s77,8192
buffer_load_dwordx4 v[204:207],v18,s[88:91],s15 offen offset:1024 sc1
qt 102,5,8704
s_add_u32 m0,s13,32768
qm 248,104,196
qr 172,18,24576
qt 104,4,12288
qw 8
qm 180,108,124
s_waitcnt vmcnt(12) lgkmcnt(1)
s_barrier
qt 106,4,12800
s_add_u32 s14,s19,294912
qm 252,108,196
qr 244,18,25600
qt 108,5,12288
qm 144,96,160
qt 110,5,12800
ql 20,48,0
qm 216,96,232
qt 96,4,16384
qm 148,100,160
qt 98,4,16896
ql 21,48,1024
qm 220,100,232
qt 100,5,16384
s_add_u32 m0,s13,40960
s_add_u32 s14,s19,295040
qw 6
qm 144,104,164
qt 102,5,16896
ql 20,48,0
qm 216,104,236
qt 104,4,20480
qw 5
qm 148,108,164
qt 106,4,20992
ql 21,48,1024
qm 220,108,236
qt 108,5,20480
s_add_u32 m0,s13,49152
s_add_u32 s14,s19,369152
qw 5
qm 152,96,160
qt 110,5,20992
ql 20,48,0
qm 224,96,232
qr 120,19,0
qt 96,4,24576
qw 6
qm 156,100,160
qt 98,4,25088
ql 21,48,1024
qm 228,100,232
qr 192,19,1024
qt 100,5,24576
qw 7
qm 152,104,164
qt 102,5,25088
qm 224,104,236
qr 124,19,8192
qt 104,4,28672
qw 8
qm 156,108,164
qt 106,4,29184
qm 228,108,236
qr 196,19,9216
qt 108,5,28672
qw 8
qm 176,96,160
qt 110,5,29184
qm 248,96,232
qt 96,2,0
qw 7
qm 180,100,160
qt 98,2,512
qm 252,100,232
qt 100,3,0
qw 6
qm 176,104,164
qt 102,3,512
s_add_u32 m0,s13,81920
qm 248,104,236
qt 104,2,4096
qw 5
qm 180,108,164
s_waitcnt vmcnt(0)
s_barrier
qt 106,2,4608
s_add_u32 s14,s19,368640
qm 252,108,236
qt 108,3,4096
qw 5
qm 144,96,168
s_add_u32 s15,s77,16384
buffer_load_dwordx4 v[136:139],v18,s[88:91],s15 offen offset:0 sc1
v_and_b32 v1,0xfffffc3f,v18
qt 110,3,4608
ql 20,48,0
qm 216,96,240
qt 96,2,8192
qw 5
qm 148,100,168
qt 98,2,8704
ql 21,48,1024
qm 220,100,240
qt 100,3,8192
qw 5
qm 144,104,172
v_lshrrev_b32 v16,4,v0
qt 102,3,8704
s_add_u32 m0,s13,90112
qm 216,104,244
qt 104,2,12288
qw 5
qm 148,108,172
s_add_u32 s15,s77,16384
buffer_load_dwordx4 v[208:211],v18,s[88:91],s15 offen offset:1024 sc1
qt 106,2,12800
s_add_u32 s14,s19,368768
qm 220,108,244
qt 108,3,12288
qw 5
qm 152,96,168
qt 110,3,12800
ql 20,48,0
qm 224,96,240
v_lshl_add_u32 v1,v16,6,v1
qt 96,2,16384
qw 5
qm 156,100,168
qt 98,2,16896
ql 21,48,1024
qm 228,100,240
qt 100,3,16384
qw 5
qm 152,104,172
s_add_u32 s15,s77,24576
buffer_load_dwordx4 v[140:143],v18,s[88:91],s15 offen offset:0 sc1
qt 102,3,16896
s_add_u32 m0,s13,57344
qm 224,104,244
qt 104,2,20480
qw 5
qm 156,108,172
v_and_b32 v16,12,v0
qt 106,2,20992
s_add_u32 s14,s19,442880
qm 228,108,244
qt 108,3,20480
qw 5
qm 176,96,168
qt 110,3,20992
ql 20,48,0
qm 248,96,240
qt 96,2,24576
qw 5
qm 180,100,168
s_add_u32 s15,s77,24576
buffer_load_dwordx4 v[212:215],v18,s[88:91],s15 offen offset:1024 sc1
qt 98,2,25088
ql 21,48,1024
qm 252,100,240
v_lshl_add_u32 v1,v16,6,v1
qt 100,3,24576
qw 5
qm 176,104,172
qt 102,3,25088
s_add_u32 m0,s13,65536
qm 248,104,244
qt 104,2,28672
qw 5
qm 180,108,172
s_barrier
qt 106,2,29184
s_add_u32 s14,s19,442368
qm 252,108,244
qt 108,3,28672
qw 5
qm 176,96,120
s_add_u32 s15,s77,65536
buffer_load_dwordx4 v[64:67],v18,s[88:91],s15 offen offset:0 sc1
qt 110,3,29184
ql 20,48,0
qm 248,96,192
qt 96,2,32768
qw 5
qm 180,100,120
qt 98,2,33280
ql 21,48,1024
qm 252,100,192
qt 100,3,32768
qw 5
qm 176,104,124
qt 102,3,33280
s_add_u32 m0,s13,73728
qm 248,104,196
qt 104,2,36864
qw 5
qm 180,108,124
s_add_u32 s15,s77,65536
buffer_load_dwordx4 v[72:75],v18,s[88:91],s15 offen offset:1024 sc1
qt 106,2,37376
s_add_u32 s14,s19,442496
qm 252,108,196
qt 108,3,36864
qw 5
qm 144,96,120
qt 110,3,37376
ql 20,48,0
qm 216,96,192
qt 96,2,40960
qw 5
qm 148,100,120
qt 98,2,41472
ql 21,48,1024
qm 220,100,192
qt 100,3,40960
qw 5
qm 144,104,124
s_add_u32 s15,s77,73728
buffer_load_dwordx4 v[68:71],v18,s[88:91],s15 offen offset:0 sc1
qt 102,3,41472
s_mov_b32 m0,s13
qm 216,104,196
qt 104,2,45056
qw 5
qm 148,108,124
qt 106,2,45568
s_add_u32 s14,s28,294912
qm 220,108,196
qt 108,3,45056
qw 5
qm 152,96,120
qt 110,3,45568
ql 8,24,0
qm 224,96,192
qt 96,2,49152
qw 5
qm 156,100,120
s_add_u32 s15,s77,73728
buffer_load_dwordx4 v[76:79],v18,s[88:91],s15 offen offset:1024 sc1
qt 98,2,49664
ql 9,24,1024
qm 228,100,192
qt 100,3,49152
qw 5
qm 152,104,124
qt 102,3,49664
s_add_u32 m0,s13,8192
qm 224,104,196
qt 104,2,53248
qw 5
qm 156,108,124
s_waitcnt vmcnt(10)
s_barrier
qt 106,2,53760
s_mov_b32 s14,s28
qm 228,108,196
qt 108,3,53248
qw 5
qm 176,96,128
s_add_u32 s15,s77,81920
buffer_load_dwordx4 v[80:83],v18,s[88:91],s15 offen offset:0 sc1
qt 110,3,53760
ql 8,24,0
qm 248,96,200
qt 96,4,16384
qw 5
qm 180,100,128
qt 98,4,16896
ql 9,24,1024
qm 252,100,200
qt 100,5,16384
qw 5
qm 176,104,132
qt 102,5,16896
s_add_u32 m0,s13,16384
qm 248,104,204
qt 104,4,20480
qw 5
qm 180,108,132
s_add_u32 s15,s77,81920
buffer_load_dwordx4 v[88:91],v18,s[88:91],s15 offen offset:1024 sc1
qt 106,4,20992
s_add_u32 s14,s28,73728
qm 252,108,204
qt 108,5,20480
qw 5
qm 144,96,128
qt 110,5,20992
ql 8,24,0
qm 216,96,200
qt 96,4,24576
qw 5
qm 148,100,128
qt 98,4,25088
ql 9,24,1024
qm 220,100,200
qt 100,5,24576
qw 5
qm 144,104,132
s_add_u32 s15,s77,90112
buffer_load_dwordx4 v[84:87],v18,s[88:91],s15 offen offset:0 sc1
qt 102,5,25088
s_add_u32 m0,s13,24576
qm 216,104,204
qt 104,4,28672
qw 5
qm 148,108,132
qt 106,4,29184
s_add_u32 s14,s28,313344
qm 220,108,204
qt 108,5,28672
qw 5
qm 152,96,128
qt 110,5,29184
ql 8,24,0
qm 224,96,200
qt 96,2,57344
qw 5
qm 156,100,128
s_add_u32 s15,s77,90112
buffer_load_dwordx4 v[92:95],v18,s[88:91],s15 offen offset:1024 sc1
qt 98,2,57856
ql 9,24,1024
qm 228,100,200
qt 100,3,57344
qw 5
qm 152,104,132
qt 102,3,57856
s_add_u32 m0,s13,81920
qm 224,104,204
qt 104,2,61440
qw 5
qm 156,108,132
s_waitcnt vmcnt(10)
s_barrier
qt 106,2,61952
s_add_u32 s14,s28,18432
qm 228,108,204
qt 108,3,61440
qw 5
qm 176,96,136
v_and_b32 v15,48,v0
qt 110,3,61952
ql 8,24,0
qm 248,96,208
qt 96,4,0
qw 5
qm 180,100,136
v_lshlrev_b32 v10,10,v15
qt 98,4,512
ql 9,24,1024
qm 252,100,208
qt 100,5,0
qw 5
qm 176,104,140
qt 102,5,512
s_add_u32 m0,s13,90112
qm 248,104,212
v_and_b32 v16,16,v0
qt 104,4,4096
qw 5
qm 180,108,140
qt 106,4,4608
s_add_u32 s14,s19,516096
qm 252,108,212
qt 108,5,4096
qw 5
qm 144,96,136
v_lshrrev_b32 v17,5,v0
qt 110,5,4608
ql 20,48,0
qm 216,96,208
qt 96,4,8192
qw 5
qm 148,100,136
v_lshl_add_u32 v12,v17,6,v16
qt 98,4,8704
ql 21,48,1024
qm 220,100,208
qt 100,5,8192
qw 5
qm 144,104,140
qt 102,5,8704
s_add_u32 m0,s13,32768
qm 216,104,212
v_and_b32 v16,3,v0
qt 104,4,12288
qw 5
qm 148,108,140
qt 106,4,12800
s_add_u32 s14,s19,589824
qm 220,108,212
qt 108,5,12288
qw 5
qm 152,96,136
v_lshl_add_u32 v12,v16,2,v12
qt 110,5,12800
ql 20,48,0
qm 224,96,208
qd 96,24,0
qw 5
qm 156,100,136
v_lshl_add_u32 v10,v16,6,v10
qd 98,24,4096
ql 21,48,1024
qm 228,100,208
qd 100,25,0
qw 5
qm 152,104,140
qd 102,25,4096
s_add_u32 m0,s13,40960
qm 224,104,212
v_bfe_u32 v11,v0,2,2
qd 104,26,0
qw 5
qm 156,108,140
s_waitcnt vmcnt(6)
s_barrier
qd 106,26,4096
s_add_u32 s14,s28,92160
qm 228,108,212
qd 108,27,0
qw 5
qm 176,96,64
s_add_u32 s15,s77,32768
buffer_load_dwordx4 v[48:51],v18,s[88:91],s15 offen offset:0 sc1
v_lshl_add_u32 v10,v11,4,v10
qd 110,27,4096
ql 8,24,0
qm 248,96,72
qd 96,24,8192
qw 5
qm 176,100,68
v_lshlrev_b32 v12,1,v12
qd 98,24,12288
ql 9,24,1024
qm 248,100,76
s_add_u32 s15,s77,32768
buffer_load_dwordx4 v[56:59],v18,s[88:91],s15 offen offset:1024 sc1
qd 100,25,8192
qw 5
qm 176,104,80
qd 102,25,12288
s_add_u32 m0,s13,49152
qm 248,104,88
v_mul_u32_u24 v11,1152,v11
qd 104,26,8192
qw 5
qm 176,108,84
s_add_u32 s15,s77,40960
buffer_load_dwordx4 v[52:55],v18,s[88:91],s15 offen offset:0 sc1
qd 106,26,12288
s_add_u32 s14,s19,516224
qm 248,108,92
qd 108,27,8192
qw 5
qm 144,96,64
v_add_u32 v11,v11,v12
qd 110,27,12288
ql 20,48,0
qm 216,96,72
s_add_u32 s15,s77,40960
buffer_load_dwordx4 v[60:63],v18,s[88:91],s15 offen offset:1024 sc1
qd 96,24,16384
qw 5
qm 144,100,68
v_lshlrev_b32 v13,1,v11
qd 98,24,20480
ql 21,48,1024
qm 216,100,76
qd 100,25,16384
qw 5
qm 144,104,80
s_add_u32 s15,s77,49152
buffer_load_dwordx4 v[120:123],v18,s[88:91],s15 offen offset:0 sc1
qd 102,25,20480
s_add_u32 m0,s13,57344
qm 216,104,88
v_lshl_or_b32 v14,v17,30,v13
qd 104,26,16384
qw 5
qm 144,108,84
qd 106,26,20480
s_add_u32 s14,s19,589952
qm 216,108,92
s_add_u32 s15,s77,49152
buffer_load_dwordx4 v[192:195],v18,s[88:91],s15 offen offset:1024 sc1
qd 108,27,16384
qw 5
qm 152,96,64
s_and_b32 s30,s8,3
s_lshl_b32 s30,s30,4
s_mov_b64 s[32:33],0xffff
s_lshl_b64 s[32:33],s[32:33],s30
s_bitcmp1_b32 s8,2
s_cselect_b64 s[40:41],0,s[32:33]
s_cselect_b64 s[42:43],s[32:33],0
qd 110,27,20480
ql 20,48,0
qm 224,96,72
qd 96,24,24576
qw 5
qm 152,100,68
s_add_u32 s15,s77,57344
buffer_load_dwordx4 v[124:127],v18,s[88:91],s15 offen offset:0 sc1
s_mov_b64 s[52:53],0
qd 98,24,28672
ql 21,48,1024
qm 224,100,76
qd 100,25,24576
qw 5
qm 152,104,80
qd 102,25,28672
s_add_u32 m0,s13,65536
qm 224,104,88
s_add_u32 s15,s77,57344
buffer_load_dwordx4 v[196:199],v18,s[88:91],s15 offen offset:1024 sc1
s_mov_b64 s[54:55],0
qd 104,26,24576
qw 5
qm 152,108,84
s_waitcnt vmcnt(14)
s_barrier
qd 106,26,28672
s_add_u32 s14,s19,256
qm 224,108,92
qd 108,27,24576
qw 5
qm 180,96,64
s_mov_b64 s[56:57],0
qd 110,27,28672
ql 20,48,0
qm 252,96,72
qd 96,28,16384
qw 5
qm 180,100,68
s_mov_b64 s[58:59],0
qd 98,28,20480
ql 21,48,1024
qm 252,100,76
qd 100,29,16384
qw 5
qm 180,104,80
qd 102,29,20480
s_add_u32 m0,s13,73728
qm 252,104,88
s_mov_b32 s76,0
qd 104,30,16384
qw 5
qm 180,108,84
qd 106,30,20480
s_add_u32 s14,s19,384
qm 252,108,92
qd 108,31,16384
qw 5
qm 148,96,64
qd 110,31,20480
ql 20,48,0
qm 220,96,72
qt 96,4,24576
qw 5
qm 148,100,68
qt 98,4,25088
ql 21,48,1024
qm 220,100,76
v_cvt_pk_bf16_f32 v176,v176,v177
qt 100,5,24576
qw 5
qm 148,104,80
v_cvt_pk_bf16_f32 v177,v178,v179
qt 102,5,25088
s_mov_b32 m0,s13
qm 220,104,88
v_cvt_pk_bf16_f32 v178,v180,v181
qt 104,4,28672
qw 5
qm 148,108,84
v_cvt_pk_bf16_f32 v179,v182,v183
qt 106,4,29184
s_add_u32 s14,s19,368896
qm 220,108,92
qt 108,5,28672
s_waitcnt vmcnt(17) lgkmcnt(5)
qm 144,96,48
v_cvt_pk_bf16_f32 v248,v248,v249
qt 110,5,29184
ql 20,48,0
s_waitcnt vmcnt(15)
qm 216,96,56
v_cvt_pk_bf16_f32 v249,v250,v251
qt 96,2,32768
qw 5
qm 148,100,48
v_cvt_pk_bf16_f32 v250,v252,v253
qt 98,2,33280
ql 21,48,1024
qm 220,100,56
v_cvt_pk_bf16_f32 v251,v254,v255
qt 100,3,32768
s_waitcnt vmcnt(15) lgkmcnt(5)
qm 144,104,52
s_add_u32 s15,s73,524288
buffer_store_dwordx4 v[176:179],v1,s[88:91],s15 offen offset:0 sc1
qt 102,3,33280
s_add_u32 m0,s13,81920
s_waitcnt vmcnt(14)
qm 216,104,60
qt 104,2,36864
qw 5
qm 148,108,52
s_waitcnt vmcnt(8)
s_barrier
qt 106,2,37376
s_add_u32 s14,s19,369024
qm 220,108,60
qt 108,3,36864
qw 5
qm 144,96,120
qt 110,3,37376
ql 20,48,0
qm 216,96,192
qd 96,24,40960
qw 5
qm 148,100,120
qd 98,24,45056
ql 21,48,1024
qm 220,100,192
qd 100,25,40960
qw 5
qm 144,104,124
s_add_u32 s15,s73,524288
buffer_store_dwordx4 v[248:251],v1,s[88:91],s15 offen offset:1024 sc1
qd 102,25,45056
s_add_u32 m0,s13,90112
s_waitcnt vmcnt(10)
qm 216,104,196
qd 104,26,40960
qw 5
qm 148,108,124
qd 106,26,45056
s_add_u32 s14,s19,442624
qm 220,108,196
qd 108,27,40960
qw 5
qm 156,96,64
qd 110,27,45056
ql 20,48,0
qm 228,96,72
qt 96,2,49152
qw 5
qm 156,100,68
qt 98,2,49664
ql 21,48,1024
qm 228,100,76
v_cvt_pk_bf16_f32 v144,v144,v145
qt 100,3,49152
qw 5
qm 156,104,80
v_cvt_pk_bf16_f32 v145,v146,v147
qt 102,3,49664
s_add_u32 m0,s13,8192
qm 228,104,88
v_cvt_pk_bf16_f32 v146,v148,v149
qt 104,2,53248
qw 5
qm 156,108,84
v_cvt_pk_bf16_f32 v147,v150,v151
qt 106,2,53760
s_add_u32 s14,s19,442752
qm 228,108,92
qt 108,3,53248
qw 5
qm 152,96,48
v_cvt_pk_bf16_f32 v216,v216,v217
qt 110,3,53760
ql 20,48,0
qm 224,96,56
v_cvt_pk_bf16_f32 v217,v218,v219
qt 96,2,57344
qw 5
qm 156,100,48
v_cvt_pk_bf16_f32 v218,v220,v221
qt 98,2,57856
ql 21,48,1024
qm 228,100,56
v_cvt_pk_bf16_f32 v219,v222,v223
qt 100,3,57344
qw 5
qm 152,104,52
s_add_u32 s15,s73,0
buffer_store_dwordx4 v[144:147],v1,s[88:91],s15 offen offset:0 sc1
qt 102,3,57856
s_add_u32 m0,s13,16384
qm 224,104,60
qt 104,2,61440
qw 5
qm 156,108,52
s_waitcnt vmcnt(9)
s_barrier
qt 106,2,61952
s_add_u32 s14,s28,147456
qm 228,108,60
qt 108,3,61440
qw 5
qm 152,96,120
qt 110,3,61952
ql 8,24,0
qm 224,96,192
qt 96,4,0
qw 5
qm 156,100,120
qt 98,4,512
ql 9,24,1024
qm 228,100,192
qt 100,5,0
qw 5
qm 152,104,124
s_add_u32 s15,s73,0
buffer_store_dwordx4 v[216:219],v1,s[88:91],s15 offen offset:1024 sc1
qt 102,5,512
s_add_u32 m0,s13,24576
qm 224,104,196
qt 104,4,4096
qw 5
qm 156,108,124
qt 106,4,4608
s_add_u32 s14,s28,221184
qm 228,108,196
qt 108,5,4096
qw 5
qz 160,96,112
qt 110,5,4608
ql 8,24,0
qz 232,96,184
qt 96,4,8192
qw 5
qz 164,100,112
qt 98,4,8704
ql 9,24,1024
qz 236,100,184
v_cvt_pk_bf16_f32 v152,v152,v153
qt 100,5,8192
qw 5
qm 160,104,116
v_cvt_pk_bf16_f32 v153,v154,v155
qt 102,5,8704
s_add_u32 m0,s13,32768
qm 232,104,188
v_cvt_pk_bf16_f32 v154,v156,v157
v_and_b32 v16,1,v0
qt 104,4,12288
qw 5
qm 164,108,116
v_cvt_pk_bf16_f32 v155,v158,v159
qt 106,4,12800
s_add_u32 s14,s28,165888
qm 236,108,188
v_lshlrev_b32 v16,4,v16
qt 108,5,12288
qw 5
qz 168,96,112
v_cvt_pk_bf16_f32 v224,v224,v225
qt 110,5,12800
ql 8,24,0
qz 240,96,184
v_cvt_pk_bf16_f32 v225,v226,v227
v_mov_b32 v17,0x3f80
qt 96,2,0
qw 5
qz 172,100,112
v_cvt_pk_bf16_f32 v226,v228,v229
qt 98,2,512
ql 9,24,1024
qz 244,100,184
v_cvt_pk_bf16_f32 v227,v230,v231
v_lshlrev_b32 v16,v16,v17
qt 100,3,0
qw 5
qm 168,104,116
s_add_u32 s15,s73,131072
buffer_store_dwordx4 v[152:155],v1,s[88:91],s15 offen offset:0 sc1
qt 102,3,512
s_add_u32 m0,s13,40960
qm 240,104,188
v_bfe_u32 v17,v0,1,3
qt 104,2,4096
qw 5
qm 172,108,116
s_waitcnt vmcnt(9)
s_barrier
qt 106,2,4608
s_add_u32 s14,s28,239616
qm 244,108,188
v_cmp_eq_u32 vcc,0,v17
s_nop 1
v_cndmask_b32 v144,0,v16,vcc
qt 108,3,4096
qw 5
qm 160,96,128
qt 110,3,4608
ql 8,24,0
qm 232,96,200
v_cmp_eq_u32 vcc,1,v17
s_nop 1
v_cndmask_b32 v145,0,v16,vcc
qt 96,4,16384
qw 5
qm 164,100,128
qt 98,4,16896
ql 9,24,1024
qm 236,100,200
v_cmp_eq_u32 vcc,2,v17
s_nop 1
v_cndmask_b32 v146,0,v16,vcc
qt 100,5,16384
s_add_u32 m0,s13,49152
s_add_u32 s14,s19,516352
qw 5
qm 160,104,132
s_add_u32 s15,s73,131072
buffer_store_dwordx4 v[224:227],v1,s[88:91],s15 offen offset:1024 sc1
qt 102,5,16896
ql 20,48,0
qm 232,104,204
v_cmp_eq_u32 vcc,3,v17
s_nop 1
v_cndmask_b32 v147,0,v16,vcc
qt 104,4,20480
qw 5
qm 164,108,132
qt 106,4,20992
ql 21,48,1024
qm 236,108,204
v_cmp_eq_u32 vcc,4,v17
s_nop 1
v_cndmask_b32 v148,0,v16,vcc
qt 108,5,20480
s_add_u32 m0,s13,57344
s_add_u32 s14,s19,516480
qw 5
qm 168,96,128
qt 110,5,20992
ql 20,48,0
qm 240,96,200
v_cmp_eq_u32 vcc,5,v17
s_nop 1
v_cndmask_b32 v149,0,v16,vcc
qt 96,4,24576
qw 5
qm 172,100,128
qt 98,4,25088
ql 21,48,1024
qm 244,100,200
v_cmp_eq_u32 vcc,6,v17
s_nop 1
v_cndmask_b32 v150,0,v16,vcc
qt 100,5,24576
qw 5
qm 168,104,132
qt 102,5,25088
qm 240,104,204
v_cmp_eq_u32 vcc,7,v17
s_nop 1
v_cndmask_b32 v151,0,v16,vcc
qt 104,4,28672
qw 5
qm 172,108,132
qt 106,4,29184
qm 244,108,204
qt 108,5,28672
qw 5
qm 160,96,136
qt 110,5,29184
qm 232,96,208
qt 96,2,8192
qw 5
qm 164,100,136
qt 98,2,8704
qm 236,100,208
qt 100,3,8192
qw 5
qm 160,104,140
qt 102,3,8704
s_add_u32 m0,s13,81920
qm 232,104,212
qt 104,2,12288
qw 5
qm 164,108,140
s_waitcnt vmcnt(0)
s_barrier
qt 106,2,12800
s_add_u32 s14,s19,590080
qm 236,108,212
qt 108,3,12288
qw 5
qm 168,96,136
buffer_load_dwordx2 v[216:217],v11,s[36:39],s70 offen offset:256
qt 110,3,12800
ql 20,48,0
qm 240,96,208
qd 96,24,16384
qw 5
qm 172,100,136
qd 98,24,20480
ql 21,48,1024
qm 244,100,208
qd 100,25,16384
qw 5
qm 168,104,140
qd 102,25,20480
s_add_u32 m0,s13,90112
qm 240,104,212
qd 104,26,16384
qw 5
qm 172,108,140
qd 106,26,20480
s_add_u32 s14,s19,590208
qm 244,108,212
qd 108,27,16384
qw 5
qm 160,96,64
qd 110,27,20480
ql 20,48,0
qm 232,96,72
qr 136,18,49152
buffer_load_dwordx2 v[218:219],v12,s[80:83],s75 offen offset:256
qd 96,24,24576
qw 6
qm 160,100,68
qd 98,24,28672
ql 21,48,1024
qm 232,100,76
qr 208,18,50176
qd 100,25,24576
qw 7
qm 160,104,80
qd 102,25,28672
s_add_u32 m0,s13,65536
qm 232,104,88
qr 140,18,57344
qd 104,26,24576
qw 8
qm 160,108,84
qd 106,26,28672
s_add_u32 s14,s19,73984
qm 232,108,92
qr 212,18,58368
qd 108,27,24576
qw 8
qm 168,96,64
qd 110,27,28672
ql 20,48,0
qm 240,96,72
qd 96,24,32768
qw 7
qm 168,100,68
buffer_load_dwordx2 v[220:221],v11,s[36:39],s70 offen offset:896
qd 98,24,36864
ql 21,48,1024
qm 240,100,76
qd 100,25,32768
qw 6
qm 168,104,80
qd 102,25,36864
s_add_u32 m0,s13,73728
qm 240,104,88
qd 104,26,32768
qw 5
qm 168,108,84
s_barrier
qd 106,26,36864
s_add_u32 s14,s19,147712
qm 240,108,92
qd 108,27,32768
qw 5
qm 164,96,64
qd 110,27,36864
ql 20,48,0
qm 236,96,72
qd 96,24,40960
qw 5
qm 164,100,68
qd 98,24,45056
ql 21,48,1024
qm 236,100,76
buffer_load_dwordx2 v[222:223],v12,s[80:83],s75 offen offset:896
qd 100,25,40960
qw 5
qm 164,104,80
qd 102,25,45056
s_mov_b32 m0,s13
qm 236,104,88
qd 104,26,40960
qw 5
qm 164,108,84
qd 106,26,45056
s_add_u32 s14,s19,221440
qm 236,108,92
qd 108,27,40960
qw 5
qm 172,96,64
qd 110,27,45056
ql 20,48,0
qm 244,96,72
qt 96,2,49152
qw 5
qm 172,100,68
qt 98,2,49664
ql 21,48,1024
qm 244,100,76
qt 100,3,49152
qw 5
qm 172,104,80
qt 102,3,49664
s_add_u32 m0,s13,8192
qm 244,104,88
qt 104,2,53248
qw 5
qm 172,108,84
qt 106,2,53760
s_add_u32 s14,s19,295168
qm 244,108,92
qt 108,3,53248
qw 5
qm 160,96,48
qt 110,3,53760
ql 20,48,0
qm 232,96,56
qr 64,18,32768
qt 96,2,57344
qw 6
qm 164,100,48
qt 98,2,57856
ql 21,48,1024
qm 236,100,56
qr 72,18,33792
qt 100,3,57344
qw 7
qm 160,104,52
qt 102,3,57856
s_add_u32 m0,s13,16384
qm 232,104,60
qr 68,18,40960
qt 104,2,61440
qw 8
qm 164,108,52
s_waitcnt vmcnt(7) lgkmcnt(1)
s_barrier
qt 106,2,61952
s_add_u32 s14,s19,74112
qm 236,108,60
qr 76,18,41984
qt 108,3,61440
qm 168,96,48
qt 110,3,61952
ql 20,48,0
qm 240,96,56
qt 96,4,16384
qm 172,100,48
qt 98,4,16896
ql 21,48,1024
qm 244,100,56
qt 100,5,16384
qw 6
qm 168,104,52
qt 102,5,16896
s_add_u32 m0,s13,24576
qm 240,104,60
qt 104,4,20480
qw 5
qm 172,108,52
qt 106,4,20992
s_add_u32 s14,s19,147840
qm 244,108,60
qt 108,5,20480
qw 5
qm 160,96,120
qt 110,5,20992
ql 20,48,0
qm 232,96,192
qr 80,18,16384
qt 96,4,24576
qw 6
qm 164,100,120
qt 98,4,25088
ql 21,48,1024
qm 236,100,192
qr 88,18,17408
qt 100,5,24576
qw 7
qm 160,104,124
qt 102,5,25088
s_add_u32 m0,s13,32768
qm 232,104,196
qr 84,18,24576
qt 104,4,28672
qw 8
qm 164,108,124
qt 106,4,29184
s_add_u32 s14,s19,221568
qm 236,108,196
qr 92,18,25600
qt 108,5,28672
qw 8
qm 168,96,120
qt 110,5,29184
ql 20,48,0
qm 240,96,192
qt 96,4,0
qw 7
qm 172,100,120
qt 98,4,512
ql 21,48,1024
qm 244,100,192
qt 100,5,0
qw 6
qm 168,104,124
qt 102,5,512
s_add_u32 m0,s13,81920
qm 240,104,196
qt 104,4,4096
qw 5
qm 172,108,124
s_waitcnt vmcnt(6)
s_barrier
qt 106,4,4608
s_add_u32 s14,s19,295296
qm 244,108,196
qt 108,5,4096
qw 5
qm 160,96,136
qt 110,5,4608
ql 20,48,0
qm 232,96,208
qr 48,19,0
qt 96,4,8192
qw 6
qm 164,100,136
qt 98,4,8704
ql 21,48,1024
qm 236,100,208
qr 56,19,1024
qt 100,5,8192
qw 7
qm 160,104,140
qt 102,5,8704
qm 232,104,212
qr 52,19,8192
qt 104,4,12288
qw 8
qm 164,108,140
qt 106,4,12800
qm 236,108,212
qr 60,19,9216
qt 108,5,12288
qw 8
qm 160,96,64
qt 110,5,12800
qm 232,96,72
qt 96,2,0
qw 7
qm 164,100,64
qt 98,2,512
qm 236,100,72
qt 100,3,0
qw 6
qm 160,104,68
qt 102,3,512
qm 232,104,76
qt 104,2,4096
qw 5
qm 164,108,68
qt 106,2,4608
qm 236,108,76
qt 108,3,4096
qw 5
qm 160,96,80
qt 110,3,4608
qm 232,96,88
qt 96,2,8192
qw 5
qm 164,100,80
qt 98,2,8704
qm 236,100,88
qt 100,3,8192
qw 5
qm 160,104,84
qt 102,3,8704
qm 232,104,92
qt 104,2,12288
qw 5
qm 164,108,84
s_waitcnt vmcnt(2)
s_barrier
qt 106,2,12800
qm 236,108,92
qt 108,3,12288
qw 5
qm 160,96,48
qt 110,3,12800
qm 232,96,56
qt 96,2,16384
qw 5
qm 164,100,48
qt 98,2,16896
qm 236,100,56
qt 100,3,16384
qw 5
qm 160,104,52
qt 102,3,16896
qm 232,104,60
qt 104,2,20480
qw 5
qm 164,108,52
qt 106,2,20992
qm 236,108,60
qt 108,3,20480
qw 5
qm 168,96,136
qt 110,3,20992
qm 240,96,208
qt 96,2,24576
qw 5
qm 172,100,136
qt 98,2,25088
qm 244,100,208
v_cvt_pk_bf16_f32 v160,v160,v161
qt 100,3,24576
qw 5
qm 168,104,140
v_cvt_pk_bf16_f32 v161,v162,v163
qt 102,3,25088
qm 240,104,212
v_cvt_pk_bf16_f32 v162,v164,v165
qt 104,2,28672
qw 5
qm 172,108,140
v_cvt_pk_bf16_f32 v163,v166,v167
qt 106,2,29184
qm 244,108,212
qt 108,3,28672
qw 5
qm 168,96,64
v_cvt_pk_bf16_f32 v232,v232,v233
qt 110,3,29184
qm 240,96,72
v_cvt_pk_bf16_f32 v233,v234,v235
qt 96,2,32768
qw 5
qm 172,100,64
v_cvt_pk_bf16_f32 v234,v236,v237
qt 98,2,33280
qm 244,100,72
v_cvt_pk_bf16_f32 v235,v238,v239
qt 100,3,32768
qw 5
qm 168,104,68
s_add_u32 s15,s73,262144
buffer_store_dwordx4 v[160:163],v1,s[88:91],s15 offen offset:0 sc1
qt 102,3,33280
qm 240,104,76
qt 104,2,36864
qw 5
qm 172,108,68
s_waitcnt vmcnt(1)
s_barrier
qt 106,2,37376
qm 244,108,76
qt 108,3,36864
qw 5
qm 168,96,80
qt 110,3,37376
qm 240,96,88
qt 96,4,16384
qw 5
qm 172,100,80
qt 98,4,16896
qm 244,100,88
qt 100,5,16384
qw 5
qm 168,104,84
s_add_u32 s15,s73,262144
buffer_store_dwordx4 v[232:235],v1,s[88:91],s15 offen offset:1024 sc1
qt 102,5,16896
qm 240,104,92
qt 104,4,20480
qw 5
qm 172,108,84
qt 106,4,20992
qm 244,108,92
qt 108,5,20480
qw 5
qm 168,96,48
qt 110,5,20992
qm 240,96,56
qw 4
qm 172,100,48
qm 244,100,56
qw 2
qm 168,104,52
qm 240,104,60
qw 0
qm 172,108,52
qm 244,108,60
s_nop 3
v_cvt_pk_bf16_f32 v168,v168,v169
v_cvt_pk_bf16_f32 v169,v170,v171
s_nop 0
v_cvt_pk_bf16_f32 v170,v172,v173
v_cvt_pk_bf16_f32 v171,v174,v175
s_add_u32 s15,s73,393216
buffer_store_dwordx4 v[168:171],v1,s[88:91],s15 offen offset:0 sc1
v_cvt_pk_bf16_f32 v240,v240,v241
v_cvt_pk_bf16_f32 v241,v242,v243
v_cvt_pk_bf16_f32 v242,v244,v245
v_cvt_pk_bf16_f32 v243,v246,v247
s_add_u32 s15,s73,393216
buffer_store_dwordx4 v[240:243],v1,s[88:91],s15 offen offset:1024 sc1
v_lshlrev_b32 v16,16,v218
v_lshlrev_b32 v176,16,v216
v_mul_f32 v16,v16,v40
v_add_f32 v17,v176,v41
v_fma_f32 v176,v17,v16,v176
v_and_b32 v16,0xffff0000,v218
v_and_b32 v177,0xffff0000,v216
v_mul_f32 v16,v16,v40
v_add_f32 v17,v177,v41
v_fma_f32 v177,v17,v16,v177
v_lshlrev_b32 v16,16,v219
v_lshlrev_b32 v178,16,v217
v_mul_f32 v16,v16,v40
v_add_f32 v17,v178,v41
v_fma_f32 v178,v17,v16,v178
v_and_b32 v16,0xffff0000,v219
v_and_b32 v179,0xffff0000,v217
v_mul_f32 v16,v16,v40
v_add_f32 v17,v179,v41
v_fma_f32 v179,v17,v16,v179
v_lshlrev_b32 v16,16,v222
v_lshlrev_b32 v180,16,v220
v_mul_f32 v16,v16,v40
v_add_f32 v17,v180,v41
v_fma_f32 v180,v17,v16,v180
v_and_b32 v16,0xffff0000,v222
v_and_b32 v181,0xffff0000,v220
v_mul_f32 v16,v16,v40
v_add_f32 v17,v181,v41
v_fma_f32 v181,v17,v16,v181
v_lshlrev_b32 v16,16,v223
v_lshlrev_b32 v182,16,v221
v_mul_f32 v16,v16,v40
v_add_f32 v17,v182,v41
v_fma_f32 v182,v17,v16,v182
v_and_b32 v16,0xffff0000,v223
v_and_b32 v183,0xffff0000,v221
v_mul_f32 v16,v16,v40
v_add_f32 v17,v183,v41
v_fma_f32 v183,v17,v16,v183
buffer_load_dwordx4 v[252:255],v15,s[96:99],s72 offen sc1
s_waitcnt vmcnt(0)
s_barrier
.L_tX00:
v_sub_u32 v252,s74,v252
v_sub_u32 v253,s74,v253
v_sub_u32 v254,s74,v254
v_sub_u32 v255,s74,v255
v_cndmask_b32 v252,v252,0,s[40:41]
v_cndmask_b32 v253,v253,0,s[42:43]
v_cmp_ge_u32 vcc,0,v252
s_nop 1
s_andn2_b64 exec,vcc,s[52:53]
s_cbranch_scc0 .L_tX00r10
s_add_u32 s31,s69,262144
buffer_load_dwordx4 v[152:155],v10,s[88:91],s31 offen sc1
s_add_u32 s31,s69,393216
buffer_load_dwordx4 v[224:227],v10,s[88:91],s31 offen sc1
.L_tX00r10:
s_mov_b64 exec,-1
s_or_b64 s[52:53],s[52:53],vcc
v_cmp_ge_u32 vcc,0,v253
s_nop 1
s_andn2_b64 exec,vcc,s[54:55]
s_cbranch_scc0 .L_tX00r11
s_add_u32 s31,s69,327680
buffer_load_dwordx4 v[156:159],v10,s[88:91],s31 offen sc1
s_add_u32 s31,s69,458752
buffer_load_dwordx4 v[228:231],v10,s[88:91],s31 offen sc1
.L_tX00r11:
s_mov_b64 exec,-1
s_or_b64 s[54:55],s[54:55],vcc
v_cmp_ge_u32 vcc,0,v254
s_nop 1
s_andn2_b64 exec,vcc,s[56:57]
s_cbranch_scc0 .L_tX00r12
s_add_u32 s31,s69,917504
buffer_load_dwordx4 v[112:115],v10,s[88:91],s31 offen sc1
s_add_u32 s31,s69,1048576
buffer_load_dwordx4 v[184:187],v10,s[88:91],s31 offen sc1
.L_tX00r12:
s_mov_b64 exec,-1
s_or_b64 s[56:57],s[56:57],vcc
v_cmp_ge_u32 vcc,0,v255
s_nop 1
s_andn2_b64 exec,vcc,s[58:59]
s_cbranch_scc0 .L_tX00r13
s_add_u32 s31,s69,983040
buffer_load_dwordx4 v[116:119],v10,s[88:91],s31 offen sc1
s_add_u32 s31,s69,1114112
buffer_load_dwordx4 v[188:191],v10,s[88:91],s31 offen sc1
.L_tX00r13:
s_mov_b64 exec,-1
s_or_b64 s[58:59],s[58:59],vcc
.L_tX00c:
s_and_b64 s[32:33],s[52:53],s[54:55]
s_and_b64 s[32:33],s[32:33],s[56:57]
s_and_b64 s[32:33],s[32:33],s[58:59]
s_cmp_eq_u64 s[32:33],-1
s_cbranch_scc1 .L_tX00x
s_add_u32 s76,s76,1
s_cmp_gt_u32 s76,1000000
s_cbranch_scc1 .L_tX00x
s_sleep 4
buffer_load_dwordx4 v[252:255],v15,s[96:99],s72 offen sc1
s_waitcnt vmcnt(0)
s_branch .L_tX00
.L_tX00x:
s_waitcnt vmcnt(0)
qm 176,144,152
qm 180,144,112
qm 176,144,156
qm 180,144,116
qm 176,148,224
qm 180,148,184
qm 176,148,228
qm 180,148,188
s_nop 6
buffer_store_dwordx4 v[176:179],v13,s[92:95],s71 offen offset:512 sc1
buffer_store_dwordx4 v[180:183],v13,s[92:95],s71 offen offset:1792 sc1
s_endpgm
.org .L_code_X0+40960
.L_code_X01:
qw 0
s_barrier
qr 112,24,24576
qr 184,24,0
qt 96,2,0
qt 98,2,512
qt 100,3,0
qt 102,3,512
qr 116,25,28672
qr 188,25,4096
qt 104,2,4096
qt 106,2,4608
qt 108,3,4096
qt 110,3,4608
qr 120,24,32768
qr 192,24,8192
qw 10
qz 48,96,112
qr 124,25,36864
qr 196,25,12288
ds_write_b128 v31,v[208:211] offset:24576
qz 52,96,184
qr 200,24,16384
ds_write_b128 v32,v[212:215] offset:24576
qt 96,2,8192
qw 14
qz 56,100,112
qr 128,24,40960
qr 204,25,20480
qt 98,2,8704
buffer_load_dword v1,v6,s[44:47],s17 offen
qz 60,100,184
ds_write_b128 v31,v[216:219] offset:32768
qt 100,3,8192
qw 15
qm 48,104,116
qr 132,25,45056
ds_write_b128 v32,v[220:223] offset:32768
qt 102,3,8704
qm 52,104,188
qt 104,2,12288
qw 15
qm 56,108,116
qr 136,24,49152
ds_write_b128 v31,v[224:227] offset:40960
qt 106,2,12800
qm 60,108,188
ds_write_b128 v32,v[228:231] offset:40960
qt 108,3,12288
qw 11
qm 48,96,120
s_barrier
qr 140,25,53248
qt 110,3,12800
qm 52,96,192
ds_write_b128 v31,v[232:235] offset:49152
qt 96,2,16384
qw 10
qm 56,100,120
qr 144,24,57344
qt 98,2,16896
buffer_load_dword v1,v6,s[40:43],s17 offen
qm 60,100,192
ds_write_b128 v32,v[236:239] offset:49152
qt 100,3,16384
qw 10
qm 48,104,124
qr 148,25,61440
qt 102,3,16896
qm 52,104,196
ds_write_b128 v31,v[240:243] offset:57344
qt 104,2,20480
qw 10
qm 56,108,124
qr 152,26,8192
qt 106,2,20992
qm 60,108,196
ds_write_b128 v32,v[244:247] offset:57344
qt 108,3,20480
qw 10
qm 48,96,128
qr 156,27,12288
qt 110,3,20992
qm 52,96,200
ds_write_b128 v38,v[248:251] offset:8192
qt 96,2,24576
qw 10
qm 56,100,128
qr 160,26,16384
qt 98,2,25088
buffer_load_dword v1,v8,s[32:35],s53 offen
qm 60,100,200
ds_write_b128 v39,v[252:255] offset:8192
qt 100,3,24576
qw 10
qm 48,104,132
qr 164,27,20480
qt 102,3,25088
qm 52,104,204
qt 104,2,28672
qw 9
qm 56,108,132
qw 2
s_barrier
qr 168,26,24576
qr 208,24,24576
qt 106,2,29184
qm 60,108,204
qr 212,25,28672
qt 108,3,28672
qm 48,96,136
qr 172,27,28672
v_add_f32 v16,v16,v17
qt 110,3,29184
qw 5
qm 52,96,208
qr 216,24,32768
v_add_f32 v17,v28,v29
qt 96,2,32768
qm 56,100,136
qr 176,26,32768
qr 220,25,36864
v_add_f32_dpp v16,v16,v16 quad_perm:[1,0,3,2] row_mask:0xf bank_mask:0xf
qt 98,2,33280
buffer_load_dword v1,v8,s[80:83],0 offen
qm 60,100,208
v_add_f32 v33,v33,v35
qt 100,3,32768
qw 10
qm 48,104,140
qr 180,27,36864
qr 224,24,40960
v_add_f32_dpp v17,v17,v17 quad_perm:[1,0,3,2] row_mask:0xf bank_mask:0xf
qt 102,3,33280
qw 12
qm 52,104,212
qr 228,25,45056
v_add_f32_dpp v16,v16,v16 quad_perm:[2,3,0,1] row_mask:0xf bank_mask:0xf
qt 104,2,36864
qw 11
qm 56,108,140
v_add_f32 v34,v34,v36
qt 106,2,37376
qm 60,108,212
qr 232,24,49152
v_add_f32_dpp v17,v17,v17 quad_perm:[2,3,0,1] row_mask:0xf bank_mask:0xf
qt 108,3,36864
qw 9
qm 48,96,144
qr 236,25,53248
qt 110,3,37376
qm 52,96,216
v_add_f32_dpp v16,v16,v16 row_half_mirror row_mask:0xf bank_mask:0xf
qt 96,2,40960
qw 8
qm 56,100,144
qr 240,24,57344
v_lshrrev_b32 v44,3,v0
qt 98,2,41472
qm 60,100,216
qr 244,25,61440
v_add_f32_dpp v17,v17,v17 row_half_mirror row_mask:0xf bank_mask:0xf
qt 100,3,40960
qw 9
qm 48,104,148
v_mul_f32 v16,0x3ae38e39,v16
qt 102,3,41472
qm 52,104,220
qr 248,26,8192
v_fma_f32 v16,-v33,v33,v16
qt 104,2,45056
qw 8
qm 56,108,148
qr 252,27,12288
v_max_f32 v16,0,v16
qt 106,2,45568
qm 60,108,220
v_mul_f32 v17,0x3ae38e39,v17
qt 108,3,45056
qw 8
qm 48,96,152
v_fma_f32 v17,-v34,v34,v17
qt 110,3,45568
s_add_u32 s15,s17,128
buffer_load_dword v1,v6,s[40:43],s15 offen
s_add_u32 s15,s17,128
buffer_load_dword v1,v6,s[44:47],s15 offen
qm 52,96,224
qt 96,2,49152
qw 7
qm 56,100,152
v_max_f32 v17,0,v17
qt 98,2,49664
qm 60,100,224
v_cvt_pk_bf16_f32 v42,v16,v17
qt 100,3,49152
qw 5
qm 48,104,156
v_lshlrev_b32 v16,16,v42
qt 102,3,49664
qm 52,104,228
v_and_b32 v17,0xffff0000,v42
qt 104,2,53248
qw 5
qm 56,108,156
s_barrier
v_add_f32 v16,0x3727c5ac,v16
qt 106,2,53760
qm 60,108,228
v_add_f32 v17,0x3727c5ac,v17
qt 108,3,53248
qw 5
qm 48,96,160
v_cvt_pk_bf16_f32 v42,v16,v17
qt 110,3,53760
qm 52,96,232
v_lshlrev_b32 v16,16,v42
qt 96,2,57344
qw 5
qm 56,100,160
qt 98,2,57856
qm 60,100,232
v_and_b32 v17,0xffff0000,v42
qt 100,3,57344
qw 5
qm 48,104,164
v_sqrt_f32 v16,v16
qt 102,3,57856
qm 52,104,236
v_sqrt_f32 v17,v17
qt 104,2,61440
qw 5
qm 56,108,164
v_and_b32 v43,4,v44
qt 106,2,61952
qm 60,108,236
v_cvt_pk_bf16_f32 v42,v16,v17
qt 108,3,61440
qw 5
qm 48,96,168
v_lshlrev_b32 v16,16,v42
qt 110,3,61952
qm 52,96,240
v_and_b32 v17,0xffff0000,v42
qt 96,4,0
qw 5
qm 56,100,168
v_readfirstlane_b32 s3,v13
s_mul_i32 s16,s3,0x9E3779B1
s_add_u32 s16,s16,0x7F4A7C15
v_rcp_f32 v16,v16
qt 98,4,512
qm 60,100,240
qt 100,5,0
qw 5
qm 48,104,172
v_rcp_f32 v17,v17
v_bfe_u32 v45,v0,2,2
qt 102,5,512
qm 52,104,244
v_add3_u32 v44,v43,v44,s29
v_lshl_add_u32 v45,s7,2,v45
qt 104,4,4096
qw 5
qm 56,108,172
v_lshlrev_b32 v43,2,v44
v_and_b32 v14,3,v0
qt 106,4,4608
qm 60,108,244
v_add_u32 v43,122880,v43
v_lshl_add_u32 v45,v45,3,v14
qt 108,5,4096
qw 5
qm 48,96,176
ds_write_b32 v43,v16
v_lshlrev_b32 v45,2,v45
qt 110,5,4608
qm 52,96,248
ds_write_b32 v43,v17 offset:64
v_add_u32 v45,122880,v45
qt 96,4,8192
qw 7
qm 56,100,176
ds_write_b32 v43,v35 offset:512
qt 98,4,8704
qm 60,100,248
ds_write_b32 v43,v36 offset:576
qt 100,5,8192
qw 9
qm 48,104,180
qt 102,5,8704
qm 52,104,252
qt 104,4,12288
qw 8
qm 56,108,180
qw 3
s_barrier
ds_read_b32 v10,v45
ds_read_b32 v11,v45 offset:16
qt 106,4,12800
qm 60,108,252
qt 108,5,12288
qz 80,96,112
qw 3
v_mul_f32 v14,v10,v10
v_mul_f32 v12,0xbdd2d3e8,v10
v_mul_f32 v10,0xc0135761,v10
v_mul_f32 v12,v12,v14
v_rcp_f32 v14,v14
qw 2
v_mul_f32 v15,v11,v11
v_mul_f32 v13,0xbdd2d3e8,v11
v_mul_f32 v11,0xc0135761,v11
v_mul_f32 v13,v13,v15
v_rcp_f32 v15,v15
qt 110,5,12800
s_setprio 0
qz 84,96,184
v_mul_f32 v16,v48,v48
qt 96,2,0
qz 88,100,112
v_fma_f32 v16,v16,v12,v10
qt 98,2,512
qz 92,100,184
v_mul_f32 v16,v16,v48
qt 100,3,0
s_add_u32 s15,s17,256
buffer_load_dword v1,v6,s[40:43],s15 offen
qw 5
qm 80,104,116
v_exp_f32 v16,v16
s_lshl_b32 s30,s10,3
s_add_u32 s30,s30,s8
s_mul_i32 s30,s30,196608
s_add_u32 s76,s30,21020672
s_add_u32 s77,s30,21118976
s_lshl_b32 s30,s7,13
s_add_u32 s79,s76,s30
qt 102,3,512
qm 84,104,188
v_fma_f32 v16,v16,v14,v14
qt 104,2,4096
qw 5
qm 88,108,116
v_rcp_f32 v16,v16
qt 106,2,4608
qm 92,108,188
v_mul_f32 v48,v48,v16
qt 108,3,4096
qw 5
qm 80,96,120
v_mul_f32 v16,v49,v49
qt 110,3,4608
qm 84,96,192
v_fma_f32 v16,v16,v12,v10
qt 96,2,8192
qw 5
qm 88,100,120
v_mul_f32 v16,v16,v49
qt 98,2,8704
qm 92,100,192
v_exp_f32 v16,v16
qt 100,3,8192
qw 5
qm 80,104,124
v_fma_f32 v16,v16,v14,v14
qt 102,3,8704
qm 84,104,196
v_rcp_f32 v16,v16
qt 104,2,12288
qw 5
qm 88,108,124
v_mul_f32 v49,v49,v16
qt 106,2,12800
qm 92,108,196
v_mul_f32 v16,v50,v50
qt 108,3,12288
qw 5
qm 80,96,128
v_fma_f32 v16,v16,v12,v10
qt 110,3,12800
qm 84,96,200
v_mul_f32 v16,v16,v50
qt 96,2,16384
qw 5
qm 88,100,128
v_exp_f32 v16,v16
qt 98,2,16896
qm 92,100,200
v_fma_f32 v16,v16,v14,v14
qt 100,3,16384
qw 5
qm 80,104,132
v_rcp_f32 v16,v16
qt 102,3,16896
qm 84,104,204
v_mul_f32 v50,v50,v16
qt 104,2,20480
qw 5
qm 88,108,132
s_barrier
v_mul_f32 v16,v51,v51
qt 106,2,20992
qm 92,108,204
v_fma_f32 v16,v16,v12,v10
qt 108,3,20480
qw 5
qm 80,96,136
v_mul_f32 v16,v16,v51
qt 110,3,20992
qm 84,96,208
v_exp_f32 v16,v16
qt 96,2,24576
qw 5
qm 88,100,136
v_fma_f32 v16,v16,v14,v14
qt 98,2,25088
qm 92,100,208
v_rcp_f32 v16,v16
qt 100,3,24576
s_add_u32 s15,s17,-768
buffer_load_dword v1,v6,s[40:43],s15 offen
qw 5
qm 80,104,140
v_mul_f32 v51,v51,v16
qt 102,3,25088
qm 84,104,212
v_mul_f32 v16,v56,v56
qt 104,2,28672
qw 5
qm 88,108,140
v_fma_f32 v16,v16,v12,v10
qt 106,2,29184
qm 92,108,212
v_mul_f32 v16,v16,v56
qt 108,3,28672
qw 5
qm 80,96,144
v_exp_f32 v16,v16
qt 110,3,29184
qm 84,96,216
v_fma_f32 v16,v16,v14,v14
qt 96,2,32768
qw 5
qm 88,100,144
v_rcp_f32 v16,v16
qt 98,2,33280
qm 92,100,216
v_mul_f32 v56,v56,v16
qt 100,3,32768
qw 5
qm 80,104,148
v_mul_f32 v16,v57,v57
qt 102,3,33280
qm 84,104,220
v_fma_f32 v16,v16,v12,v10
qt 104,2,36864
qw 5
qm 88,108,148
v_mul_f32 v16,v16,v57
qt 106,2,37376
qm 92,108,220
v_exp_f32 v16,v16
qt 108,3,36864
qw 5
qm 80,96,152
v_fma_f32 v16,v16,v14,v14
qt 110,3,37376
qm 84,96,224
v_rcp_f32 v16,v16
qt 96,2,40960
qw 5
qm 88,100,152
v_mul_f32 v57,v57,v16
qt 98,2,41472
qm 92,100,224
v_mul_f32 v16,v58,v58
qt 100,3,40960
qw 5
qm 80,104,156
v_fma_f32 v16,v16,v12,v10
qt 102,3,41472
qm 84,104,228
v_mul_f32 v16,v16,v58
qt 104,2,45056
qw 5
qm 88,108,156
s_barrier
v_exp_f32 v16,v16
qt 106,2,45568
qm 92,108,228
v_fma_f32 v16,v16,v14,v14
qt 108,3,45056
qw 5
qm 80,96,160
v_rcp_f32 v16,v16
qt 110,3,45568
qm 84,96,232
v_mul_f32 v58,v58,v16
qt 96,4,16384
qw 5
qm 88,100,160
v_mul_f32 v16,v59,v59
qt 98,4,16896
qm 92,100,232
v_fma_f32 v16,v16,v12,v10
qt 100,5,16384
qw 5
qm 80,104,164
v_mul_f32 v16,v16,v59
qt 102,5,16896
qm 84,104,236
v_exp_f32 v16,v16
qt 104,4,20480
qw 5
qm 88,108,164
v_fma_f32 v16,v16,v14,v14
qt 106,4,20992
qm 92,108,236
v_rcp_f32 v16,v16
qt 108,5,20480
qw 5
qm 80,96,168
v_mul_f32 v59,v59,v16
qt 110,5,20992
qm 84,96,240
v_mul_f32 v16,v52,v52
qt 96,4,24576
qw 5
qm 88,100,168
v_fma_f32 v16,v16,v13,v11
qt 98,4,25088
qm 92,100,240
v_mul_f32 v16,v16,v52
qt 100,5,24576
qw 5
qm 80,104,172
v_exp_f32 v16,v16
qt 102,5,25088
qm 84,104,244
v_fma_f32 v16,v16,v15,v15
qt 104,4,28672
qw 5
qm 88,108,172
v_rcp_f32 v16,v16
qt 106,4,29184
qm 92,108,244
v_mul_f32 v52,v52,v16
qt 108,5,28672
qw 5
qm 80,96,176
v_mul_f32 v16,v53,v53
qt 110,5,29184
qm 84,96,248
v_fma_f32 v16,v16,v13,v11
qt 96,4,32768
qw 5
qm 88,100,176
v_mul_f32 v16,v16,v53
qt 98,4,33280
qm 92,100,248
v_exp_f32 v16,v16
qt 100,5,32768
qw 5
qm 80,104,180
v_fma_f32 v16,v16,v15,v15
qt 102,5,33280
qm 84,104,252
v_rcp_f32 v16,v16
qt 104,4,36864
qw 5
qm 88,108,180
s_barrier
v_mul_f32 v53,v53,v16
qt 106,4,37376
qm 92,108,252
qt 108,5,36864
qw 5
qz 64,96,112
v_mul_f32 v16,v54,v54
qt 110,5,37376
qz 68,96,184
v_fma_f32 v16,v16,v13,v11
qt 96,4,40960
qw 5
qz 72,100,112
v_mul_f32 v16,v16,v54
qt 98,4,41472
qz 76,100,184
v_exp_f32 v16,v16
qt 100,5,40960
s_add_u32 s15,s17,256
buffer_load_dword v1,v6,s[44:47],s15 offen
qw 5
qm 64,104,116
v_fma_f32 v16,v16,v15,v15
qt 102,5,41472
qm 68,104,188
v_rcp_f32 v16,v16
qt 104,4,45056
qw 5
qm 72,108,116
v_mul_f32 v54,v54,v16
qt 106,4,45568
qm 76,108,188
v_mul_f32 v16,v55,v55
qt 108,5,45056
qw 5
qm 64,96,120
v_fma_f32 v16,v16,v13,v11
qt 110,5,45568
qm 68,96,192
v_mul_f32 v16,v16,v55
qt 96,4,49152
qw 5
qm 72,100,120
v_exp_f32 v16,v16
qt 98,4,49664
qm 76,100,192
v_fma_f32 v16,v16,v15,v15
qt 100,5,49152
qw 5
qm 64,104,124
v_rcp_f32 v16,v16
qt 102,5,49664
qm 68,104,196
v_mul_f32 v55,v55,v16
qt 104,4,53248
qw 5
qm 72,108,124
v_mul_f32 v16,v60,v60
qt 106,4,53760
qm 76,108,196
v_fma_f32 v16,v16,v13,v11
qt 108,5,53248
qw 5
qm 64,96,128
v_mul_f32 v16,v16,v60
qt 110,5,53760
qm 68,96,200
v_exp_f32 v16,v16
qt 96,2,49152
qw 5
qm 72,100,128
v_fma_f32 v16,v16,v15,v15
qt 98,2,49664
qm 76,100,200
v_rcp_f32 v16,v16
qt 100,3,49152
qw 5
qm 64,104,132
v_mul_f32 v60,v60,v16
qt 102,3,49664
qm 68,104,204
v_mul_f32 v16,v61,v61
qt 104,2,53248
qw 5
qm 72,108,132
s_barrier
v_fma_f32 v16,v16,v13,v11
qt 106,2,53760
qm 76,108,204
v_mul_f32 v16,v16,v61
qt 108,3,53248
qw 5
qm 64,96,136
v_exp_f32 v16,v16
qt 110,3,53760
qm 68,96,208
v_fma_f32 v16,v16,v15,v15
qt 96,2,57344
qw 5
qm 72,100,136
v_rcp_f32 v16,v16
qt 98,2,57856
qm 76,100,208
v_mul_f32 v61,v61,v16
qt 100,3,57344
s_add_u32 s15,s17,-768
buffer_load_dword v1,v6,s[44:47],s15 offen
qw 5
qm 64,104,140
v_mul_f32 v16,v62,v62
qt 102,3,57856
qm 68,104,212
v_fma_f32 v16,v16,v13,v11
qt 104,2,61440
qw 5
qm 72,108,140
v_mul_f32 v16,v16,v62
qt 106,2,61952
qm 76,108,212
v_exp_f32 v16,v16
qt 108,3,61440
qw 5
qm 64,96,144
v_fma_f32 v16,v16,v15,v15
qt 110,3,61952
qm 68,96,216
v_rcp_f32 v16,v16
qt 96,4,0
qw 5
qm 72,100,144
v_mul_f32 v62,v62,v16
qt 98,4,512
qm 76,100,216
v_mul_f32 v16,v63,v63
qt 100,5,0
qw 5
qm 64,104,148
v_fma_f32 v16,v16,v13,v11
qt 102,5,512
qm 68,104,220
v_mul_f32 v16,v16,v63
qt 104,4,4096
qw 5
qm 72,108,148
v_exp_f32 v16,v16
qt 106,4,4608
qm 76,108,220
v_fma_f32 v16,v16,v15,v15
qt 108,5,4096
qw 5
qm 64,96,152
v_rcp_f32 v16,v16
qt 110,5,4608
qm 68,96,224
v_mul_f32 v63,v63,v16
qt 96,4,8192
qw 5
qm 72,100,152
v_mul_f32 v48,v48,v80
qt 98,4,8704
qm 76,100,224
v_mul_f32 v49,v49,v81
qt 100,5,8192
qw 5
qm 64,104,156
v_cvt_pk_bf16_f32 v48,v48,v49
qt 102,5,8704
qm 68,104,228
v_mul_f32 v50,v50,v82
qt 104,4,12288
qw 5
qm 72,108,156
s_barrier
v_mul_f32 v51,v51,v83
qt 106,4,12800
qm 76,108,228
v_cvt_pk_bf16_f32 v49,v50,v51
qt 108,5,12288
qw 5
qm 64,96,160
v_mul_f32 v56,v56,v88
qt 110,5,12800
qm 68,96,232
v_mul_f32 v57,v57,v89
qt 96,4,16384
qw 5
qm 72,100,160
v_cvt_pk_bf16_f32 v50,v56,v57
qt 98,4,16896
qm 76,100,232
v_mul_f32 v58,v58,v90
qt 100,5,16384
qw 5
qm 64,104,164
v_mul_f32 v59,v59,v91
qt 102,5,16896
qm 68,104,236
v_cvt_pk_bf16_f32 v51,v58,v59
qt 104,4,20480
qw 5
qm 72,108,164
ds_write_b128 v19,v[48:51] offset:8192
v_mul_f32 v52,v52,v84
qt 106,4,20992
qm 76,108,236
s_add_u32 s15,s76,8192
buffer_store_dwordx4 v[48:51],v18,s[88:91],s15 offen offset:0 sc1
v_mul_f32 v53,v53,v85
qt 108,5,20480
qw 6
qm 64,96,168
v_cvt_pk_bf16_f32 v52,v52,v53
qt 110,5,20992
qm 68,96,240
v_mul_f32 v54,v54,v86
qt 96,4,24576
qw 6
qm 72,100,168
v_mul_f32 v55,v55,v87
qt 98,4,25088
qm 76,100,240
v_cvt_pk_bf16_f32 v53,v54,v55
qt 100,5,24576
qw 5
qm 64,104,172
v_mul_f32 v60,v60,v92
qt 102,5,25088
qm 68,104,244
v_mul_f32 v61,v61,v93
qt 104,4,28672
qw 5
qm 72,108,172
v_cvt_pk_bf16_f32 v54,v60,v61
qt 106,4,29184
qm 76,108,244
v_mul_f32 v62,v62,v94
qt 108,5,28672
qw 5
qm 64,96,176
v_mul_f32 v63,v63,v95
qt 110,5,29184
qm 68,96,248
v_cvt_pk_bf16_f32 v55,v62,v63
qt 96,2,0
qw 5
qm 72,100,176
ds_write_b128 v19,v[52:55] offset:9216
qt 98,2,512
qm 76,100,248
s_add_u32 s15,s76,8192
buffer_store_dwordx4 v[52:55],v18,s[88:91],s15 offen offset:1024 sc1
qt 100,3,0
qw 6
qm 64,104,180
qt 102,3,512
qm 68,104,252
qt 104,2,4096
qw 6
qm 72,108,180
qw 4
s_barrier
qt 106,2,4608
qm 76,108,252
qt 108,3,4096
qw 5
qz 80,96,112
qt 110,3,4608
qz 84,96,184
v_mul_f32 v16,v64,v64
qt 96,4,32768
qw 5
qz 88,100,112
v_fma_f32 v16,v16,v12,v10
qt 98,4,33280
qz 92,100,184
v_mul_f32 v16,v16,v64
qt 100,5,32768
s_add_u32 s15,s17,384
buffer_load_dword v1,v6,s[40:43],s15 offen
qw 5
qm 80,104,116
v_exp_f32 v16,v16
qt 102,5,33280
qm 84,104,188
v_fma_f32 v16,v16,v14,v14
qt 104,4,36864
qw 5
qm 88,108,116
v_rcp_f32 v16,v16
qt 106,4,37376
qm 92,108,188
v_mul_f32 v64,v64,v16
qt 108,5,36864
qw 5
qm 80,96,120
v_mul_f32 v16,v65,v65
qt 110,5,37376
qm 84,96,192
v_fma_f32 v16,v16,v12,v10
qt 96,4,40960
qw 5
qm 88,100,120
v_mul_f32 v16,v16,v65
qt 98,4,41472
qm 92,100,192
v_exp_f32 v16,v16
qt 100,5,40960
qw 5
qm 80,104,124
v_fma_f32 v16,v16,v14,v14
qt 102,5,41472
qm 84,104,196
v_rcp_f32 v16,v16
qt 104,4,45056
qw 5
qm 88,108,124
v_mul_f32 v65,v65,v16
qt 106,4,45568
qm 92,108,196
v_mul_f32 v16,v66,v66
qt 108,5,45056
qw 5
qm 80,96,128
v_fma_f32 v16,v16,v12,v10
qt 110,5,45568
qm 84,96,200
v_mul_f32 v16,v16,v66
qt 96,4,49152
qw 5
qm 88,100,128
v_exp_f32 v16,v16
qt 98,4,49664
qm 92,100,200
v_fma_f32 v16,v16,v14,v14
qt 100,5,49152
qw 5
qm 80,104,132
v_rcp_f32 v16,v16
qt 102,5,49664
qm 84,104,204
v_mul_f32 v66,v66,v16
qt 104,4,53248
qw 5
qm 88,108,132
s_barrier
v_mul_f32 v16,v67,v67
qt 106,4,53760
qm 92,108,204
v_fma_f32 v16,v16,v12,v10
qt 108,5,53248
qw 5
qm 80,96,136
v_mul_f32 v16,v16,v67
qt 110,5,53760
qm 84,96,208
v_exp_f32 v16,v16
qt 96,2,8192
qw 5
qm 88,100,136
v_fma_f32 v16,v16,v14,v14
qt 98,2,8704
qm 92,100,208
v_rcp_f32 v16,v16
qt 100,3,8192
s_add_u32 s15,s17,-640
buffer_load_dword v1,v6,s[40:43],s15 offen
qw 5
qm 80,104,140
v_mul_f32 v67,v67,v16
qt 102,3,8704
qm 84,104,212
v_mul_f32 v16,v72,v72
qt 104,2,12288
qw 5
qm 88,108,140
v_fma_f32 v16,v16,v12,v10
qt 106,2,12800
qm 92,108,212
v_mul_f32 v16,v16,v72
qt 108,3,12288
qw 5
qm 80,96,144
v_exp_f32 v16,v16
qt 110,3,12800
qm 84,96,216
v_fma_f32 v16,v16,v14,v14
qt 96,2,16384
qw 5
qm 88,100,144
v_rcp_f32 v16,v16
qt 98,2,16896
qm 92,100,216
v_mul_f32 v72,v72,v16
qt 100,3,16384
qw 5
qm 80,104,148
v_mul_f32 v16,v73,v73
qt 102,3,16896
qm 84,104,220
v_fma_f32 v16,v16,v12,v10
qt 104,2,20480
qw 5
qm 88,108,148
v_mul_f32 v16,v16,v73
qt 106,2,20992
qm 92,108,220
v_exp_f32 v16,v16
qt 108,3,20480
qw 5
qm 80,96,152
v_fma_f32 v16,v16,v14,v14
qt 110,3,20992
qm 84,96,224
v_rcp_f32 v16,v16
qt 96,2,24576
qw 5
qm 88,100,152
v_mul_f32 v73,v73,v16
qt 98,2,25088
qm 92,100,224
v_mul_f32 v16,v74,v74
qt 100,3,24576
qw 5
qm 80,104,156
v_fma_f32 v16,v16,v12,v10
qt 102,3,25088
qm 84,104,228
v_mul_f32 v16,v16,v74
qt 104,2,28672
qw 5
qm 88,108,156
s_barrier
v_exp_f32 v16,v16
qt 106,2,29184
qm 92,108,228
v_fma_f32 v16,v16,v14,v14
qt 108,3,28672
qw 5
qm 80,96,160
v_rcp_f32 v16,v16
qt 110,3,29184
qm 84,96,232
v_mul_f32 v74,v74,v16
qt 96,4,16384
qw 5
qm 88,100,160
v_mul_f32 v16,v75,v75
qt 98,4,16896
qm 92,100,232
v_fma_f32 v16,v16,v12,v10
qt 100,5,16384
qw 5
qm 80,104,164
v_mul_f32 v16,v16,v75
qt 102,5,16896
qm 84,104,236
v_exp_f32 v16,v16
qt 104,4,20480
qw 5
qm 88,108,164
v_fma_f32 v16,v16,v14,v14
qt 106,4,20992
qm 92,108,236
v_rcp_f32 v16,v16
qt 108,5,20480
qw 5
qm 80,96,168
v_mul_f32 v75,v75,v16
qt 110,5,20992
qm 84,96,240
v_mul_f32 v16,v68,v68
qt 96,4,24576
qw 5
qm 88,100,168
v_fma_f32 v16,v16,v13,v11
qt 98,4,25088
qm 92,100,240
v_mul_f32 v16,v16,v68
qt 100,5,24576
qw 5
qm 80,104,172
v_exp_f32 v16,v16
qt 102,5,25088
qm 84,104,244
v_fma_f32 v16,v16,v15,v15
qt 104,4,28672
qw 5
qm 88,108,172
v_rcp_f32 v16,v16
qt 106,4,29184
qm 92,108,244
v_mul_f32 v68,v68,v16
qt 108,5,28672
qw 5
qm 80,96,176
v_mul_f32 v16,v69,v69
qt 110,5,29184
qm 84,96,248
v_fma_f32 v16,v16,v13,v11
qt 96,2,32768
qw 5
qm 88,100,176
v_mul_f32 v16,v16,v69
qt 98,2,33280
qm 92,100,248
v_exp_f32 v16,v16
qt 100,3,32768
qw 5
qm 80,104,180
v_fma_f32 v16,v16,v15,v15
qt 102,3,33280
qm 84,104,252
v_rcp_f32 v16,v16
qt 104,2,36864
qw 5
qm 88,108,180
s_barrier
v_mul_f32 v69,v69,v16
qt 106,2,37376
qm 92,108,252
qt 108,3,36864
qw 5
qz 48,96,112
v_mul_f32 v16,v70,v70
qt 110,3,37376
qz 52,96,184
v_fma_f32 v16,v16,v13,v11
qt 96,4,32768
qw 5
qz 56,100,112
v_mul_f32 v16,v16,v70
qt 98,4,33280
qz 60,100,184
v_exp_f32 v16,v16
qt 100,5,32768
s_add_u32 s15,s17,384
buffer_load_dword v1,v6,s[44:47],s15 offen
qw 5
qm 48,104,116
v_fma_f32 v16,v16,v15,v15
qt 102,5,33280
qm 52,104,188
v_rcp_f32 v16,v16
qt 104,4,36864
qw 5
qm 56,108,116
v_mul_f32 v70,v70,v16
qt 106,4,37376
qm 60,108,188
v_mul_f32 v16,v71,v71
qt 108,5,36864
qw 5
qm 48,96,120
v_fma_f32 v16,v16,v13,v11
qt 110,5,37376
qm 52,96,192
v_mul_f32 v16,v16,v71
qt 96,4,40960
qw 5
qm 56,100,120
v_exp_f32 v16,v16
qt 98,4,41472
qm 60,100,192
v_fma_f32 v16,v16,v15,v15
qt 100,5,40960
qw 5
qm 48,104,124
v_rcp_f32 v16,v16
qt 102,5,41472
qm 52,104,196
v_mul_f32 v71,v71,v16
qt 104,4,45056
qw 5
qm 56,108,124
v_mul_f32 v16,v76,v76
qt 106,4,45568
qm 60,108,196
v_fma_f32 v16,v16,v13,v11
qt 108,5,45056
qw 5
qm 48,96,128
v_mul_f32 v16,v16,v76
qt 110,5,45568
qm 52,96,200
v_exp_f32 v16,v16
qt 96,2,40960
qw 5
qm 56,100,128
v_fma_f32 v16,v16,v15,v15
qt 98,2,41472
qm 60,100,200
v_rcp_f32 v16,v16
qt 100,3,40960
qw 5
qm 48,104,132
v_mul_f32 v76,v76,v16
qt 102,3,41472
qm 52,104,204
v_mul_f32 v16,v77,v77
qt 104,2,45056
qw 5
qm 56,108,132
s_barrier
v_fma_f32 v16,v16,v13,v11
qt 106,2,45568
qm 60,108,204
v_mul_f32 v16,v16,v77
qt 108,3,45056
qw 5
qm 48,96,136
v_exp_f32 v16,v16
qt 110,3,45568
qm 52,96,208
v_fma_f32 v16,v16,v15,v15
qt 96,4,49152
qw 5
qm 56,100,136
v_rcp_f32 v16,v16
qt 98,4,49664
qm 60,100,208
v_mul_f32 v77,v77,v16
qt 100,5,49152
s_add_u32 s15,s17,-640
buffer_load_dword v1,v6,s[44:47],s15 offen
qw 5
qm 48,104,140
v_mul_f32 v16,v78,v78
qt 102,5,49664
qm 52,104,212
v_fma_f32 v16,v16,v13,v11
qt 104,4,53248
qw 5
qm 56,108,140
v_mul_f32 v16,v16,v78
qt 106,4,53760
qm 60,108,212
v_exp_f32 v16,v16
qt 108,5,53248
qw 5
qm 48,96,144
v_fma_f32 v16,v16,v15,v15
qt 110,5,53760
qm 52,96,216
v_rcp_f32 v16,v16
qt 96,2,49152
qw 5
qm 56,100,144
v_mul_f32 v78,v78,v16
qt 98,2,49664
qm 60,100,216
v_mul_f32 v16,v79,v79
qt 100,3,49152
qw 5
qm 48,104,148
v_fma_f32 v16,v16,v13,v11
qt 102,3,49664
qm 52,104,220
v_mul_f32 v16,v16,v79
qt 104,2,53248
qw 5
qm 56,108,148
v_exp_f32 v16,v16
qt 106,2,53760
qm 60,108,220
v_fma_f32 v16,v16,v15,v15
qt 108,3,53248
qw 5
qm 48,96,152
v_rcp_f32 v16,v16
qt 110,3,53760
qm 52,96,224
v_mul_f32 v79,v79,v16
qt 96,2,57344
qw 5
qm 56,100,152
v_mul_f32 v64,v64,v80
qt 98,2,57856
qm 60,100,224
v_mul_f32 v65,v65,v81
qt 100,3,57344
qw 5
qm 48,104,156
v_cvt_pk_bf16_f32 v64,v64,v65
qt 102,3,57856
qm 52,104,228
v_mul_f32 v66,v66,v82
qt 104,2,61440
qw 5
qm 56,108,156
s_barrier
v_mul_f32 v67,v67,v83
qt 106,2,61952
qm 60,108,228
v_cvt_pk_bf16_f32 v65,v66,v67
qt 108,3,61440
qw 5
qm 48,96,160
v_mul_f32 v72,v72,v88
qt 110,3,61952
qm 52,96,232
v_mul_f32 v73,v73,v89
qt 96,4,16384
qw 5
qm 56,100,160
v_cvt_pk_bf16_f32 v66,v72,v73
qt 98,4,16896
qm 60,100,232
v_mul_f32 v74,v74,v90
qt 100,5,16384
qw 5
qm 48,104,164
v_mul_f32 v75,v75,v91
qt 102,5,16896
qm 52,104,236
v_cvt_pk_bf16_f32 v67,v74,v75
qt 104,4,20480
qw 5
qm 56,108,164
ds_write_b128 v18,v[64:67] offset:57344
v_mul_f32 v68,v68,v84
qt 106,4,20992
qm 60,108,236
s_add_u32 s15,s76,24576
buffer_store_dwordx4 v[64:67],v18,s[88:91],s15 offen offset:0 sc1
v_mul_f32 v69,v69,v85
qt 108,5,20480
qw 6
qm 48,96,168
v_cvt_pk_bf16_f32 v68,v68,v69
qt 110,5,20992
qm 52,96,240
v_mul_f32 v70,v70,v86
qt 96,4,24576
qw 6
qm 56,100,168
v_mul_f32 v71,v71,v87
qt 98,4,25088
qm 60,100,240
v_cvt_pk_bf16_f32 v69,v70,v71
qt 100,5,24576
qw 5
qm 48,104,172
v_mul_f32 v76,v76,v92
qt 102,5,25088
qm 52,104,244
v_mul_f32 v77,v77,v93
qt 104,4,28672
qw 5
qm 56,108,172
v_cvt_pk_bf16_f32 v70,v76,v77
qt 106,4,29184
qm 60,108,244
v_mul_f32 v78,v78,v94
qt 108,5,28672
qw 5
qm 48,96,176
v_mul_f32 v79,v79,v95
qt 110,5,29184
qm 52,96,248
v_cvt_pk_bf16_f32 v71,v78,v79
qt 96,4,0
qw 5
qm 56,100,176
ds_write_b128 v18,v[68:71] offset:58368
qt 98,4,512
qm 60,100,248
s_add_u32 s15,s76,24576
buffer_store_dwordx4 v[68:71],v18,s[88:91],s15 offen offset:1024 sc1
qt 100,5,0
qw 6
qm 48,104,180
qt 102,5,512
qm 52,104,252
qt 104,4,4096
qw 6
qm 56,108,180
qw 4
s_barrier
qt 106,4,4608
qm 60,108,252
qt 108,5,4096
qw 5
qz 80,96,112
qt 110,5,4608
qz 84,96,184
v_mul_f32 v16,v48,v48
qt 96,4,32768
qw 5
qz 88,100,112
v_fma_f32 v16,v16,v12,v10
qt 98,4,33280
qz 92,100,184
v_mul_f32 v16,v16,v48
qt 100,5,32768
v_lshrrev_b32 v8,5,v0
v_mul_u32_u24 v8,640,v8
v_sub_u32 v6,v6,v8
s_add_u32 s15,s17,512
buffer_load_dword v1,v6,s[40:43],s15 offen
qw 5
qm 80,104,116
v_exp_f32 v16,v16
qt 102,5,33280
qm 84,104,188
v_fma_f32 v16,v16,v14,v14
qt 104,4,36864
qw 5
qm 88,108,116
v_rcp_f32 v16,v16
qt 106,4,37376
qm 92,108,188
v_mul_f32 v48,v48,v16
qt 108,5,36864
qw 5
qm 80,96,120
v_mul_f32 v16,v49,v49
qt 110,5,37376
qm 84,96,192
v_fma_f32 v16,v16,v12,v10
qt 96,4,40960
qw 5
qm 88,100,120
v_mul_f32 v16,v16,v49
qt 98,4,41472
qm 92,100,192
v_exp_f32 v16,v16
qt 100,5,40960
qw 5
qm 80,104,124
v_fma_f32 v16,v16,v14,v14
qt 102,5,41472
qm 84,104,196
v_rcp_f32 v16,v16
qt 104,4,45056
qw 5
qm 88,108,124
v_mul_f32 v49,v49,v16
qt 106,4,45568
qm 92,108,196
v_mul_f32 v16,v50,v50
qt 108,5,45056
qw 5
qm 80,96,128
v_fma_f32 v16,v16,v12,v10
qt 110,5,45568
qm 84,96,200
v_mul_f32 v16,v16,v50
qt 96,4,8192
qw 5
qm 88,100,128
v_exp_f32 v16,v16
qt 98,4,8704
qm 92,100,200
v_fma_f32 v16,v16,v14,v14
qt 100,5,8192
qw 5
qm 80,104,132
v_rcp_f32 v16,v16
qt 102,5,8704
qm 84,104,204
v_mul_f32 v50,v50,v16
qt 104,4,12288
qw 5
qm 88,108,132
s_barrier
v_mul_f32 v16,v51,v51
qt 106,4,12800
qm 92,108,204
v_fma_f32 v16,v16,v12,v10
qt 108,5,12288
qw 5
qm 80,96,136
v_mul_f32 v16,v16,v51
qt 110,5,12800
qm 84,96,208
v_exp_f32 v16,v16
qt 96,4,49152
qw 5
qm 88,100,136
v_fma_f32 v16,v16,v14,v14
qt 98,4,49664
qm 92,100,208
v_rcp_f32 v16,v16
qt 100,5,49152
qw 5
qm 80,104,140
v_mul_f32 v51,v51,v16
qt 102,5,49664
qm 84,104,212
v_mul_f32 v16,v56,v56
qt 104,4,53248
qw 5
qm 88,108,140
v_fma_f32 v16,v16,v12,v10
qt 106,4,53760
qm 92,108,212
v_mul_f32 v16,v16,v56
qt 108,5,53248
qw 5
qm 80,96,144
v_exp_f32 v16,v16
qt 110,5,53760
qm 84,96,216
v_fma_f32 v16,v16,v14,v14
qt 96,2,0
qw 5
qm 88,100,144
v_rcp_f32 v16,v16
qt 98,2,512
qm 92,100,216
v_mul_f32 v56,v56,v16
qt 100,3,0
qw 5
qm 80,104,148
v_mul_f32 v16,v57,v57
qt 102,3,512
qm 84,104,220
v_fma_f32 v16,v16,v12,v10
qt 104,2,4096
qw 5
qm 88,108,148
v_mul_f32 v16,v16,v57
qt 106,2,4608
qm 92,108,220
v_exp_f32 v16,v16
qt 108,3,4096
qw 5
qm 80,96,152
v_fma_f32 v16,v16,v14,v14
qt 110,3,4608
qm 84,96,224
v_rcp_f32 v16,v16
qt 96,2,8192
qw 5
qm 88,100,152
v_mul_f32 v57,v57,v16
qt 98,2,8704
qm 92,100,224
v_mul_f32 v16,v58,v58
qt 100,3,8192
qw 5
qm 80,104,156
v_fma_f32 v16,v16,v12,v10
qt 102,3,8704
qm 84,104,228
v_mul_f32 v16,v16,v58
qt 104,2,12288
qw 5
qm 88,108,156
s_barrier
v_exp_f32 v16,v16
qt 106,2,12800
qm 92,108,228
v_fma_f32 v16,v16,v14,v14
qt 108,3,12288
qw 5
qm 80,96,160
v_rcp_f32 v16,v16
qt 110,3,12800
qm 84,96,232
v_mul_f32 v58,v58,v16
qt 96,4,16384
qw 5
qm 88,100,160
v_mul_f32 v16,v59,v59
qt 98,4,16896
qm 92,100,232
v_fma_f32 v16,v16,v12,v10
qt 100,5,16384
qw 5
qm 80,104,164
v_mul_f32 v16,v16,v59
qt 102,5,16896
qm 84,104,236
v_exp_f32 v16,v16
qt 104,4,20480
qw 5
qm 88,108,164
v_fma_f32 v16,v16,v14,v14
qt 106,4,20992
qm 92,108,236
v_rcp_f32 v16,v16
qt 108,5,20480
qw 5
qm 80,96,168
v_mul_f32 v59,v59,v16
qt 110,5,20992
qm 84,96,240
v_mul_f32 v16,v52,v52
qt 96,4,24576
qw 5
qm 88,100,168
v_fma_f32 v16,v16,v13,v11
qt 98,4,25088
qm 92,100,240
v_mul_f32 v16,v16,v52
qt 100,5,24576
qw 5
qm 80,104,172
v_exp_f32 v16,v16
qt 102,5,25088
qm 84,104,244
v_fma_f32 v16,v16,v15,v15
qt 104,4,28672
qw 5
qm 88,108,172
v_rcp_f32 v16,v16
qt 106,4,29184
qm 92,108,244
v_mul_f32 v52,v52,v16
qt 108,5,28672
qw 5
qm 80,96,176
v_mul_f32 v16,v53,v53
qt 110,5,29184
qm 84,96,248
v_fma_f32 v16,v16,v13,v11
qt 96,2,16384
qw 5
qm 88,100,176
v_mul_f32 v16,v16,v53
qt 98,2,16896
qm 92,100,248
v_exp_f32 v16,v16
qt 100,3,16384
qw 5
qm 80,104,180
v_fma_f32 v16,v16,v15,v15
qt 102,3,16896
qm 84,104,252
v_rcp_f32 v16,v16
qt 104,2,20480
qw 5
qm 88,108,180
s_barrier
v_mul_f32 v53,v53,v16
qt 106,2,20992
qm 92,108,252
qt 108,3,20480
qw 5
qz 64,96,112
v_mul_f32 v16,v54,v54
qt 110,3,20992
qz 68,96,184
v_fma_f32 v16,v16,v13,v11
v_bfe_u32 v44,v0,2,2
qt 96,4,32768
qw 5
qz 72,100,112
v_mul_f32 v16,v16,v54
qt 98,4,33280
qz 76,100,184
v_exp_f32 v16,v16
qt 100,5,32768
s_add_u32 s15,s17,512
buffer_load_dword v1,v6,s[44:47],s15 offen
s_lshl_b32 s54,s10,2
s_add_u32 s54,s54,s7
s_mul_i32 s54,s54,6912
s_mul_i32 s53,s8,663552
s_add_u32 s54,s54,s53
s_add_u32 s54,s54,221184
v_min_u32 v7,3328,v7
buffer_load_dword v1,v7,s[48:51],s54 offen
qw 5
qm 64,104,116
v_fma_f32 v16,v16,v15,v15
qt 102,5,33280
qm 68,104,188
v_rcp_f32 v16,v16
s_and_b32 s33,s8,3
s_lshl_b32 s33,s33,2
s_add_u32 s33,s33,s7
s_lshl_b32 s33,s33,3
s_lshl_b32 s33,s33,2
s_add_u32 s33,s33,122880
v_lshl_add_u32 v45,v44,2,s33
qt 104,4,36864
qw 5
qm 72,108,116
v_mul_f32 v54,v54,v16
qt 106,4,37376
qm 76,108,188
v_mul_f32 v16,v55,v55
qt 108,5,36864
qw 5
qm 64,96,120
v_fma_f32 v16,v16,v13,v11
qt 110,5,37376
qm 68,96,192
v_mul_f32 v16,v16,v55
qt 96,4,40960
qw 5
qm 72,100,120
v_exp_f32 v16,v16
ds_read_b32 v40,v45 offset:0
qt 98,4,41472
qm 76,100,192
v_fma_f32 v16,v16,v15,v15
qt 100,5,40960
qw 6
qm 64,104,124
v_rcp_f32 v16,v16
qt 102,5,41472
qm 68,104,196
v_mul_f32 v55,v55,v16
qt 104,4,45056
qw 6
qm 72,108,124
v_mul_f32 v16,v60,v60
ds_read_b32 v41,v45 offset:512
qt 106,4,45568
qm 76,108,196
v_fma_f32 v16,v16,v13,v11
qt 108,5,45056
qw 6
qm 64,96,128
v_mul_f32 v16,v16,v60
qt 110,5,45568
qm 68,96,200
v_exp_f32 v16,v16
qt 96,2,24576
qw 6
qm 72,100,128
v_fma_f32 v16,v16,v15,v15
qt 98,2,25088
qm 76,100,200
v_rcp_f32 v16,v16
qt 100,3,24576
qw 5
qm 64,104,132
v_mul_f32 v60,v60,v16
qt 102,3,25088
qm 68,104,204
v_mul_f32 v16,v61,v61
qt 104,2,28672
qw 5
qm 72,108,132
s_barrier
v_fma_f32 v16,v16,v13,v11
qt 106,2,29184
qm 76,108,204
v_mul_f32 v16,v16,v61
qt 108,3,28672
qw 5
qm 64,96,136
v_exp_f32 v16,v16
qt 110,3,29184
qm 68,96,208
v_fma_f32 v16,v16,v15,v15
qt 96,4,49152
qw 5
qm 72,100,136
v_rcp_f32 v16,v16
qt 98,4,49664
qm 76,100,208
v_mul_f32 v61,v61,v16
qt 100,5,49152
qw 5
qm 64,104,140
v_mul_f32 v16,v62,v62
qt 102,5,49664
qm 68,104,212
v_fma_f32 v16,v16,v13,v11
qt 104,4,53248
qw 5
qm 72,108,140
v_mul_f32 v16,v16,v62
qt 106,4,53760
qm 76,108,212
v_exp_f32 v16,v16
qt 108,5,53248
qw 5
qm 64,96,144
v_fma_f32 v16,v16,v15,v15
qt 110,5,53760
qm 68,96,216
v_rcp_f32 v16,v16
qt 96,2,32768
qw 5
qm 72,100,144
v_mul_f32 v62,v62,v16
qt 98,2,33280
qm 76,100,216
v_mul_f32 v16,v63,v63
qt 100,3,32768
qw 5
qm 64,104,148
v_fma_f32 v16,v16,v13,v11
qt 102,3,33280
qm 68,104,220
v_mul_f32 v16,v16,v63
qt 104,2,36864
qw 5
qm 72,108,148
v_exp_f32 v16,v16
qt 106,2,37376
qm 76,108,220
v_fma_f32 v16,v16,v15,v15
qt 108,3,36864
qw 5
qm 64,96,152
v_rcp_f32 v16,v16
qt 110,3,37376
qm 68,96,224
v_mul_f32 v63,v63,v16
qt 96,2,40960
qw 5
qm 72,100,152
v_mul_f32 v48,v48,v80
qt 98,2,41472
qm 76,100,224
v_mul_f32 v49,v49,v81
qt 100,3,40960
qw 5
qm 64,104,156
v_cvt_pk_bf16_f32 v48,v48,v49
qt 102,3,41472
qm 68,104,228
v_mul_f32 v50,v50,v82
qt 104,2,45056
qw 5
qm 72,108,156
s_barrier
v_mul_f32 v51,v51,v83
qt 106,2,45568
qm 76,108,228
v_cvt_pk_bf16_f32 v49,v50,v51
qt 108,3,45056
qw 5
qm 64,96,160
v_mul_f32 v56,v56,v88
qt 110,3,45568
qm 68,96,232
v_mul_f32 v57,v57,v89
qt 96,4,16384
qw 5
qm 72,100,160
v_cvt_pk_bf16_f32 v50,v56,v57
qt 98,4,16896
qm 76,100,232
v_mul_f32 v58,v58,v90
qt 100,5,16384
qw 5
qm 64,104,164
v_mul_f32 v59,v59,v91
qt 102,5,16896
qm 68,104,236
v_cvt_pk_bf16_f32 v51,v58,v59
qt 104,4,20480
qw 5
qm 72,108,164
ds_write_b128 v18,v[48:51] offset:40960
v_mul_f32 v52,v52,v84
qt 106,4,20992
qm 76,108,236
s_add_u32 s15,s76,40960
buffer_store_dwordx4 v[48:51],v18,s[88:91],s15 offen offset:0 sc1
v_mul_f32 v53,v53,v85
qt 108,5,20480
qw 6
qm 64,96,168
v_cvt_pk_bf16_f32 v52,v52,v53
qt 110,5,20992
qm 68,96,240
v_mul_f32 v54,v54,v86
qt 96,4,24576
qw 6
qm 72,100,168
v_mul_f32 v55,v55,v87
qt 98,4,25088
qm 76,100,240
v_cvt_pk_bf16_f32 v53,v54,v55
qt 100,5,24576
qw 5
qm 64,104,172
v_mul_f32 v60,v60,v92
qt 102,5,25088
qm 68,104,244
v_mul_f32 v61,v61,v93
qt 104,4,28672
qw 5
qm 72,108,172
v_cvt_pk_bf16_f32 v54,v60,v61
qt 106,4,29184
qm 76,108,244
v_mul_f32 v62,v62,v94
qt 108,5,28672
qw 5
qm 64,96,176
v_mul_f32 v63,v63,v95
qt 110,5,29184
qm 68,96,248
v_cvt_pk_bf16_f32 v55,v62,v63
qt 96,2,49152
qw 5
qm 72,100,176
ds_write_b128 v18,v[52:55] offset:41984
qt 98,2,49664
qm 76,100,248
s_add_u32 s15,s76,40960
buffer_store_dwordx4 v[52:55],v18,s[88:91],s15 offen offset:1024 sc1
qt 100,3,49152
qw 6
qm 64,104,180
qt 102,3,49664
qm 68,104,252
qt 104,2,53248
qw 6
qm 72,108,180
qw 4
s_barrier
qt 106,2,53760
qm 76,108,252
qt 108,3,53248
qw 5
qz 80,96,112
qt 110,3,53760
qz 84,96,184
v_mul_f32 v16,v64,v64
qt 96,4,32768
qw 5
qz 88,100,112
v_fma_f32 v16,v16,v12,v10
qt 98,4,33280
qz 92,100,184
v_mul_f32 v16,v16,v64
qt 100,5,32768
s_add_u32 s54,s54,3456
buffer_load_dword v1,v7,s[48:51],s54 offen
qw 5
qm 80,104,116
v_exp_f32 v16,v16
qt 102,5,33280
qm 84,104,188
v_fma_f32 v16,v16,v14,v14
qt 104,4,36864
qw 5
qm 88,108,116
v_rcp_f32 v16,v16
qt 106,4,37376
qm 92,108,188
v_mul_f32 v64,v64,v16
qt 108,5,36864
qw 5
qm 80,96,120
v_mul_f32 v16,v65,v65
qt 110,5,37376
qm 84,96,192
v_fma_f32 v16,v16,v12,v10
qt 96,4,40960
qw 5
qm 88,100,120
v_mul_f32 v16,v16,v65
qt 98,4,41472
qm 92,100,192
v_exp_f32 v16,v16
qt 100,5,40960
qw 5
qm 80,104,124
v_fma_f32 v16,v16,v14,v14
qt 102,5,41472
qm 84,104,196
v_rcp_f32 v16,v16
qt 104,4,45056
qw 5
qm 88,108,124
v_mul_f32 v65,v65,v16
qt 106,4,45568
qm 92,108,196
v_mul_f32 v16,v66,v66
qt 108,5,45056
qw 5
qm 80,96,128
v_fma_f32 v16,v16,v12,v10
qt 110,5,45568
qm 84,96,200
v_mul_f32 v16,v16,v66
qt 96,2,57344
qw 5
qm 88,100,128
v_exp_f32 v16,v16
qt 98,2,57856
qm 92,100,200
v_fma_f32 v16,v16,v14,v14
qt 100,3,57344
qw 5
qm 80,104,132
v_rcp_f32 v16,v16
qt 102,3,57856
qm 84,104,204
v_mul_f32 v66,v66,v16
qt 104,2,61440
qw 5
qm 88,108,132
s_barrier
v_mul_f32 v16,v67,v67
qt 106,2,61952
qm 92,108,204
v_fma_f32 v16,v16,v12,v10
qt 108,3,61440
qw 5
qm 80,96,136
v_mul_f32 v16,v16,v67
qt 110,3,61952
qm 84,96,208
v_exp_f32 v16,v16
qt 96,4,0
qw 5
qm 88,100,136
v_fma_f32 v16,v16,v14,v14
qt 98,4,512
qm 92,100,208
v_rcp_f32 v16,v16
qt 100,5,0
qw 5
qm 80,104,140
v_mul_f32 v67,v67,v16
qt 102,5,512
qm 84,104,212
v_mul_f32 v16,v72,v72
qt 104,4,4096
qw 5
qm 88,108,140
v_fma_f32 v16,v16,v12,v10
qt 106,4,4608
qm 92,108,212
v_mul_f32 v16,v16,v72
qt 108,5,4096
qw 5
qm 80,96,144
v_exp_f32 v16,v16
qt 110,5,4608
qm 84,96,216
v_fma_f32 v16,v16,v14,v14
qt 96,4,8192
qw 5
qm 88,100,144
v_rcp_f32 v16,v16
qt 98,4,8704
qm 92,100,216
v_mul_f32 v72,v72,v16
qt 100,5,8192
qw 5
qm 80,104,148
v_mul_f32 v16,v73,v73
qt 102,5,8704
qm 84,104,220
v_fma_f32 v16,v16,v12,v10
qt 104,4,12288
qw 5
qm 88,108,148
v_mul_f32 v16,v16,v73
qt 106,4,12800
qm 92,108,220
v_exp_f32 v16,v16
qt 108,5,12288
qw 5
qm 80,96,152
v_fma_f32 v16,v16,v14,v14
qt 110,5,12800
qm 84,96,224
v_rcp_f32 v16,v16
qt 96,2,0
qw 5
qm 88,100,152
v_mul_f32 v73,v73,v16
qt 98,2,512
qm 92,100,224
v_mul_f32 v16,v74,v74
qt 100,3,0
qw 5
qm 80,104,156
v_fma_f32 v16,v16,v12,v10
qt 102,3,512
qm 84,104,228
v_mul_f32 v16,v16,v74
qt 104,2,4096
qw 5
qm 88,108,156
s_barrier
v_exp_f32 v16,v16
qt 106,2,4608
qm 92,108,228
v_fma_f32 v16,v16,v14,v14
qt 108,3,4096
qw 5
qm 80,96,160
v_rcp_f32 v16,v16
qt 110,3,4608
qm 84,96,232
v_mul_f32 v74,v74,v16
qt 96,4,16384
qw 5
qm 88,100,160
v_mul_f32 v16,v75,v75
qt 98,4,16896
qm 92,100,232
v_fma_f32 v16,v16,v12,v10
qt 100,5,16384
qw 5
qm 80,104,164
v_mul_f32 v16,v16,v75
qt 102,5,16896
qm 84,104,236
v_exp_f32 v16,v16
qt 104,4,20480
qw 5
qm 88,108,164
v_fma_f32 v16,v16,v14,v14
qt 106,4,20992
qm 92,108,236
v_rcp_f32 v16,v16
qt 108,5,20480
qw 5
qm 80,96,168
v_mul_f32 v75,v75,v16
qt 110,5,20992
qm 84,96,240
v_mul_f32 v16,v68,v68
qt 96,4,24576
qw 5
qm 88,100,168
v_fma_f32 v16,v16,v13,v11
qt 98,4,25088
qm 92,100,240
v_mul_f32 v16,v16,v68
qt 100,5,24576
qw 5
qm 80,104,172
v_exp_f32 v16,v16
qt 102,5,25088
qm 84,104,244
v_fma_f32 v16,v16,v15,v15
qt 104,4,28672
qw 5
qm 88,108,172
v_rcp_f32 v16,v16
qt 106,4,29184
qm 92,108,244
v_mul_f32 v68,v68,v16
qt 108,5,28672
qw 5
qm 80,96,176
v_mul_f32 v16,v69,v69
qt 110,5,29184
qm 84,96,248
v_fma_f32 v16,v16,v13,v11
qt 96,2,8192
qw 5
qm 88,100,176
v_mul_f32 v16,v16,v69
qt 98,2,8704
qm 92,100,248
v_exp_f32 v16,v16
qt 100,3,8192
qw 5
qm 80,104,180
v_fma_f32 v16,v16,v15,v15
qt 102,3,8704
qm 84,104,252
v_rcp_f32 v16,v16
qt 104,2,12288
qw 5
qm 88,108,180
s_barrier
v_mul_f32 v69,v69,v16
qt 106,2,12800
qm 92,108,252
qt 108,3,12288
qw 5
qz 48,96,112
v_mul_f32 v16,v70,v70
qt 110,3,12800
qz 52,96,184
v_fma_f32 v16,v16,v13,v11
qt 96,4,32768
qw 5
qz 56,100,112
v_mul_f32 v16,v16,v70
v_mov_b32 v38,s16
qt 98,4,33280
qz 60,100,184
v_exp_f32 v16,v16
v_and_b32 v37,48,v0
qt 100,5,32768
qw 5
qm 48,104,116
v_fma_f32 v16,v16,v15,v15
qt 102,5,33280
qm 52,104,188
v_rcp_f32 v16,v16
v_lshlrev_b32 v37,24,v37
qt 104,4,36864
qw 5
qm 56,108,116
v_mul_f32 v70,v70,v16
qt 106,4,37376
qm 60,108,188
v_mul_f32 v16,v71,v71
v_lshl_or_b32 v37,v0,7,v37
qt 108,5,36864
qw 5
qm 48,96,120
v_fma_f32 v16,v16,v13,v11
s_mul_i32 s72,s10,2048
qt 110,5,37376
qm 52,96,192
v_mul_f32 v16,v16,v71
qt 96,4,40960
qw 5
qm 56,100,120
v_exp_f32 v16,v16
s_add_u32 s72,s72,8192
qt 98,4,41472
qm 60,100,192
v_fma_f32 v16,v16,v15,v15
qt 100,5,40960
qw 5
qm 48,104,124
v_rcp_f32 v16,v16
s_and_b32 s30,s8,3
qt 102,5,41472
qm 52,104,196
v_mul_f32 v71,v71,v16
qt 104,4,45056
qw 5
qm 56,108,124
v_mul_f32 v16,v76,v76
s_lshl_b32 s30,s30,4
qt 106,4,45568
qm 60,108,196
)ASM"
R"ASM(v_fma_f32 v16,v16,v13,v11
s_and_b32 s31,s8,4
qt 108,5,45056
qw 5
qm 48,96,128
v_mul_f32 v16,v16,v76
qt 110,5,45568
qm 52,96,200
v_exp_f32 v16,v16
s_add_u32 s30,s30,s31
qt 96,2,16384
qw 5
qm 56,100,128
v_fma_f32 v16,v16,v15,v15
qt 98,2,16896
qm 60,100,200
v_rcp_f32 v16,v16
s_add_u32 s68,s30,s72
qt 100,3,16384
qw 5
qm 48,104,132
v_mul_f32 v76,v76,v16
qt 102,3,16896
qm 52,104,204
v_mul_f32 v16,v77,v77
s_lshl_b32 s31,s8,8
qt 104,2,20480
qw 5
qm 56,108,132
s_barrier
v_fma_f32 v16,v16,v13,v11
s_add_u32 s78,s68,s31
qt 106,2,20992
qm 60,108,204
v_mul_f32 v16,v16,v77
qt 108,3,20480
qw 5
qm 48,96,136
v_exp_f32 v16,v16
s_add_u32 s78,s78,8
qt 110,3,20992
qm 52,96,208
v_fma_f32 v16,v16,v15,v15
qt 96,2,24576
qw 5
qm 56,100,136
v_rcp_f32 v16,v16
s_add_u32 s68,s68,0
qt 98,2,25088
qm 60,100,208
v_mul_f32 v77,v77,v16
qt 100,3,24576
qw 5
qm 48,104,140
v_mul_f32 v16,v78,v78
s_cmp_eq_u32 s6,4
s_cselect_b32 s68,s68,0x7ff00000
qt 102,3,25088
qm 52,104,212
v_fma_f32 v16,v16,v13,v11
s_add_u32 s72,s72,s31
qt 104,2,28672
qw 5
qm 56,108,140
v_mul_f32 v16,v16,v78
qt 106,2,29184
qm 60,108,212
v_exp_f32 v16,v16
s_add_u32 s72,s72,0
qt 108,3,28672
qw 5
qm 48,96,144
v_fma_f32 v16,v16,v15,v15
qt 110,3,29184
qm 52,96,216
v_rcp_f32 v16,v16
s_add_u32 s74,s16,8
qt 96,2,32768
qw 5
qm 56,100,144
v_mul_f32 v78,v78,v16
qt 98,2,33280
qm 60,100,216
v_mul_f32 v16,v79,v79
v_and_b32 v42,15,v0
qt 100,3,32768
qw 5
qm 48,104,148
v_fma_f32 v16,v16,v13,v11
v_lshrrev_b32 v43,5,v0
qt 102,3,33280
qm 52,104,220
v_mul_f32 v16,v16,v79
qt 104,2,36864
qw 5
qm 56,108,148
v_exp_f32 v16,v16
v_bfe_u32 v25,v0,4,1
qt 106,2,37376
qm 60,108,220
v_fma_f32 v16,v16,v15,v15
qt 108,3,36864
qw 5
qm 48,96,152
v_rcp_f32 v16,v16
v_lshlrev_b32 v25,3,v25
qt 110,3,37376
qm 52,96,224
v_mul_f32 v79,v79,v16
qt 96,2,40960
qw 5
qm 56,100,152
v_mul_f32 v64,v64,v80
v_add_u32 v25,2048,v25
qt 98,2,41472
qm 60,100,224
v_mul_f32 v65,v65,v81
v_lshl_add_u32 v24,v42,7,v25
qt 100,3,40960
qw 5
qm 48,104,156
v_cvt_pk_bf16_f32 v64,v64,v65
qt 102,3,41472
qm 52,104,228
v_mul_f32 v66,v66,v82
v_lshl_add_u32 v22,v42,5,v25
qt 104,2,45056
qw 5
qm 56,108,156
s_waitcnt vmcnt(0)
s_barrier
v_mul_f32 v67,v67,v83
v_add_u32 v38,1,v38
buffer_store_dword v38,v37,s[96:99],s68 offen sc1
qt 106,2,45568
qm 60,108,228
v_cvt_pk_bf16_f32 v65,v66,v67
v_lshrrev_b32 v42,1,v42
qt 108,3,45056
qw 5
qm 48,96,160
v_mul_f32 v72,v72,v88
qt 110,3,45568
qm 52,96,232
v_mul_f32 v73,v73,v89
v_xor_b32 v25,v42,v43
qt 96,4,16384
qw 5
qm 56,100,160
v_cvt_pk_bf16_f32 v66,v72,v73
v_lshl_add_u32 v24,v25,4,v24
qt 98,4,16896
qm 60,100,232
v_mul_f32 v74,v74,v90
qt 100,5,16384
qw 5
qm 48,104,164
v_mul_f32 v75,v75,v91
v_lshrrev_b32 v42,2,v42
qt 102,5,16896
qm 52,104,236
v_cvt_pk_bf16_f32 v67,v74,v75
qt 104,4,20480
qw 5
qm 56,108,164
ds_write_b128 v18,v[64:67] offset:24576
v_mul_f32 v68,v68,v84
v_xor_b32 v42,v42,v43
qt 106,4,20992
qm 60,108,236
s_add_u32 s15,s76,57344
buffer_store_dwordx4 v[64:67],v18,s[88:91],s15 offen offset:0 sc1
v_mul_f32 v69,v69,v85
qt 108,5,20480
qw 6
qm 48,96,168
v_cvt_pk_bf16_f32 v68,v68,v69
v_lshl_add_u32 v22,v42,4,v22
qt 110,5,20992
qm 52,96,240
v_mul_f32 v70,v70,v86
v_add_u32 v23,0x10000,v22
qt 96,4,24576
qw 6
qm 56,100,168
v_mul_f32 v71,v71,v87
qt 98,4,25088
qm 60,100,240
v_cvt_pk_bf16_f32 v69,v70,v71
v_xor_b32 v25,32,v24
qt 100,5,24576
qw 5
qm 48,104,172
v_mul_f32 v76,v76,v92
qt 102,5,25088
qm 52,104,244
v_mul_f32 v77,v77,v93
v_xor_b32 v26,64,v24
qt 104,4,28672
qw 5
qm 56,108,172
v_cvt_pk_bf16_f32 v70,v76,v77
qt 106,4,29184
qm 60,108,244
v_mul_f32 v78,v78,v94
v_xor_b32 v27,0x60,v24
qt 108,5,28672
qw 5
qm 48,96,176
v_mul_f32 v79,v79,v95
v_lshlrev_b32 v36,3,v0
qt 110,5,29184
qm 52,96,248
v_cvt_pk_bf16_f32 v71,v78,v79
qt 96,2,49152
qw 5
qm 56,100,176
ds_write_b128 v18,v[68:71] offset:25600
v_add_u32 v36,s13,v36
qt 98,2,49664
qm 60,100,248
s_add_u32 s15,s76,57344
buffer_store_dwordx4 v[68:71],v18,s[88:91],s15 offen offset:1024 sc1
qt 100,3,49152
qw 6
qm 48,104,180
v_add_u32 v36,147456,v36
qt 102,3,49664
qm 52,104,252
qt 104,2,53248
qw 6
qm 56,108,180
qw 4
s_barrier
qt 106,2,53760
qm 60,108,252
qt 108,3,53248
qw 5
qz 80,96,112
qt 110,3,53760
qz 84,96,184
v_mul_f32 v16,v48,v48
qt 96,2,57344
qw 5
qz 88,100,112
v_fma_f32 v16,v16,v12,v10
qt 98,2,57856
qz 92,100,184
v_mul_f32 v16,v16,v48
qt 100,3,57344
qw 5
qm 80,104,116
v_exp_f32 v16,v16
qt 102,3,57856
qm 84,104,188
v_fma_f32 v16,v16,v14,v14
qt 104,2,61440
qw 5
qm 88,108,116
v_rcp_f32 v16,v16
qt 106,2,61952
qm 92,108,188
v_mul_f32 v48,v48,v16
qt 108,3,61440
qw 5
qm 80,96,120
v_mul_f32 v16,v49,v49
qt 110,3,61952
qm 84,96,192
v_fma_f32 v16,v16,v12,v10
qr 112,19,0
qt 96,4,0
qw 6
qm 88,100,120
v_mul_f32 v16,v16,v49
qt 98,4,512
qm 92,100,192
v_exp_f32 v16,v16
qr 184,19,1024
qt 100,5,0
qw 7
qm 80,104,124
v_fma_f32 v16,v16,v14,v14
qt 102,5,512
qm 84,104,196
v_rcp_f32 v16,v16
qr 116,19,8192
qt 104,4,4096
qw 8
qm 88,108,124
v_mul_f32 v49,v49,v16
qt 106,4,4608
qm 92,108,196
v_mul_f32 v16,v50,v50
qr 188,19,9216
qt 108,5,4096
qw 8
qm 80,96,128
v_fma_f32 v16,v16,v12,v10
qt 110,5,4608
qm 84,96,200
v_mul_f32 v16,v16,v50
qr 120,18,49152
qt 96,4,8192
qw 8
qm 88,100,128
v_exp_f32 v16,v16
qt 98,4,8704
qm 92,100,200
v_fma_f32 v16,v16,v14,v14
qr 192,18,50176
qt 100,5,8192
qw 8
qm 80,104,132
v_rcp_f32 v16,v16
qt 102,5,8704
qm 84,104,204
v_mul_f32 v50,v50,v16
qr 124,18,57344
qt 104,4,12288
qw 8
qm 88,108,132
qw 1
s_barrier
v_mul_f32 v16,v51,v51
qt 106,4,12800
qm 92,108,204
v_fma_f32 v16,v16,v12,v10
qr 196,18,58368
qt 108,5,12288
qm 80,96,136
v_mul_f32 v16,v16,v51
v_add_u32 v28,0x10000,v24
qt 110,5,12800
qm 84,96,208
v_exp_f32 v16,v16
v_add_u32 v29,0x10000,v25
qt 96,2,0
qm 88,100,136
v_fma_f32 v16,v16,v14,v14
qt 98,2,512
qm 92,100,208
v_rcp_f32 v16,v16
v_add_u32 v30,0x10000,v26
qt 100,3,0
qw 6
qm 80,104,140
v_mul_f32 v51,v51,v16
v_add_u32 v31,0x10000,v27
qt 102,3,512
qm 84,104,212
v_mul_f32 v16,v56,v56
qt 104,2,4096
qw 5
qm 88,108,140
v_fma_f32 v16,v16,v12,v10
qt 106,2,4608
qm 92,108,212
v_mul_f32 v16,v16,v56
qt 108,3,4096
qw 5
qm 80,96,144
v_exp_f32 v16,v16
qt 110,3,4608
qm 84,96,216
v_fma_f32 v16,v16,v14,v14
qt 96,2,8192
qw 5
qm 88,100,144
v_rcp_f32 v16,v16
qt 98,2,8704
qm 92,100,216
v_mul_f32 v56,v56,v16
qt 100,3,8192
qw 5
qm 80,104,148
v_mul_f32 v16,v57,v57
qt 102,3,8704
qm 84,104,220
v_fma_f32 v16,v16,v12,v10
qt 104,2,12288
qw 5
qm 88,108,148
v_mul_f32 v16,v16,v57
qt 106,2,12800
qm 92,108,220
v_exp_f32 v16,v16
qt 108,3,12288
qw 5
qm 80,96,152
v_fma_f32 v16,v16,v14,v14
qt 110,3,12800
qm 84,96,224
v_rcp_f32 v16,v16
qt 96,2,16384
qw 5
qm 88,100,152
v_mul_f32 v57,v57,v16
s_mul_i32 s30,s10,1310720
qt 98,2,16896
qm 92,100,224
v_mul_f32 v16,v58,v58
s_lshl_b32 s31,s8,14
qt 100,3,16384
qw 5
qm 80,104,156
v_fma_f32 v16,v16,v12,v10
qt 102,3,16896
qm 84,104,228
v_mul_f32 v16,v16,v58
s_add_u32 s73,s30,s31
qt 104,2,20480
qw 5
qm 88,108,156
s_barrier
v_exp_f32 v16,v16
qt 106,2,20992
qm 92,108,228
v_fma_f32 v16,v16,v14,v14
s_add_u32 s73,s73,57344
qt 108,3,20480
qw 5
qm 80,96,160
v_rcp_f32 v16,v16
qt 110,3,20992
qm 84,96,232
v_mul_f32 v58,v58,v16
s_lshl_b32 s31,s8,11
qt 96,4,16384
qw 5
qm 88,100,160
v_mul_f32 v16,v59,v59
s_add_u32 s69,s30,s31
qt 98,4,16896
qm 92,100,232
v_fma_f32 v16,v16,v12,v10
qt 100,5,16384
qw 5
qm 80,104,164
v_mul_f32 v16,v16,v59
s_lshl_b32 s31,s7,8
qt 102,5,16896
qm 84,104,236
v_exp_f32 v16,v16
qt 104,4,20480
qw 5
qm 88,108,164
v_fma_f32 v16,v16,v14,v14
s_add_u32 s69,s69,s31
qt 106,4,20992
qm 92,108,236
v_rcp_f32 v16,v16
qt 108,5,20480
qw 5
qm 80,96,168
v_mul_f32 v59,v59,v16
s_add_u32 s69,s69,131072
qt 110,5,20992
qm 84,96,240
v_mul_f32 v16,v52,v52
s_and_b32 s75,s8,4
qt 96,4,24576
qw 5
qm 88,100,168
v_fma_f32 v16,v16,v13,v11
qt 98,4,25088
qm 92,100,240
v_mul_f32 v16,v16,v52
s_lshl_b32 s75,s75,4
qt 100,5,24576
qw 5
qm 80,104,172
v_exp_f32 v16,v16
qt 102,5,25088
qm 84,104,244
v_fma_f32 v16,v16,v15,v15
s_and_b32 s33,s8,3
s_lshl_b32 s33,s33,2
s_add_u32 s33,s33,s7
s_lshl_b32 s33,s33,3
s_lshl_b32 s30,s10,7
s_add_u32 s30,s30,s33
qt 104,4,28672
qw 5
qm 88,108,172
v_rcp_f32 v16,v16
qt 106,4,29184
qm 92,108,244
v_mul_f32 v52,v52,v16
s_add_u32 s30,s30,0
qt 108,5,28672
qw 5
qm 80,96,176
v_mul_f32 v16,v53,v53
s_mul_i32 s30,s30,1152
qt 110,5,29184
qm 84,96,248
v_fma_f32 v16,v16,v13,v11
qt 96,2,24576
qw 5
qm 88,100,176
v_mul_f32 v16,v16,v53
s_add_u32 s70,s30,s75
qt 98,2,25088
qm 92,100,248
v_exp_f32 v16,v16
qt 100,3,24576
qw 5
qm 80,104,180
buffer_load_dword v39,off,s[96:99],s78 sc1
v_fma_f32 v16,v16,v15,v15
s_lshl_b32 s71,s70,1
qt 102,3,25088
qm 84,104,252
v_rcp_f32 v16,v16
qt 104,2,28672
qw 5
qm 88,108,180
s_barrier
v_mul_f32 v53,v53,v16
qt 106,2,29184
qm 92,108,252
qt 108,3,28672
qw 5
qz 144,96,112
v_mul_f32 v16,v54,v54
v_mul_f32 v17,v55,v55
v_fma_f32 v16,v16,v13,v11
qt 110,3,29184
qz 216,96,184
v_fma_f32 v17,v17,v13,v11
v_mul_f32 v16,v16,v54
qt 96,2,32768
qw 5
qz 148,100,112
v_mul_f32 v17,v17,v55
v_exp_f32 v16,v16
qt 98,2,33280
qz 220,100,184
v_exp_f32 v17,v17
v_fma_f32 v16,v16,v15,v15
v_fma_f32 v17,v17,v15,v15
qt 100,3,32768
qw 5
qm 144,104,116
v_rcp_f32 v16,v16
v_rcp_f32 v17,v17
qt 102,3,33280
qm 216,104,188
v_mul_f32 v54,v54,v16
v_mul_f32 v55,v55,v17
qt 104,2,36864
qw 5
qm 148,108,116
v_mul_f32 v16,v60,v60
v_mul_f32 v17,v61,v61
v_fma_f32 v16,v16,v13,v11
qt 106,2,37376
qm 220,108,188
v_fma_f32 v17,v17,v13,v11
v_mul_f32 v16,v16,v60
qt 108,3,36864
qw 5
qz 152,96,112
v_mul_f32 v17,v17,v61
v_exp_f32 v16,v16
qt 110,3,37376
qz 224,96,184
v_exp_f32 v17,v17
v_fma_f32 v16,v16,v15,v15
v_fma_f32 v17,v17,v15,v15
qt 96,2,40960
qw 5
qz 156,100,112
v_rcp_f32 v16,v16
v_rcp_f32 v17,v17
qt 98,2,41472
qz 228,100,184
v_mul_f32 v60,v60,v16
v_mul_f32 v61,v61,v17
qt 100,3,40960
qw 5
qm 152,104,116
v_mul_f32 v16,v62,v62
v_mul_f32 v17,v63,v63
v_fma_f32 v16,v16,v13,v11
qt 102,3,41472
qm 224,104,188
v_fma_f32 v17,v17,v13,v11
v_mul_f32 v16,v16,v62
qt 104,2,45056
qw 5
qm 156,108,116
v_mul_f32 v17,v17,v63
v_exp_f32 v16,v16
qt 106,2,45568
qm 228,108,188
v_exp_f32 v17,v17
v_fma_f32 v16,v16,v15,v15
v_fma_f32 v17,v17,v15,v15
qt 108,3,45056
qw 5
qz 176,96,112
v_rcp_f32 v16,v16
v_rcp_f32 v17,v17
qt 110,3,45568
qz 248,96,184
v_mul_f32 v62,v62,v16
v_mul_f32 v63,v63,v17
qr 160,18,32768
qt 96,2,49152
qw 6
qz 180,100,112
v_mul_f32 v48,v48,v80
v_mul_f32 v49,v49,v81
v_cvt_pk_bf16_f32 v48,v48,v49
qt 98,2,49664
qz 252,100,184
v_mul_f32 v50,v50,v82
v_mul_f32 v51,v51,v83
qr 232,18,33792
qt 100,3,49152
qw 7
qm 176,104,116
v_cvt_pk_bf16_f32 v49,v50,v51
v_mul_f32 v56,v56,v88
qt 102,3,49664
qm 248,104,188
v_mul_f32 v57,v57,v89
v_cvt_pk_bf16_f32 v50,v56,v57
v_mul_f32 v58,v58,v90
qr 164,18,40960
qt 104,2,53248
qw 8
qm 180,108,116
s_waitcnt vmcnt(0)
s_mov_b32 s35,0
.L_wX01:
v_readfirstlane_b32 s34,v39
s_sub_u32 s34,s74,s34
s_cmp_le_u32 s34,7
s_cbranch_scc1 .L_wX01d
s_add_u32 s35,s35,1
s_cmp_gt_u32 s35,1000000
s_cbranch_scc1 .L_wX01d
s_sleep 2
buffer_load_dword v39,off,s[96:99],s78 sc1
s_waitcnt vmcnt(0)
s_branch .L_wX01
.L_wX01d:
qw 1
s_barrier
v_mul_f32 v59,v59,v91
v_cvt_pk_bf16_f32 v51,v58,v59
qt 106,2,53760
qm 252,108,188
s_add_u32 s15,s76,73728
buffer_store_dwordx4 v[48:51],v18,s[88:91],s15 offen offset:0 sc1
ds_write_b128 v19,v[48:51] offset:8192
qr 236,18,41984
qt 108,3,53248
qm 144,96,120
v_mul_f32 v52,v52,v84
v_mul_f32 v53,v53,v85
v_cvt_pk_bf16_f32 v52,v52,v53
qt 110,3,53760
qm 216,96,192
v_mul_f32 v54,v54,v86
v_mul_f32 v55,v55,v87
qt 96,2,57344
qm 148,100,120
v_cvt_pk_bf16_f32 v53,v54,v55
v_mul_f32 v60,v60,v92
qt 98,2,57856
qm 220,100,192
v_mul_f32 v61,v61,v93
v_cvt_pk_bf16_f32 v54,v60,v61
v_mul_f32 v62,v62,v94
s_add_u32 s15,s77,0
buffer_load_dwordx4 v[128:131],v18,s[88:91],s15 offen offset:0 sc1
qt 100,3,57344
qw 7
qm 144,104,124
v_mul_f32 v63,v63,v95
v_cvt_pk_bf16_f32 v55,v62,v63
qt 102,3,57856
qm 216,104,196
s_add_u32 s15,s76,73728
buffer_store_dwordx4 v[52:55],v18,s[88:91],s15 offen offset:1024 sc1
ds_write_b128 v19,v[52:55] offset:9216
qt 104,2,61440
qw 6
qm 148,108,124
qt 106,2,61952
qm 220,108,196
qt 108,3,61440
qw 6
qm 152,96,120
s_add_u32 s15,s77,0
buffer_load_dwordx4 v[200:203],v18,s[88:91],s15 offen offset:1024 sc1
qt 110,3,61952
qm 224,96,192
qt 96,4,0
qw 6
qm 156,100,120
qt 98,4,512
qm 228,100,192
qt 100,5,0
qw 5
qm 152,104,124
qt 102,5,512
qm 224,104,196
qt 104,4,4096
qw 5
qm 156,108,124
s_add_u32 s15,s77,8192
buffer_load_dwordx4 v[132:135],v18,s[88:91],s15 offen offset:0 sc1
qt 106,4,4608
qm 228,108,196
qt 108,5,4096
qw 5
qm 176,96,120
qt 110,5,4608
qm 248,96,192
qr 168,18,16384
qt 96,4,8192
qw 6
qm 180,100,120
qt 98,4,8704
qm 252,100,192
qr 240,18,17408
qt 100,5,8192
qw 7
qm 176,104,124
s_add_u32 s15,s77,8192
buffer_load_dwordx4 v[204:207],v18,s[88:91],s15 offen offset:1024 sc1
qt 102,5,8704
qm 248,104,196
qr 172,18,24576
qt 104,4,12288
qw 8
qm 180,108,124
qw 1
s_barrier
qt 106,4,12800
qm 252,108,196
qr 244,18,25600
qt 108,5,12288
qm 144,96,160
qt 110,5,12800
qm 216,96,232
qt 96,4,16384
qm 148,100,160
qt 98,4,16896
qm 220,100,232
qt 100,5,16384
qw 6
qm 144,104,164
qt 102,5,16896
qm 216,104,236
qt 104,4,20480
qw 5
qm 148,108,164
qt 106,4,20992
qm 220,108,236
qt 108,5,20480
qw 5
qm 152,96,160
qt 110,5,20992
qm 224,96,232
qr 120,19,0
qt 96,4,24576
qw 6
qm 156,100,160
qt 98,4,25088
qm 228,100,232
qr 192,19,1024
qt 100,5,24576
qw 7
qm 152,104,164
qt 102,5,25088
qm 224,104,236
qr 124,19,8192
qt 104,4,28672
qw 8
qm 156,108,164
qt 106,4,29184
qm 228,108,236
qr 196,19,9216
qt 108,5,28672
qw 8
qm 176,96,160
qt 110,5,29184
qm 248,96,232
qt 96,2,0
qw 7
qm 180,100,160
qt 98,2,512
qm 252,100,232
qt 100,3,0
qw 6
qm 176,104,164
qt 102,3,512
qm 248,104,236
qt 104,2,4096
qw 5
qm 180,108,164
s_waitcnt vmcnt(0)
s_barrier
v_add_u32 v38,1,v38
buffer_store_dword v38,v37,s[96:99],s68 offen sc1
qt 106,2,4608
qm 252,108,236
qt 108,3,4096
qw 5
qm 144,96,168
s_add_u32 s15,s77,16384
buffer_load_dwordx4 v[136:139],v18,s[88:91],s15 offen offset:0 sc1
v_and_b32 v1,0xfffffc3f,v18
qt 110,3,4608
qm 216,96,240
qt 96,2,8192
qw 5
qm 148,100,168
qt 98,2,8704
qm 220,100,240
qt 100,3,8192
qw 5
qm 144,104,172
v_lshrrev_b32 v16,4,v0
qt 102,3,8704
qm 216,104,244
qt 104,2,12288
qw 5
qm 148,108,172
s_add_u32 s15,s77,16384
buffer_load_dwordx4 v[208:211],v18,s[88:91],s15 offen offset:1024 sc1
qt 106,2,12800
qm 220,108,244
qt 108,3,12288
qw 5
qm 152,96,168
qt 110,3,12800
qm 224,96,240
v_lshl_add_u32 v1,v16,6,v1
qt 96,2,16384
qw 5
qm 156,100,168
qt 98,2,16896
qm 228,100,240
qt 100,3,16384
qw 5
qm 152,104,172
s_add_u32 s15,s77,24576
buffer_load_dwordx4 v[140:143],v18,s[88:91],s15 offen offset:0 sc1
qt 102,3,16896
qm 224,104,244
qt 104,2,20480
qw 5
qm 156,108,172
v_and_b32 v16,12,v0
qt 106,2,20992
qm 228,108,244
qt 108,3,20480
qw 5
qm 176,96,168
qt 110,3,20992
qm 248,96,240
qt 96,2,24576
qw 5
qm 180,100,168
s_add_u32 s15,s77,24576
buffer_load_dwordx4 v[212:215],v18,s[88:91],s15 offen offset:1024 sc1
qt 98,2,25088
qm 252,100,240
v_lshl_add_u32 v1,v16,6,v1
qt 100,3,24576
qw 5
qm 176,104,172
qt 102,3,25088
qm 248,104,244
qt 104,2,28672
qw 5
qm 180,108,172
s_barrier
qt 106,2,29184
qm 252,108,244
qt 108,3,28672
qw 5
qm 176,96,120
s_add_u32 s15,s77,65536
buffer_load_dwordx4 v[64:67],v18,s[88:91],s15 offen offset:0 sc1
qt 110,3,29184
qm 248,96,192
qt 96,2,32768
qw 5
qm 180,100,120
qt 98,2,33280
qm 252,100,192
qt 100,3,32768
qw 5
qm 176,104,124
qt 102,3,33280
qm 248,104,196
qt 104,2,36864
qw 5
qm 180,108,124
s_add_u32 s15,s77,65536
buffer_load_dwordx4 v[72:75],v18,s[88:91],s15 offen offset:1024 sc1
qt 106,2,37376
qm 252,108,196
qt 108,3,36864
qw 5
qm 144,96,120
qt 110,3,37376
qm 216,96,192
qt 96,2,40960
qw 5
qm 148,100,120
qt 98,2,41472
qm 220,100,192
qt 100,3,40960
qw 5
qm 144,104,124
s_add_u32 s15,s77,73728
buffer_load_dwordx4 v[68:71],v18,s[88:91],s15 offen offset:0 sc1
qt 102,3,41472
qm 216,104,196
qt 104,2,45056
qw 5
qm 148,108,124
qt 106,2,45568
qm 220,108,196
qt 108,3,45056
qw 5
qm 152,96,120
qt 110,3,45568
qm 224,96,192
qt 96,2,49152
qw 5
qm 156,100,120
s_add_u32 s15,s77,73728
buffer_load_dwordx4 v[76:79],v18,s[88:91],s15 offen offset:1024 sc1
qt 98,2,49664
qm 228,100,192
qt 100,3,49152
qw 5
qm 152,104,124
qt 102,3,49664
qm 224,104,196
qt 104,2,53248
qw 5
qm 156,108,124
s_barrier
qt 106,2,53760
qm 228,108,196
qt 108,3,53248
qw 5
qm 176,96,128
s_add_u32 s15,s77,81920
buffer_load_dwordx4 v[80:83],v18,s[88:91],s15 offen offset:0 sc1
qt 110,3,53760
qm 248,96,200
qt 96,4,16384
qw 5
qm 180,100,128
qt 98,4,16896
qm 252,100,200
qt 100,5,16384
qw 5
qm 176,104,132
qt 102,5,16896
qm 248,104,204
qt 104,4,20480
qw 5
qm 180,108,132
s_add_u32 s15,s77,81920
buffer_load_dwordx4 v[88:91],v18,s[88:91],s15 offen offset:1024 sc1
qt 106,4,20992
qm 252,108,204
qt 108,5,20480
qw 5
qm 144,96,128
qt 110,5,20992
qm 216,96,200
qt 96,4,24576
qw 5
qm 148,100,128
qt 98,4,25088
qm 220,100,200
qt 100,5,24576
qw 5
qm 144,104,132
s_add_u32 s15,s77,90112
buffer_load_dwordx4 v[84:87],v18,s[88:91],s15 offen offset:0 sc1
qt 102,5,25088
qm 216,104,204
qt 104,4,28672
qw 5
qm 148,108,132
qt 106,4,29184
qm 220,108,204
qt 108,5,28672
qw 5
qm 152,96,128
qt 110,5,29184
qm 224,96,200
qt 96,2,57344
qw 5
qm 156,100,128
s_add_u32 s15,s77,90112
buffer_load_dwordx4 v[92:95],v18,s[88:91],s15 offen offset:1024 sc1
qt 98,2,57856
qm 228,100,200
qt 100,3,57344
qw 5
qm 152,104,132
buffer_load_dword v39,off,s[96:99],s78 sc1
qt 102,3,57856
qm 224,104,204
qt 104,2,61440
qw 5
qm 156,108,132
s_barrier
qt 106,2,61952
qm 228,108,204
qt 108,3,61440
s_waitcnt vmcnt(12) lgkmcnt(5)
qm 176,96,136
v_and_b32 v15,48,v0
qt 110,3,61952
s_waitcnt vmcnt(11)
qm 248,96,208
qt 96,4,0
qw 5
qm 180,100,136
v_lshlrev_b32 v10,10,v15
qt 98,4,512
qm 252,100,208
qt 100,5,0
s_waitcnt vmcnt(10) lgkmcnt(5)
qm 176,104,140
qt 102,5,512
s_waitcnt vmcnt(9)
qm 248,104,212
v_and_b32 v16,16,v0
qt 104,4,4096
qw 5
qm 180,108,140
qt 106,4,4608
qm 252,108,212
qt 108,5,4096
qw 5
qm 144,96,136
v_lshrrev_b32 v17,5,v0
qt 110,5,4608
qm 216,96,208
qt 96,4,8192
qw 5
qm 148,100,136
v_lshl_add_u32 v12,v17,6,v16
qt 98,4,8704
qm 220,100,208
qt 100,5,8192
qw 5
qm 144,104,140
qt 102,5,8704
qm 216,104,212
v_and_b32 v16,3,v0
qt 104,4,12288
qw 5
qm 148,108,140
qt 106,4,12800
qm 220,108,212
qt 108,5,12288
qw 5
qm 152,96,136
v_lshl_add_u32 v12,v16,2,v12
qt 110,5,12800
qm 224,96,208
qd 96,24,0
qw 5
qm 156,100,136
v_lshl_add_u32 v10,v16,6,v10
qd 98,24,4096
qm 228,100,208
qd 100,25,0
qw 5
qm 152,104,140
qd 102,25,4096
qm 224,104,212
v_bfe_u32 v11,v0,2,2
qd 104,26,0
qw 5
qm 156,108,140
s_waitcnt vmcnt(0)
s_mov_b32 s35,0
.L_wX02:
v_readfirstlane_b32 s34,v39
s_sub_u32 s34,s74,s34
s_cmp_le_u32 s34,6
s_cbranch_scc1 .L_wX02d
s_add_u32 s35,s35,1
s_cmp_gt_u32 s35,1000000
s_cbranch_scc1 .L_wX02d
s_sleep 2
buffer_load_dword v39,off,s[96:99],s78 sc1
s_waitcnt vmcnt(0)
s_branch .L_wX02
.L_wX02d:
s_barrier
qd 106,26,4096
qm 228,108,212
qd 108,27,0
qw 5
qm 176,96,64
s_add_u32 s15,s77,32768
buffer_load_dwordx4 v[48:51],v18,s[88:91],s15 offen offset:0 sc1
v_lshl_add_u32 v10,v11,4,v10
qd 110,27,4096
qm 248,96,72
qd 96,24,8192
qw 5
qm 176,100,68
v_lshlrev_b32 v12,1,v12
qd 98,24,12288
qm 248,100,76
s_add_u32 s15,s77,32768
buffer_load_dwordx4 v[56:59],v18,s[88:91],s15 offen offset:1024 sc1
qd 100,25,8192
qw 5
qm 176,104,80
qd 102,25,12288
qm 248,104,88
v_mul_u32_u24 v11,1152,v11
qd 104,26,8192
qw 5
qm 176,108,84
s_add_u32 s15,s77,40960
buffer_load_dwordx4 v[52:55],v18,s[88:91],s15 offen offset:0 sc1
qd 106,26,12288
qm 248,108,92
qd 108,27,8192
qw 5
qm 144,96,64
v_add_u32 v11,v11,v12
qd 110,27,12288
qm 216,96,72
s_add_u32 s15,s77,40960
buffer_load_dwordx4 v[60:63],v18,s[88:91],s15 offen offset:1024 sc1
qd 96,24,16384
qw 5
qm 144,100,68
v_lshlrev_b32 v13,1,v11
qd 98,24,20480
qm 216,100,76
qd 100,25,16384
qw 5
qm 144,104,80
s_add_u32 s15,s77,49152
buffer_load_dwordx4 v[120:123],v18,s[88:91],s15 offen offset:0 sc1
qd 102,25,20480
qm 216,104,88
v_lshl_or_b32 v14,v17,30,v13
qd 104,26,16384
qw 5
qm 144,108,84
qd 106,26,20480
qm 216,108,92
s_add_u32 s15,s77,49152
buffer_load_dwordx4 v[192:195],v18,s[88:91],s15 offen offset:1024 sc1
qd 108,27,16384
qw 5
qm 152,96,64
s_and_b32 s30,s8,3
s_lshl_b32 s30,s30,4
s_mov_b64 s[32:33],0xffff
s_lshl_b64 s[32:33],s[32:33],s30
s_bitcmp1_b32 s8,2
s_cselect_b64 s[40:41],0,s[32:33]
s_cselect_b64 s[42:43],s[32:33],0
qd 110,27,20480
qm 224,96,72
qd 96,24,24576
qw 5
qm 152,100,68
s_add_u32 s15,s77,57344
buffer_load_dwordx4 v[124:127],v18,s[88:91],s15 offen offset:0 sc1
s_mov_b64 s[52:53],0
qd 98,24,28672
qm 224,100,76
qd 100,25,24576
qw 5
qm 152,104,80
qd 102,25,28672
qm 224,104,88
s_add_u32 s15,s77,57344
buffer_load_dwordx4 v[196:199],v18,s[88:91],s15 offen offset:1024 sc1
s_mov_b64 s[54:55],0
qd 104,26,24576
qw 5
qm 152,108,84
s_barrier
qd 106,26,28672
qm 224,108,92
qd 108,27,24576
qw 5
qm 180,96,64
s_mov_b64 s[56:57],0
qd 110,27,28672
qm 252,96,72
qd 96,28,16384
qw 5
qm 180,100,68
s_mov_b64 s[58:59],0
qd 98,28,20480
qm 252,100,76
qd 100,29,16384
qw 5
qm 180,104,80
qd 102,29,20480
qm 252,104,88
s_mov_b32 s76,0
qd 104,30,16384
qw 5
qm 180,108,84
qd 106,30,20480
qm 252,108,92
qd 108,31,16384
qw 5
qm 148,96,64
qd 110,31,20480
qm 220,96,72
qt 96,4,24576
qw 5
qm 148,100,68
qt 98,4,25088
qm 220,100,76
v_cvt_pk_bf16_f32 v176,v176,v177
qt 100,5,24576
qw 5
qm 148,104,80
v_cvt_pk_bf16_f32 v177,v178,v179
qt 102,5,25088
qm 220,104,88
v_cvt_pk_bf16_f32 v178,v180,v181
qt 104,4,28672
qw 5
qm 148,108,84
v_cvt_pk_bf16_f32 v179,v182,v183
qt 106,4,29184
qm 220,108,92
qt 108,5,28672
s_waitcnt vmcnt(7) lgkmcnt(5)
qm 144,96,48
v_cvt_pk_bf16_f32 v248,v248,v249
s_add_u32 s15,s73,524288
buffer_store_dwordx4 v[176:179],v1,s[88:91],s15 offen offset:0 sc1
qt 110,5,29184
s_waitcnt vmcnt(7)
qm 216,96,56
v_cvt_pk_bf16_f32 v249,v250,v251
qt 96,2,32768
qw 5
qm 148,100,48
v_cvt_pk_bf16_f32 v250,v252,v253
qt 98,2,33280
qm 220,100,56
v_cvt_pk_bf16_f32 v251,v254,v255
qt 100,3,32768
s_waitcnt vmcnt(6) lgkmcnt(5)
qm 144,104,52
qt 102,3,33280
s_waitcnt vmcnt(5)
qm 216,104,60
qt 104,2,36864
qw 5
qm 148,108,52
s_barrier
qt 106,2,37376
qm 220,108,60
qt 108,3,36864
s_waitcnt vmcnt(4) lgkmcnt(5)
qm 144,96,120
s_add_u32 s15,s73,524288
buffer_store_dwordx4 v[248:251],v1,s[88:91],s15 offen offset:1024 sc1
qt 110,3,37376
s_waitcnt vmcnt(4)
qm 216,96,192
qd 96,24,40960
qw 5
qm 148,100,120
qd 98,24,45056
qm 220,100,192
qd 100,25,40960
s_waitcnt vmcnt(3) lgkmcnt(5)
qm 144,104,124
qd 102,25,45056
s_waitcnt vmcnt(2)
qm 216,104,196
qd 104,26,40960
qw 5
qm 148,108,124
qd 106,26,45056
qm 220,108,196
qd 108,27,40960
qw 5
qm 156,96,64
qd 110,27,45056
qm 228,96,72
qt 96,2,49152
qw 5
qm 156,100,68
qt 98,2,49664
qm 228,100,76
v_cvt_pk_bf16_f32 v144,v144,v145
qt 100,3,49152
qw 5
qm 156,104,80
v_cvt_pk_bf16_f32 v145,v146,v147
qt 102,3,49664
qm 228,104,88
v_cvt_pk_bf16_f32 v146,v148,v149
qt 104,2,53248
qw 5
qm 156,108,84
v_cvt_pk_bf16_f32 v147,v150,v151
qt 106,2,53760
qm 228,108,92
qt 108,3,53248
qw 5
qm 152,96,48
v_cvt_pk_bf16_f32 v216,v216,v217
s_add_u32 s15,s73,0
buffer_store_dwordx4 v[144:147],v1,s[88:91],s15 offen offset:0 sc1
qt 110,3,53760
qm 224,96,56
v_cvt_pk_bf16_f32 v217,v218,v219
qt 96,2,57344
qw 5
qm 156,100,48
v_cvt_pk_bf16_f32 v218,v220,v221
qt 98,2,57856
qm 228,100,56
v_cvt_pk_bf16_f32 v219,v222,v223
qt 100,3,57344
qw 5
qm 152,104,52
qt 102,3,57856
qm 224,104,60
qt 104,2,61440
qw 5
qm 156,108,52
s_barrier
qt 106,2,61952
qm 228,108,60
qt 108,3,61440
qw 5
qm 152,96,120
s_add_u32 s15,s73,0
buffer_store_dwordx4 v[216:219],v1,s[88:91],s15 offen offset:1024 sc1
qt 110,3,61952
qm 224,96,192
qt 96,4,0
qw 5
qm 156,100,120
qt 98,4,512
qm 228,100,192
qt 100,5,0
qw 5
qm 152,104,124
qt 102,5,512
qm 224,104,196
qt 104,4,4096
qw 5
qm 156,108,124
qt 106,4,4608
qm 228,108,196
qt 108,5,4096
qw 5
qz 160,96,112
qt 110,5,4608
qz 232,96,184
qt 96,4,8192
qw 5
qz 164,100,112
qt 98,4,8704
qz 236,100,184
v_cvt_pk_bf16_f32 v152,v152,v153
qt 100,5,8192
qw 5
qm 160,104,116
v_cvt_pk_bf16_f32 v153,v154,v155
qt 102,5,8704
qm 232,104,188
v_cvt_pk_bf16_f32 v154,v156,v157
v_and_b32 v16,1,v0
qt 104,4,12288
qw 5
qm 164,108,116
v_cvt_pk_bf16_f32 v155,v158,v159
qt 106,4,12800
qm 236,108,188
v_lshlrev_b32 v16,4,v16
qt 108,5,12288
qw 5
qz 168,96,112
v_cvt_pk_bf16_f32 v224,v224,v225
s_add_u32 s15,s73,131072
buffer_store_dwordx4 v[152:155],v1,s[88:91],s15 offen offset:0 sc1
qt 110,5,12800
qz 240,96,184
v_cvt_pk_bf16_f32 v225,v226,v227
v_mov_b32 v17,0x3f80
qt 96,2,0
qw 5
qz 172,100,112
v_cvt_pk_bf16_f32 v226,v228,v229
qt 98,2,512
qz 244,100,184
v_cvt_pk_bf16_f32 v227,v230,v231
v_lshlrev_b32 v16,v16,v17
qt 100,3,0
qw 5
qm 168,104,116
qt 102,3,512
qm 240,104,188
v_bfe_u32 v17,v0,1,3
qt 104,2,4096
qw 5
qm 172,108,116
s_barrier
qt 106,2,4608
qm 244,108,188
v_cmp_eq_u32 vcc,0,v17
s_nop 1
v_cndmask_b32 v144,0,v16,vcc
qt 108,3,4096
qw 5
qm 160,96,128
s_add_u32 s15,s73,131072
buffer_store_dwordx4 v[224:227],v1,s[88:91],s15 offen offset:1024 sc1
qt 110,3,4608
qm 232,96,200
v_cmp_eq_u32 vcc,1,v17
s_nop 1
v_cndmask_b32 v145,0,v16,vcc
qt 96,4,16384
qw 5
qm 164,100,128
qt 98,4,16896
qm 236,100,200
v_cmp_eq_u32 vcc,2,v17
s_nop 1
v_cndmask_b32 v146,0,v16,vcc
qt 100,5,16384
qw 5
qm 160,104,132
qt 102,5,16896
qm 232,104,204
v_cmp_eq_u32 vcc,3,v17
s_nop 1
v_cndmask_b32 v147,0,v16,vcc
qt 104,4,20480
qw 5
qm 164,108,132
qt 106,4,20992
qm 236,108,204
v_cmp_eq_u32 vcc,4,v17
s_nop 1
v_cndmask_b32 v148,0,v16,vcc
qt 108,5,20480
qw 5
qm 168,96,128
qt 110,5,20992
qm 240,96,200
v_cmp_eq_u32 vcc,5,v17
s_nop 1
v_cndmask_b32 v149,0,v16,vcc
qt 96,4,24576
qw 5
qm 172,100,128
qt 98,4,25088
qm 244,100,200
v_cmp_eq_u32 vcc,6,v17
s_nop 1
v_cndmask_b32 v150,0,v16,vcc
qt 100,5,24576
qw 5
qm 168,104,132
qt 102,5,25088
qm 240,104,204
v_cmp_eq_u32 vcc,7,v17
s_nop 1
v_cndmask_b32 v151,0,v16,vcc
qt 104,4,28672
qw 5
qm 172,108,132
qt 106,4,29184
qm 244,108,204
qt 108,5,28672
qw 5
qm 160,96,136
qt 110,5,29184
qm 232,96,208
qt 96,2,8192
qw 5
qm 164,100,136
qt 98,2,8704
qm 236,100,208
qt 100,3,8192
qw 5
qm 160,104,140
qt 102,3,8704
qm 232,104,212
qt 104,2,12288
qw 5
qm 164,108,140
s_waitcnt vmcnt(0)
s_barrier
v_add_u32 v38,1,v38
buffer_store_dword v38,v37,s[96:99],s68 offen sc1
qt 106,2,12800
qm 236,108,212
qt 108,3,12288
qw 5
qm 168,96,136
buffer_load_dwordx2 v[216:217],v11,s[36:39],s70 offen offset:512
qt 110,3,12800
qm 240,96,208
qd 96,24,16384
qw 5
qm 172,100,136
qd 98,24,20480
qm 244,100,208
qd 100,25,16384
qw 5
qm 168,104,140
qd 102,25,20480
qm 240,104,212
qd 104,26,16384
qw 5
qm 172,108,140
buffer_load_dwordx2 v[218:219],v12,s[80:83],s75 offen offset:512
qd 106,26,20480
qm 244,108,212
qd 108,27,16384
qw 5
qm 160,96,64
qd 110,27,20480
qm 232,96,72
qr 136,18,49152
qd 96,24,24576
qw 6
qm 160,100,68
qd 98,24,28672
qm 232,100,76
qr 208,18,50176
qd 100,25,24576
qw 7
qm 160,104,80
buffer_load_dwordx2 v[220:221],v11,s[36:39],s70 offen offset:0
qd 102,25,28672
qm 232,104,88
qr 140,18,57344
qd 104,26,24576
qw 8
qm 160,108,84
qd 106,26,28672
qm 232,108,92
qr 212,18,58368
qd 108,27,24576
qw 8
qm 168,96,64
buffer_load_dwordx4 v[252:255],v15,s[96:99],s72 offen sc1
qd 110,27,28672
qm 240,96,72
qd 96,24,32768
qw 7
qm 168,100,68
buffer_load_dwordx2 v[222:223],v12,s[80:83],s75 offen offset:0
qd 98,24,36864
qm 240,100,76
qd 100,25,32768
qw 6
qm 168,104,80
qd 102,25,36864
qm 240,104,88
qd 104,26,32768
qw 5
qm 168,108,84
s_barrier
qd 106,26,36864
qm 240,108,92
qd 108,27,32768
qw 5
qm 164,96,64
buffer_load_dwordx2 v[42:43],v11,s[36:39],s70 offen offset:640
qd 110,27,36864
qm 236,96,72
qd 96,24,40960
qw 5
qm 164,100,68
qd 98,24,45056
qm 236,100,76
qd 100,25,40960
qw 5
qm 164,104,80
qd 102,25,45056
qm 236,104,88
qd 104,26,40960
qw 5
qm 164,108,84
buffer_load_dwordx2 v[44:45],v12,s[80:83],s75 offen offset:640
qd 106,26,45056
qm 236,108,92
qd 108,27,40960
qw 5
qm 172,96,64
qd 110,27,45056
qm 244,96,72
qt 96,2,49152
qw 5
qm 172,100,68
qt 98,2,49664
qm 244,100,76
qt 100,3,49152
qw 5
qm 172,104,80
qt 102,3,49664
qm 244,104,88
qt 104,2,53248
qw 5
qm 172,108,84
qt 106,2,53760
qm 244,108,92
qt 108,3,53248
v_mov_b32 v16,0x7ff00000
s_waitcnt vmcnt(3)
v_sub_u32 v252,s74,v252
v_sub_u32 v253,s74,v253
v_sub_u32 v254,s74,v254
v_sub_u32 v255,s74,v255
v_cndmask_b32 v252,v252,0,s[40:41]
v_cndmask_b32 v253,v253,0,s[42:43]
qw 5
qm 160,96,48
qt 110,3,53760
v_cmp_ge_u32 s[52:53],5,v252
v_cmp_ge_u32 s[54:55],5,v253
v_cmp_ge_u32 s[56:57],5,v254
v_cmp_ge_u32 s[58:59],5,v255
qm 232,96,56
qr 64,18,32768
qt 96,2,57344
v_cndmask_b32 v32,v16,v10,s[52:53]
v_cndmask_b32 v33,v16,v10,s[54:55]
v_cndmask_b32 v34,v16,v10,s[56:57]
v_cndmask_b32 v35,v16,v10,s[58:59]
qw 6
qm 164,100,48
s_add_u32 s31,s69,524288
buffer_load_dwordx4 v[152:155],v32,s[88:91],s31 offen sc1
qt 98,2,57856
qm 236,100,56
qr 72,18,33792
qt 100,3,57344
qw 7
qm 160,104,52
qt 102,3,57856
qm 232,104,60
qr 68,18,40960
qt 104,2,61440
qw 8
qm 164,108,52
qw 1
s_barrier
s_add_u32 s31,s69,0
buffer_load_dwordx4 v[112:115],v32,s[88:91],s31 offen sc1
qt 106,2,61952
qm 236,108,60
qr 76,18,41984
qt 108,3,61440
qm 168,96,48
qt 110,3,61952
qm 240,96,56
qt 96,4,16384
qm 172,100,48
s_add_u32 s31,s69,131072
buffer_load_dwordx4 v[184:187],v32,s[88:91],s31 offen sc1
qt 98,4,16896
qm 244,100,56
qt 100,5,16384
qw 6
qm 168,104,52
qt 102,5,16896
qm 240,104,60
qt 104,4,20480
qw 5
qm 172,108,52
s_add_u32 s31,s69,589824
buffer_load_dwordx4 v[156:159],v33,s[88:91],s31 offen sc1
qt 106,4,20992
qm 244,108,60
qt 108,5,20480
qw 5
qm 160,96,120
qt 110,5,20992
qm 232,96,192
qr 80,18,16384
qt 96,4,24576
qw 6
qm 164,100,120
s_add_u32 s31,s69,65536
buffer_load_dwordx4 v[116:119],v33,s[88:91],s31 offen sc1
qt 98,4,25088
qm 236,100,192
qr 88,18,17408
qt 100,5,24576
qw 7
qm 160,104,124
qt 102,5,25088
qm 232,104,196
qr 84,18,24576
qt 104,4,28672
qw 8
qm 164,108,124
s_add_u32 s31,s69,196608
buffer_load_dwordx4 v[188:191],v33,s[88:91],s31 offen sc1
qt 106,4,29184
qm 236,108,196
qr 92,18,25600
qt 108,5,28672
qw 8
qm 168,96,120
qt 110,5,29184
qm 240,96,192
qt 96,4,0
qw 7
qm 172,100,120
s_add_u32 s31,s69,1179648
buffer_load_dwordx4 v[224:227],v34,s[88:91],s31 offen sc1
qt 98,4,512
qm 244,100,192
qt 100,5,0
qw 6
qm 168,104,124
qt 102,5,512
qm 240,104,196
qt 104,4,4096
qw 5
qm 172,108,124
s_barrier
s_add_u32 s31,s69,655360
buffer_load_dwordx4 v[128:131],v34,s[88:91],s31 offen sc1
qt 106,4,4608
qm 244,108,196
qt 108,5,4096
qw 5
qm 160,96,136
qt 110,5,4608
qm 232,96,208
qr 48,19,0
qt 96,4,8192
qw 6
qm 164,100,136
s_add_u32 s31,s69,786432
buffer_load_dwordx4 v[200:203],v34,s[88:91],s31 offen sc1
qt 98,4,8704
qm 236,100,208
qr 56,19,1024
qt 100,5,8192
qw 7
qm 160,104,140
qt 102,5,8704
qm 232,104,212
qr 52,19,8192
qt 104,4,12288
qw 8
qm 164,108,140
s_add_u32 s31,s69,1245184
buffer_load_dwordx4 v[228:231],v35,s[88:91],s31 offen sc1
qt 106,4,12800
qm 236,108,212
qr 60,19,9216
qt 108,5,12288
qw 8
qm 160,96,64
qt 110,5,12800
qm 232,96,72
qt 96,2,0
qw 7
qm 164,100,64
s_add_u32 s31,s69,720896
buffer_load_dwordx4 v[132:135],v35,s[88:91],s31 offen sc1
qt 98,2,512
qm 236,100,72
qt 100,3,0
qw 6
qm 160,104,68
qt 102,3,512
qm 232,104,76
qt 104,2,4096
qw 5
qm 164,108,68
s_add_u32 s31,s69,851968
buffer_load_dwordx4 v[204:207],v35,s[88:91],s31 offen sc1
qt 106,2,4608
qm 236,108,76
qt 108,3,4096
qw 5
qm 160,96,80
qt 110,3,4608
qm 232,96,88
qt 96,2,8192
qw 5
qm 164,100,80
qt 98,2,8704
qm 236,100,88
qt 100,3,8192
qw 5
qm 160,104,84
qt 102,3,8704
qm 232,104,92
qt 104,2,12288
qw 5
qm 164,108,84
s_barrier
qt 106,2,12800
qm 236,108,92
qt 108,3,12288
qw 5
qm 160,96,48
qt 110,3,12800
qm 232,96,56
qt 96,2,16384
qw 5
qm 164,100,48
qt 98,2,16896
qm 236,100,56
qt 100,3,16384
qw 5
qm 160,104,52
qt 102,3,16896
qm 232,104,60
qt 104,2,20480
qw 5
qm 164,108,52
qt 106,2,20992
qm 236,108,60
qt 108,3,20480
qw 5
qm 168,96,136
qt 110,3,20992
qm 240,96,208
qt 96,2,24576
qw 5
qm 172,100,136
qt 98,2,25088
qm 244,100,208
v_cvt_pk_bf16_f32 v160,v160,v161
qt 100,3,24576
qw 5
qm 168,104,140
v_cvt_pk_bf16_f32 v161,v162,v163
qt 102,3,25088
qm 240,104,212
v_cvt_pk_bf16_f32 v162,v164,v165
qt 104,2,28672
qw 5
qm 172,108,140
v_cvt_pk_bf16_f32 v163,v166,v167
qt 106,2,29184
qm 244,108,212
qt 108,3,28672
qw 5
qm 168,96,64
v_cvt_pk_bf16_f32 v232,v232,v233
s_add_u32 s15,s73,262144
buffer_store_dwordx4 v[160:163],v1,s[88:91],s15 offen offset:0 sc1
qt 110,3,29184
qm 240,96,72
v_cvt_pk_bf16_f32 v233,v234,v235
qt 96,2,32768
qw 5
qm 172,100,64
v_cvt_pk_bf16_f32 v234,v236,v237
qt 98,2,33280
qm 244,100,72
v_cvt_pk_bf16_f32 v235,v238,v239
qt 100,3,32768
qw 5
qm 168,104,68
qt 102,3,33280
qm 240,104,76
qt 104,2,36864
qw 5
qm 172,108,68
s_barrier
qt 106,2,37376
qm 244,108,76
qt 108,3,36864
qw 5
qm 168,96,80
s_add_u32 s15,s73,262144
buffer_store_dwordx4 v[232:235],v1,s[88:91],s15 offen offset:1024 sc1
qt 110,3,37376
qm 240,96,88
qt 96,4,16384
qw 5
qm 172,100,80
qt 98,4,16896
qm 244,100,88
qt 100,5,16384
qw 5
qm 168,104,84
qt 102,5,16896
qm 240,104,92
qt 104,4,20480
qw 5
qm 172,108,84
qt 106,4,20992
qm 244,108,92
qt 108,5,20480
qw 5
qm 168,96,48
qt 110,5,20992
qm 240,96,56
qw 4
qm 172,100,48
qm 244,100,56
qw 2
qm 168,104,52
qm 240,104,60
qw 0
qm 172,108,52
qm 244,108,60
s_nop 3
v_cvt_pk_bf16_f32 v168,v168,v169
v_cvt_pk_bf16_f32 v169,v170,v171
s_nop 0
v_cvt_pk_bf16_f32 v170,v172,v173
v_cvt_pk_bf16_f32 v171,v174,v175
s_add_u32 s15,s73,393216
buffer_store_dwordx4 v[168:171],v1,s[88:91],s15 offen offset:0 sc1
v_cvt_pk_bf16_f32 v240,v240,v241
v_cvt_pk_bf16_f32 v241,v242,v243
v_cvt_pk_bf16_f32 v242,v244,v245
v_cvt_pk_bf16_f32 v243,v246,v247
s_add_u32 s15,s73,393216
buffer_store_dwordx4 v[240:243],v1,s[88:91],s15 offen offset:1024 sc1
v_lshrrev_b32 v16,2,v0
v_mul_u32_u24 v16,2048,v16
v_and_b32 v17,3,v0
v_lshl_add_u32 v212,v17,4,v16
s_lshl_b32 s30,s2,3
s_add_u32 s29,s30,s6
s_movk_i32 s30,8192
s_cmp_eq_u32 s29,15
s_cselect_b32 s30,s30,0x7ff00000
buffer_load_dwordx4 v[208:211],v212,s[96:99],s30 offen sc1
v_lshlrev_b32 v16,16,v218
v_lshlrev_b32 v176,16,v216
v_mul_f32 v16,v16,v40
v_add_f32 v17,v176,v41
v_fma_f32 v176,v17,v16,v176
v_and_b32 v16,0xffff0000,v218
v_and_b32 v177,0xffff0000,v216
v_mul_f32 v16,v16,v40
v_add_f32 v17,v177,v41
v_fma_f32 v177,v17,v16,v177
v_lshlrev_b32 v16,16,v219
v_lshlrev_b32 v178,16,v217
v_mul_f32 v16,v16,v40
v_add_f32 v17,v178,v41
v_fma_f32 v178,v17,v16,v178
v_and_b32 v16,0xffff0000,v219
v_and_b32 v179,0xffff0000,v217
v_mul_f32 v16,v16,v40
v_add_f32 v17,v179,v41
v_fma_f32 v179,v17,v16,v179
s_waitcnt vmcnt(18)
v_lshlrev_b32 v16,16,v222
v_lshlrev_b32 v180,16,v220
v_mul_f32 v16,v16,v40
v_add_f32 v17,v180,v41
v_fma_f32 v180,v17,v16,v180
v_and_b32 v16,0xffff0000,v222
v_and_b32 v181,0xffff0000,v220
v_mul_f32 v16,v16,v40
v_add_f32 v17,v181,v41
v_fma_f32 v181,v17,v16,v181
v_lshlrev_b32 v16,16,v223
v_lshlrev_b32 v182,16,v221
v_mul_f32 v16,v16,v40
v_add_f32 v17,v182,v41
v_fma_f32 v182,v17,v16,v182
v_and_b32 v16,0xffff0000,v223
v_and_b32 v183,0xffff0000,v221
v_mul_f32 v16,v16,v40
v_add_f32 v17,v183,v41
v_fma_f32 v183,v17,v16,v183
s_waitcnt vmcnt(16)
v_lshlrev_b32 v16,16,v44
v_lshlrev_b32 v248,16,v42
v_mul_f32 v16,v16,v40
v_add_f32 v17,v248,v41
v_fma_f32 v248,v17,v16,v248
v_and_b32 v16,0xffff0000,v44
v_and_b32 v249,0xffff0000,v42
v_mul_f32 v16,v16,v40
v_add_f32 v17,v249,v41
v_fma_f32 v249,v17,v16,v249
v_lshlrev_b32 v16,16,v45
v_lshlrev_b32 v250,16,v43
v_mul_f32 v16,v16,v40
v_add_f32 v17,v250,v41
v_fma_f32 v250,v17,v16,v250
v_and_b32 v16,0xffff0000,v45
v_and_b32 v251,0xffff0000,v43
v_mul_f32 v16,v16,v40
v_add_f32 v17,v251,v41
v_fma_f32 v251,v17,v16,v251
buffer_load_dwordx4 v[252:255],v15,s[96:99],s72 offen sc1
v_add_u32 v38,5,v38
s_setprio 3
s_waitcnt vmcnt(0)
s_barrier
buffer_store_dword v38,v37,s[96:99],s68 offen sc1
s_setprio 0
.L_tX01:
v_sub_u32 v252,s74,v252
v_sub_u32 v253,s74,v253
v_sub_u32 v254,s74,v254
v_sub_u32 v255,s74,v255
v_cndmask_b32 v252,v252,0,s[40:41]
v_cndmask_b32 v253,v253,0,s[42:43]
v_cmp_ge_u32 vcc,5,v252
s_nop 1
s_andn2_b64 exec,vcc,s[52:53]
s_cbranch_scc0 .L_tX01r10
s_add_u32 s31,s69,524288
buffer_load_dwordx4 v[152:155],v10,s[88:91],s31 offen sc1
s_add_u32 s31,s69,0
buffer_load_dwordx4 v[112:115],v10,s[88:91],s31 offen sc1
s_add_u32 s31,s69,131072
buffer_load_dwordx4 v[184:187],v10,s[88:91],s31 offen sc1
.L_tX01r10:
s_mov_b64 exec,-1
s_or_b64 s[52:53],s[52:53],vcc
v_cmp_ge_u32 vcc,5,v253
s_nop 1
s_andn2_b64 exec,vcc,s[54:55]
s_cbranch_scc0 .L_tX01r11
s_add_u32 s31,s69,589824
buffer_load_dwordx4 v[156:159],v10,s[88:91],s31 offen sc1
s_add_u32 s31,s69,65536
buffer_load_dwordx4 v[116:119],v10,s[88:91],s31 offen sc1
s_add_u32 s31,s69,196608
buffer_load_dwordx4 v[188:191],v10,s[88:91],s31 offen sc1
.L_tX01r11:
s_mov_b64 exec,-1
s_or_b64 s[54:55],s[54:55],vcc
v_cmp_ge_u32 vcc,5,v254
s_nop 1
s_andn2_b64 exec,vcc,s[56:57]
s_cbranch_scc0 .L_tX01r12
s_add_u32 s31,s69,1179648
buffer_load_dwordx4 v[224:227],v10,s[88:91],s31 offen sc1
s_add_u32 s31,s69,655360
buffer_load_dwordx4 v[128:131],v10,s[88:91],s31 offen sc1
s_add_u32 s31,s69,786432
buffer_load_dwordx4 v[200:203],v10,s[88:91],s31 offen sc1
.L_tX01r12:
s_mov_b64 exec,-1
s_or_b64 s[56:57],s[56:57],vcc
v_cmp_ge_u32 vcc,5,v255
s_nop 1
s_andn2_b64 exec,vcc,s[58:59]
s_cbranch_scc0 .L_tX01r13
s_add_u32 s31,s69,1245184
buffer_load_dwordx4 v[228:231],v10,s[88:91],s31 offen sc1
s_add_u32 s31,s69,720896
buffer_load_dwordx4 v[132:135],v10,s[88:91],s31 offen sc1
s_add_u32 s31,s69,851968
buffer_load_dwordx4 v[204:207],v10,s[88:91],s31 offen sc1
.L_tX01r13:
s_mov_b64 exec,-1
s_or_b64 s[58:59],s[58:59],vcc
.L_tX01c:
s_and_b64 s[32:33],s[52:53],s[54:55]
s_and_b64 s[32:33],s[32:33],s[56:57]
s_and_b64 s[32:33],s[32:33],s[58:59]
s_cmp_eq_u64 s[32:33],-1
s_cbranch_scc1 .L_tX01x
s_add_u32 s76,s76,1
s_cmp_gt_u32 s76,1000000
s_cbranch_scc1 .L_tX01x
s_sleep 4
buffer_load_dwordx4 v[252:255],v15,s[96:99],s72 offen sc1
s_waitcnt vmcnt(0)
s_branch .L_tX01
.L_tX01x:
s_waitcnt vmcnt(0)
qm 176,144,152
qm 180,144,112
qm 248,144,128
qm 176,144,156
qm 180,144,116
qm 248,144,132
qm 176,144,224
qm 180,148,184
qm 248,148,200
qm 176,144,228
qm 180,148,188
qm 248,148,204
s_nop 5
buffer_store_dwordx4 v[176:179],v14,s[92:95],s71 offen offset:1024 sc1
buffer_store_dwordx4 v[180:183],v13,s[92:95],s71 offen offset:0 sc1
buffer_store_dwordx4 v[248:251],v13,s[92:95],s71 offen offset:1280 sc1
s_cmp_lg_u32 s29,15
s_cbranch_scc1 .L_ebd
s_movk_i32 s30,8192
s_mov_b32 s31,0
.L_eb:
v_sub_u32 v208,s74,v208
v_sub_u32 v209,s74,v209
v_sub_u32 v210,s74,v210
v_sub_u32 v211,s74,v211
v_max_u32 v16,v208,v209
v_max3_u32 v16,v16,v210,v211
v_cmp_lt_u32 vcc,7,v16
s_add_u32 s31,s31,1
s_cmp_gt_u32 s31,1000000
s_cbranch_scc1 .L_ebs
s_cmp_eq_u64 vcc,0
s_cbranch_scc1 .L_ebs
buffer_load_dwordx4 v[208:211],v212,s[96:99],s30 offen sc1
s_waitcnt vmcnt(0)
s_branch .L_eb
.L_ebs:
s_add_u32 s30,s3,1
v_mov_b32 v16,s30
v_lshlrev_b32 v17,24,v0
buffer_store_dword v16,v17,s[96:99],0 offen sc1
.L_ebd:
s_endpgm
.org .L_code_Y+245760
.L_code_Yo:
.L_code_Yo0:
s_waitcnt vmcnt(11) lgkmcnt(0)
s_barrier
qr 184,24,0
qr 96,20,0
qr 100,20,2048
qr 188,25,4096
qr 104,21,0
qr 108,21,2048
qr 192,24,8192
qw 5
qz 48,112,96
qr 196,25,12288
qz 52,184,96
qr 200,24,16384
qr 96,20,8192
qw 7
qz 56,112,100
qr 204,25,20480
qz 60,184,100
qr 100,20,10240
qw 7
qm 48,116,104
qm 52,188,104
qr 104,21,8192
s_add_u32 m0,s13,57344
qw 7
qm 56,116,108
qm 60,188,108
qr 108,21,10240
s_add_u32 s14,s18,896
qw 4
qm 48,120,96
s_waitcnt vmcnt(7) lgkmcnt(3)
s_barrier
qm 52,192,96
qr 96,20,16384
ql 8,20,0
qw 3
qm 56,120,100
qm 60,192,100
qr 100,20,18432
ql 9,20,1024
qw 3
qm 48,124,104
s_add_u32 m0,s13,65536
s_add_u32 s14,s18,1024
qm 52,196,104
qr 104,21,16384
ql 8,20,0
qw 3
qm 56,124,108
qm 60,196,108
qr 108,21,18432
ql 9,20,1024
qw 3
qm 48,128,96
s_add_u32 m0,s13,73728
s_add_u32 s14,s18,36864
qm 52,200,96
qr 96,20,24576
ql 8,20,0
qw 3
qm 56,128,100
qm 60,200,100
qr 100,20,26624
ql 9,20,1024
qw 3
qm 48,132,104
qm 52,204,104
qr 104,21,24576
qw 3
qm 56,132,108
s_waitcnt vmcnt(7)
s_barrier
qr 208,24,24576
qm 60,204,108
qr 212,25,28672
qr 108,21,26624
s_mov_b32 m0,s13
s_add_u32 s14,s18,36992
qw 5
qm 48,136,96
ql 8,20,0
qw 2
qm 52,208,96
qr 216,24,32768
qr 96,20,32768
qm 56,136,100
qr 220,25,36864
ql 9,20,1024
qm 60,208,100
qr 100,20,34816
qm 48,140,104
qr 224,24,40960
s_add_u32 m0,s13,8192
qw 6
qm 52,212,104
qr 228,25,45056
qr 104,21,32768
qw 7
qm 56,140,108
s_add_u32 s14,s18,37120
qm 60,212,108
qr 232,24,49152
qr 108,21,34816
qw 7
qm 48,144,96
qr 236,25,53248
ql 8,20,0
qm 52,216,96
qr 96,20,40960
qw 7
qm 56,144,100
qr 240,24,57344
ql 9,20,1024
qm 60,216,100
qr 244,25,61440
qr 100,20,43008
qw 7
qm 48,148,104
s_add_u32 m0,s13,16384
qm 52,220,104
qr 248,26,8192
qr 104,21,40960
qw 7
qm 56,148,108
qr 252,27,12288
s_add_u32 s14,s18,37248
qm 60,220,108
qr 108,21,43008
qw 7
qm 48,152,96
ql 8,20,0
qm 52,224,96
qr 96,20,49152
qw 5
qm 56,152,100
ql 9,20,1024
qm 60,224,100
qr 100,20,51200
qw 4
qm 48,156,104
s_add_u32 m0,s13,24576
qm 52,228,104
qr 104,21,49152
qw 3
qm 56,156,108
s_waitcnt vmcnt(6)
s_barrier
s_add_u32 s14,s18,37376
qm 60,228,108
qr 108,21,51200
qw 3
qm 48,160,96
ql 8,20,0
qm 52,232,96
qr 96,20,57344
qw 3
qm 56,160,100
ql 9,20,1024
qm 60,232,100
qr 100,20,59392
qw 3
qm 48,164,104
s_add_u32 m0,s13,32768
qm 52,236,104
qr 104,21,57344
qw 3
qm 56,164,108
s_add_u32 s14,s18,37504
qm 60,236,108
qr 108,21,59392
qw 3
qm 48,168,96
ql 8,20,0
qm 52,240,96
qr 96,22,0
qw 3
qm 56,168,100
v_readfirstlane_b32 s3,v13
s_mul_i32 s16,s3,0x9E3779B1
s_add_u32 s16,s16,0x7F4A7C15
ql 9,20,1024
qm 60,240,100
qr 100,22,2048
qw 3
qm 48,172,104
s_add_u32 m0,s13,40960
qm 52,244,104
qr 104,23,0
qw 3
qm 56,172,108
s_add_u32 s14,s18,37632
qm 60,244,108
qr 108,23,2048
qw 3
qm 48,176,96
ql 8,20,0
qm 52,248,96
qr 96,22,8192
qw 3
qm 56,176,100
ql 9,20,1024
qm 60,248,100
qr 100,22,10240
qw 3
qm 48,180,104
s_add_u32 m0,s13,81920
qm 52,252,104
qr 104,23,8192
qw 3
qm 56,180,108
s_waitcnt vmcnt(6)
s_barrier
s_add_u32 s14,s18,37760
qm 60,252,108
qr 108,23,10240
qw 3
qz 64,112,96
v_lshrrev_b32 v42,4,v0
v_and_b32 v43,15,v0
v_bfe_u32 v45,v0,2,2
ql 8,20,0
qz 68,184,96
v_lshl_add_u32 v36,s7,2,v42
v_lshl_add_u32 v45,s7,2,v45
qr 96,20,0
qw 3
qz 72,112,100
v_lshlrev_b32 v42,5,v36
v_and_b32 v14,3,v0
ql 9,20,1024
qz 76,184,100
v_lshlrev_b32 v37,3,v43
v_lshl_add_u32 v45,v45,3,v14
qr 100,20,2048
qw 3
qm 64,116,104
v_lshl_add_u32 v43,v36,9,v37
v_lshlrev_b32 v45,2,v45
s_lshl_b32 s30,s10,3
s_add_u32 s30,s30,s8
s_mul_i32 s30,s30,196608
s_add_u32 s76,s30,21118976
s_add_u32 s77,s30,21020672
s_lshl_b32 s30,s7,13
s_add_u32 s79,s76,s30
s_add_u32 m0,s13,90112
qm 68,188,104
v_add_u32 v42,122880,v42
v_add_u32 v45,122880,v45
qr 104,21,0
qw 3
qm 72,116,108
v_add_u32 v37,124928,v37
ds_read_b32 v10,v45
s_add_u32 s14,s18,37888
qm 76,188,108
v_add_u32 v43,114688,v43
ds_read_b32 v11,v45 offset:16
qr 108,21,2048
qw 5
qm 64,120,96
qr 28,42,0
ql 8,20,0
qm 68,192,96
qr 32,42,16
qr 96,20,8192
qw 7
qm 72,120,100
qd 36,37,0
ql 9,20,1024
qm 76,192,100
s_nop 0
qr 100,20,10240
qw 8
qm 64,124,104
s_nop 0
qw 7
v_mul_f32 v14,v10,v10
s_add_u32 m0,s13,98304
qm 68,196,104
s_nop 0
v_mul_f32 v12,0xbdd2d3e8,v10
qr 104,21,8192
qw 6
qm 72,124,108
qw 5
v_mul_f32 v48,v48,v28
v_mul_f32 v10,0xc0135761,v10
s_mov_b32 s14,s17
qm 76,196,108
v_mul_f32 v56,v56,v28
v_mul_f32 v12,v12,v14
qr 108,21,10240
qw 4
qm 64,128,96
v_mov_b32 v38,v48
v_rcp_f32 v14,v14
ql 6,40,0
qm 68,200,96
v_mov_b32 v39,v56
v_cvt_pk_bf16_f32 v48,v48,v56
v_mul_f32 v15,v11,v11
qr 96,20,16384
qw 3
qm 72,128,100
v_mul_f32 v49,v49,v29
v_mul_f32 v13,0xbdd2d3e8,v11
ql 7,40,1024
qm 76,200,100
v_mul_f32 v57,v57,v29
v_mul_f32 v11,0xc0135761,v11
qr 100,20,18432
qw 3
qm 64,132,104
v_fma_f32 v40,v36,v38,v49
v_mul_f32 v13,v13,v15
s_add_u32 m0,s13,106496
qm 68,204,104
v_fma_f32 v41,v36,v39,v57
v_rcp_f32 v15,v15
qr 104,21,16384
qw 3
qm 72,132,108
s_waitcnt vmcnt(6)
s_barrier
v_fma_f32 v40,-v37,v39,v40
s_add_u32 s14,s17,589824
qm 76,204,108
v_fma_f32 v41,v37,v38,v41
qr 108,21,18432
qw 3
qm 64,136,96
v_cvt_pk_bf16_f32 v49,v49,v57
ql 6,40,0
qm 68,208,96
v_mul_f32 v50,v50,v30
qr 96,20,24576
qw 3
qm 72,136,100
v_mul_f32 v58,v58,v30
ql 7,40,1024
qm 76,208,100
v_fma_f32 v38,v36,v40,v50
qr 100,20,26624
qw 3
qm 64,140,104
v_fma_f32 v39,v36,v41,v58
s_add_u32 m0,s13,49152
qm 68,212,104
v_fma_f32 v38,-v37,v41,v38
qr 104,21,24576
qw 3
qm 72,140,108
v_fma_f32 v39,v37,v40,v39
s_add_u32 s14,s17,1179648
qm 76,212,108
v_cvt_pk_bf16_f32 v50,v50,v58
qr 108,21,26624
qw 3
qm 64,144,96
v_mul_f32 v51,v51,v31
ql 6,40,0
qm 68,216,96
v_mul_f32 v59,v59,v31
qr 96,20,32768
qw 3
qm 72,144,100
v_fma_f32 v40,v36,v38,v51
v_fma_f32 v41,v36,v39,v59
ql 7,40,1024
qm 76,216,100
v_fma_f32 v40,-v37,v39,v40
qr 100,20,34816
qw 3
qm 64,148,104
v_fma_f32 v41,v37,v38,v41
s_add_u32 m0,s13,57344
qm 68,220,104
v_cvt_pk_bf16_f32 v51,v51,v59
qr 104,21,32768
qw 3
qm 72,148,108
ds_write_b128 v18,v[48:51] offset:49152
s_add_u32 s14,s17,1769472
qm 76,220,108
v_mul_f32 v52,v52,v32
qr 108,21,34816
qw 4
qm 64,152,96
v_mul_f32 v60,v60,v32
ql 6,40,0
qm 68,224,96
v_fma_f32 v38,v36,v40,v52
qr 96,20,40960
qw 4
qm 72,152,100
v_fma_f32 v39,v36,v41,v60
ql 7,40,1024
qm 76,224,100
v_fma_f32 v38,-v37,v41,v38
qr 100,20,43008
qw 4
qm 64,156,104
v_fma_f32 v39,v37,v40,v39
s_add_u32 m0,s13,65536
qm 68,228,104
v_cvt_pk_bf16_f32 v52,v52,v60
qr 104,21,40960
qw 3
qm 72,156,108
s_waitcnt vmcnt(6)
s_barrier
v_mul_f32 v53,v53,v33
s_add_u32 s14,s17,2359296
qm 76,228,108
v_mul_f32 v61,v61,v33
qr 108,21,43008
qw 3
qm 64,160,96
v_fma_f32 v40,v36,v38,v53
ql 6,40,0
qm 68,232,96
v_fma_f32 v41,v36,v39,v61
qr 96,22,16384
qw 3
qm 72,160,100
v_fma_f32 v40,-v37,v39,v40
ql 7,40,1024
qm 76,232,100
v_fma_f32 v41,v37,v38,v41
v_cvt_pk_bf16_f32 v53,v53,v61
qr 100,22,18432
qw 3
qm 64,164,104
v_mul_f32 v54,v54,v34
s_add_u32 m0,s13,73728
qm 68,236,104
v_mul_f32 v62,v62,v34
qr 104,23,16384
qw 3
qm 72,164,108
v_fma_f32 v38,v36,v40,v54
s_add_u32 s14,s17,2949120
qm 76,236,108
v_fma_f32 v39,v36,v41,v62
qr 108,23,18432
qw 3
qm 64,168,96
v_fma_f32 v38,-v37,v41,v38
ql 6,40,0
qm 68,240,96
v_fma_f32 v39,v37,v40,v39
qr 96,22,24576
qw 3
qm 72,168,100
v_cvt_pk_bf16_f32 v54,v54,v62
ql 7,40,1024
qm 76,240,100
v_mul_f32 v55,v55,v35
qr 100,22,26624
qw 3
qm 64,172,104
v_mul_f32 v63,v63,v35
s_mov_b32 m0,s13
qm 68,244,104
v_fma_f32 v40,v36,v38,v55
qr 104,23,24576
qw 3
qm 72,172,108
v_fma_f32 v41,v36,v39,v63
s_add_u32 s14,s17,3538944
qm 76,244,108
v_fma_f32 v40,-v37,v39,v40
qr 108,23,26624
qw 3
qm 64,176,96
v_fma_f32 v41,v37,v38,v41
ql 6,40,0
qm 68,248,96
v_cvt_pk_bf16_f32 v55,v55,v63
qt 96,4,32768
qw 3
qm 72,176,100
ds_write_b128 v18,v[52:55] offset:50176
qt 98,4,33280
ql 7,40,1024
qm 76,248,100
ds_write_b64 v43,v[40:41] offset:0
qt 100,5,32768
qw 6
qm 64,180,104
qt 102,5,33280
s_add_u32 m0,s13,81920
qm 68,252,104
qt 104,4,36864
qw 7
qm 72,180,108
s_waitcnt vmcnt(6) lgkmcnt(3)
s_barrier
qt 106,4,37376
s_add_u32 s14,s17,4128768
qm 76,252,108
qt 108,5,36864
qz 48,96,112
v_lshrrev_b32 v42,4,v0
v_and_b32 v43,15,v0
qt 110,5,37376
ql 6,40,0
qz 52,96,184
v_lshl_add_u32 v36,s7,2,v42
v_lshlrev_b32 v42,5,v36
qt 96,4,40960
qw 5
qz 56,100,112
v_lshlrev_b32 v37,3,v43
qt 98,4,41472
ql 7,40,1024
qz 60,100,184
v_lshl_add_u32 v43,v36,9,v37
v_add_u32 v42,122880,v42
qt 100,5,40960
qw 5
qm 48,104,116
v_add_u32 v37,124928,v37
v_add_u32 v43,114688,v43
qt 102,5,41472
s_add_u32 m0,s13,90112
qm 52,104,188
qr 28,42,0
qt 104,4,45056
qw 6
qm 56,108,116
qr 32,42,16
qd 36,37,256
qt 106,4,45568
s_add_u32 s14,s17,4718592
qm 60,108,188
s_nop 0
s_nop 0
qt 108,5,45056
qw 8
qm 48,96,120
s_nop 0
qt 110,5,45568
ql 6,40,0
qm 52,96,192
qw 6
v_mul_f32 v64,v64,v28
v_mul_f32 v72,v72,v28
qt 96,2,49152
qm 56,100,120
v_mov_b32 v38,v64
qt 98,2,49664
ql 7,40,1024
qm 60,100,192
v_mov_b32 v39,v72
v_cvt_pk_bf16_f32 v64,v64,v72
qt 100,3,49152
qw 5
qm 48,104,124
v_mul_f32 v65,v65,v29
v_mul_f32 v73,v73,v29
qt 102,3,49664
s_add_u32 m0,s13,8192
qm 52,104,196
v_fma_f32 v40,v36,v38,v65
qt 104,2,53248
qw 5
qm 56,108,124
v_fma_f32 v41,v36,v39,v73
v_fma_f32 v40,-v37,v39,v40
qt 106,2,53760
s_mov_b32 s14,s17
qm 60,108,196
v_fma_f32 v41,v37,v38,v41
v_cvt_pk_bf16_f32 v65,v65,v73
qt 108,3,53248
qw 5
qm 48,96,128
v_mul_f32 v66,v66,v30
qt 110,3,53760
ql 6,44,0
qm 52,96,200
v_mul_f32 v74,v74,v30
v_fma_f32 v38,v36,v40,v66
qt 96,2,57344
qw 5
qm 56,100,128
v_fma_f32 v39,v36,v41,v74
v_fma_f32 v38,-v37,v41,v38
qt 98,2,57856
ql 7,44,1024
qm 60,100,200
v_fma_f32 v39,v37,v40,v39
qt 100,3,57344
qw 5
qm 48,104,132
v_cvt_pk_bf16_f32 v66,v66,v74
v_mul_f32 v67,v67,v31
qt 102,3,57856
s_add_u32 m0,s13,98304
qm 52,104,204
v_mul_f32 v75,v75,v31
qt 104,2,61440
qw 5
qm 56,108,132
s_waitcnt vmcnt(6)
s_barrier
v_fma_f32 v40,v36,v38,v67
v_fma_f32 v41,v36,v39,v75
qt 106,2,61952
s_add_u32 s14,s17,589824
qm 60,108,204
v_fma_f32 v40,-v37,v39,v40
v_fma_f32 v41,v37,v38,v41
qt 108,3,61440
qw 5
qm 48,96,136
v_cvt_pk_bf16_f32 v67,v67,v75
qt 110,3,61952
ql 6,44,0
qm 52,96,208
ds_write_b128 v19,v[64:67]
v_mul_f32 v68,v68,v32
qt 96,4,0
qw 6
qm 56,100,136
v_mul_f32 v76,v76,v32
v_fma_f32 v38,v36,v40,v68
qt 98,4,512
ql 7,44,1024
qm 60,100,208
v_fma_f32 v39,v36,v41,v76
qt 100,5,0
qw 6
qm 48,104,140
v_fma_f32 v38,-v37,v41,v38
v_fma_f32 v39,v37,v40,v39
qt 102,5,512
s_add_u32 m0,s13,106496
qm 52,104,212
v_cvt_pk_bf16_f32 v68,v68,v76
v_mul_f32 v69,v69,v33
qt 104,4,4096
qw 6
qm 56,108,140
v_mul_f32 v77,v77,v33
qt 106,4,4608
s_add_u32 s14,s17,1179648
qm 60,108,212
v_fma_f32 v40,v36,v38,v69
v_fma_f32 v41,v36,v39,v77
qt 108,5,4096
qw 5
qm 48,96,144
v_fma_f32 v40,-v37,v39,v40
qt 110,5,4608
ql 6,44,0
qm 52,96,216
v_fma_f32 v41,v37,v38,v41
v_cvt_pk_bf16_f32 v69,v69,v77
qt 96,4,8192
qw 5
qm 56,100,144
v_mul_f32 v70,v70,v34
v_mul_f32 v78,v78,v34
qt 98,4,8704
ql 7,44,1024
qm 60,100,216
v_fma_f32 v38,v36,v40,v70
qt 100,5,8192
qw 5
qm 48,104,148
v_fma_f32 v39,v36,v41,v78
v_fma_f32 v38,-v37,v41,v38
qt 102,5,8704
s_add_u32 m0,s13,16384
qm 52,104,220
v_fma_f32 v39,v37,v40,v39
v_cvt_pk_bf16_f32 v70,v70,v78
qt 104,4,12288
qw 5
qm 56,108,148
v_mul_f32 v71,v71,v35
qt 106,4,12800
s_add_u32 s14,s17,1769472
qm 60,108,220
v_mul_f32 v79,v79,v35
v_fma_f32 v40,v36,v38,v71
qt 108,5,12288
qw 5
qm 48,96,152
v_fma_f32 v41,v36,v39,v79
v_fma_f32 v40,-v37,v39,v40
qt 110,5,12800
ql 6,44,0
qm 52,96,224
v_fma_f32 v41,v37,v38,v41
qt 96,2,0
qw 5
qm 56,100,152
v_cvt_pk_bf16_f32 v71,v71,v79
ds_write_b128 v19,v[68:71] offset:1024
qt 98,2,512
ql 7,44,1024
qm 60,100,224
ds_write_b64 v43,v[40:41] offset:256
qt 100,3,0
qw 7
qm 48,104,156
qt 102,3,512
s_add_u32 m0,s13,24576
qm 52,104,228
qt 104,2,4096
qw 7
qm 56,108,156
s_waitcnt vmcnt(6) lgkmcnt(3)
s_barrier
qt 106,2,4608
s_add_u32 s14,s17,2359296
qm 60,108,228
qt 108,3,4096
qm 48,96,160
qt 110,3,4608
ql 6,44,0
qm 52,96,232
qt 96,4,16384
qw 5
qm 56,100,160
qt 98,4,16896
ql 7,44,1024
qm 60,100,232
qt 100,5,16384
qw 5
qm 48,104,164
qt 102,5,16896
s_add_u32 m0,s13,32768
qm 52,104,236
qt 104,4,20480
qw 5
qm 56,108,164
qt 106,4,20992
s_add_u32 s14,s17,2949120
qm 60,108,236
qt 108,5,20480
qw 5
qm 48,96,168
qt 110,5,20992
ql 6,44,0
qm 52,96,240
qt 96,4,24576
qw 5
qm 56,100,168
qt 98,4,25088
ql 7,44,1024
qm 60,100,240
qt 100,5,24576
qw 5
qm 48,104,172
qt 102,5,25088
s_add_u32 m0,s13,40960
qm 52,104,244
qt 104,4,28672
qw 5
qm 56,108,172
qt 106,4,29184
s_add_u32 s14,s17,3538944
qm 60,108,244
qt 108,5,28672
qw 5
qm 48,96,176
qt 110,5,29184
ql 6,44,0
qm 52,96,248
qt 96,2,8192
qw 5
qm 56,100,176
qt 98,2,8704
ql 7,44,1024
qm 60,100,248
qt 100,3,8192
qw 5
qm 48,104,180
qt 102,3,8704
s_add_u32 m0,s13,81920
qm 52,104,252
qt 104,2,12288
qw 5
qm 56,108,180
s_waitcnt vmcnt(6)
s_barrier
qt 106,2,12800
s_add_u32 s14,s17,4128768
qm 60,108,252
qt 108,3,12288
qw 5
qz 80,96,112
qt 110,3,12800
ql 6,44,0
qz 84,96,184
v_mul_f32 v16,v48,v48
qt 96,4,32768
qw 5
qz 88,100,112
v_fma_f32 v16,v16,v12,v10
qt 98,4,33280
ql 7,44,1024
qz 92,100,184
v_mul_f32 v16,v16,v48
qt 100,5,32768
qw 5
qm 80,104,116
v_exp_f32 v16,v16
qt 102,5,33280
s_add_u32 m0,s13,90112
qm 84,104,188
v_fma_f32 v16,v16,v14,v14
qt 104,4,36864
qw 5
qm 88,108,116
v_rcp_f32 v16,v16
qt 106,4,37376
s_add_u32 s14,s17,4718592
qm 92,108,188
v_mul_f32 v48,v48,v16
qt 108,5,36864
qw 5
qm 80,96,120
v_mul_f32 v16,v49,v49
qt 110,5,37376
ql 6,44,0
qm 84,96,192
v_fma_f32 v16,v16,v12,v10
qt 96,4,40960
qw 5
qm 88,100,120
v_mul_f32 v16,v16,v49
qt 98,4,41472
ql 7,44,1024
qm 92,100,192
v_exp_f32 v16,v16
qt 100,5,40960
qw 5
qm 80,104,124
v_fma_f32 v16,v16,v14,v14
qt 102,5,41472
s_add_u32 m0,s13,49152
qm 84,104,196
v_rcp_f32 v16,v16
qt 104,4,45056
qw 5
qm 88,108,124
v_mul_f32 v49,v49,v16
qt 106,4,45568
s_add_u32 s14,s17,128
qm 92,108,196
v_mul_f32 v16,v50,v50
qt 108,5,45056
qw 5
qm 80,96,128
v_fma_f32 v16,v16,v12,v10
qt 110,5,45568
ql 6,40,0
qm 84,96,200
v_mul_f32 v16,v16,v50
qt 96,2,16384
qw 5
qm 88,100,128
v_exp_f32 v16,v16
qt 98,2,16896
ql 7,40,1024
qm 92,100,200
v_fma_f32 v16,v16,v14,v14
qt 100,3,16384
qw 5
qm 80,104,132
v_rcp_f32 v16,v16
qt 102,3,16896
s_add_u32 m0,s13,98304
qm 84,104,204
v_mul_f32 v50,v50,v16
qt 104,2,20480
qw 5
qm 88,108,132
s_waitcnt vmcnt(6)
s_barrier
v_mul_f32 v16,v51,v51
qt 106,2,20992
s_add_u32 s14,s17,589952
qm 92,108,204
v_fma_f32 v16,v16,v12,v10
qt 108,3,20480
qw 5
qm 80,96,136
v_mul_f32 v16,v16,v51
qt 110,3,20992
ql 6,40,0
qm 84,96,208
v_exp_f32 v16,v16
qt 96,2,24576
qw 5
qm 88,100,136
v_fma_f32 v16,v16,v14,v14
qt 98,2,25088
ql 7,40,1024
qm 92,100,208
v_rcp_f32 v16,v16
qt 100,3,24576
qw 5
qm 80,104,140
v_mul_f32 v51,v51,v16
qt 102,3,25088
s_add_u32 m0,s13,106496
qm 84,104,212
v_mul_f32 v16,v56,v56
qt 104,2,28672
qw 5
qm 88,108,140
v_fma_f32 v16,v16,v12,v10
qt 106,2,29184
s_add_u32 s14,s17,1179776
qm 92,108,212
v_mul_f32 v16,v16,v56
qt 108,3,28672
qw 5
qm 80,96,144
v_exp_f32 v16,v16
qt 110,3,29184
ql 6,40,0
qm 84,96,216
v_fma_f32 v16,v16,v14,v14
qt 96,2,32768
qw 5
qm 88,100,144
v_rcp_f32 v16,v16
qt 98,2,33280
ql 7,40,1024
qm 92,100,216
v_mul_f32 v56,v56,v16
qt 100,3,32768
qw 5
qm 80,104,148
v_mul_f32 v16,v57,v57
qt 102,3,33280
s_add_u32 m0,s13,57344
qm 84,104,220
v_fma_f32 v16,v16,v12,v10
qt 104,2,36864
qw 5
qm 88,108,148
v_mul_f32 v16,v16,v57
qt 106,2,37376
s_add_u32 s14,s17,1769600
qm 92,108,220
v_exp_f32 v16,v16
qt 108,3,36864
qw 5
qm 80,96,152
v_fma_f32 v16,v16,v14,v14
qt 110,3,37376
ql 6,40,0
qm 84,96,224
v_rcp_f32 v16,v16
qt 96,2,40960
qw 5
qm 88,100,152
v_mul_f32 v57,v57,v16
qt 98,2,41472
ql 7,40,1024
qm 92,100,224
v_mul_f32 v16,v58,v58
qt 100,3,40960
qw 5
qm 80,104,156
v_fma_f32 v16,v16,v12,v10
qt 102,3,41472
s_add_u32 m0,s13,65536
qm 84,104,228
v_mul_f32 v16,v16,v58
qt 104,2,45056
qw 5
qm 88,108,156
s_waitcnt vmcnt(6)
s_barrier
v_exp_f32 v16,v16
qt 106,2,45568
s_add_u32 s14,s17,2359424
qm 92,108,228
v_fma_f32 v16,v16,v14,v14
qt 108,3,45056
qw 5
qm 80,96,160
v_rcp_f32 v16,v16
qt 110,3,45568
ql 6,40,0
qm 84,96,232
v_mul_f32 v58,v58,v16
qt 96,4,16384
qw 5
qm 88,100,160
v_mul_f32 v16,v59,v59
qt 98,4,16896
ql 7,40,1024
qm 92,100,232
v_fma_f32 v16,v16,v12,v10
qt 100,5,16384
qw 5
qm 80,104,164
v_mul_f32 v16,v16,v59
qt 102,5,16896
s_add_u32 m0,s13,73728
qm 84,104,236
v_exp_f32 v16,v16
qt 104,4,20480
qw 5
qm 88,108,164
v_fma_f32 v16,v16,v14,v14
qt 106,4,20992
s_add_u32 s14,s17,2949248
qm 92,108,236
v_rcp_f32 v16,v16
qt 108,5,20480
qw 5
qm 80,96,168
v_mul_f32 v59,v59,v16
qt 110,5,20992
ql 6,40,0
qm 84,96,240
v_mul_f32 v16,v52,v52
qt 96,4,24576
qw 5
qm 88,100,168
v_fma_f32 v16,v16,v13,v11
qt 98,4,25088
ql 7,40,1024
qm 92,100,240
v_mul_f32 v16,v16,v52
qt 100,5,24576
qw 5
qm 80,104,172
v_exp_f32 v16,v16
qt 102,5,25088
s_mov_b32 m0,s13
qm 84,104,244
v_fma_f32 v16,v16,v15,v15
qt 104,4,28672
qw 5
qm 88,108,172
v_rcp_f32 v16,v16
qt 106,4,29184
s_add_u32 s14,s17,3539072
qm 92,108,244
v_mul_f32 v52,v52,v16
qt 108,5,28672
qw 5
qm 80,96,176
v_mul_f32 v16,v53,v53
qt 110,5,29184
ql 6,40,0
qm 84,96,248
v_fma_f32 v16,v16,v13,v11
qt 96,2,49152
qw 5
qm 88,100,176
v_mul_f32 v16,v16,v53
qt 98,2,49664
ql 7,40,1024
qm 92,100,248
v_exp_f32 v16,v16
qt 100,3,49152
qw 5
qm 80,104,180
v_fma_f32 v16,v16,v15,v15
qt 102,3,49664
s_add_u32 m0,s13,8192
qm 84,104,252
v_rcp_f32 v16,v16
qt 104,2,53248
qw 5
qm 88,108,180
s_waitcnt vmcnt(6)
s_barrier
v_mul_f32 v53,v53,v16
qt 106,2,53760
s_add_u32 s14,s17,4128896
qm 92,108,252
qt 108,3,53248
qw 5
qz 64,96,112
v_mul_f32 v16,v54,v54
qt 110,3,53760
ql 6,40,0
qz 68,96,184
v_fma_f32 v16,v16,v13,v11
qt 96,4,32768
qw 5
qz 72,100,112
v_mul_f32 v16,v16,v54
qt 98,4,33280
ql 7,40,1024
qz 76,100,184
v_exp_f32 v16,v16
qt 100,5,32768
qw 5
qm 64,104,116
v_fma_f32 v16,v16,v15,v15
qt 102,5,33280
s_add_u32 m0,s13,16384
qm 68,104,188
v_rcp_f32 v16,v16
qt 104,4,36864
qw 5
qm 72,108,116
v_mul_f32 v54,v54,v16
qt 106,4,37376
s_add_u32 s14,s17,4718720
qm 76,108,188
v_mul_f32 v16,v55,v55
qt 108,5,36864
qw 5
qm 64,96,120
v_fma_f32 v16,v16,v13,v11
qt 110,5,37376
ql 6,40,0
qm 68,96,192
v_mul_f32 v16,v16,v55
qt 96,4,40960
qw 5
qm 72,100,120
v_exp_f32 v16,v16
qt 98,4,41472
ql 7,40,1024
qm 76,100,192
v_fma_f32 v16,v16,v15,v15
qt 100,5,40960
qw 5
qm 64,104,124
v_rcp_f32 v16,v16
qt 102,5,41472
s_add_u32 m0,s13,24576
qm 68,104,196
v_mul_f32 v55,v55,v16
qt 104,4,45056
qw 5
qm 72,108,124
v_mul_f32 v16,v60,v60
qt 106,4,45568
s_add_u32 s14,s17,128
qm 76,108,196
v_fma_f32 v16,v16,v13,v11
qt 108,5,45056
qw 5
qm 64,96,128
v_mul_f32 v16,v16,v60
qt 110,5,45568
ql 6,44,0
qm 68,96,200
v_exp_f32 v16,v16
qt 96,2,57344
qw 5
qm 72,100,128
v_fma_f32 v16,v16,v15,v15
qt 98,2,57856
ql 7,44,1024
qm 76,100,200
v_rcp_f32 v16,v16
qt 100,3,57344
qw 5
qm 64,104,132
v_mul_f32 v60,v60,v16
qt 102,3,57856
s_add_u32 m0,s13,98304
qm 68,104,204
v_mul_f32 v16,v61,v61
qt 104,2,61440
qw 5
qm 72,108,132
s_waitcnt vmcnt(6)
s_barrier
v_fma_f32 v16,v16,v13,v11
qt 106,2,61952
s_add_u32 s14,s17,589952
qm 76,108,204
v_mul_f32 v16,v16,v61
qt 108,3,61440
qw 5
qm 64,96,136
v_exp_f32 v16,v16
qt 110,3,61952
ql 6,44,0
qm 68,96,208
v_fma_f32 v16,v16,v15,v15
qt 96,4,0
qw 5
qm 72,100,136
v_rcp_f32 v16,v16
qt 98,4,512
ql 7,44,1024
qm 76,100,208
v_mul_f32 v61,v61,v16
qt 100,5,0
qw 5
qm 64,104,140
v_mul_f32 v16,v62,v62
qt 102,5,512
s_add_u32 m0,s13,106496
qm 68,104,212
v_fma_f32 v16,v16,v13,v11
qt 104,4,4096
qw 5
qm 72,108,140
v_mul_f32 v16,v16,v62
qt 106,4,4608
s_add_u32 s14,s17,1179776
qm 76,108,212
v_exp_f32 v16,v16
qt 108,5,4096
qw 5
qm 64,96,144
v_fma_f32 v16,v16,v15,v15
qt 110,5,4608
ql 6,44,0
qm 68,96,216
v_rcp_f32 v16,v16
qt 96,4,8192
qw 5
qm 72,100,144
v_mul_f32 v62,v62,v16
qt 98,4,8704
ql 7,44,1024
qm 76,100,216
v_mul_f32 v16,v63,v63
qt 100,5,8192
qw 5
qm 64,104,148
v_fma_f32 v16,v16,v13,v11
qt 102,5,8704
s_add_u32 m0,s13,32768
qm 68,104,220
v_mul_f32 v16,v16,v63
qt 104,4,12288
qw 5
qm 72,108,148
v_exp_f32 v16,v16
qt 106,4,12800
s_add_u32 s14,s17,1769600
qm 76,108,220
v_fma_f32 v16,v16,v15,v15
qt 108,5,12288
qw 5
qm 64,96,152
v_rcp_f32 v16,v16
qt 110,5,12800
ql 6,44,0
qm 68,96,224
v_mul_f32 v63,v63,v16
qt 96,2,0
qw 5
qm 72,100,152
v_mul_f32 v48,v48,v80
qt 98,2,512
ql 7,44,1024
qm 76,100,224
v_mul_f32 v49,v49,v81
qt 100,3,0
qw 5
qm 64,104,156
v_cvt_pk_bf16_f32 v48,v48,v49
qt 102,3,512
s_add_u32 m0,s13,40960
qm 68,104,228
v_mul_f32 v50,v50,v82
qt 104,2,4096
qw 5
qm 72,108,156
s_waitcnt vmcnt(6)
s_barrier
v_mul_f32 v51,v51,v83
qt 106,2,4608
s_add_u32 s14,s17,2359424
qm 76,108,228
v_cvt_pk_bf16_f32 v49,v50,v51
qt 108,3,4096
qw 5
qm 64,96,160
v_mul_f32 v56,v56,v88
qt 110,3,4608
ql 6,44,0
qm 68,96,232
v_mul_f32 v57,v57,v89
qt 96,2,8192
qw 5
qm 72,100,160
v_cvt_pk_bf16_f32 v50,v56,v57
qt 98,2,8704
ql 7,44,1024
qm 76,100,232
v_mul_f32 v58,v58,v90
qt 100,3,8192
qw 5
qm 64,104,164
v_mul_f32 v59,v59,v91
qt 102,3,8704
s_add_u32 m0,s13,49152
qm 68,104,236
v_cvt_pk_bf16_f32 v51,v58,v59
qt 104,2,12288
qw 5
qm 72,108,164
ds_write_b128 v18,v[48:51]
v_mul_f32 v52,v52,v84
qt 106,2,12800
s_add_u32 s14,s17,2949248
qm 76,108,236
s_add_u32 s15,s76,0
buffer_store_dwordx4 v[48:51],v18,s[88:91],s15 offen offset:0 sc1
v_mul_f32 v53,v53,v85
qt 108,3,12288
qw 6
qm 64,96,168
v_cvt_pk_bf16_f32 v52,v52,v53
qt 110,3,12800
ql 6,44,0
qm 68,96,240
v_mul_f32 v54,v54,v86
qt 96,2,16384
qw 6
qm 72,100,168
v_mul_f32 v55,v55,v87
qt 98,2,16896
ql 7,44,1024
qm 76,100,240
v_cvt_pk_bf16_f32 v53,v54,v55
qt 100,3,16384
qw 5
qm 64,104,172
v_mul_f32 v60,v60,v92
qt 102,3,16896
s_add_u32 m0,s13,57344
qm 68,104,244
v_mul_f32 v61,v61,v93
qt 104,2,20480
qw 5
qm 72,108,172
v_cvt_pk_bf16_f32 v54,v60,v61
qt 106,2,20992
s_add_u32 s14,s17,3539072
qm 76,108,244
v_mul_f32 v62,v62,v94
qt 108,3,20480
qw 5
qm 64,96,176
v_mul_f32 v63,v63,v95
qt 110,3,20992
ql 6,44,0
qm 68,96,248
v_cvt_pk_bf16_f32 v55,v62,v63
qt 96,2,24576
qw 5
qm 72,100,176
ds_write_b128 v18,v[52:55] offset:1024
qt 98,2,25088
ql 7,44,1024
qm 76,100,248
s_add_u32 s15,s76,0
buffer_store_dwordx4 v[52:55],v18,s[88:91],s15 offen offset:1024 sc1
qt 100,3,24576
qw 6
qm 64,104,180
qt 102,3,25088
s_add_u32 m0,s13,65536
qm 68,104,252
qt 104,2,28672
qw 6
qm 72,108,180
s_waitcnt vmcnt(8) lgkmcnt(4)
s_barrier
qt 106,2,29184
s_add_u32 s14,s17,4128896
qm 76,108,252
qt 108,3,28672
qw 5
qz 80,96,112
qt 110,3,29184
ql 6,44,0
qz 84,96,184
v_mul_f32 v16,v64,v64
qt 96,4,32768
qw 5
qz 88,100,112
v_fma_f32 v16,v16,v12,v10
qt 98,4,33280
)ASM"
R"ASM(ql 7,44,1024
qz 92,100,184
v_mul_f32 v16,v16,v64
qt 100,5,32768
qw 5
qm 80,104,116
v_exp_f32 v16,v16
qt 102,5,33280
s_add_u32 m0,s13,73728
qm 84,104,188
v_fma_f32 v16,v16,v14,v14
qt 104,4,36864
qw 5
qm 88,108,116
v_rcp_f32 v16,v16
qt 106,4,37376
s_add_u32 s14,s17,4718720
qm 92,108,188
v_mul_f32 v64,v64,v16
qt 108,5,36864
qw 5
qm 80,96,120
v_mul_f32 v16,v65,v65
qt 110,5,37376
ql 6,44,0
qm 84,96,192
v_fma_f32 v16,v16,v12,v10
qt 96,4,40960
qw 5
qm 88,100,120
v_mul_f32 v16,v16,v65
qt 98,4,41472
ql 7,44,1024
qm 92,100,192
v_exp_f32 v16,v16
qt 100,5,40960
qw 5
qm 80,104,124
v_fma_f32 v16,v16,v14,v14
qt 102,5,41472
s_mov_b32 m0,s13
qm 84,104,196
v_rcp_f32 v16,v16
qt 104,4,45056
qw 5
qm 88,108,124
v_mul_f32 v65,v65,v16
qt 106,4,45568
s_add_u32 s14,s17,256
qm 92,108,196
v_mul_f32 v16,v66,v66
qt 108,5,45056
qw 5
qm 80,96,128
v_fma_f32 v16,v16,v12,v10
qt 110,5,45568
ql 6,40,0
qm 84,96,200
v_mul_f32 v16,v16,v66
qt 96,2,32768
qw 5
qm 88,100,128
v_exp_f32 v16,v16
qt 98,2,33280
ql 7,40,1024
qm 92,100,200
v_fma_f32 v16,v16,v14,v14
qt 100,3,32768
qw 5
qm 80,104,132
v_rcp_f32 v16,v16
qt 102,3,33280
s_add_u32 m0,s13,98304
qm 84,104,204
v_mul_f32 v66,v66,v16
qt 104,2,36864
qw 5
qm 88,108,132
s_waitcnt vmcnt(7)
s_barrier
v_mul_f32 v16,v67,v67
qt 106,2,37376
s_add_u32 s14,s17,590080
qm 92,108,204
v_fma_f32 v16,v16,v12,v10
qt 108,3,36864
qw 5
qm 80,96,136
v_mul_f32 v16,v16,v67
qt 110,3,37376
ql 6,40,0
qm 84,96,208
v_exp_f32 v16,v16
qt 96,2,40960
qw 5
qm 88,100,136
v_fma_f32 v16,v16,v14,v14
qt 98,2,41472
ql 7,40,1024
qm 92,100,208
v_rcp_f32 v16,v16
qt 100,3,40960
qw 5
qm 80,104,140
v_mul_f32 v67,v67,v16
qt 102,3,41472
s_add_u32 m0,s13,106496
qm 84,104,212
v_mul_f32 v16,v72,v72
qt 104,2,45056
qw 5
qm 88,108,140
v_fma_f32 v16,v16,v12,v10
qt 106,2,45568
s_add_u32 s14,s17,1179904
qm 92,108,212
v_mul_f32 v16,v16,v72
qt 108,3,45056
qw 5
qm 80,96,144
v_exp_f32 v16,v16
qt 110,3,45568
ql 6,40,0
qm 84,96,216
v_fma_f32 v16,v16,v14,v14
qt 96,2,49152
qw 5
qm 88,100,144
v_rcp_f32 v16,v16
qt 98,2,49664
ql 7,40,1024
qm 92,100,216
v_mul_f32 v72,v72,v16
qt 100,3,49152
qw 5
qm 80,104,148
v_mul_f32 v16,v73,v73
qt 102,3,49664
s_add_u32 m0,s13,8192
qm 84,104,220
v_fma_f32 v16,v16,v12,v10
qt 104,2,53248
qw 5
qm 88,108,148
v_mul_f32 v16,v16,v73
qt 106,2,53760
s_add_u32 s14,s17,1769728
qm 92,108,220
v_exp_f32 v16,v16
qt 108,3,53248
qw 5
qm 80,96,152
v_fma_f32 v16,v16,v14,v14
qt 110,3,53760
ql 6,40,0
qm 84,96,224
v_rcp_f32 v16,v16
qt 96,2,57344
qw 5
qm 88,100,152
v_mul_f32 v73,v73,v16
qt 98,2,57856
ql 7,40,1024
qm 92,100,224
v_mul_f32 v16,v74,v74
qt 100,3,57344
qw 5
qm 80,104,156
v_fma_f32 v16,v16,v12,v10
qt 102,3,57856
s_add_u32 m0,s13,16384
qm 84,104,228
v_mul_f32 v16,v16,v74
qt 104,2,61440
qw 5
qm 88,108,156
s_waitcnt vmcnt(6)
s_barrier
v_exp_f32 v16,v16
qt 106,2,61952
s_add_u32 s14,s17,2359552
qm 92,108,228
v_fma_f32 v16,v16,v14,v14
qt 108,3,61440
qw 5
qm 80,96,160
v_rcp_f32 v16,v16
qt 110,3,61952
ql 6,40,0
qm 84,96,232
v_mul_f32 v74,v74,v16
qt 96,4,0
qw 5
qm 88,100,160
v_mul_f32 v16,v75,v75
qt 98,4,512
ql 7,40,1024
qm 92,100,232
v_fma_f32 v16,v16,v12,v10
qt 100,5,0
qw 5
qm 80,104,164
v_mul_f32 v16,v16,v75
qt 102,5,512
s_add_u32 m0,s13,24576
qm 84,104,236
v_exp_f32 v16,v16
qt 104,4,4096
qw 5
qm 88,108,164
v_fma_f32 v16,v16,v14,v14
qt 106,4,4608
s_add_u32 s14,s17,2949376
qm 92,108,236
v_rcp_f32 v16,v16
qt 108,5,4096
qw 5
qm 80,96,168
v_mul_f32 v75,v75,v16
qt 110,5,4608
ql 6,40,0
qm 84,96,240
v_mul_f32 v16,v68,v68
qt 96,4,8192
qw 5
qm 88,100,168
v_fma_f32 v16,v16,v13,v11
qt 98,4,8704
ql 7,40,1024
qm 92,100,240
v_mul_f32 v16,v16,v68
qt 100,5,8192
qw 5
qm 80,104,172
v_exp_f32 v16,v16
qt 102,5,8704
s_add_u32 m0,s13,32768
qm 84,104,244
v_fma_f32 v16,v16,v15,v15
qt 104,4,12288
qw 5
qm 88,108,172
v_rcp_f32 v16,v16
qt 106,4,12800
s_add_u32 s14,s17,3539200
qm 92,108,244
v_mul_f32 v68,v68,v16
qt 108,5,12288
qw 5
qm 80,96,176
v_mul_f32 v16,v69,v69
qt 110,5,12800
ql 6,40,0
qm 84,96,248
v_fma_f32 v16,v16,v13,v11
qt 96,2,0
qw 5
qm 88,100,176
v_mul_f32 v16,v16,v69
qt 98,2,512
ql 7,40,1024
qm 92,100,248
v_exp_f32 v16,v16
qt 100,3,0
qw 5
qm 80,104,180
v_fma_f32 v16,v16,v15,v15
qt 102,3,512
s_add_u32 m0,s13,40960
qm 84,104,252
v_rcp_f32 v16,v16
qt 104,2,4096
qw 5
qm 88,108,180
s_waitcnt vmcnt(6)
s_barrier
v_mul_f32 v69,v69,v16
qt 106,2,4608
s_add_u32 s14,s17,4129024
qm 92,108,252
qt 108,3,4096
qw 5
qz 48,96,112
v_mul_f32 v16,v70,v70
qt 110,3,4608
ql 6,40,0
qz 52,96,184
v_fma_f32 v16,v16,v13,v11
qt 96,4,32768
qw 5
qz 56,100,112
v_mul_f32 v16,v16,v70
qt 98,4,33280
ql 7,40,1024
qz 60,100,184
v_exp_f32 v16,v16
qt 100,5,32768
qw 5
qm 48,104,116
v_fma_f32 v16,v16,v15,v15
qt 102,5,33280
s_add_u32 m0,s13,49152
qm 52,104,188
v_rcp_f32 v16,v16
qt 104,4,36864
qw 5
qm 56,108,116
v_mul_f32 v70,v70,v16
qt 106,4,37376
s_add_u32 s14,s17,4718848
qm 60,108,188
v_mul_f32 v16,v71,v71
qt 108,5,36864
qw 5
qm 48,96,120
v_fma_f32 v16,v16,v13,v11
qt 110,5,37376
ql 6,40,0
qm 52,96,192
v_mul_f32 v16,v16,v71
qt 96,4,40960
qw 5
qm 56,100,120
v_exp_f32 v16,v16
qt 98,4,41472
ql 7,40,1024
qm 60,100,192
v_fma_f32 v16,v16,v15,v15
qt 100,5,40960
qw 5
qm 48,104,124
v_rcp_f32 v16,v16
qt 102,5,41472
s_add_u32 m0,s13,57344
qm 52,104,196
v_mul_f32 v71,v71,v16
qt 104,4,45056
qw 5
qm 56,108,124
v_mul_f32 v16,v76,v76
qt 106,4,45568
s_add_u32 s14,s17,256
qm 60,108,196
v_fma_f32 v16,v16,v13,v11
qt 108,5,45056
qw 5
qm 48,96,128
v_mul_f32 v16,v16,v76
qt 110,5,45568
ql 6,44,0
qm 52,96,200
v_exp_f32 v16,v16
qt 96,2,8192
qw 5
qm 56,100,128
v_fma_f32 v16,v16,v15,v15
qt 98,2,8704
ql 7,44,1024
qm 60,100,200
v_rcp_f32 v16,v16
qt 100,3,8192
qw 5
qm 48,104,132
v_mul_f32 v76,v76,v16
qt 102,3,8704
s_add_u32 m0,s13,65536
qm 52,104,204
v_mul_f32 v16,v77,v77
qt 104,2,12288
qw 5
qm 56,108,132
s_waitcnt vmcnt(6)
s_barrier
v_fma_f32 v16,v16,v13,v11
qt 106,2,12800
s_add_u32 s14,s17,590080
qm 60,108,204
v_mul_f32 v16,v16,v77
qt 108,3,12288
qw 5
qm 48,96,136
v_exp_f32 v16,v16
qt 110,3,12800
ql 6,44,0
qm 52,96,208
v_fma_f32 v16,v16,v15,v15
qt 96,2,16384
qw 5
qm 56,100,136
v_rcp_f32 v16,v16
qt 98,2,16896
ql 7,44,1024
qm 60,100,208
v_mul_f32 v77,v77,v16
qt 100,3,16384
qw 5
qm 48,104,140
v_mul_f32 v16,v78,v78
qt 102,3,16896
s_add_u32 m0,s13,73728
qm 52,104,212
v_fma_f32 v16,v16,v13,v11
qt 104,2,20480
qw 5
qm 56,108,140
v_mul_f32 v16,v16,v78
qt 106,2,20992
s_add_u32 s14,s17,1179904
qm 60,108,212
v_exp_f32 v16,v16
qt 108,3,20480
qw 5
qm 48,96,144
v_fma_f32 v16,v16,v15,v15
qt 110,3,20992
ql 6,44,0
qm 52,96,216
v_rcp_f32 v16,v16
qt 96,2,24576
qw 5
qm 56,100,144
v_mul_f32 v78,v78,v16
qt 98,2,25088
ql 7,44,1024
qm 60,100,216
v_mul_f32 v16,v79,v79
qt 100,3,24576
qw 5
qm 48,104,148
v_fma_f32 v16,v16,v13,v11
qt 102,3,25088
s_mov_b32 m0,s13
qm 52,104,220
v_mul_f32 v16,v16,v79
qt 104,2,28672
qw 5
qm 56,108,148
v_exp_f32 v16,v16
qt 106,2,29184
s_add_u32 s14,s17,1769728
qm 60,108,220
v_fma_f32 v16,v16,v15,v15
qt 108,3,28672
qw 5
qm 48,96,152
v_rcp_f32 v16,v16
qt 110,3,29184
ql 6,44,0
qm 52,96,224
v_mul_f32 v79,v79,v16
qt 96,2,32768
qw 5
qm 56,100,152
v_mul_f32 v64,v64,v80
qt 98,2,33280
ql 7,44,1024
qm 60,100,224
v_mul_f32 v65,v65,v81
qt 100,3,32768
qw 5
qm 48,104,156
v_cvt_pk_bf16_f32 v64,v64,v65
qt 102,3,33280
s_add_u32 m0,s13,8192
qm 52,104,228
v_mul_f32 v66,v66,v82
qt 104,2,36864
qw 5
qm 56,108,156
s_waitcnt vmcnt(6)
s_barrier
v_mul_f32 v67,v67,v83
qt 106,2,37376
s_add_u32 s14,s17,2359552
qm 60,108,228
v_cvt_pk_bf16_f32 v65,v66,v67
qt 108,3,36864
qw 5
qm 48,96,160
v_mul_f32 v72,v72,v88
v_and_b32 v44,15,v0
v_lshrrev_b32 v45,4,v0
qt 110,3,37376
ql 6,44,0
qm 52,96,232
v_mul_f32 v73,v73,v89
v_lshl_add_u32 v17,v44,2,v45
qt 96,2,40960
qw 5
qm 56,100,160
v_cvt_pk_bf16_f32 v66,v72,v73
v_lshl_add_u32 v45,s7,2,v45
qt 98,2,41472
ql 7,44,1024
qm 60,100,232
v_mul_f32 v74,v74,v90
v_lshlrev_b32 v28,4,v44
qt 100,3,40960
qw 5
qm 48,104,164
v_mul_f32 v75,v75,v91
v_add_u32 v28,123904,v28
qt 102,3,41472
s_add_u32 m0,s13,16384
qm 52,104,236
v_cvt_pk_bf16_f32 v67,v74,v75
v_lshlrev_b32 v44,3,v44
v_lshl_add_u32 v42,v45,9,v44
qt 104,2,45056
qw 5
qm 56,108,164
ds_write_b128 v18,v[64:67] offset:16384
v_mul_f32 v68,v68,v84
v_add_u32 v42,114688,v42
qt 106,2,45568
s_add_u32 s14,s17,2949376
qm 60,108,236
s_add_u32 s15,s76,16384
buffer_store_dwordx4 v[64:67],v18,s[88:91],s15 offen offset:0 sc1
v_mul_f32 v69,v69,v85
v_add_u32 v44,124928,v44
qt 108,3,45056
qw 6
qm 48,96,168
v_cvt_pk_bf16_f32 v68,v68,v69
qr 28,28,0
qt 110,3,45568
ql 6,44,0
qm 52,96,240
v_mul_f32 v70,v70,v86
qd 32,44,0
qt 96,2,49152
qw 8
qm 56,100,168
v_mul_f32 v71,v71,v87
qd 34,44,512
qd 36,44,1024
qt 98,2,49664
ql 7,44,1024
qm 60,100,240
v_cvt_pk_bf16_f32 v69,v70,v71
qd 38,44,1536
qt 100,3,49152
qw 10
qm 48,104,172
v_mul_f32 v76,v76,v92
qd 40,44,2048
qt 102,3,49664
s_add_u32 m0,s13,24576
qm 52,104,244
v_mul_f32 v77,v77,v93
qd 42,42,0
qt 104,2,53248
qw 11
qm 56,108,172
v_cvt_pk_bf16_f32 v70,v76,v77
qr 20,18,49152
qt 106,2,53760
s_add_u32 s14,s17,3539200
qm 60,108,244
v_mul_f32 v78,v78,v94
qr 24,18,50176
v_lshlrev_b32 v17,3,v17
qt 108,3,53248
qw 10
qm 48,96,176
v_mul_f32 v79,v79,v95
v_add_u32 v17,s13,v17
qt 110,3,53760
ql 6,44,0
qm 52,96,248
v_cvt_pk_bf16_f32 v71,v78,v79
v_add_u32 v17,131072,v17
qt 96,2,57344
qw 8
qm 56,100,176
ds_write_b128 v18,v[68:71] offset:17408
v_lshlrev_b32 v45,3,v45
qt 98,2,57856
ql 7,44,1024
qm 60,100,248
s_add_u32 s15,s76,16384
buffer_store_dwordx4 v[68:71],v18,s[88:91],s15 offen offset:1024 sc1
v_cvt_f32_u32 v45,v45
qt 100,3,57344
qw 7
qm 48,104,180
qt 102,3,57856
s_add_u32 m0,s13,32768
qm 52,104,252
qt 104,2,61440
qw 6
qm 56,108,180
s_waitcnt vmcnt(8) lgkmcnt(4)
s_barrier
qt 106,2,61952
s_add_u32 s14,s17,4129024
qm 60,108,252
qt 108,3,61440
v_add_f32 v34,v34,v36
v_add_f32 v35,v35,v37
v_add_f32 v38,v38,v40
v_add_f32 v39,v39,v41
v_add_f32 v34,v34,v38
v_add_f32 v35,v35,v39
v_mul_f32 v44,v45,v28
v_mul_f32 v37,v45,v29
v_exp_f32 v44,v44
v_fract_f32 v37,v37
v_cos_f32 v36,v37
v_sin_f32 v37,v37
v_mul_f32 v36,v36,v44
v_mul_f32 v37,v37,v44
v_fma_f32 v38,v36,v34,v42
v_fma_f32 v39,v36,v35,v43
v_fma_f32 v38,-v37,v35,v38
v_fma_f32 v39,v37,v34,v39
v_lshlrev_b32 v34,16,v20
v_and_b32 v35,0xffff0000,v20
v_fma_f32 v40,v32,v38,v34
v_fma_f32 v41,v32,v39,v35
v_fma_f32 v40,-v33,v39,v40
v_fma_f32 v41,v33,v38,v41
v_mul_f32 v36,v30,v40
v_mul_f32 v42,v30,v41
v_fma_f32 v36,-v31,v41,v36
v_fma_f32 v42,v31,v40,v42
v_lshlrev_b32 v34,16,v21
v_and_b32 v35,0xffff0000,v21
v_fma_f32 v38,v32,v40,v34
v_fma_f32 v39,v32,v41,v35
v_fma_f32 v38,-v33,v41,v38
v_fma_f32 v39,v33,v40,v39
v_mul_f32 v37,v30,v38
v_mul_f32 v43,v30,v39
v_fma_f32 v37,-v31,v39,v37
v_fma_f32 v43,v31,v38,v43
v_cvt_pk_bf16_f32 v20,v36,v37
v_cvt_pk_bf16_f32 v28,-v42,-v43
v_lshlrev_b32 v34,16,v22
v_and_b32 v35,0xffff0000,v22
v_fma_f32 v40,v32,v38,v34
v_fma_f32 v41,v32,v39,v35
v_fma_f32 v40,-v33,v39,v40
v_fma_f32 v41,v33,v38,v41
v_mul_f32 v36,v30,v40
v_mul_f32 v42,v30,v41
v_fma_f32 v36,-v31,v41,v36
v_fma_f32 v42,v31,v40,v42
v_lshlrev_b32 v34,16,v23
v_and_b32 v35,0xffff0000,v23
v_fma_f32 v38,v32,v40,v34
v_fma_f32 v39,v32,v41,v35
v_fma_f32 v38,-v33,v41,v38
v_fma_f32 v39,v33,v40,v39
v_mul_f32 v37,v30,v38
v_mul_f32 v43,v30,v39
v_fma_f32 v37,-v31,v39,v37
v_fma_f32 v43,v31,v38,v43
v_cvt_pk_bf16_f32 v21,v36,v37
v_cvt_pk_bf16_f32 v29,-v42,-v43
ds_write_b64 v17,v[20:21] offset:0
ds_write_b64 v17,v[28:29] offset:512
v_lshlrev_b32 v34,16,v24
v_and_b32 v35,0xffff0000,v24
v_fma_f32 v40,v32,v38,v34
v_fma_f32 v41,v32,v39,v35
v_fma_f32 v40,-v33,v39,v40
v_fma_f32 v41,v33,v38,v41
v_mul_f32 v36,v30,v40
v_mul_f32 v42,v30,v41
v_fma_f32 v36,-v31,v41,v36
v_fma_f32 v42,v31,v40,v42
v_lshlrev_b32 v34,16,v25
v_and_b32 v35,0xffff0000,v25
v_fma_f32 v38,v32,v40,v34
v_fma_f32 v39,v32,v41,v35
v_fma_f32 v38,-v33,v41,v38
v_fma_f32 v39,v33,v40,v39
v_mul_f32 v37,v30,v38
v_mul_f32 v43,v30,v39
v_fma_f32 v37,-v31,v39,v37
v_fma_f32 v43,v31,v38,v43
v_cvt_pk_bf16_f32 v24,v36,v37
v_cvt_pk_bf16_f32 v28,-v42,-v43
v_lshlrev_b32 v34,16,v26
v_and_b32 v35,0xffff0000,v26
v_fma_f32 v40,v32,v38,v34
v_fma_f32 v41,v32,v39,v35
v_fma_f32 v40,-v33,v39,v40
v_fma_f32 v41,v33,v38,v41
v_mul_f32 v36,v30,v40
v_mul_f32 v42,v30,v41
v_fma_f32 v36,-v31,v41,v36
v_fma_f32 v42,v31,v40,v42
v_lshlrev_b32 v34,16,v27
v_and_b32 v35,0xffff0000,v27
v_fma_f32 v38,v32,v40,v34
v_fma_f32 v39,v32,v41,v35
v_fma_f32 v38,-v33,v41,v38
v_fma_f32 v39,v33,v40,v39
v_mul_f32 v37,v30,v38
v_mul_f32 v43,v30,v39
v_fma_f32 v37,-v31,v39,v37
v_fma_f32 v43,v31,v38,v43
v_cvt_pk_bf16_f32 v25,v36,v37
v_cvt_pk_bf16_f32 v29,-v42,-v43
ds_write_b64 v17,v[24:25] offset:1024
ds_write_b64 v17,v[28:29] offset:1536
qw 9
qz 80,96,112
qt 110,3,61952
ql 6,44,0
qz 84,96,184
v_mul_f32 v16,v48,v48
qt 96,4,0
qw 9
qz 88,100,112
v_fma_f32 v16,v16,v12,v10
v_lshlrev_b32 v36,3,v0
v_add_u32 v36,s13,v36
qt 98,4,512
ql 7,44,1024
qz 92,100,184
v_mul_f32 v16,v16,v48
v_add_u32 v36,131072,v36
qt 100,5,0
qw 9
qm 80,104,116
v_exp_f32 v16,v16
qt 28,36,0
qt 102,5,512
s_add_u32 m0,s13,40960
qm 84,104,188
v_fma_f32 v16,v16,v14,v14
qt 30,36,512
qt 104,4,4096
qw 7
qm 88,108,116
v_rcp_f32 v16,v16
qt 32,36,1024
qt 106,4,4608
s_add_u32 s14,s17,4718848
qm 92,108,188
v_mul_f32 v48,v48,v16
qt 34,36,1536
qt 108,5,4096
qw 9
qm 80,96,120
v_mul_f32 v16,v49,v49
qt 110,5,4608
ql 6,44,0
qm 84,96,192
v_fma_f32 v16,v16,v12,v10
qt 96,4,8192
qw 8
qm 88,100,120
v_mul_f32 v16,v16,v49
qt 98,4,8704
ql 7,44,1024
qm 92,100,192
v_exp_f32 v16,v16
qt 100,5,8192
qw 6
qm 80,104,124
v_fma_f32 v16,v16,v14,v14
qt 102,5,8704
s_add_u32 m0,s13,49152
qm 84,104,196
v_rcp_f32 v16,v16
qt 104,4,12288
qw 5
qm 88,108,124
v_mul_f32 v49,v49,v16
qt 106,4,12800
s_add_u32 s14,s17,384
qm 92,108,196
v_mul_f32 v16,v50,v50
s_add_u32 s15,s76,65536
buffer_store_dwordx4 v[28:31],v18,s[88:91],s15 offen offset:0 sc1
qt 108,5,12288
qw 5
qm 80,96,128
v_fma_f32 v16,v16,v12,v10
s_add_u32 s15,s76,65536
buffer_store_dwordx4 v[32:35],v18,s[88:91],s15 offen offset:1024 sc1
qt 110,5,12800
ql 6,40,0
qm 84,96,200
v_mul_f32 v16,v16,v50
qt 96,2,0
qw 5
qm 88,100,128
v_exp_f32 v16,v16
qt 98,2,512
ql 7,40,1024
qm 92,100,200
v_fma_f32 v16,v16,v14,v14
qt 100,3,0
qw 5
qm 80,104,132
v_rcp_f32 v16,v16
qt 102,3,512
s_add_u32 m0,s13,57344
qm 84,104,204
v_mul_f32 v50,v50,v16
qt 104,2,4096
qw 5
qm 88,108,132
s_waitcnt vmcnt(9)
s_barrier
v_mul_f32 v16,v51,v51
qt 106,2,4608
s_add_u32 s14,s17,590208
qm 92,108,204
v_fma_f32 v16,v16,v12,v10
qt 108,3,4096
qw 5
qm 80,96,136
v_mul_f32 v16,v16,v51
qt 110,3,4608
ql 6,40,0
qm 84,96,208
v_exp_f32 v16,v16
qt 96,2,8192
qw 5
qm 88,100,136
v_fma_f32 v16,v16,v14,v14
qt 98,2,8704
ql 7,40,1024
qm 92,100,208
v_rcp_f32 v16,v16
qt 100,3,8192
qw 5
qm 80,104,140
v_mul_f32 v51,v51,v16
qt 102,3,8704
s_add_u32 m0,s13,65536
qm 84,104,212
v_mul_f32 v16,v56,v56
qt 104,2,12288
qw 5
qm 88,108,140
v_fma_f32 v16,v16,v12,v10
qt 106,2,12800
s_add_u32 s14,s17,1180032
qm 92,108,212
v_mul_f32 v16,v16,v56
qt 108,3,12288
qw 5
qm 80,96,144
v_exp_f32 v16,v16
qt 110,3,12800
ql 6,40,0
qm 84,96,216
v_fma_f32 v16,v16,v14,v14
qt 96,2,16384
qw 5
qm 88,100,144
v_rcp_f32 v16,v16
qt 98,2,16896
ql 7,40,1024
qm 92,100,216
v_mul_f32 v56,v56,v16
qt 100,3,16384
qw 5
qm 80,104,148
v_mul_f32 v16,v57,v57
qt 102,3,16896
s_add_u32 m0,s13,73728
qm 84,104,220
v_fma_f32 v16,v16,v12,v10
qt 104,2,20480
qw 5
qm 88,108,148
v_mul_f32 v16,v16,v57
qt 106,2,20992
s_add_u32 s14,s17,1769856
qm 92,108,220
v_exp_f32 v16,v16
qt 108,3,20480
qw 5
qm 80,96,152
v_fma_f32 v16,v16,v14,v14
qt 110,3,20992
ql 6,40,0
qm 84,96,224
v_rcp_f32 v16,v16
qt 96,2,24576
qw 5
qm 88,100,152
v_mul_f32 v57,v57,v16
qt 98,2,25088
ql 7,40,1024
qm 92,100,224
v_mul_f32 v16,v58,v58
qt 100,3,24576
qw 5
qm 80,104,156
v_fma_f32 v16,v16,v12,v10
qt 102,3,25088
s_mov_b32 m0,s13
qm 84,104,228
v_mul_f32 v16,v16,v58
qt 104,2,28672
qw 5
qm 88,108,156
s_waitcnt vmcnt(6)
s_barrier
v_exp_f32 v16,v16
qt 106,2,29184
s_add_u32 s14,s17,2359680
qm 92,108,228
v_fma_f32 v16,v16,v14,v14
qt 108,3,28672
qw 5
qm 80,96,160
v_rcp_f32 v16,v16
v_and_b32 v44,15,v0
v_lshrrev_b32 v45,4,v0
qt 110,3,29184
ql 6,40,0
qm 84,96,232
v_mul_f32 v58,v58,v16
v_lshl_add_u32 v17,v44,2,v45
qt 96,2,32768
qw 5
qm 88,100,160
v_mul_f32 v16,v59,v59
v_lshl_add_u32 v45,s7,2,v45
qt 98,2,33280
ql 7,40,1024
qm 92,100,232
v_fma_f32 v16,v16,v12,v10
v_lshlrev_b32 v28,4,v44
qt 100,3,32768
qw 5
qm 80,104,164
v_mul_f32 v16,v16,v59
v_add_u32 v28,123904,v28
qt 102,3,33280
s_add_u32 m0,s13,8192
qm 84,104,236
v_exp_f32 v16,v16
v_lshlrev_b32 v44,3,v44
v_lshl_add_u32 v42,v45,9,v44
qt 104,2,36864
qw 5
qm 88,108,164
v_fma_f32 v16,v16,v14,v14
v_add_u32 v42,114688,v42
qt 106,2,37376
s_add_u32 s14,s17,2949504
qm 92,108,236
v_rcp_f32 v16,v16
v_add_u32 v44,124928,v44
qt 108,3,36864
qw 5
qm 80,96,168
v_mul_f32 v59,v59,v16
qr 28,28,512
qt 110,3,37376
ql 6,40,0
qm 84,96,240
v_mul_f32 v16,v52,v52
qd 32,44,256
qt 96,2,40960
qw 7
qm 88,100,168
v_fma_f32 v16,v16,v13,v11
qd 34,44,768
qd 36,44,1280
qt 98,2,41472
ql 7,40,1024
qm 92,100,240
v_mul_f32 v16,v16,v52
qd 38,44,1792
qt 100,3,40960
qw 10
qm 80,104,172
v_exp_f32 v16,v16
qd 40,44,2304
qt 102,3,41472
s_add_u32 m0,s13,16384
qm 84,104,244
v_fma_f32 v16,v16,v15,v15
qd 42,42,256
qt 104,2,45056
qw 11
qm 88,108,172
v_rcp_f32 v16,v16
qr 20,19,0
qt 106,2,45568
s_add_u32 s14,s17,3539328
qm 92,108,244
v_mul_f32 v52,v52,v16
qr 24,19,1024
v_lshlrev_b32 v17,3,v17
qt 108,3,45056
qw 10
qm 80,96,176
v_mul_f32 v16,v53,v53
v_add_u32 v17,s13,v17
qt 110,3,45568
ql 6,40,0
qm 84,96,248
v_fma_f32 v16,v16,v13,v11
v_add_u32 v17,147456,v17
qt 96,2,49152
qw 8
qm 88,100,176
v_mul_f32 v16,v16,v53
v_lshlrev_b32 v45,3,v45
qt 98,2,49664
ql 7,40,1024
qm 92,100,248
v_exp_f32 v16,v16
v_cvt_f32_u32 v45,v45
qt 100,3,49152
qw 6
qm 80,104,180
v_fma_f32 v16,v16,v15,v15
qt 102,3,49664
s_add_u32 m0,s13,24576
qm 84,104,252
v_rcp_f32 v16,v16
qt 104,2,53248
qw 5
qm 88,108,180
s_waitcnt vmcnt(6)
s_barrier
v_mul_f32 v53,v53,v16
qt 106,2,53760
s_add_u32 s14,s17,4129152
qm 92,108,252
qt 108,3,53248
v_add_f32 v34,v34,v36
v_add_f32 v35,v35,v37
v_add_f32 v38,v38,v40
v_add_f32 v39,v39,v41
v_add_f32 v34,v34,v38
v_add_f32 v35,v35,v39
v_mul_f32 v44,v45,v28
v_mul_f32 v37,v45,v29
v_exp_f32 v44,v44
v_fract_f32 v37,v37
v_cos_f32 v36,v37
v_sin_f32 v37,v37
v_mul_f32 v36,v36,v44
v_mul_f32 v37,v37,v44
v_fma_f32 v38,v36,v34,v42
v_fma_f32 v39,v36,v35,v43
v_fma_f32 v38,-v37,v35,v38
v_fma_f32 v39,v37,v34,v39
v_lshlrev_b32 v34,16,v20
v_and_b32 v35,0xffff0000,v20
v_fma_f32 v40,v32,v38,v34
v_fma_f32 v41,v32,v39,v35
v_fma_f32 v40,-v33,v39,v40
v_fma_f32 v41,v33,v38,v41
v_mul_f32 v36,v30,v40
v_mul_f32 v42,v30,v41
v_fma_f32 v36,-v31,v41,v36
v_fma_f32 v42,v31,v40,v42
v_lshlrev_b32 v34,16,v21
v_and_b32 v35,0xffff0000,v21
v_fma_f32 v38,v32,v40,v34
v_fma_f32 v39,v32,v41,v35
v_fma_f32 v38,-v33,v41,v38
v_fma_f32 v39,v33,v40,v39
v_mul_f32 v37,v30,v38
v_mul_f32 v43,v30,v39
v_fma_f32 v37,-v31,v39,v37
v_fma_f32 v43,v31,v38,v43
v_cvt_pk_bf16_f32 v20,v36,v37
v_cvt_pk_bf16_f32 v28,-v42,-v43
v_lshlrev_b32 v34,16,v22
v_and_b32 v35,0xffff0000,v22
v_fma_f32 v40,v32,v38,v34
v_fma_f32 v41,v32,v39,v35
v_fma_f32 v40,-v33,v39,v40
v_fma_f32 v41,v33,v38,v41
v_mul_f32 v36,v30,v40
v_mul_f32 v42,v30,v41
v_fma_f32 v36,-v31,v41,v36
v_fma_f32 v42,v31,v40,v42
v_lshlrev_b32 v34,16,v23
v_and_b32 v35,0xffff0000,v23
v_fma_f32 v38,v32,v40,v34
v_fma_f32 v39,v32,v41,v35
v_fma_f32 v38,-v33,v41,v38
v_fma_f32 v39,v33,v40,v39
v_mul_f32 v37,v30,v38
v_mul_f32 v43,v30,v39
v_fma_f32 v37,-v31,v39,v37
v_fma_f32 v43,v31,v38,v43
v_cvt_pk_bf16_f32 v21,v36,v37
v_cvt_pk_bf16_f32 v29,-v42,-v43
ds_write_b64 v17,v[20:21] offset:0
ds_write_b64 v17,v[28:29] offset:512
v_lshlrev_b32 v34,16,v24
v_and_b32 v35,0xffff0000,v24
v_fma_f32 v40,v32,v38,v34
v_fma_f32 v41,v32,v39,v35
v_fma_f32 v40,-v33,v39,v40
v_fma_f32 v41,v33,v38,v41
v_mul_f32 v36,v30,v40
v_mul_f32 v42,v30,v41
v_fma_f32 v36,-v31,v41,v36
v_fma_f32 v42,v31,v40,v42
v_lshlrev_b32 v34,16,v25
v_and_b32 v35,0xffff0000,v25
v_fma_f32 v38,v32,v40,v34
v_fma_f32 v39,v32,v41,v35
v_fma_f32 v38,-v33,v41,v38
v_fma_f32 v39,v33,v40,v39
v_mul_f32 v37,v30,v38
v_mul_f32 v43,v30,v39
v_fma_f32 v37,-v31,v39,v37
v_fma_f32 v43,v31,v38,v43
v_cvt_pk_bf16_f32 v24,v36,v37
v_cvt_pk_bf16_f32 v28,-v42,-v43
v_lshlrev_b32 v34,16,v26
v_and_b32 v35,0xffff0000,v26
v_fma_f32 v40,v32,v38,v34
v_fma_f32 v41,v32,v39,v35
v_fma_f32 v40,-v33,v39,v40
v_fma_f32 v41,v33,v38,v41
v_mul_f32 v36,v30,v40
v_mul_f32 v42,v30,v41
v_fma_f32 v36,-v31,v41,v36
v_fma_f32 v42,v31,v40,v42
v_lshlrev_b32 v34,16,v27
v_and_b32 v35,0xffff0000,v27
v_fma_f32 v38,v32,v40,v34
v_fma_f32 v39,v32,v41,v35
v_fma_f32 v38,-v33,v41,v38
v_fma_f32 v39,v33,v40,v39
v_mul_f32 v37,v30,v38
v_mul_f32 v43,v30,v39
v_fma_f32 v37,-v31,v39,v37
v_fma_f32 v43,v31,v38,v43
v_cvt_pk_bf16_f32 v25,v36,v37
v_cvt_pk_bf16_f32 v29,-v42,-v43
ds_write_b64 v17,v[24:25] offset:1024
ds_write_b64 v17,v[28:29] offset:1536
qw 9
qz 64,96,112
v_mul_f32 v16,v54,v54
qt 110,3,53760
ql 6,40,0
qz 68,96,184
v_fma_f32 v16,v16,v13,v11
v_bfe_u32 v44,v0,2,2
qt 96,2,57344
qw 9
qz 72,100,112
v_mul_f32 v16,v16,v54
v_lshlrev_b32 v36,3,v0
v_add_u32 v36,s13,v36
v_mov_b32 v38,s16
v_and_b32 v37,48,v0
qt 98,2,57856
ql 7,40,1024
qz 76,100,184
v_exp_f32 v16,v16
v_add_u32 v36,131072,v36
v_lshlrev_b32 v37,24,v37
qt 100,3,57344
qw 9
qm 64,104,116
v_fma_f32 v16,v16,v15,v15
qt 28,36,16384
v_lshl_or_b32 v37,v0,7,v37
qt 102,3,57856
s_add_u32 m0,s13,32768
qm 68,104,188
v_rcp_f32 v16,v16
qt 30,36,16896
s_mul_i32 s72,s10,2048
s_and_b32 s33,s8,3
s_lshl_b32 s33,s33,2
s_add_u32 s33,s33,s7
s_lshl_b32 s33,s33,3
s_lshl_b32 s33,s33,2
s_add_u32 s33,s33,122896
v_lshl_add_u32 v45,v44,2,s33
qt 104,2,61440
qw 7
qm 72,108,116
v_mul_f32 v54,v54,v16
qt 32,36,17408
s_add_u32 s72,s72,8192
qt 106,2,61952
s_add_u32 s14,s17,4718976
qm 76,108,188
v_mul_f32 v16,v55,v55
qt 34,36,17920
s_and_b32 s30,s8,3
qt 108,3,61440
qw 9
qm 64,96,120
v_fma_f32 v16,v16,v13,v11
s_lshl_b32 s30,s30,4
qt 110,3,61952
ql 6,40,0
qm 68,96,192
v_mul_f32 v16,v16,v55
s_and_b32 s31,s8,4
qt 96,4,0
qw 8
qm 72,100,120
v_exp_f32 v16,v16
s_add_u32 s30,s30,s31
ds_read_b32 v40,v45 offset:0
qt 98,4,512
ql 7,40,1024
qm 76,100,192
v_fma_f32 v16,v16,v15,v15
s_add_u32 s68,s30,s72
qt 100,5,0
qw 7
qm 64,104,124
v_rcp_f32 v16,v16
s_lshl_b32 s31,s8,8
qt 102,5,512
s_add_u32 m0,s13,40960
qm 68,104,196
v_mul_f32 v55,v55,v16
s_add_u32 s78,s68,s31
qt 104,4,4096
qw 6
qm 72,108,124
v_mul_f32 v16,v60,v60
s_add_u32 s78,s78,128
ds_read_b32 v41,v45 offset:512
qt 106,4,4608
s_add_u32 s14,s17,384
qm 76,108,196
v_fma_f32 v16,v16,v13,v11
s_add_u32 s15,s76,81920
buffer_store_dwordx4 v[28:31],v18,s[88:91],s15 offen offset:0 sc1
s_add_u32 s68,s68,8
s_cmp_eq_u32 s6,4
s_cselect_b32 s68,s68,0x7ff00000
qt 108,5,4096
qw 6
qm 64,96,128
v_mul_f32 v16,v16,v60
s_add_u32 s15,s76,81920
buffer_store_dwordx4 v[32:35],v18,s[88:91],s15 offen offset:1024 sc1
s_add_u32 s72,s72,s31
qt 110,5,4608
ql 6,44,0
qm 68,96,200
v_exp_f32 v16,v16
s_add_u32 s72,s72,128
qt 96,4,8192
qw 6
qm 72,100,128
v_fma_f32 v16,v16,v15,v15
s_add_u32 s74,s16,8
qt 98,4,8704
ql 7,44,1024
qm 76,100,200
v_rcp_f32 v16,v16
v_bfe_u32 v20,v0,3,2
qt 100,5,8192
qw 5
qm 64,104,132
v_mul_f32 v60,v60,v16
v_lshrrev_b32 v21,1,v0
qt 102,5,8704
s_add_u32 m0,s13,49152
qm 68,104,204
v_mul_f32 v16,v61,v61
v_and_or_b32 v20,v21,16,v20
qt 104,4,12288
qw 5
qm 72,108,132
s_waitcnt vmcnt(8)
s_barrier
v_fma_f32 v16,v16,v13,v11
v_mul_u32_u24 v20,1152,v20
qt 106,4,12800
s_add_u32 s14,s17,590208
qm 76,108,204
v_mul_f32 v16,v16,v61
v_lshrrev_b32 v21,3,v0
qt 108,5,12288
qw 5
qm 64,96,136
v_exp_f32 v16,v16
v_and_b32 v21,2,v21
qt 110,5,12800
ql 6,44,0
qm 68,96,208
v_fma_f32 v16,v16,v15,v15
v_xor_b32 v21,v21,v0
qt 96,2,0
qw 5
qm 72,100,136
v_rcp_f32 v16,v16
v_and_b32 v21,7,v21
qt 98,2,512
ql 7,44,1024
qm 76,100,208
v_mul_f32 v61,v61,v16
v_lshl_add_u32 v20,v21,4,v20
qt 100,3,0
s_add_u32 m0,s13,57344
s_add_u32 s14,s17,1180032
qw 5
qm 64,104,140
v_mul_f32 v16,v62,v62
v_xor_b32 v21,64,v20
qt 102,3,512
ql 6,44,0
qm 68,104,212
v_fma_f32 v16,v16,v13,v11
v_add_u32 v21,3584,v21
v_and_b32 v42,15,v0
qt 104,2,4096
qw 5
qm 72,108,140
v_mul_f32 v16,v16,v62
v_lshrrev_b32 v43,5,v0
qt 106,2,4608
ql 7,44,1024
qm 76,108,212
v_exp_f32 v16,v16
v_bfe_u32 v25,v0,4,1
qt 108,3,4096
s_add_u32 m0,s13,65536
s_add_u32 s14,s17,1769856
qw 5
qm 64,96,144
v_fma_f32 v16,v16,v15,v15
v_lshlrev_b32 v25,3,v25
qt 110,3,4608
ql 6,44,0
qm 68,96,216
v_rcp_f32 v16,v16
v_add_u32 v25,0,v25
qt 96,2,8192
qw 5
qm 72,100,144
v_mul_f32 v62,v62,v16
v_lshl_add_u32 v24,v42,7,v25
qt 98,2,8704
ql 7,44,1024
qm 76,100,216
v_mul_f32 v16,v63,v63
v_lshl_add_u32 v22,v42,5,v25
qt 100,3,8192
qw 5
qm 64,104,148
v_fma_f32 v16,v16,v13,v11
v_lshrrev_b32 v42,1,v42
qt 102,3,8704
qm 68,104,220
v_mul_f32 v16,v16,v63
v_xor_b32 v25,v42,v43
qt 104,2,12288
qw 5
qm 72,108,148
v_exp_f32 v16,v16
v_lshl_add_u32 v24,v25,4,v24
qt 106,2,12800
qm 76,108,220
v_fma_f32 v16,v16,v15,v15
v_lshrrev_b32 v42,2,v42
qt 108,3,12288
qw 5
qm 64,96,152
v_rcp_f32 v16,v16
v_xor_b32 v42,v42,v43
qt 110,3,12800
qm 68,96,224
v_mul_f32 v63,v63,v16
v_lshl_add_u32 v22,v42,4,v22
qt 96,2,16384
qw 5
qm 72,100,152
v_mul_f32 v48,v48,v80
v_add_u32 v23,0x10000,v22
v_xor_b32 v25,32,v24
qt 98,2,16896
qm 76,100,224
v_mul_f32 v49,v49,v81
v_xor_b32 v26,64,v24
qt 100,3,16384
qw 5
qm 64,104,156
v_cvt_pk_bf16_f32 v48,v48,v49
v_xor_b32 v27,0x60,v24
qt 102,3,16896
s_add_u32 m0,s13,73728
qm 68,104,228
v_mul_f32 v50,v50,v82
v_lshlrev_b32 v36,3,v0
qt 104,2,20480
qw 5
qm 72,108,156
s_waitcnt vmcnt(0)
s_barrier
v_mul_f32 v51,v51,v83
v_add_u32 v36,s13,v36
qt 106,2,20992
s_add_u32 s14,s17,2359680
qm 76,108,228
v_cvt_pk_bf16_f32 v49,v50,v51
v_add_u32 v36,131072,v36
qt 108,3,20480
qw 5
qm 64,96,160
v_mul_f32 v56,v56,v88
v_lshrrev_b32 v8,3,v0
qt 110,3,20992
ql 6,44,0
qm 68,96,232
v_mul_f32 v57,v57,v89
v_mul_u32_u24 v8,1152,v8
qt 96,2,24576
qw 5
qm 72,100,160
v_cvt_pk_bf16_f32 v50,v56,v57
v_lshrrev_b32 v9,4,v0
qt 98,2,25088
ql 7,44,1024
qm 76,100,232
v_mul_f32 v58,v58,v90
v_xor_b32 v9,v9,v0
qt 100,3,24576
qw 5
qm 64,104,164
v_mul_f32 v59,v59,v91
v_and_b32 v9,7,v9
qt 102,3,25088
s_mov_b32 m0,s13
qm 68,104,236
v_cvt_pk_bf16_f32 v51,v58,v59
v_lshl_add_u32 v8,v9,4,v8
qt 104,2,28672
qw 5
qm 72,108,164
ds_write_b128 v18,v[48:51] offset:32768
v_mul_f32 v52,v52,v84
v_xor_b32 v9,64,v8
qt 106,2,29184
s_add_u32 s14,s17,2949504
qm 76,108,236
s_add_u32 s15,s76,32768
buffer_store_dwordx4 v[48:51],v18,s[88:91],s15 offen offset:0 sc1
v_mul_f32 v53,v53,v85
v_add_u32 v9,8192,v9
qt 108,3,28672
qw 6
qm 64,96,168
v_cvt_pk_bf16_f32 v52,v52,v53
s_lshr_b32 s30,s7,1
s_lshl_b32 s30,s30,5
qt 110,3,29184
ql 6,44,0
qm 68,96,240
v_mul_f32 v54,v54,v86
s_and_b32 s31,s7,1
qt 96,2,32768
qw 6
qm 72,100,168
v_mul_f32 v55,v55,v87
s_lshl_b32 s32,s31,3
qt 98,2,33280
ql 7,44,1024
qm 76,100,240
v_cvt_pk_bf16_f32 v53,v54,v55
s_add_u32 s30,s30,s32
qt 100,3,32768
qw 5
qm 64,104,172
v_mul_f32 v60,v60,v92
s_mul_i32 s32,s8,576
qt 102,3,33280
s_add_u32 m0,s13,8192
qm 68,104,244
v_mul_f32 v61,v61,v93
s_add_u32 s30,s30,s32
qt 104,2,36864
qw 5
qm 72,108,172
v_cvt_pk_bf16_f32 v54,v60,v61
s_mul_i32 s19,s30,1152
qt 106,2,37376
s_add_u32 s14,s17,3539328
qm 76,108,244
v_mul_f32 v62,v62,v94
s_mul_i32 s31,s31,36864
qt 108,3,36864
qw 5
qm 64,96,176
v_mul_f32 v63,v63,v95
s_and_b32 s30,s8,6
qt 110,3,37376
ql 6,44,0
qm 68,96,248
v_cvt_pk_bf16_f32 v55,v62,v63
s_lshl_b32 s30,s30,4
qt 96,2,40960
qw 5
qm 72,100,176
ds_write_b128 v18,v[52:55] offset:33792
s_add_u32 s18,s31,s30
qt 98,2,41472
ql 7,44,1024
qm 76,100,248
s_add_u32 s15,s76,32768
buffer_store_dwordx4 v[52:55],v18,s[88:91],s15 offen offset:1024 sc1
s_add_u32 s18,s18,1024
qt 100,3,40960
qw 6
qm 64,104,180
s_lshl_b32 s30,s8,7
qt 102,3,41472
s_add_u32 m0,s13,16384
qm 68,104,252
s_add_u32 s28,s31,s30
qt 104,2,45056
qw 6
qm 72,108,180
qw 4
s_barrier
qt 106,2,45568
s_add_u32 s14,s17,4129152
qm 76,108,252
qt 108,3,45056
qw 5
qz 80,96,112
qt 110,3,45568
ql 6,44,0
qz 84,96,184
v_mul_f32 v16,v64,v64
qt 96,2,49152
qw 5
qz 88,100,112
v_fma_f32 v16,v16,v12,v10
qt 98,2,49664
ql 7,44,1024
qz 92,100,184
v_mul_f32 v16,v16,v64
qt 100,3,49152
qw 5
qm 80,104,116
v_exp_f32 v16,v16
qt 102,3,49664
s_add_u32 m0,s13,24576
qm 84,104,188
v_fma_f32 v16,v16,v14,v14
qt 104,2,53248
qw 5
qm 88,108,116
v_rcp_f32 v16,v16
qt 106,2,53760
s_add_u32 s14,s17,4718976
qm 92,108,188
v_mul_f32 v64,v64,v16
qt 108,3,53248
qw 5
qm 80,96,120
v_mul_f32 v16,v65,v65
qt 110,3,53760
ql 6,44,0
qm 84,96,192
v_fma_f32 v16,v16,v12,v10
qr 112,18,0
qt 96,2,57344
qw 6
qm 88,100,120
v_mul_f32 v16,v16,v65
qt 98,2,57856
ql 7,44,1024
qm 92,100,192
v_exp_f32 v16,v16
qr 184,18,1024
qt 100,3,57344
qw 7
qm 80,104,124
v_fma_f32 v16,v16,v14,v14
qt 102,3,57856
s_add_u32 m0,s13,32768
qm 84,104,196
v_rcp_f32 v16,v16
qr 116,18,8192
qt 104,2,61440
qw 8
qm 88,108,124
v_mul_f32 v65,v65,v16
qt 106,2,61952
s_add_u32 s14,s28,368640
qm 92,108,196
v_mul_f32 v16,v66,v66
qr 188,18,9216
qt 108,3,61440
qw 8
qm 80,96,128
v_fma_f32 v16,v16,v12,v10
qt 110,3,61952
ql 8,24,0
qm 84,96,200
v_mul_f32 v16,v16,v66
qt 96,4,0
qw 7
qm 88,100,128
v_exp_f32 v16,v16
qt 98,4,512
ql 9,24,1024
qm 92,100,200
v_fma_f32 v16,v16,v14,v14
qt 100,5,0
qw 6
qm 80,104,132
v_rcp_f32 v16,v16
qt 102,5,512
s_add_u32 m0,s13,40960
qm 84,104,204
v_mul_f32 v66,v66,v16
qt 104,4,4096
qw 5
qm 88,108,132
s_waitcnt vmcnt(7)
s_barrier
v_mul_f32 v16,v67,v67
qt 106,4,4608
s_add_u32 s14,s28,387072
qm 92,108,204
v_fma_f32 v16,v16,v12,v10
qt 108,5,4096
qw 5
qm 80,96,136
v_mul_f32 v16,v16,v67
v_lshrrev_b32 v6,1,v0
v_add_u32 v28,0x10000,v24
qt 110,5,4608
ql 8,24,0
qm 84,96,208
v_exp_f32 v16,v16
v_add_u32 v29,0x10000,v25
qt 128,36,0
qt 96,4,8192
qw 6
qm 88,100,136
v_fma_f32 v16,v16,v14,v14
v_mul_u32_u24 v6,1152,v6
qt 130,36,512
qt 98,4,8704
ql 9,24,1024
qm 92,100,208
v_rcp_f32 v16,v16
v_add_u32 v30,0x10000,v26
qt 200,36,1024
s_add_u32 s15,s77,0
buffer_load_dwordx4 v[48:51],v18,s[88:91],s15 offen offset:0 sc1
qt 100,5,8192
qw 8
qm 80,104,140
v_mul_f32 v67,v67,v16
v_lshrrev_b32 v7,4,v0
v_add_u32 v31,0x10000,v27
qt 202,36,1536
qt 102,5,8704
s_add_u32 m0,s13,49152
qm 84,104,212
v_mul_f32 v16,v72,v72
qt 132,36,8192
qt 104,4,12288
qw 10
qm 88,108,140
v_fma_f32 v16,v16,v12,v10
v_xor_b32 v7,v7,v0
qt 134,36,8704
qt 106,4,12800
s_add_u32 s14,s28,442368
qm 92,108,212
v_mul_f32 v16,v16,v72
qt 204,36,9216
qt 108,5,12288
qw 10
qm 80,96,144
v_exp_f32 v16,v16
qt 206,36,9728
qt 110,5,12800
ql 8,24,0
qm 84,96,216
v_fma_f32 v16,v16,v14,v14
v_and_b32 v7,1,v7
qt 136,36,16384
qt 96,2,0
qw 10
qm 88,100,144
v_rcp_f32 v16,v16
qt 138,36,16896
qt 98,2,512
ql 9,24,1024
qm 92,100,216
v_mul_f32 v72,v72,v16
v_lshl_add_u32 v6,v7,4,v6
qt 208,36,17408
qt 100,3,0
qw 10
qm 80,104,148
v_mul_f32 v16,v73,v73
qt 210,36,17920
s_add_u32 s15,s77,0
buffer_load_dwordx4 v[56:59],v18,s[88:91],s15 offen offset:1024 sc1
qt 102,3,512
s_add_u32 m0,s13,57344
qm 84,104,220
v_fma_f32 v16,v16,v12,v10
v_add_u32 v7,72704,v6
qt 140,36,24576
qt 104,2,4096
qw 10
qm 88,108,148
v_mul_f32 v16,v16,v73
qt 142,36,25088
qt 106,2,4608
s_add_u32 s14,s28,460800
qm 92,108,220
v_exp_f32 v16,v16
qt 212,36,25600
qt 108,3,4096
qw 10
qm 80,96,152
v_fma_f32 v16,v16,v14,v14
qt 214,36,26112
qt 110,3,4608
ql 8,24,0
qm 84,96,224
v_rcp_f32 v16,v16
qt 96,2,8192
qw 9
qm 88,100,152
v_mul_f32 v73,v73,v16
s_mul_i32 s30,s10,1310720
qt 98,2,8704
ql 9,24,1024
qm 92,100,224
v_mul_f32 v16,v74,v74
s_lshl_b32 s31,s8,14
qt 100,3,8192
qw 7
qm 80,104,156
v_fma_f32 v16,v16,v12,v10
qt 102,3,8704
s_add_u32 m0,s13,65536
qm 84,104,228
v_mul_f32 v16,v16,v74
s_add_u32 s73,s30,s31
qt 104,2,12288
qw 5
qm 88,108,156
s_waitcnt vmcnt(8)
s_barrier
v_exp_f32 v16,v16
s_add_u32 s15,s77,8192
buffer_load_dwordx4 v[52:55],v18,s[88:91],s15 offen offset:0 sc1
qt 106,2,12800
s_add_u32 s14,s19,369280
qm 92,108,228
v_fma_f32 v16,v16,v14,v14
s_add_u32 s73,s73,49152
qt 108,3,12288
qw 5
qm 80,96,160
v_rcp_f32 v16,v16
qt 110,3,12800
ql 20,48,0
qm 84,96,232
v_mul_f32 v74,v74,v16
s_lshl_b32 s31,s8,11
qt 96,2,16384
qw 5
qm 88,100,160
v_mul_f32 v16,v75,v75
s_add_u32 s69,s30,s31
qt 98,2,16896
ql 21,48,1024
qm 92,100,232
v_fma_f32 v16,v16,v12,v10
qt 100,3,16384
qw 5
qm 80,104,164
v_mul_f32 v16,v16,v75
s_lshl_b32 s31,s7,8
qt 102,3,16896
s_add_u32 m0,s13,73728
qm 84,104,236
v_exp_f32 v16,v16
qt 104,2,20480
qw 5
qm 88,108,164
v_fma_f32 v16,v16,v14,v14
s_add_u32 s69,s69,s31
qt 106,2,20992
s_add_u32 s14,s19,369408
qm 92,108,236
v_rcp_f32 v16,v16
s_add_u32 s15,s77,8192
buffer_load_dwordx4 v[60:63],v18,s[88:91],s15 offen offset:1024 sc1
qt 108,3,20480
qw 5
qm 80,96,168
v_mul_f32 v75,v75,v16
s_add_u32 s69,s69,132096
qt 110,3,20992
ql 20,48,0
qm 84,96,240
v_mul_f32 v16,v68,v68
s_and_b32 s75,s8,4
qt 96,2,24576
qw 5
qm 88,100,168
v_fma_f32 v16,v16,v13,v11
qt 98,2,25088
ql 21,48,1024
qm 92,100,240
v_mul_f32 v16,v16,v68
s_lshl_b32 s75,s75,4
qt 100,3,24576
qw 5
qm 80,104,172
v_exp_f32 v16,v16
qt 102,3,25088
s_mov_b32 m0,s13
qm 84,104,244
v_fma_f32 v16,v16,v15,v15
s_and_b32 s33,s8,3
s_lshl_b32 s33,s33,2
s_add_u32 s33,s33,s7
s_lshl_b32 s33,s33,3
s_lshl_b32 s30,s10,7
s_add_u32 s30,s30,s33
qt 104,2,28672
qw 5
qm 88,108,172
v_rcp_f32 v16,v16
qt 106,2,29184
s_add_u32 s14,s19,443008
qm 92,108,244
v_mul_f32 v68,v68,v16
s_add_u32 s30,s30,4
qt 108,3,28672
qw 5
qm 80,96,176
v_mul_f32 v16,v69,v69
s_mul_i32 s30,s30,1152
qt 110,3,29184
ql 20,48,0
qm 84,96,248
v_fma_f32 v16,v16,v13,v11
s_add_u32 s15,s77,16384
buffer_load_dwordx4 v[120:123],v18,s[88:91],s15 offen offset:0 sc1
qd 96,24,32768
qw 5
qm 88,100,176
v_mul_f32 v16,v16,v69
s_add_u32 s70,s30,s75
qd 98,24,36864
ql 21,48,1024
qm 92,100,248
v_exp_f32 v16,v16
qd 100,25,32768
qw 5
qm 80,104,180
v_fma_f32 v16,v16,v15,v15
s_lshl_b32 s71,s70,1
qd 102,25,36864
s_add_u32 m0,s13,8192
qm 84,104,252
v_rcp_f32 v16,v16
qd 104,26,32768
qw 5
qm 88,108,180
s_waitcnt vmcnt(9)
s_barrier
v_mul_f32 v69,v69,v16
qd 106,26,36864
s_add_u32 s14,s19,443136
qm 92,108,252
qd 108,27,32768
qw 5
qz 152,96,128
v_mul_f32 v16,v70,v70
v_mul_f32 v17,v71,v71
v_fma_f32 v16,v16,v13,v11
qd 110,27,36864
ql 20,48,0
qz 224,96,200
v_fma_f32 v17,v17,v13,v11
v_mul_f32 v16,v16,v70
qd 96,24,40960
qw 5
qm 152,100,132
v_mul_f32 v17,v17,v71
v_exp_f32 v16,v16
qd 98,24,45056
ql 21,48,1024
qm 224,100,204
v_exp_f32 v17,v17
v_fma_f32 v16,v16,v15,v15
v_fma_f32 v17,v17,v15,v15
s_add_u32 s15,s77,16384
buffer_load_dwordx4 v[192:195],v18,s[88:91],s15 offen offset:1024 sc1
qd 100,25,40960
qw 5
qm 152,104,136
v_rcp_f32 v16,v16
v_rcp_f32 v17,v17
qd 102,25,45056
s_add_u32 m0,s13,16384
qm 224,104,208
v_mul_f32 v70,v70,v16
v_mul_f32 v71,v71,v17
qd 104,26,40960
qw 5
qm 152,108,140
v_mul_f32 v16,v76,v76
v_mul_f32 v17,v77,v77
v_fma_f32 v16,v16,v13,v11
qd 106,26,45056
s_add_u32 s14,s19,516736
qm 224,108,212
v_fma_f32 v17,v17,v13,v11
v_mul_f32 v16,v16,v76
qd 108,27,40960
qw 5
qz 156,96,128
v_mul_f32 v17,v17,v77
v_exp_f32 v16,v16
qd 110,27,45056
ql 20,48,0
qz 228,96,200
v_exp_f32 v17,v17
v_fma_f32 v16,v16,v15,v15
v_fma_f32 v17,v17,v15,v15
qd 96,24,49152
qw 5
qm 156,100,132
v_rcp_f32 v16,v16
v_rcp_f32 v17,v17
qd 98,24,53248
ql 21,48,1024
qm 228,100,204
v_mul_f32 v76,v76,v16
v_mul_f32 v77,v77,v17
qd 100,25,49152
qw 5
qm 156,104,136
v_mul_f32 v16,v78,v78
v_mul_f32 v17,v79,v79
v_fma_f32 v16,v16,v13,v11
s_add_u32 s15,s77,24576
buffer_load_dwordx4 v[124:127],v18,s[88:91],s15 offen offset:0 sc1
qd 102,25,53248
s_add_u32 m0,s13,24576
qm 228,104,208
v_fma_f32 v17,v17,v13,v11
v_mul_f32 v16,v16,v78
qd 104,26,49152
qw 5
qm 156,108,140
v_mul_f32 v17,v17,v79
v_exp_f32 v16,v16
qd 106,26,53248
s_add_u32 s14,s19,516864
qm 228,108,212
v_exp_f32 v17,v17
v_fma_f32 v16,v16,v15,v15
v_fma_f32 v17,v17,v15,v15
qd 108,27,49152
qw 5
qz 160,96,128
v_rcp_f32 v16,v16
v_rcp_f32 v17,v17
qd 110,27,53248
ql 20,48,0
qz 232,96,200
v_mul_f32 v78,v78,v16
v_mul_f32 v79,v79,v17
qr 176,18,16384
qd 96,24,57344
qw 6
qm 160,100,132
v_mul_f32 v64,v64,v80
v_mul_f32 v65,v65,v81
v_cvt_pk_bf16_f32 v64,v64,v65
qd 98,24,61440
ql 21,48,1024
qm 232,100,204
v_mul_f32 v66,v66,v82
v_mul_f32 v67,v67,v83
qr 248,18,17408
qd 100,25,57344
qw 7
qm 160,104,136
v_cvt_pk_bf16_f32 v65,v66,v67
v_mul_f32 v72,v72,v88
qd 102,25,61440
s_add_u32 m0,s13,32768
qm 232,104,208
v_mul_f32 v73,v73,v89
v_cvt_pk_bf16_f32 v66,v72,v73
v_mul_f32 v74,v74,v90
qr 180,18,24576
qd 104,26,57344
qw 8
qm 160,108,140
s_waitcnt vmcnt(8) lgkmcnt(1)
s_barrier
v_mul_f32 v75,v75,v91
v_cvt_pk_bf16_f32 v67,v74,v75
s_add_u32 s15,s77,24576
buffer_load_dwordx4 v[196:199],v18,s[88:91],s15 offen offset:1024 sc1
qd 106,26,61440
s_add_u32 s14,s19,516608
qm 232,108,212
s_add_u32 s15,s76,49152
buffer_store_dwordx4 v[64:67],v18,s[88:91],s15 offen offset:0 sc1
ds_write_b128 v18,v[64:67]
qr 252,18,25600
qd 108,27,57344
qz 164,96,128
v_mul_f32 v68,v68,v84
v_mul_f32 v69,v69,v85
v_cvt_pk_bf16_f32 v68,v68,v69
qd 110,27,61440
ql 20,48,0
qz 236,96,200
v_mul_f32 v70,v70,v86
v_mul_f32 v71,v71,v87
qt 96,4,0
qm 164,100,132
v_cvt_pk_bf16_f32 v69,v70,v71
v_mul_f32 v76,v76,v92
qt 98,4,512
ql 21,48,1024
qm 236,100,204
v_mul_f32 v77,v77,v93
v_cvt_pk_bf16_f32 v70,v76,v77
v_mul_f32 v78,v78,v94
qt 100,5,0
qw 7
qm 164,104,136
v_mul_f32 v79,v79,v95
v_cvt_pk_bf16_f32 v71,v78,v79
qt 102,5,512
s_add_u32 m0,s13,40960
qm 236,104,208
s_add_u32 s15,s76,49152
buffer_store_dwordx4 v[68:71],v18,s[88:91],s15 offen offset:1024 sc1
ds_write_b128 v18,v[68:71] offset:1024
qt 104,4,4096
qw 6
qm 164,108,140
qt 106,4,4608
s_add_u32 s14,s19,590336
qm 236,108,212
qt 108,5,4096
qw 6
qm 152,96,112
s_add_u32 s15,s77,32768
buffer_load_dwordx4 v[80:83],v18,s[88:91],s15 offen offset:0 sc1
qt 110,5,4608
ql 20,48,0
qm 224,96,184
qr 168,18,32768
qt 96,4,8192
qw 7
qm 156,100,112
qt 98,4,8704
ql 21,48,1024
qm 228,100,184
qr 240,18,33792
qt 100,5,8192
qw 7
qm 152,104,116
qt 102,5,8704
s_add_u32 m0,s13,49152
qm 224,104,188
qr 172,18,40960
qt 104,4,12288
qw 8
qm 156,108,116
qt 106,4,12800
s_add_u32 s14,s19,590464
qm 228,108,188
qr 244,18,41984
qt 108,5,12288
qw 8
qm 160,96,112
qt 110,5,12800
ql 20,48,0
qm 232,96,184
qt 96,2,0
qw 7
qm 164,100,112
s_add_u32 s15,s77,32768
buffer_load_dwordx4 v[88:91],v18,s[88:91],s15 offen offset:1024 sc1
qt 98,2,512
ql 21,48,1024
qm 236,100,184
qt 100,3,0
qw 6
qm 160,104,116
qt 102,3,512
s_add_u32 m0,s13,57344
qm 232,104,188
qt 104,2,4096
qw 5
qm 164,108,116
s_waitcnt vmcnt(11)
s_barrier
qt 106,2,4608
s_add_u32 s14,s19,590592
qm 236,108,188
qt 108,3,4096
qw 5
qm 152,96,176
qt 110,3,4608
ql 20,48,0
qm 224,96,248
qt 96,2,8192
qw 5
qm 156,100,176
qt 98,2,8704
ql 21,48,1024
qm 228,100,248
qt 100,3,8192
s_add_u32 m0,s13,65536
s_add_u32 s14,s19,640
qw 5
qm 152,104,180
qt 102,3,8704
ql 20,48,0
qm 224,104,252
qt 104,2,12288
qw 5
qm 156,108,180
qt 106,2,12800
ql 21,48,1024
qm 228,108,252
qt 108,3,12288
s_add_u32 m0,s13,73728
s_add_u32 s14,s19,768
qw 5
qm 160,96,176
qt 110,3,12800
ql 20,48,0
qm 232,96,248
qt 96,2,16384
qw 5
qm 164,100,176
qt 98,2,16896
ql 21,48,1024
qm 236,100,248
qt 100,3,16384
qw 5
qm 160,104,180
qt 102,3,16896
qm 232,104,252
qt 104,2,20480
qw 5
qm 164,108,180
qt 106,2,20992
qm 236,108,252
qt 108,3,20480
qw 5
qm 152,96,168
qt 110,3,20992
qm 224,96,240
qt 96,2,24576
qw 5
qm 156,100,168
qt 98,2,25088
qm 228,100,240
qt 100,3,24576
qw 5
qm 152,104,172
qt 102,3,25088
s_mov_b32 m0,s13
qm 224,104,244
qt 104,2,28672
qw 5
qm 156,108,172
s_waitcnt vmcnt(0)
s_barrier
qt 106,2,29184
s_add_u32 s14,s19,74368
qm 228,108,244
qt 108,3,28672
qw 5
qm 160,96,168
v_and_b32 v1,0xfffffc3f,v18
qt 110,3,29184
ql 20,48,0
qm 232,96,240
qr 64,18,0
qt 96,2,32768
qw 6
qm 164,100,168
qt 98,2,33280
ql 21,48,1024
qm 236,100,240
qr 72,18,1024
s_add_u32 s15,s77,40960
buffer_load_dwordx4 v[84:87],v18,s[88:91],s15 offen offset:0 sc1
qt 100,3,32768
qw 7
qm 160,104,172
v_lshrrev_b32 v16,4,v0
qt 102,3,33280
s_add_u32 m0,s13,8192
qm 232,104,244
qr 68,18,8192
qt 104,2,36864
qw 8
qm 164,108,172
qt 106,2,37376
s_add_u32 s14,s19,74496
qm 236,108,244
qr 76,18,9216
qt 108,3,36864
qw 8
qz 144,96,168
qt 110,3,37376
ql 20,48,0
qz 216,96,240
v_lshl_add_u32 v1,v16,6,v1
qt 96,2,40960
qw 7
qz 148,100,168
qt 98,2,41472
ql 21,48,1024
qz 220,100,240
qt 100,3,40960
qw 6
qm 144,104,172
qt 102,3,41472
s_add_u32 m0,s13,16384
qm 216,104,244
s_add_u32 s15,s77,40960
buffer_load_dwordx4 v[92:95],v18,s[88:91],s15 offen offset:1024 sc1
qt 104,2,45056
qw 5
qm 148,108,172
v_and_b32 v16,12,v0
qt 106,2,45568
s_add_u32 s14,s19,148096
qm 220,108,244
qt 108,3,45056
qw 5
qm 144,96,64
qt 110,3,45568
ql 20,48,0
qm 216,96,72
qt 96,2,49152
qw 5
qm 148,100,64
qt 98,2,49664
ql 21,48,1024
qm 220,100,72
v_lshl_add_u32 v1,v16,6,v1
qt 100,3,49152
qw 5
qm 144,104,68
qt 102,3,49664
s_add_u32 m0,s13,24576
qm 216,104,76
qt 104,2,53248
qw 5
qm 148,108,68
s_barrier
qt 106,2,53760
s_add_u32 s14,s19,148224
qm 220,108,76
qt 108,3,53248
qw 5
qm 152,96,64
s_add_u32 s15,s77,49152
buffer_load_dwordx4 v[128:131],v18,s[88:91],s15 offen offset:0 sc1
qt 110,3,53760
ql 20,48,0
qm 224,96,72
qt 96,2,57344
qw 5
qm 156,100,64
qt 98,2,57856
ql 21,48,1024
qm 228,100,72
v_cvt_pk_bf16_f32 v144,v144,v145
qt 100,3,57344
qw 5
qm 152,104,68
v_cvt_pk_bf16_f32 v145,v146,v147
qt 102,3,57856
s_add_u32 m0,s13,32768
qm 224,104,76
v_cvt_pk_bf16_f32 v146,v148,v149
qt 104,2,61440
qw 5
qm 156,108,68
s_add_u32 s15,s77,49152
buffer_load_dwordx4 v[200:203],v18,s[88:91],s15 offen offset:1024 sc1
v_cvt_pk_bf16_f32 v147,v150,v151
qt 106,2,61952
s_add_u32 s14,s19,221824
qm 228,108,76
qt 108,3,61440
qw 5
qm 160,96,64
v_cvt_pk_bf16_f32 v216,v216,v217
qt 110,3,61952
ql 20,48,0
qm 232,96,72
v_cvt_pk_bf16_f32 v217,v218,v219
qt 96,4,0
qw 5
qm 164,100,64
v_cvt_pk_bf16_f32 v218,v220,v221
qt 98,4,512
ql 21,48,1024
qm 236,100,72
v_cvt_pk_bf16_f32 v219,v222,v223
qt 100,5,0
qw 5
qm 160,104,68
s_add_u32 s15,s77,57344
buffer_load_dwordx4 v[132:135],v18,s[88:91],s15 offen offset:0 sc1
qt 102,5,512
s_add_u32 m0,s13,40960
qm 232,104,76
qt 104,4,4096
qw 5
qm 164,108,68
qt 106,4,4608
s_add_u32 s14,s19,295552
qm 236,108,76
qt 108,5,4096
qw 5
qm 152,96,48
qt 110,5,4608
ql 20,48,0
qm 224,96,56
qt 96,4,8192
qw 5
qm 156,100,48
s_add_u32 s15,s77,57344
buffer_load_dwordx4 v[204:207],v18,s[88:91],s15 offen offset:1024 sc1
qt 98,4,8704
ql 21,48,1024
qm 228,100,56
qt 100,5,8192
qw 5
qm 152,104,52
qt 102,5,8704
s_add_u32 m0,s13,49152
qm 224,104,60
qt 104,4,12288
qw 5
qm 156,108,52
s_waitcnt vmcnt(10)
s_barrier
qt 106,4,12800
s_add_u32 s14,s19,221952
qm 228,108,60
qt 108,5,12288
qw 5
qm 160,96,48
s_add_u32 s15,s77,65536
buffer_load_dwordx4 v[136:139],v18,s[88:91],s15 offen offset:0 sc1
qt 110,5,12800
ql 20,48,0
qm 232,96,56
qt 96,2,0
qw 5
qm 164,100,48
qt 98,2,512
ql 21,48,1024
qm 236,100,56
qt 100,3,0
qw 5
qm 160,104,52
qt 102,3,512
s_add_u32 m0,s13,57344
qm 232,104,60
qt 104,2,4096
qw 5
qm 164,108,52
s_add_u32 s15,s77,65536
buffer_load_dwordx4 v[208:211],v18,s[88:91],s15 offen offset:1024 sc1
qt 106,2,4608
s_add_u32 s14,s19,295680
qm 236,108,60
qt 108,3,4096
qw 5
qm 152,96,120
qt 110,3,4608
ql 20,48,0
qm 224,96,192
qt 96,2,8192
qw 5
qm 156,100,120
qt 98,2,8704
ql 21,48,1024
qm 228,100,192
qt 100,3,8192
qw 5
qm 152,104,124
s_add_u32 s15,s77,73728
buffer_load_dwordx4 v[140:143],v18,s[88:91],s15 offen offset:0 sc1
qt 102,3,8704
s_add_u32 m0,s13,65536
qm 224,104,196
qt 104,2,12288
qw 5
qm 156,108,124
qt 106,2,12800
s_add_u32 s14,s19,369536
qm 228,108,196
qt 108,3,12288
qw 5
qm 160,96,120
qt 110,3,12800
ql 20,48,0
qm 232,96,192
qt 96,2,16384
qw 5
qm 164,100,120
s_add_u32 s15,s77,73728
buffer_load_dwordx4 v[212:215],v18,s[88:91],s15 offen offset:1024 sc1
qt 98,2,16896
ql 21,48,1024
qm 236,100,192
qt 100,3,16384
qw 5
qm 160,104,124
qt 102,3,16896
s_add_u32 m0,s13,73728
qm 232,104,196
qt 104,2,20480
qw 5
qm 164,108,124
s_waitcnt vmcnt(10)
s_barrier
qt 106,2,20992
s_add_u32 s14,s19,369664
qm 236,108,196
qt 108,3,20480
qw 5
qm 152,96,80
v_and_b32 v15,48,v0
qt 110,3,20992
ql 20,48,0
qm 224,96,88
qt 96,2,24576
qw 5
qm 156,100,80
v_lshlrev_b32 v10,10,v15
qt 98,2,25088
ql 21,48,1024
qm 228,100,88
qt 100,3,24576
qw 5
qm 152,104,84
qt 102,3,25088
s_mov_b32 m0,s13
qm 224,104,92
s_add_u32 s15,s73,1179648
buffer_store_dwordx4 v[144:147],v1,s[88:91],s15 offen offset:0 sc1
v_and_b32 v16,16,v0
qt 104,2,28672
qw 5
qm 156,108,84
qt 106,2,29184
s_add_u32 s14,s19,896
qm 228,108,92
qt 108,3,28672
qw 5
qm 160,96,80
v_lshrrev_b32 v17,5,v0
qt 110,3,29184
ql 20,48,0
qm 232,96,88
qt 96,2,32768
qw 5
qm 164,100,80
v_lshl_add_u32 v12,v17,6,v16
qt 98,2,33280
ql 21,48,1024
qm 236,100,88
qt 100,3,32768
qw 5
qm 160,104,84
qt 102,3,33280
s_add_u32 m0,s13,8192
qm 232,104,92
s_add_u32 s15,s73,1179648
buffer_store_dwordx4 v[216:219],v1,s[88:91],s15 offen offset:1024 sc1
v_and_b32 v16,3,v0
qt 104,2,36864
qw 5
qm 164,108,84
qt 106,2,37376
s_add_u32 s14,s19,1024
qm 236,108,92
qt 108,3,36864
qw 5
qm 152,96,128
v_lshl_add_u32 v12,v16,2,v12
qt 110,3,37376
ql 20,48,0
qm 224,96,200
qt 96,2,40960
qw 5
qm 156,100,128
v_lshl_add_u32 v10,v16,6,v10
qt 98,2,41472
ql 21,48,1024
qm 228,100,200
qt 100,3,40960
qw 5
qm 152,104,132
qt 102,3,41472
s_add_u32 m0,s13,16384
qm 224,104,204
v_bfe_u32 v11,v0,2,2
qt 104,2,45056
qw 5
qm 156,108,132
s_waitcnt vmcnt(8)
s_barrier
qt 106,2,45568
s_add_u32 s14,s19,74624
qm 228,108,204
qt 108,3,45056
qw 5
qm 152,96,136
v_lshl_add_u32 v10,v11,4,v10
qt 110,3,45568
ql 20,48,0
qm 224,96,208
qt 96,2,49152
qw 5
qm 156,100,136
v_lshlrev_b32 v12,1,v12
qt 98,2,49664
ql 21,48,1024
qm 228,100,208
qt 100,3,49152
qw 5
qm 152,104,140
qt 102,3,49664
s_add_u32 m0,s13,24576
qm 224,104,212
v_mul_u32_u24 v11,1152,v11
qt 104,2,53248
qw 5
qm 156,108,140
qt 106,2,53760
s_add_u32 s14,s19,74752
qm 228,108,212
qt 108,3,53248
qw 5
qm 160,96,128
v_add_u32 v11,v11,v12
qt 110,3,53760
ql 20,48,0
qm 232,96,200
qt 96,2,57344
qw 5
qm 164,100,128
v_lshlrev_b32 v13,1,v11
qt 98,2,57856
ql 21,48,1024
qm 236,100,200
v_cvt_pk_bf16_f32 v152,v152,v153
qt 100,3,57344
qw 5
qm 160,104,132
v_cvt_pk_bf16_f32 v153,v154,v155
qt 102,3,57856
s_add_u32 m0,s13,32768
qm 232,104,204
v_cvt_pk_bf16_f32 v154,v156,v157
v_lshl_or_b32 v14,v17,30,v13
qt 104,2,61440
qw 5
qm 164,108,132
v_cvt_pk_bf16_f32 v155,v158,v159
qt 106,2,61952
s_add_u32 s14,s19,148352
qm 236,108,204
qt 108,3,61440
qw 5
qm 160,96,136
v_cvt_pk_bf16_f32 v224,v224,v225
s_and_b32 s30,s8,3
s_lshl_b32 s30,s30,4
s_mov_b64 s[32:33],0xffff
s_lshl_b64 s[32:33],s[32:33],s30
s_bitcmp1_b32 s8,2
s_cselect_b64 s[40:41],0,s[32:33]
s_cselect_b64 s[42:43],s[32:33],0
qt 110,3,61952
ql 20,48,0
qm 232,96,208
v_cvt_pk_bf16_f32 v225,v226,v227
qt 96,4,0
qw 5
qm 164,100,136
v_cvt_pk_bf16_f32 v226,v228,v229
s_mov_b64 s[52:53],0
qt 98,4,512
ql 21,48,1024
qm 236,100,208
v_cvt_pk_bf16_f32 v227,v230,v231
qt 100,5,0
qw 5
qm 160,104,140
s_add_u32 s15,s73,655360
buffer_store_dwordx4 v[152:155],v1,s[88:91],s15 offen offset:0 sc1
qt 102,5,512
s_add_u32 m0,s13,40960
qm 232,104,212
s_mov_b64 s[54:55],0
qt 104,4,4096
qw 5
qm 164,108,140
s_waitcnt vmcnt(7)
s_barrier
qt 106,4,4608
s_add_u32 s14,s19,148480
qm 236,108,212
qt 108,5,4096
qw 5
qz 168,96,112
s_mov_b64 s[56:57],0
qt 110,5,4608
ql 20,48,0
qz 240,96,184
qt 96,4,8192
qw 5
qz 172,100,112
s_mov_b64 s[58:59],0
qt 98,4,8704
ql 21,48,1024
qz 244,100,184
v_cvt_pk_bf16_f32 v160,v160,v161
qt 100,5,8192
qw 5
qm 168,104,116
s_add_u32 s15,s73,655360
buffer_store_dwordx4 v[224:227],v1,s[88:91],s15 offen offset:1024 sc1
v_cvt_pk_bf16_f32 v161,v162,v163
qt 102,5,8704
s_add_u32 m0,s13,49152
qm 240,104,188
v_cvt_pk_bf16_f32 v162,v164,v165
s_mov_b32 s76,0
qt 104,4,12288
qw 5
qm 172,108,116
v_cvt_pk_bf16_f32 v163,v166,v167
qt 106,4,12800
s_add_u32 s14,s19,222080
qm 244,108,188
qt 108,5,12288
qw 5
qz 176,96,112
v_cvt_pk_bf16_f32 v232,v232,v233
qt 110,5,12800
ql 20,48,0
qz 248,96,184
v_cvt_pk_bf16_f32 v233,v234,v235
qt 96,2,0
qw 5
qz 180,100,112
v_cvt_pk_bf16_f32 v234,v236,v237
qt 98,2,512
ql 21,48,1024
qz 252,100,184
v_cvt_pk_bf16_f32 v235,v238,v239
qt 100,3,0
qw 5
qm 176,104,116
s_add_u32 s15,s73,786432
buffer_store_dwordx4 v[160:163],v1,s[88:91],s15 offen offset:0 sc1
qt 102,3,512
s_add_u32 m0,s13,57344
qm 248,104,188
v_and_b32 v16,1,v0
qt 104,2,4096
qw 5
qm 180,108,116
qt 106,2,4608
s_add_u32 s14,s19,222208
qm 252,108,188
v_lshlrev_b32 v16,4,v16
qt 108,3,4096
qw 5
qm 168,96,48
qt 110,3,4608
ql 20,48,0
qm 240,96,56
v_mov_b32 v17,0x3f80
qt 96,2,8192
qw 5
qm 172,100,48
qt 98,2,8704
ql 21,48,1024
qm 244,100,56
v_lshlrev_b32 v16,v16,v17
qt 100,3,8192
qw 5
qm 168,104,52
s_add_u32 s15,s73,786432
buffer_store_dwordx4 v[232:235],v1,s[88:91],s15 offen offset:1024 sc1
qt 102,3,8704
s_add_u32 m0,s13,65536
qm 240,104,60
v_bfe_u32 v17,v0,1,3
qt 104,2,12288
qw 5
qm 172,108,52
s_waitcnt vmcnt(10)
s_barrier
qt 106,2,12800
s_add_u32 s14,s19,295808
qm 244,108,60
v_cmp_eq_u32 vcc,0,v17
s_nop 1
v_cndmask_b32 v152,0,v16,vcc
qt 108,3,12288
qw 5
qm 176,96,48
qt 110,3,12800
ql 20,48,0
qm 248,96,56
v_cmp_eq_u32 vcc,1,v17
s_nop 1
v_cndmask_b32 v153,0,v16,vcc
qt 96,2,16384
qw 5
qm 180,100,48
qt 98,2,16896
ql 21,48,1024
qm 252,100,56
v_cmp_eq_u32 vcc,2,v17
s_nop 1
v_cndmask_b32 v154,0,v16,vcc
qt 100,3,16384
s_add_u32 m0,s13,73728
s_add_u32 s14,s19,295936
qw 5
qm 176,104,52
qt 102,3,16896
ql 20,48,0
qm 248,104,60
v_cmp_eq_u32 vcc,3,v17
s_nop 1
v_cndmask_b32 v155,0,v16,vcc
qt 104,2,20480
qw 5
qm 180,108,52
qt 106,2,20992
ql 21,48,1024
qm 252,108,60
v_cmp_eq_u32 vcc,4,v17
s_nop 1
v_cndmask_b32 v156,0,v16,vcc
qt 108,3,20480
s_mov_b32 m0,s13
s_add_u32 s14,s19,590720
qw 5
qm 168,96,120
qt 110,3,20992
ql 20,48,0
qm 240,96,192
v_cmp_eq_u32 vcc,5,v17
s_nop 1
v_cndmask_b32 v157,0,v16,vcc
qt 96,2,24576
qw 5
qm 172,100,120
qt 98,2,25088
ql 21,48,1024
qm 244,100,192
v_cmp_eq_u32 vcc,6,v17
s_nop 1
v_cndmask_b32 v158,0,v16,vcc
qt 100,3,24576
qw 5
qm 168,104,124
qt 102,3,25088
qm 240,104,196
v_cmp_eq_u32 vcc,7,v17
s_nop 1
v_cndmask_b32 v159,0,v16,vcc
qt 104,2,28672
qw 5
qm 172,108,124
qt 106,2,29184
qm 244,108,196
qt 108,3,28672
qw 5
qm 176,96,120
qt 110,3,29184
qm 248,96,192
qt 96,2,32768
qw 5
qm 180,100,120
qt 98,2,33280
qm 252,100,192
qt 100,3,32768
qw 5
qm 176,104,124
qt 102,3,33280
s_add_u32 m0,s13,8192
qm 248,104,196
qt 104,2,36864
qw 5
qm 180,108,124
s_waitcnt vmcnt(0)
s_barrier
qt 106,2,37376
s_add_u32 s14,s19,443264
qm 252,108,196
qt 108,3,36864
qw 5
qm 168,96,80
buffer_load_dwordx2 v[224:225],v11,s[36:39],s70 offen offset:256
qt 110,3,37376
ql 20,48,0
qm 240,96,88
qr 120,18,16384
qt 96,2,40960
qw 6
qm 172,100,80
qt 98,2,41472
ql 21,48,1024
qm 244,100,88
qr 192,18,17408
qt 100,3,40960
qw 7
qm 168,104,84
qt 102,3,41472
s_add_u32 m0,s13,16384
qm 240,104,92
qr 124,18,24576
qt 104,2,45056
qw 8
qm 172,108,84
qt 106,2,45568
s_add_u32 s14,s28,516096
qm 244,108,92
qr 196,18,25600
qt 108,3,45056
qw 8
qm 176,96,80
qt 110,3,45568
ql 8,24,0
qm 248,96,88
buffer_load_dwordx2 v[226:227],v12,s[80:83],s75 offen offset:256
qt 96,2,49152
qw 7
qm 180,100,80
qt 98,2,49664
ql 9,24,1024
qm 252,100,88
qt 100,3,49152
qw 6
qm 176,104,84
qt 102,3,49664
s_add_u32 m0,s13,24576
qm 248,104,92
qt 104,2,53248
qw 5
qm 180,108,84
qt 106,2,53760
s_add_u32 s14,s28,534528
qm 252,108,92
qt 108,3,53248
qw 5
qm 168,96,128
qt 110,3,53760
ql 8,24,0
qm 240,96,200
qt 80,36,0
qt 96,2,57344
qw 6
qm 172,100,128
qt 82,36,512
buffer_load_dwordx2 v[228:229],v11,s[36:39],s70 offen offset:896
qt 98,2,57856
ql 9,24,1024
qm 244,100,200
qt 88,36,1024
qt 100,3,57344
qw 8
qm 168,104,132
qt 90,36,1536
qt 102,3,57856
s_add_u32 m0,s13,32768
qm 240,104,204
qt 84,36,8192
qt 104,2,61440
qw 10
qm 172,108,132
qw 1
s_barrier
qt 86,36,8704
qt 106,2,61952
s_add_u32 s14,s19,516992
qm 244,108,204
qt 92,36,9216
qt 108,3,61440
qm 176,96,128
qt 94,36,9728
qt 110,3,61952
ql 20,48,0
qm 248,96,200
qt 96,4,0
qm 180,100,128
qt 98,4,512
ql 21,48,1024
qm 252,100,200
buffer_load_dwordx2 v[230:231],v12,s[80:83],s75 offen offset:896
qt 100,5,0
qw 7
qm 176,104,132
qt 102,5,512
s_add_u32 m0,s13,40960
qm 248,104,204
qt 104,4,4096
qw 5
qm 180,108,132
qt 106,4,4608
s_add_u32 s14,s19,590848
qm 252,108,204
qt 108,5,4096
qw 5
qm 168,96,136
qt 110,5,4608
ql 20,48,0
qm 240,96,208
qt 128,36,16384
qt 96,4,8192
qw 6
qm 172,100,136
qt 130,36,16896
qt 98,4,8704
ql 21,48,1024
qm 244,100,208
qt 200,36,17408
qt 100,5,8192
qw 8
qm 168,104,140
qt 202,36,17920
qt 102,5,8704
s_add_u32 m0,s13,49152
qm 240,104,212
qt 132,36,24576
qt 104,4,12288
qw 10
qm 172,108,140
qt 134,36,25088
qt 106,4,12800
s_add_u32 s14,s19,443392
qm 244,108,212
qt 204,36,25600
qt 108,5,12288
qw 10
qm 176,96,136
qt 206,36,26112
qt 110,5,12800
ql 20,48,0
qm 248,96,208
qt 96,2,0
qw 9
qm 180,100,136
qt 98,2,512
ql 21,48,1024
qm 252,100,208
qt 100,3,0
qw 7
qm 176,104,140
qt 102,3,512
s_add_u32 m0,s13,57344
qm 248,104,212
qt 104,2,4096
qw 5
qm 180,108,140
s_waitcnt vmcnt(7)
s_barrier
qt 106,2,4608
s_add_u32 s14,s28,589824
qm 252,108,212
qt 108,3,4096
qw 5
qm 168,96,64
qt 110,3,4608
ql 8,24,0
qm 240,96,72
qr 136,18,32768
qt 96,2,8192
qw 6
qm 172,100,64
qt 98,2,8704
ql 9,24,1024
qm 244,100,72
qr 208,18,33792
qt 100,3,8192
qw 7
qm 168,104,68
qt 102,3,8704
s_add_u32 m0,s13,65536
qm 240,104,76
qr 140,18,40960
qt 104,2,12288
qw 8
qm 172,108,68
qt 106,2,12800
s_add_u32 s14,s28,608256
qm 244,108,76
qr 212,18,41984
qt 108,3,12288
qw 8
qm 168,96,120
qt 110,3,12800
ql 8,24,0
qm 240,96,192
qd 96,24,16384
qw 7
qm 172,100,120
qd 98,24,20480
ql 9,24,1024
qm 244,100,192
qd 100,25,16384
qw 6
qm 168,104,124
qd 102,25,20480
s_add_u32 m0,s13,73728
qm 240,104,196
qd 104,26,16384
qw 5
qm 172,108,124
qd 106,26,20480
s_add_u32 s14,s19,517120
qm 244,108,196
qd 108,27,16384
qw 5
qm 168,96,80
qd 110,27,20480
ql 20,48,0
qm 240,96,88
qd 96,24,24576
qw 5
qm 168,100,84
qd 98,24,28672
ql 21,48,1024
qm 240,100,92
qd 100,25,24576
qw 5
qm 168,104,128
qd 102,25,28672
qm 240,104,200
qd 104,26,24576
qw 5
qm 168,108,132
s_waitcnt vmcnt(6)
s_barrier
qd 106,26,28672
qm 240,108,204
qd 108,27,24576
qw 5
qm 172,96,80
qd 110,27,28672
qm 244,96,88
qt 96,2,32768
qw 5
qm 172,100,84
qt 98,2,33280
qm 244,100,92
qt 100,3,32768
qw 5
qm 172,104,128
qt 102,3,33280
qm 244,104,200
qt 104,2,36864
qw 5
qm 172,108,132
qt 106,2,37376
qm 244,108,204
qt 108,3,36864
qw 5
qm 168,96,136
qt 110,3,37376
qm 240,96,208
qt 96,2,40960
qw 5
qm 172,100,136
qt 98,2,41472
qm 244,100,208
qt 100,3,40960
qw 5
qm 168,104,140
qt 102,3,41472
qm 240,104,212
qt 104,2,45056
qw 5
qm 172,108,140
qt 106,2,45568
qm 244,108,212
qt 108,3,45056
qw 5
qm 176,96,64
qt 110,3,45568
qm 248,96,72
qt 96,2,49152
qw 5
qm 180,100,64
qt 98,2,49664
qm 252,100,72
v_cvt_pk_bf16_f32 v168,v168,v169
qt 100,3,49152
qw 5
qm 176,104,68
v_cvt_pk_bf16_f32 v169,v170,v171
qt 102,3,49664
qm 248,104,76
v_cvt_pk_bf16_f32 v170,v172,v173
qt 104,2,53248
qw 5
qm 180,108,68
s_waitcnt vmcnt(0)
s_barrier
v_cvt_pk_bf16_f32 v171,v174,v175
qt 106,2,53760
qm 252,108,76
qt 108,3,53248
qw 5
qm 176,96,120
v_cvt_pk_bf16_f32 v240,v240,v241
qt 110,3,53760
qm 248,96,192
v_cvt_pk_bf16_f32 v241,v242,v243
qd 96,24,57344
qw 5
qm 180,100,120
v_cvt_pk_bf16_f32 v242,v244,v245
qd 98,24,61440
qm 252,100,192
v_cvt_pk_bf16_f32 v243,v246,v247
qd 100,25,57344
qw 5
qm 176,104,124
s_add_u32 s15,s73,917504
buffer_store_dwordx4 v[168:171],v1,s[88:91],s15 offen offset:0 sc1
qd 102,25,61440
qm 248,104,196
qd 104,26,57344
qw 5
qm 180,108,124
qd 106,26,61440
qm 252,108,196
qd 108,27,57344
qw 5
qm 176,96,80
qd 110,27,61440
qm 248,96,88
qd 96,28,0
qw 5
qm 176,100,84
qd 98,28,4096
qm 248,100,92
qd 100,29,0
qw 5
qm 176,104,128
s_add_u32 s15,s73,917504
buffer_store_dwordx4 v[240:243],v1,s[88:91],s15 offen offset:1024 sc1
qd 102,29,4096
qm 248,104,200
qd 104,30,0
qw 5
qm 176,108,132
qd 106,30,4096
qm 248,108,204
qd 108,31,0
qw 5
qm 180,96,80
qd 110,31,4096
qm 252,96,88
qt 96,4,8192
qw 5
qm 180,100,84
qt 98,4,8704
qm 252,100,92
qt 100,5,8192
qw 5
qm 180,104,128
qt 102,5,8704
qm 252,104,200
qt 104,4,12288
qw 5
qm 180,108,132
s_barrier
qt 106,4,12800
qm 252,108,204
qt 108,5,12288
qw 5
qm 176,96,136
qt 110,5,12800
qm 248,96,208
qw 4
qm 180,100,136
qm 252,100,208
qw 2
qm 176,104,140
qm 248,104,212
qw 0
qm 180,108,140
qm 252,108,212
s_nop 3
v_cvt_pk_bf16_f32 v176,v176,v177
v_cvt_pk_bf16_f32 v177,v178,v179
s_nop 0
v_cvt_pk_bf16_f32 v178,v180,v181
v_cvt_pk_bf16_f32 v179,v182,v183
s_add_u32 s15,s73,1048576
buffer_store_dwordx4 v[176:179],v1,s[88:91],s15 offen offset:0 sc1
v_cvt_pk_bf16_f32 v248,v248,v249
v_cvt_pk_bf16_f32 v249,v250,v251
v_cvt_pk_bf16_f32 v250,v252,v253
v_cvt_pk_bf16_f32 v251,v254,v255
s_add_u32 s15,s73,1048576
buffer_store_dwordx4 v[248:251],v1,s[88:91],s15 offen offset:1024 sc1
v_lshlrev_b32 v16,16,v226
v_lshlrev_b32 v144,16,v224
v_mul_f32 v16,v16,v40
v_add_f32 v17,v144,v41
v_fma_f32 v144,v17,v16,v144
v_and_b32 v16,0xffff0000,v226
v_and_b32 v145,0xffff0000,v224
v_mul_f32 v16,v16,v40
v_add_f32 v17,v145,v41
v_fma_f32 v145,v17,v16,v145
v_lshlrev_b32 v16,16,v227
v_lshlrev_b32 v146,16,v225
v_mul_f32 v16,v16,v40
v_add_f32 v17,v146,v41
v_fma_f32 v146,v17,v16,v146
v_and_b32 v16,0xffff0000,v227
v_and_b32 v147,0xffff0000,v225
v_mul_f32 v16,v16,v40
v_add_f32 v17,v147,v41
v_fma_f32 v147,v17,v16,v147
v_lshlrev_b32 v16,16,v230
v_lshlrev_b32 v148,16,v228
v_mul_f32 v16,v16,v40
v_add_f32 v17,v148,v41
v_fma_f32 v148,v17,v16,v148
v_and_b32 v16,0xffff0000,v230
v_and_b32 v149,0xffff0000,v228
v_mul_f32 v16,v16,v40
v_add_f32 v17,v149,v41
v_fma_f32 v149,v17,v16,v149
v_lshlrev_b32 v16,16,v231
v_lshlrev_b32 v150,16,v229
v_mul_f32 v16,v16,v40
v_add_f32 v17,v150,v41
v_fma_f32 v150,v17,v16,v150
v_and_b32 v16,0xffff0000,v231
v_and_b32 v151,0xffff0000,v229
v_mul_f32 v16,v16,v40
v_add_f32 v17,v151,v41
v_fma_f32 v151,v17,v16,v151
buffer_load_dwordx4 v[220:223],v15,s[96:99],s72 offen sc1
s_waitcnt vmcnt(0)
s_barrier
.L_tYo0:
v_sub_u32 v220,s74,v220
v_sub_u32 v221,s74,v221
v_sub_u32 v222,s74,v222
v_sub_u32 v223,s74,v223
v_cndmask_b32 v222,v222,0,s[40:41]
v_cndmask_b32 v223,v223,0,s[42:43]
v_cmp_ge_u32 vcc,0,v220
s_nop 1
s_andn2_b64 exec,vcc,s[52:53]
s_cbranch_scc0 .L_tYo0r10
s_add_u32 s31,s69,262144
buffer_load_dwordx4 v[160:163],v10,s[88:91],s31 offen sc1
s_add_u32 s31,s69,393216
buffer_load_dwordx4 v[232:235],v10,s[88:91],s31 offen sc1
.L_tYo0r10:
s_mov_b64 exec,-1
s_or_b64 s[52:53],s[52:53],vcc
v_cmp_ge_u32 vcc,0,v221
s_nop 1
s_andn2_b64 exec,vcc,s[54:55]
s_cbranch_scc0 .L_tYo0r11
s_add_u32 s31,s69,327680
buffer_load_dwordx4 v[164:167],v10,s[88:91],s31 offen sc1
s_add_u32 s31,s69,458752
buffer_load_dwordx4 v[236:239],v10,s[88:91],s31 offen sc1
.L_tYo0r11:
s_mov_b64 exec,-1
s_or_b64 s[54:55],s[54:55],vcc
v_cmp_ge_u32 vcc,0,v222
s_nop 1
s_andn2_b64 exec,vcc,s[56:57]
s_cbranch_scc0 .L_tYo0r12
s_add_u32 s31,s69,917504
buffer_load_dwordx4 v[112:115],v10,s[88:91],s31 offen sc1
s_add_u32 s31,s69,1048576
buffer_load_dwordx4 v[184:187],v10,s[88:91],s31 offen sc1
.L_tYo0r12:
s_mov_b64 exec,-1
s_or_b64 s[56:57],s[56:57],vcc
v_cmp_ge_u32 vcc,0,v223
s_nop 1
s_andn2_b64 exec,vcc,s[58:59]
s_cbranch_scc0 .L_tYo0r13
s_add_u32 s31,s69,983040
buffer_load_dwordx4 v[116:119],v10,s[88:91],s31 offen sc1
s_add_u32 s31,s69,1114112
buffer_load_dwordx4 v[188:191],v10,s[88:91],s31 offen sc1
.L_tYo0r13:
s_mov_b64 exec,-1
s_or_b64 s[58:59],s[58:59],vcc
.L_tYo0c:
s_and_b64 s[32:33],s[52:53],s[54:55]
s_and_b64 s[32:33],s[32:33],s[56:57]
s_and_b64 s[32:33],s[32:33],s[58:59]
s_cmp_eq_u64 s[32:33],-1
s_cbranch_scc1 .L_tYo0x
s_add_u32 s76,s76,1
s_cmp_gt_u32 s76,1000000
s_cbranch_scc1 .L_tYo0x
s_sleep 4
buffer_load_dwordx4 v[220:223],v15,s[96:99],s72 offen sc1
s_waitcnt vmcnt(0)
s_branch .L_tYo0
.L_tYo0x:
s_waitcnt vmcnt(0)
qm 144,152,160
qm 148,152,112
qm 144,152,164
qm 148,152,116
qm 144,156,232
qm 148,156,184
qm 144,156,236
qm 148,156,188
s_nop 6
buffer_store_dwordx4 v[144:147],v13,s[92:95],s71 offen offset:512 sc1
buffer_store_dwordx4 v[148:151],v13,s[92:95],s71 offen offset:1792 sc1
s_endpgm
.org .L_code_Yo+40960
.L_code_Yo1:
qw 0
s_barrier
qr 112,24,24576
qr 184,24,0
qr 96,20,4096
qr 100,20,6144
qr 116,25,28672
qr 188,25,4096
qr 104,21,4096
qr 108,21,6144
qr 120,24,32768
qr 192,24,8192
qw 7
qz 48,112,96
qr 124,25,36864
qr 196,25,12288
ds_write_b128 v31,v[208:211] offset:24576
qz 52,184,96
qr 200,24,16384
ds_write_b128 v32,v[212:215] offset:24576
qr 96,20,12288
qw 12
qz 56,112,100
qr 128,24,40960
qr 204,25,20480
buffer_load_dword v1,v8,s[32:35],s53 offen
qz 60,184,100
ds_write_b128 v31,v[216:219] offset:32768
qr 100,20,14336
qw 13
qm 48,116,104
qr 132,25,45056
ds_write_b128 v32,v[220:223] offset:32768
qm 52,188,104
qr 104,21,12288
qw 15
qm 56,116,108
qr 136,24,49152
ds_write_b128 v31,v[224:227] offset:40960
qm 60,188,108
ds_write_b128 v32,v[228:231] offset:40960
qr 108,21,14336
qw 11
qm 48,120,96
qw 9
s_barrier
qr 140,25,53248
qm 52,192,96
ds_write_b128 v31,v[232:235] offset:49152
qr 96,20,20480
qw 10
qm 56,120,100
qr 144,24,57344
buffer_load_dword v1,v8,s[80:83],0 offen
qm 60,192,100
ds_write_b128 v32,v[236:239] offset:49152
qr 100,20,22528
qw 10
qm 48,124,104
qr 148,25,61440
qm 52,196,104
ds_write_b128 v31,v[240:243] offset:57344
qr 104,21,20480
qw 9
qm 56,124,108
qr 152,26,8192
qm 60,196,108
ds_write_b128 v32,v[244:247] offset:57344
qr 108,21,22528
qw 9
qm 48,128,96
qr 156,27,12288
)ASM"
R"ASM(qm 52,200,96
ds_write_b128 v38,v[248:251] offset:8192
qr 96,20,28672
qw 9
qm 56,128,100
qr 160,26,16384
qm 60,200,100
ds_write_b128 v39,v[252:255] offset:8192
qr 100,20,30720
qw 9
qm 48,132,104
qr 164,27,20480
qm 52,204,104
qr 104,21,28672
qw 8
qm 56,132,108
qw 1
s_barrier
qr 168,26,24576
qr 208,24,24576
qm 60,204,108
qr 212,25,28672
qr 108,21,30720
qm 48,136,96
qr 172,27,28672
v_add_f32 v16,v16,v17
qw 3
qm 52,208,96
qr 216,24,32768
v_add_f32 v17,v28,v29
qr 96,20,36864
qm 56,136,100
qr 176,26,32768
qr 220,25,36864
v_add_f32_dpp v16,v16,v16 quad_perm:[1,0,3,2] row_mask:0xf bank_mask:0xf
qm 60,208,100
v_add_f32 v33,v33,v35
qr 100,20,38912
qm 48,140,104
qr 180,27,36864
qr 224,24,40960
v_add_f32_dpp v17,v17,v17 quad_perm:[1,0,3,2] row_mask:0xf bank_mask:0xf
qw 9
qm 52,212,104
qr 228,25,45056
v_add_f32_dpp v16,v16,v16 quad_perm:[2,3,0,1] row_mask:0xf bank_mask:0xf
qr 104,21,36864
qw 10
qm 56,140,108
v_add_f32 v34,v34,v36
qm 60,212,108
qr 232,24,49152
v_add_f32_dpp v17,v17,v17 quad_perm:[2,3,0,1] row_mask:0xf bank_mask:0xf
qr 108,21,38912
qw 9
qm 48,144,96
qr 236,25,53248
qm 52,216,96
v_add_f32_dpp v16,v16,v16 row_half_mirror row_mask:0xf bank_mask:0xf
qr 96,20,45056
qw 8
qm 56,144,100
qr 240,24,57344
v_lshrrev_b32 v44,3,v0
qm 60,216,100
qr 244,25,61440
v_add_f32_dpp v17,v17,v17 row_half_mirror row_mask:0xf bank_mask:0xf
qr 100,20,47104
qw 7
qm 48,148,104
v_mul_f32 v16,0x3ae38e39,v16
v_mov_b32 v9,v6
qm 52,220,104
qr 248,26,8192
v_fma_f32 v16,-v33,v33,v16
qr 104,21,45056
qw 7
qm 56,148,108
qr 252,27,12288
v_max_f32 v16,0,v16
qm 60,220,108
v_mul_f32 v17,0x3ae38e39,v17
qr 108,21,47104
qw 7
qm 48,152,96
v_fma_f32 v17,-v34,v34,v17
qm 52,224,96
qr 96,20,53248
qw 5
qm 56,152,100
v_max_f32 v17,0,v17
qm 60,224,100
v_cvt_pk_bf16_f32 v42,v16,v17
qr 100,20,55296
qw 4
qm 48,156,104
v_lshlrev_b32 v16,16,v42
qm 52,228,104
v_and_b32 v17,0xffff0000,v42
qr 104,21,53248
qw 3
qm 56,156,108
s_barrier
v_add_f32 v16,0x3727c5ac,v16
qm 60,228,108
v_add_f32 v17,0x3727c5ac,v17
qr 108,21,55296
qw 3
qm 48,160,96
v_cvt_pk_bf16_f32 v42,v16,v17
qm 52,232,96
v_lshlrev_b32 v16,16,v42
qr 96,20,61440
qw 3
qm 56,160,100
qm 60,232,100
v_and_b32 v17,0xffff0000,v42
qr 100,20,63488
qw 3
qm 48,164,104
v_sqrt_f32 v16,v16
qm 52,236,104
v_sqrt_f32 v17,v17
qr 104,21,61440
qw 3
qm 56,164,108
v_and_b32 v43,4,v44
qm 60,236,108
v_cvt_pk_bf16_f32 v42,v16,v17
qr 108,21,63488
qw 3
qm 48,168,96
v_lshlrev_b32 v16,16,v42
qm 52,240,96
v_and_b32 v17,0xffff0000,v42
qr 96,22,4096
qw 3
qm 56,168,100
v_readfirstlane_b32 s3,v13
s_mul_i32 s16,s3,0x9E3779B1
s_add_u32 s16,s16,0x7F4A7C15
v_rcp_f32 v16,v16
qm 60,240,100
qr 100,22,6144
qw 3
qm 48,172,104
v_rcp_f32 v17,v17
qm 52,244,104
v_add3_u32 v44,v43,v44,s29
qr 104,23,4096
qw 3
qm 56,172,108
v_lshlrev_b32 v43,2,v44
qm 60,244,108
v_add_u32 v43,122880,v43
qr 108,23,6144
qw 3
qm 48,176,96
ds_write_b32 v43,v16
qm 52,248,96
ds_write_b32 v43,v17 offset:64
qr 96,22,12288
qw 5
qm 56,176,100
ds_write_b32 v43,v35 offset:512
qm 60,248,100
ds_write_b32 v43,v36 offset:576
qr 100,22,14336
qw 7
qm 48,180,104
qm 52,252,104
qr 104,23,12288
qw 7
qm 56,180,108
qw 2
s_barrier
qm 60,252,108
qr 108,23,14336
qz 64,112,96
v_lshrrev_b32 v42,4,v0
v_and_b32 v43,15,v0
v_bfe_u32 v45,v0,2,2
s_setprio 0
qz 68,184,96
v_lshl_add_u32 v36,s7,2,v42
v_lshl_add_u32 v45,s7,2,v45
qr 96,20,4096
qw 3
qz 72,112,100
v_lshlrev_b32 v42,5,v36
v_and_b32 v14,3,v0
qz 76,184,100
v_lshlrev_b32 v37,3,v43
v_lshl_add_u32 v45,v45,3,v14
qr 100,20,6144
qw 3
qm 64,116,104
v_lshl_add_u32 v43,v36,9,v37
v_lshlrev_b32 v45,2,v45
s_lshl_b32 s30,s10,3
s_add_u32 s30,s30,s8
s_mul_i32 s30,s30,196608
s_add_u32 s76,s30,21118976
s_add_u32 s77,s30,21020672
s_lshl_b32 s30,s7,13
s_add_u32 s79,s76,s30
qm 68,188,104
v_add_u32 v42,122880,v42
v_add_u32 v45,122880,v45
qr 104,21,4096
qw 3
qm 72,116,108
v_add_u32 v37,124928,v37
ds_read_b32 v10,v45
qm 76,188,108
v_add_u32 v43,114688,v43
ds_read_b32 v11,v45 offset:16
qr 108,21,6144
qw 5
qm 64,120,96
qr 28,42,0
qm 68,192,96
qr 32,42,16
qr 96,20,12288
qw 7
qm 72,120,100
qd 36,37,128
qm 76,192,100
s_nop 0
qr 100,20,14336
qw 8
qm 64,124,104
s_nop 0
qw 7
v_mul_f32 v14,v10,v10
qm 68,196,104
s_nop 0
v_mul_f32 v12,0xbdd2d3e8,v10
qr 104,21,12288
qw 6
qm 72,124,108
qw 5
v_mul_f32 v48,v48,v28
v_mul_f32 v10,0xc0135761,v10
qm 76,196,108
v_mul_f32 v56,v56,v28
v_mul_f32 v12,v12,v14
qr 108,21,14336
qw 4
qm 64,128,96
v_mov_b32 v38,v48
v_rcp_f32 v14,v14
qm 68,200,96
v_mov_b32 v39,v56
v_cvt_pk_bf16_f32 v48,v48,v56
v_mul_f32 v15,v11,v11
qr 96,20,20480
qw 3
qm 72,128,100
v_mul_f32 v49,v49,v29
v_mul_f32 v13,0xbdd2d3e8,v11
qm 76,200,100
v_mul_f32 v57,v57,v29
v_mul_f32 v11,0xc0135761,v11
qr 100,20,22528
qw 3
qm 64,132,104
v_fma_f32 v40,v36,v38,v49
v_mul_f32 v13,v13,v15
qm 68,204,104
v_fma_f32 v41,v36,v39,v57
v_rcp_f32 v15,v15
qr 104,21,20480
qw 3
qm 72,132,108
s_barrier
v_fma_f32 v40,-v37,v39,v40
qm 76,204,108
v_fma_f32 v41,v37,v38,v41
qr 108,21,22528
qw 3
qm 64,136,96
v_cvt_pk_bf16_f32 v49,v49,v57
qm 68,208,96
v_mul_f32 v50,v50,v30
qr 96,20,28672
qw 3
qm 72,136,100
v_mul_f32 v58,v58,v30
qm 76,208,100
v_fma_f32 v38,v36,v40,v50
qr 100,20,30720
qw 3
qm 64,140,104
v_fma_f32 v39,v36,v41,v58
qm 68,212,104
v_fma_f32 v38,-v37,v41,v38
qr 104,21,28672
qw 3
qm 72,140,108
v_fma_f32 v39,v37,v40,v39
qm 76,212,108
v_cvt_pk_bf16_f32 v50,v50,v58
qr 108,21,30720
qw 3
qm 64,144,96
v_mul_f32 v51,v51,v31
qm 68,216,96
v_mul_f32 v59,v59,v31
qr 96,20,36864
qw 3
qm 72,144,100
v_fma_f32 v40,v36,v38,v51
v_fma_f32 v41,v36,v39,v59
qm 76,216,100
v_fma_f32 v40,-v37,v39,v40
qr 100,20,38912
qw 3
qm 64,148,104
v_fma_f32 v41,v37,v38,v41
qm 68,220,104
v_cvt_pk_bf16_f32 v51,v51,v59
qr 104,21,36864
qw 3
qm 72,148,108
ds_write_b128 v18,v[48:51] offset:57344
qm 76,220,108
v_mul_f32 v52,v52,v32
qr 108,21,38912
qw 4
qm 64,152,96
v_mul_f32 v60,v60,v32
qm 68,224,96
v_fma_f32 v38,v36,v40,v52
qr 96,20,45056
qw 4
qm 72,152,100
v_fma_f32 v39,v36,v41,v60
s_mov_b32 s53,s10
qm 76,224,100
v_fma_f32 v38,-v37,v41,v38
qr 100,20,47104
qw 4
qm 64,156,104
v_fma_f32 v39,v37,v40,v39
s_lshl_b32 s54,s53,2
qm 68,228,104
v_cvt_pk_bf16_f32 v52,v52,v60
qr 104,21,45056
qw 3
qm 72,156,108
s_barrier
v_mul_f32 v53,v53,v33
s_add_u32 s54,s54,s7
qm 76,228,108
v_mul_f32 v61,v61,v33
qr 108,21,47104
qw 3
qm 64,160,96
v_fma_f32 v40,v36,v38,v53
s_mul_i32 s54,s54,3456
qm 68,232,96
v_fma_f32 v41,v36,v39,v61
qr 96,22,20480
qw 3
qm 72,160,100
v_fma_f32 v40,-v37,v39,v40
s_mul_i32 s52,s8,663552
qm 76,232,100
v_fma_f32 v41,v37,v38,v41
v_cvt_pk_bf16_f32 v53,v53,v61
qr 100,22,22528
qw 3
qm 64,164,104
v_mul_f32 v54,v54,v34
qm 68,236,104
v_mul_f32 v62,v62,v34
s_add_u32 s54,s54,s52
qr 104,23,20480
qw 3
qm 72,164,108
v_fma_f32 v38,v36,v40,v54
qm 76,236,108
v_fma_f32 v39,v36,v41,v62
s_and_b32 s53,s53,3
qr 108,23,22528
qw 3
qm 64,168,96
v_fma_f32 v38,-v37,v41,v38
qm 68,240,96
v_fma_f32 v39,v37,v40,v39
v_min_u32 v8,53,v0
qr 96,22,28672
qw 3
qm 72,168,100
v_cvt_pk_bf16_f32 v54,v54,v62
qm 76,240,100
v_mul_f32 v55,v55,v35
v_lshlrev_b32 v8,7,v8
qr 100,22,30720
qw 3
qm 64,172,104
v_mul_f32 v63,v63,v35
qm 68,244,104
v_fma_f32 v40,v36,v38,v55
qr 104,23,28672
qw 3
qm 72,172,108
v_fma_f32 v41,v36,v39,v63
qm 76,244,108
v_fma_f32 v40,-v37,v39,v40
qr 108,23,30720
qw 3
qm 64,176,96
v_fma_f32 v41,v37,v38,v41
qm 68,248,96
v_cvt_pk_bf16_f32 v55,v55,v63
qt 96,4,32768
qw 3
qm 72,176,100
ds_write_b128 v18,v[52:55] offset:58368
qt 98,4,33280
qm 76,248,100
ds_write_b64 v43,v[40:41] offset:128
qt 100,5,32768
qw 6
qm 64,180,104
qt 102,5,33280
qm 68,252,104
qt 104,4,36864
qw 7
qm 72,180,108
qw 3
s_barrier
qt 106,4,37376
qm 76,252,108
qt 108,5,36864
qz 48,96,112
v_lshrrev_b32 v42,4,v0
v_and_b32 v43,15,v0
qt 110,5,37376
qz 52,96,184
v_lshl_add_u32 v36,s7,2,v42
v_lshlrev_b32 v42,5,v36
qt 96,4,40960
qw 5
qz 56,100,112
v_lshlrev_b32 v37,3,v43
qt 98,4,41472
qz 60,100,184
v_lshl_add_u32 v43,v36,9,v37
v_add_u32 v42,122880,v42
qt 100,5,40960
qw 5
qm 48,104,116
v_add_u32 v37,124928,v37
v_add_u32 v43,114688,v43
qt 102,5,41472
qm 52,104,188
qr 28,42,0
qt 104,4,45056
qw 6
qm 56,108,116
qr 32,42,16
qd 36,37,384
qt 106,4,45568
qm 60,108,188
s_nop 0
s_nop 0
qt 108,5,45056
qw 8
qm 48,96,120
s_nop 0
qt 110,5,45568
qm 52,96,192
qw 6
v_mul_f32 v64,v64,v28
v_mul_f32 v72,v72,v28
qt 96,2,49152
qm 56,100,120
v_mov_b32 v38,v64
qt 98,2,49664
qm 60,100,192
v_mov_b32 v39,v72
v_cvt_pk_bf16_f32 v64,v64,v72
qt 100,3,49152
qw 5
qm 48,104,124
v_mul_f32 v65,v65,v29
v_mul_f32 v73,v73,v29
qt 102,3,49664
qm 52,104,196
v_fma_f32 v40,v36,v38,v65
qt 104,2,53248
qw 5
qm 56,108,124
v_fma_f32 v41,v36,v39,v73
v_fma_f32 v40,-v37,v39,v40
qt 106,2,53760
qm 60,108,196
v_fma_f32 v41,v37,v38,v41
v_cvt_pk_bf16_f32 v65,v65,v73
qt 108,3,53248
qw 5
qm 48,96,128
v_mul_f32 v66,v66,v30
qt 110,3,53760
qm 52,96,200
v_mul_f32 v74,v74,v30
v_fma_f32 v38,v36,v40,v66
qt 96,2,57344
qw 5
qm 56,100,128
v_fma_f32 v39,v36,v41,v74
v_fma_f32 v38,-v37,v41,v38
qt 98,2,57856
qm 60,100,200
v_fma_f32 v39,v37,v40,v39
qt 100,3,57344
qw 5
qm 48,104,132
v_cvt_pk_bf16_f32 v66,v66,v74
v_mul_f32 v67,v67,v31
qt 102,3,57856
qm 52,104,204
v_mul_f32 v75,v75,v31
qt 104,2,61440
qw 5
qm 56,108,132
s_barrier
v_fma_f32 v40,v36,v38,v67
v_fma_f32 v41,v36,v39,v75
qt 106,2,61952
qm 60,108,204
v_fma_f32 v40,-v37,v39,v40
v_fma_f32 v41,v37,v38,v41
qt 108,3,61440
qw 5
qm 48,96,136
v_cvt_pk_bf16_f32 v67,v67,v75
qt 110,3,61952
qm 52,96,208
ds_write_b128 v19,v[64:67] offset:8192
v_mul_f32 v68,v68,v32
qt 96,4,0
qw 6
qm 56,100,136
v_mul_f32 v76,v76,v32
v_fma_f32 v38,v36,v40,v68
qt 98,4,512
qm 60,100,208
v_fma_f32 v39,v36,v41,v76
qt 100,5,0
qw 6
qm 48,104,140
v_fma_f32 v38,-v37,v41,v38
v_fma_f32 v39,v37,v40,v39
qt 102,5,512
qm 52,104,212
v_cvt_pk_bf16_f32 v68,v68,v76
v_mul_f32 v69,v69,v33
qt 104,4,4096
qw 6
qm 56,108,140
v_mul_f32 v77,v77,v33
qt 106,4,4608
qm 60,108,212
v_fma_f32 v40,v36,v38,v69
v_fma_f32 v41,v36,v39,v77
qt 108,5,4096
qw 5
qm 48,96,144
v_fma_f32 v40,-v37,v39,v40
qt 110,5,4608
qm 52,96,216
v_fma_f32 v41,v37,v38,v41
v_cvt_pk_bf16_f32 v69,v69,v77
qt 96,4,8192
qw 5
qm 56,100,144
v_mul_f32 v70,v70,v34
v_mul_f32 v78,v78,v34
qt 98,4,8704
qm 60,100,216
v_fma_f32 v38,v36,v40,v70
qt 100,5,8192
qw 5
qm 48,104,148
v_fma_f32 v39,v36,v41,v78
v_fma_f32 v38,-v37,v41,v38
qt 102,5,8704
qm 52,104,220
v_fma_f32 v39,v37,v40,v39
v_cvt_pk_bf16_f32 v70,v70,v78
qt 104,4,12288
qw 5
qm 56,108,148
v_mul_f32 v71,v71,v35
qt 106,4,12800
qm 60,108,220
v_mul_f32 v79,v79,v35
v_fma_f32 v40,v36,v38,v71
qt 108,5,12288
qw 5
qm 48,96,152
v_fma_f32 v41,v36,v39,v79
v_fma_f32 v40,-v37,v39,v40
qt 110,5,12800
qm 52,96,224
v_fma_f32 v41,v37,v38,v41
qt 96,2,0
qw 5
qm 56,100,152
v_cvt_pk_bf16_f32 v71,v71,v79
ds_write_b128 v19,v[68:71] offset:9216
qt 98,2,512
qm 60,100,224
ds_write_b64 v43,v[40:41] offset:384
qt 100,3,0
qw 7
qm 48,104,156
qt 102,3,512
qm 52,104,228
qt 104,2,4096
qw 7
qm 56,108,156
qw 3
s_barrier
qt 106,2,4608
qm 60,108,228
v_lshrrev_b32 v43,4,v0
v_and_b32 v76,15,v0
qt 108,3,4096
qm 48,96,160
v_lshl_add_u32 v76,s7,4,v76
qt 110,3,4608
qm 52,96,232
v_lshlrev_b32 v43,11,v43
v_lshl_add_u32 v43,v76,3,v43
qt 96,4,16384
qw 5
qm 56,100,160
v_add_u32 v43,114688,v43
qt 98,4,16896
qm 60,100,232
v_lshlrev_b32 v76,4,v76
v_add_u32 v42,123904,v76
qt 100,5,16384
qw 5
qm 48,104,164
qd 36,42,0
qt 102,5,16896
qm 52,104,236
qd 28,43,0
qd 30,43,512
qt 104,4,20480
qw 8
qm 56,108,164
qd 32,43,1024
qt 106,4,20992
qm 60,108,236
qd 34,43,1536
s_mov_b32 s30,0
qt 108,5,20480
qw 10
qm 48,96,168
s_mov_b32 s31,-1
qt 110,5,20992
qm 52,96,240
s_mov_b32 s32,0xffff0000
s_mov_b32 s33,0xffff0000
qt 96,4,24576
qw 9
qm 56,100,168
v_mov_b32 v73,s16
qt 98,4,25088
qm 60,100,240
v_mov_b32 v75,s16
qt 100,5,24576
qw 6
qm 48,104,172
v_mul_f32 v42,0x41000000,v36
v_mul_f32 v44,0x42000000,v36
qt 102,5,25088
qm 52,104,244
v_mul_f32 v39,0x41000000,v37
qt 104,4,28672
qw 5
qm 56,108,172
v_mul_f32 v41,0x42000000,v37
v_exp_f32 v42,v42
qt 106,4,29184
qm 60,108,244
v_exp_f32 v44,v44
qt 108,5,28672
qw 5
qm 48,96,176
v_fract_f32 v39,v39
v_fract_f32 v41,v41
qt 110,5,29184
qm 52,96,248
v_cos_f32 v38,v39
qt 96,2,8192
qw 5
qm 56,100,176
v_cos_f32 v40,v41
v_sin_f32 v39,v39
qt 98,2,8704
qm 60,100,248
v_sin_f32 v41,v41
qt 100,3,8192
qw 5
qm 48,104,180
v_mul_f32 v38,v38,v42
v_mul_f32 v40,v40,v44
qt 102,3,8704
qm 52,104,252
v_mul_f32 v39,v39,v42
qt 104,2,12288
qw 5
qm 56,108,180
s_barrier
v_mul_f32 v41,v41,v44
v_fma_f32 v44,v38,v28,v30
qt 106,2,12800
qm 60,108,252
v_fma_f32 v45,v38,v29,v31
qt 108,3,12288
qw 5
qz 80,96,112
v_fma_f32 v44,-v39,v29,v44
qt 110,3,12800
qz 84,96,184
v_fma_f32 v45,v39,v28,v45
v_fma_f32 v64,v38,v44,v32
v_mul_f32 v16,v48,v48
qt 96,4,32768
qw 5
qz 88,100,112
v_fma_f32 v65,v38,v45,v33
v_fma_f32 v16,v16,v12,v10
v_and_b32 v6,31,v0
qt 98,4,33280
qz 92,100,184
v_fma_f32 v64,-v39,v45,v64
v_fma_f32 v65,v39,v44,v65
v_mul_f32 v16,v16,v48
qt 100,5,32768
qw 5
qm 80,104,116
v_fma_f32 v44,v38,v64,v34
v_exp_f32 v16,v16
v_min_u32 v6,8,v6
qt 102,5,33280
qm 84,104,188
v_fma_f32 v45,v38,v65,v35
v_fma_f32 v44,-v39,v65,v44
v_fma_f32 v16,v16,v14,v14
qt 104,4,36864
qw 5
qm 88,108,116
v_fma_f32 v45,v39,v64,v45
v_rcp_f32 v16,v16
v_mul_u32_u24 v6,1152,v6
qt 106,4,37376
qm 92,108,188
v_mov_b32 v64,v44
v_mov_b32 v65,v45
v_mul_f32 v48,v48,v16
qt 108,5,36864
qw 5
qm 80,96,120
v_permlane16_swap_b32 v44,v64
v_mul_f32 v16,v49,v49
s_lshl_b32 s52,s8,7
qt 110,5,37376
qm 84,96,192
v_permlane16_swap_b32 v45,v65
v_fma_f32 v66,v40,v44,v64
v_fma_f32 v16,v16,v12,v10
qt 96,4,40960
qw 5
qm 88,100,120
v_fma_f32 v67,v40,v45,v65
v_mul_f32 v16,v16,v49
v_add_u32 v6,s52,v6
qt 98,4,41472
qm 92,100,192
v_fma_f32 v66,-v41,v45,v66
v_fma_f32 v67,v41,v44,v67
v_exp_f32 v16,v16
qt 100,5,40960
qw 5
qm 80,104,124
v_mov_b32 v64,v66
v_fma_f32 v16,v16,v14,v14
s_sub_u32 s52,0x400,s52
qt 102,5,41472
qm 84,104,196
v_mov_b32 v65,v67
v_rcp_f32 v16,v16
qt 104,4,45056
qw 5
qm 88,108,124
v_permlane32_swap_b32 v66,v64
v_permlane32_swap_b32 v67,v65
v_mul_f32 v49,v49,v16
qt 106,4,45568
qm 92,108,196
v_cndmask_b32 v66,0,v66,s[30:31]
v_mul_f32 v16,v50,v50
v_lshrrev_b32 v7,5,v0
qt 108,5,45056
qw 5
qm 80,96,128
v_cndmask_b32 v67,0,v67,s[30:31]
v_fma_f32 v64,v40,v66,v44
v_fma_f32 v16,v16,v12,v10
qt 110,5,45568
qm 84,96,200
v_fma_f32 v65,v40,v67,v45
v_mul_f32 v16,v16,v50
v_mul_u32_u24 v7,s52,v7
qt 96,2,16384
qw 5
qm 88,100,128
v_fma_f32 v64,-v41,v67,v64
v_fma_f32 v65,v41,v66,v65
v_exp_f32 v16,v16
qt 98,2,16896
qm 92,100,200
v_cndmask_b32 v66,v66,v64,s[32:33]
v_fma_f32 v16,v16,v14,v14
v_add_u32 v6,v6,v7
qt 100,3,16384
qw 5
qm 80,104,132
v_cndmask_b32 v67,v67,v65,s[32:33]
ds_write_b64 v43,v[66:67] offset:0
v_rcp_f32 v16,v16
qt 102,3,16896
qm 84,104,204
v_fma_f32 v64,v38,v66,v28
v_mul_f32 v50,v50,v16
s_lshl_b32 s52,s10,2
qt 104,2,20480
qw 6
qm 88,108,132
qw 2
s_barrier
v_fma_f32 v65,v38,v67,v29
v_fma_f32 v64,-v39,v67,v64
v_mul_f32 v16,v51,v51
qt 106,2,20992
qm 92,108,204
v_fma_f32 v65,v39,v66,v65
v_fma_f32 v16,v16,v12,v10
s_add_u32 s52,s52,s7
qt 108,3,20480
qm 80,96,136
ds_write_b64 v43,v[64:65] offset:512
v_fma_f32 v44,v38,v64,v30
v_mul_f32 v16,v16,v51
qt 110,3,20992
qm 84,96,208
v_fma_f32 v45,v38,v65,v31
v_exp_f32 v16,v16
s_mul_i32 s52,s52,10368
qt 96,2,24576
qw 6
qm 88,100,136
v_fma_f32 v44,-v39,v65,v44
v_fma_f32 v16,v16,v14,v14
qt 98,2,25088
qm 92,100,208
v_fma_f32 v45,v39,v64,v45
ds_write_b64 v43,v[44:45] offset:1024
v_rcp_f32 v16,v16
qt 100,3,24576
qw 7
qm 80,104,140
v_fma_f32 v68,v38,v44,v32
v_mul_f32 v51,v51,v16
qt 102,3,25088
qm 84,104,212
v_fma_f32 v69,v38,v45,v33
v_fma_f32 v68,-v39,v45,v68
v_mul_f32 v16,v56,v56
qt 104,2,28672
qw 6
qm 88,108,140
v_fma_f32 v69,v39,v44,v69
v_fma_f32 v16,v16,v12,v10
qt 106,2,29184
qm 92,108,212
ds_write_b64 v43,v[68:69] offset:1536
v_fma_f32 v72,v38,v68,v34
v_mul_f32 v16,v16,v56
qt 108,3,28672
qw 7
qm 80,96,144
v_fma_f32 v74,v38,v69,v35
v_exp_f32 v16,v16
qt 110,3,29184
qm 84,96,216
v_fma_f32 v72,-v39,v69,v72
v_fma_f32 v74,v39,v68,v74
v_fma_f32 v16,v16,v14,v14
qt 96,2,32768
qw 6
qm 88,100,144
v_cmp_gt_u32 vcc,48,v0
v_rcp_f32 v16,v16
qt 98,2,33280
qm 92,100,216
v_mov_b32 v42,0x7ff00000
s_mul_i32 s30,s10,9216
v_mul_f32 v56,v56,v16
qt 100,3,32768
qw 6
qm 80,104,148
s_lshl_b32 s31,s12,8
v_mul_f32 v16,v57,v57
qt 102,3,33280
qm 84,104,220
s_add_u32 s30,s30,s31
v_cndmask_b32 v76,v76,v42,vcc
v_fma_f32 v16,v16,v12,v10
qt 104,2,36864
qw 5
qm 88,108,148
buffer_store_dwordx2 v[72:73],v76,s[84:87],s30 offen sc1
v_mul_f32 v16,v16,v57
qt 106,2,37376
qm 92,108,220
buffer_store_dwordx2 v[74:75],v76,s[84:87],s30 offen offset:8 sc1
v_exp_f32 v16,v16
qt 108,3,36864
qw 5
qm 80,96,152
v_fma_f32 v16,v16,v14,v14
qt 110,3,37376
qm 84,96,224
v_rcp_f32 v16,v16
qt 96,2,40960
qw 5
qm 88,100,152
v_mul_f32 v57,v57,v16
qt 98,2,41472
qm 92,100,224
v_mul_f32 v16,v58,v58
qt 100,3,40960
qw 5
qm 80,104,156
v_fma_f32 v16,v16,v12,v10
qt 102,3,41472
qm 84,104,228
v_mul_f32 v16,v16,v58
qt 104,2,45056
qw 5
qm 88,108,156
s_barrier
v_exp_f32 v16,v16
qt 106,2,45568
qm 92,108,228
v_fma_f32 v16,v16,v14,v14
qt 108,3,45056
qw 5
qm 80,96,160
v_rcp_f32 v16,v16
qt 110,3,45568
qm 84,96,232
v_mul_f32 v58,v58,v16
qt 96,4,16384
qw 5
qm 88,100,160
v_mul_f32 v16,v59,v59
qt 98,4,16896
qm 92,100,232
v_fma_f32 v16,v16,v12,v10
qt 100,5,16384
qw 5
qm 80,104,164
v_mul_f32 v16,v16,v59
qt 102,5,16896
qm 84,104,236
v_exp_f32 v16,v16
qt 104,4,20480
qw 5
qm 88,108,164
v_fma_f32 v16,v16,v14,v14
qt 106,4,20992
qm 92,108,236
v_rcp_f32 v16,v16
qt 108,5,20480
qw 5
qm 80,96,168
v_mul_f32 v59,v59,v16
qt 110,5,20992
qm 84,96,240
v_mul_f32 v16,v52,v52
qt 96,4,24576
qw 5
qm 88,100,168
v_fma_f32 v16,v16,v13,v11
qt 98,4,25088
qm 92,100,240
v_mul_f32 v16,v16,v52
qt 100,5,24576
qw 5
qm 80,104,172
v_exp_f32 v16,v16
qt 102,5,25088
qm 84,104,244
v_fma_f32 v16,v16,v15,v15
qt 104,4,28672
qw 5
qm 88,108,172
v_rcp_f32 v16,v16
qt 106,4,29184
qm 92,108,244
v_mul_f32 v52,v52,v16
qt 108,5,28672
qw 5
qm 80,96,176
v_mul_f32 v16,v53,v53
qt 110,5,29184
qm 84,96,248
v_fma_f32 v16,v16,v13,v11
qt 96,2,49152
qw 5
qm 88,100,176
v_mul_f32 v16,v16,v53
qt 98,2,49664
qm 92,100,248
v_exp_f32 v16,v16
qt 100,3,49152
qw 5
qm 80,104,180
v_fma_f32 v16,v16,v15,v15
qt 102,3,49664
qm 84,104,252
v_rcp_f32 v16,v16
qt 104,2,53248
qw 5
qm 88,108,180
s_barrier
v_mul_f32 v53,v53,v16
qt 106,2,53760
qm 92,108,252
qt 108,3,53248
qw 5
qz 64,96,112
v_mul_f32 v16,v54,v54
qt 110,3,53760
qz 68,96,184
v_fma_f32 v16,v16,v13,v11
qt 96,4,32768
qw 5
qz 72,100,112
v_mul_f32 v16,v16,v54
qt 98,4,33280
qz 76,100,184
v_exp_f32 v16,v16
qt 100,5,32768
qw 5
qm 64,104,116
v_fma_f32 v16,v16,v15,v15
qt 102,5,33280
qm 68,104,188
v_rcp_f32 v16,v16
qt 104,4,36864
qw 5
qm 72,108,116
v_mul_f32 v54,v54,v16
qt 106,4,37376
qm 76,108,188
v_mul_f32 v16,v55,v55
qt 108,5,36864
qw 5
qm 64,96,120
v_fma_f32 v16,v16,v13,v11
qt 110,5,37376
qm 68,96,192
v_mul_f32 v16,v16,v55
qt 96,4,40960
qw 5
qm 72,100,120
v_exp_f32 v16,v16
qt 98,4,41472
qm 76,100,192
v_fma_f32 v16,v16,v15,v15
qt 100,5,40960
qw 5
qm 64,104,124
v_rcp_f32 v16,v16
qt 102,5,41472
qm 68,104,196
v_mul_f32 v55,v55,v16
qt 104,4,45056
qw 5
qm 72,108,124
v_mul_f32 v16,v60,v60
qt 106,4,45568
qm 76,108,196
v_fma_f32 v16,v16,v13,v11
qt 108,5,45056
qw 5
qm 64,96,128
v_mul_f32 v16,v16,v60
qt 110,5,45568
qm 68,96,200
v_exp_f32 v16,v16
qt 96,2,57344
qw 5
qm 72,100,128
v_fma_f32 v16,v16,v15,v15
qt 98,2,57856
qm 76,100,200
v_rcp_f32 v16,v16
qt 100,3,57344
qw 5
qm 64,104,132
v_mul_f32 v60,v60,v16
qt 102,3,57856
qm 68,104,204
v_mul_f32 v16,v61,v61
qt 104,2,61440
qw 5
qm 72,108,132
s_barrier
v_fma_f32 v16,v16,v13,v11
qt 106,2,61952
qm 76,108,204
v_mul_f32 v16,v16,v61
qt 108,3,61440
qw 5
qm 64,96,136
v_exp_f32 v16,v16
qt 110,3,61952
qm 68,96,208
v_fma_f32 v16,v16,v15,v15
qt 96,4,0
qw 5
qm 72,100,136
v_rcp_f32 v16,v16
qt 98,4,512
qm 76,100,208
v_mul_f32 v61,v61,v16
qt 100,5,0
qw 5
qm 64,104,140
v_mul_f32 v16,v62,v62
qt 102,5,512
qm 68,104,212
v_fma_f32 v16,v16,v13,v11
qt 104,4,4096
qw 5
qm 72,108,140
v_mul_f32 v16,v16,v62
qt 106,4,4608
qm 76,108,212
v_exp_f32 v16,v16
qt 108,5,4096
qw 5
qm 64,96,144
v_fma_f32 v16,v16,v15,v15
qt 110,5,4608
qm 68,96,216
v_rcp_f32 v16,v16
qt 96,4,8192
qw 5
qm 72,100,144
v_mul_f32 v62,v62,v16
qt 98,4,8704
qm 76,100,216
v_mul_f32 v16,v63,v63
qt 100,5,8192
qw 5
qm 64,104,148
v_fma_f32 v16,v16,v13,v11
qt 102,5,8704
qm 68,104,220
v_mul_f32 v16,v16,v63
qt 104,4,12288
qw 5
qm 72,108,148
v_exp_f32 v16,v16
qt 106,4,12800
qm 76,108,220
v_fma_f32 v16,v16,v15,v15
qt 108,5,12288
qw 5
qm 64,96,152
v_rcp_f32 v16,v16
qt 110,5,12800
qm 68,96,224
v_mul_f32 v63,v63,v16
qt 96,2,0
qw 5
qm 72,100,152
v_mul_f32 v48,v48,v80
qt 98,2,512
qm 76,100,224
v_mul_f32 v49,v49,v81
qt 100,3,0
qw 5
qm 64,104,156
v_cvt_pk_bf16_f32 v48,v48,v49
qt 102,3,512
qm 68,104,228
v_mul_f32 v50,v50,v82
qt 104,2,4096
qw 5
qm 72,108,156
s_barrier
v_mul_f32 v51,v51,v83
qt 106,2,4608
qm 76,108,228
v_cvt_pk_bf16_f32 v49,v50,v51
qt 108,3,4096
qw 5
qm 64,96,160
v_mul_f32 v56,v56,v88
qt 110,3,4608
qm 68,96,232
v_mul_f32 v57,v57,v89
qt 96,2,8192
qw 5
qm 72,100,160
v_cvt_pk_bf16_f32 v50,v56,v57
qt 98,2,8704
qm 76,100,232
v_mul_f32 v58,v58,v90
qt 100,3,8192
qw 5
qm 64,104,164
v_mul_f32 v59,v59,v91
qt 102,3,8704
qm 68,104,236
v_cvt_pk_bf16_f32 v51,v58,v59
qt 104,2,12288
qw 5
qm 72,108,164
ds_write_b128 v18,v[48:51] offset:8192
v_mul_f32 v52,v52,v84
qt 106,2,12800
qm 76,108,236
s_add_u32 s15,s76,8192
buffer_store_dwordx4 v[48:51],v18,s[88:91],s15 offen offset:0 sc1
v_mul_f32 v53,v53,v85
qt 108,3,12288
qw 6
qm 64,96,168
v_cvt_pk_bf16_f32 v52,v52,v53
qt 110,3,12800
qm 68,96,240
v_mul_f32 v54,v54,v86
qt 96,2,16384
qw 6
qm 72,100,168
v_mul_f32 v55,v55,v87
qt 98,2,16896
qm 76,100,240
v_cvt_pk_bf16_f32 v53,v54,v55
qt 100,3,16384
qw 5
qm 64,104,172
v_mul_f32 v60,v60,v92
qt 102,3,16896
qm 68,104,244
v_mul_f32 v61,v61,v93
qt 104,2,20480
qw 5
qm 72,108,172
v_cvt_pk_bf16_f32 v54,v60,v61
qt 106,2,20992
qm 76,108,244
v_mul_f32 v62,v62,v94
qt 108,3,20480
qw 5
qm 64,96,176
v_mul_f32 v63,v63,v95
qt 110,3,20992
qm 68,96,248
v_cvt_pk_bf16_f32 v55,v62,v63
qt 96,2,24576
qw 5
qm 72,100,176
ds_write_b128 v18,v[52:55] offset:9216
qt 98,2,25088
qm 76,100,248
s_add_u32 s15,s76,8192
buffer_store_dwordx4 v[52:55],v18,s[88:91],s15 offen offset:1024 sc1
qt 100,3,24576
qw 6
qm 64,104,180
qt 102,3,25088
qm 68,104,252
qt 104,2,28672
qw 6
qm 72,108,180
qw 4
s_barrier
qt 106,2,29184
qm 76,108,252
qt 108,3,28672
qw 5
qz 80,96,112
qt 110,3,29184
qz 84,96,184
v_mul_f32 v16,v64,v64
qt 96,4,32768
qw 5
qz 88,100,112
v_fma_f32 v16,v16,v12,v10
v_lshlrev_b32 v44,4,v0
qt 98,4,33280
qz 92,100,184
v_mul_f32 v16,v16,v64
s_lshl_b32 s30,s7,2
qt 100,5,32768
qw 5
qm 80,104,116
v_exp_f32 v16,v16
s_sub_i32 s30,s10,s30
qt 102,5,33280
qm 84,104,188
v_fma_f32 v16,v16,v14,v14
qt 104,4,36864
qw 5
qm 88,108,116
v_rcp_f32 v16,v16
s_lshl_b32 s31,s12,8
qt 106,4,37376
qm 92,108,188
v_mul_f32 v64,v64,v16
s_sub_i32 s30,s30,1
qt 108,5,36864
qw 5
qm 80,96,120
v_mul_f32 v16,v65,v65
qt 110,5,37376
qm 84,96,192
v_fma_f32 v16,v16,v12,v10
s_mul_i32 s68,s30,9216
qt 96,4,40960
qw 5
qm 88,100,120
v_mul_f32 v16,v16,v65
s_add_u32 s68,s68,s31
qt 98,4,41472
qm 92,100,192
v_exp_f32 v16,v16
s_cmp_ge_i32 s30,0
s_cselect_b32 s68,s68,0x7ff00000
s_cselect_b32 s72,s16,0
qt 100,5,40960
qw 5
qm 80,104,124
v_fma_f32 v16,v16,v14,v14
qt 102,5,41472
qm 84,104,196
v_rcp_f32 v16,v16
s_sub_i32 s30,s30,1
qt 104,4,45056
qw 5
qm 88,108,124
v_mul_f32 v65,v65,v16
s_mul_i32 s69,s30,9216
qt 106,4,45568
qm 92,108,196
v_mul_f32 v16,v66,v66
qt 108,5,45056
qw 5
qm 80,96,128
v_fma_f32 v16,v16,v12,v10
s_add_u32 s69,s69,s31
qt 110,5,45568
qm 84,96,200
v_mul_f32 v16,v16,v66
s_cmp_ge_i32 s30,0
s_cselect_b32 s69,s69,0x7ff00000
s_cselect_b32 s73,s16,0
qt 96,2,32768
qw 5
qm 88,100,128
v_exp_f32 v16,v16
s_sub_i32 s30,s30,1
qt 98,2,33280
qm 92,100,200
v_fma_f32 v16,v16,v14,v14
qt 100,3,32768
qw 5
qm 80,104,132
v_rcp_f32 v16,v16
s_mul_i32 s70,s30,9216
qt 102,3,33280
qm 84,104,204
v_mul_f32 v66,v66,v16
s_add_u32 s70,s70,s31
qt 104,2,36864
qw 5
qm 88,108,132
s_barrier
v_mul_f32 v16,v67,v67
qt 106,2,37376
qm 92,108,204
v_fma_f32 v16,v16,v12,v10
s_cmp_ge_i32 s30,0
s_cselect_b32 s70,s70,0x7ff00000
s_cselect_b32 s74,s16,0
qt 108,3,36864
qw 5
qm 80,96,136
v_mul_f32 v16,v16,v67
s_sub_i32 s30,s30,1
qt 110,3,37376
qm 84,96,208
v_exp_f32 v16,v16
s_mul_i32 s71,s30,9216
qt 96,2,40960
qw 5
qm 88,100,136
v_fma_f32 v16,v16,v14,v14
qt 98,2,41472
qm 92,100,208
v_rcp_f32 v16,v16
s_add_u32 s71,s71,s31
qt 100,3,40960
qw 5
qm 80,104,140
v_mul_f32 v67,v67,v16
s_cmp_ge_i32 s30,0
s_cselect_b32 s71,s71,0x7ff00000
s_cselect_b32 s75,s16,0
qt 102,3,41472
qm 84,104,212
v_mul_f32 v16,v72,v72
qt 104,2,45056
qw 5
qm 88,108,140
v_fma_f32 v16,v16,v12,v10
buffer_load_dwordx4 v[28:31],v44,s[84:87],s68 offen sc1
qt 106,2,45568
qm 92,108,212
v_mul_f32 v16,v16,v72
qt 108,3,45056
qw 5
qm 80,96,144
v_exp_f32 v16,v16
buffer_load_dwordx4 v[32:35],v44,s[84:87],s69 offen sc1
qt 110,3,45568
qm 84,96,216
v_fma_f32 v16,v16,v14,v14
qt 96,2,49152
qw 5
qm 88,100,144
v_rcp_f32 v16,v16
buffer_load_dwordx4 v[36:39],v44,s[84:87],s70 offen sc1
qt 98,2,49664
qm 92,100,216
v_mul_f32 v72,v72,v16
qt 100,3,49152
qw 5
qm 80,104,148
v_mul_f32 v16,v73,v73
buffer_load_dwordx4 v[40:43],v44,s[84:87],s71 offen sc1
qt 102,3,49664
qm 84,104,220
v_fma_f32 v16,v16,v12,v10
qt 104,2,53248
qw 5
qm 88,108,148
v_mul_f32 v16,v16,v73
qt 106,2,53760
qm 92,108,220
v_exp_f32 v16,v16
qt 108,3,53248
qw 5
qm 80,96,152
v_fma_f32 v16,v16,v14,v14
v_add_u32 v26,123904,v44
qd 20,26,0
qt 110,3,53760
qm 84,96,224
v_rcp_f32 v16,v16
qt 96,2,57344
qw 6
qm 88,100,152
v_mul_f32 v73,v73,v16
qt 98,2,57856
qm 92,100,224
v_mul_f32 v16,v74,v74
qt 100,3,57344
qw 6
qm 80,104,156
v_fma_f32 v16,v16,v12,v10
qt 102,3,57856
qm 84,104,228
v_mul_f32 v16,v16,v74
qt 104,2,61440
qw 5
qm 88,108,156
s_barrier
v_exp_f32 v16,v16
s_lshl_b32 s30,s7,9
v_cvt_f32_u32 v17,s30
qt 106,2,61952
qm 92,108,228
v_fma_f32 v16,v16,v14,v14
v_mul_f32 v26,0x43000000,v20
qt 108,3,61440
qw 5
qm 80,96,160
v_rcp_f32 v16,v16
v_mul_f32 v27,v17,v20
qt 110,3,61952
qm 84,96,232
v_mul_f32 v74,v74,v16
v_mul_f32 v23,0x43000000,v21
qt 96,4,0
qw 5
qm 88,100,160
v_mul_f32 v16,v75,v75
qt 98,4,512
qm 92,100,232
v_fma_f32 v16,v16,v12,v10
v_mul_f32 v25,v17,v21
qt 100,5,0
qw 5
qm 80,104,164
v_mul_f32 v16,v16,v75
v_exp_f32 v26,v26
qt 102,5,512
qm 84,104,236
v_exp_f32 v16,v16
v_exp_f32 v27,v27
qt 104,4,4096
qw 5
qm 88,108,164
v_fma_f32 v16,v16,v14,v14
qt 106,4,4608
qm 92,108,236
v_rcp_f32 v16,v16
v_fract_f32 v23,v23
qt 108,5,4096
qw 5
qm 80,96,168
v_mul_f32 v75,v75,v16
v_fract_f32 v25,v25
qt 110,5,4608
qm 84,96,240
v_mul_f32 v16,v68,v68
v_cos_f32 v22,v23
qt 96,4,8192
qw 5
qm 88,100,168
v_fma_f32 v16,v16,v13,v11
v_cos_f32 v24,v25
qt 98,4,8704
qm 92,100,240
v_mul_f32 v16,v16,v68
qt 100,5,8192
qw 5
qm 80,104,172
v_exp_f32 v16,v16
v_sin_f32 v23,v23
qt 102,5,8704
qm 84,104,244
v_fma_f32 v16,v16,v15,v15
v_sin_f32 v25,v25
qt 104,4,12288
qw 5
qm 88,108,172
v_rcp_f32 v16,v16
v_mul_f32 v22,v22,v26
qt 106,4,12800
qm 92,108,244
v_mul_f32 v68,v68,v16
qt 108,5,12288
qw 5
qm 80,96,176
v_mul_f32 v16,v69,v69
v_mul_f32 v24,v24,v27
qt 110,5,12800
qm 84,96,248
v_fma_f32 v16,v16,v13,v11
v_mul_f32 v23,v23,v26
qt 96,2,0
qw 5
qm 88,100,176
v_mul_f32 v16,v16,v69
v_mul_f32 v25,v25,v27
qt 98,2,512
qm 92,100,248
v_exp_f32 v16,v16
qt 100,3,0
qw 5
qm 80,104,180
v_fma_f32 v16,v16,v15,v15
qt 102,3,512
qm 84,104,252
v_rcp_f32 v16,v16
qt 104,2,4096
qw 5
qm 88,108,180
s_barrier
v_mul_f32 v69,v69,v16
qt 106,2,4608
qm 92,108,252
qt 108,3,4096
s_mov_b32 s34,0
s_mov_b32 s32,-1
s_mov_b32 s33,-1
s_waitcnt vmcnt(0)
.L_gYo:
s_waitcnt vmcnt(0)
v_xor_b32 v26,s72,v29
v_xor_b32 v27,s72,v31
v_or3_b32 v17,v26,v27,0
v_xor_b32 v26,s73,v33
v_xor_b32 v27,s73,v35
v_or3_b32 v17,v26,v27,v17
v_xor_b32 v26,s74,v37
v_xor_b32 v27,s74,v39
v_or3_b32 v17,v26,v27,v17
v_xor_b32 v26,s75,v41
v_xor_b32 v27,s75,v43
v_or3_b32 v17,v26,v27,v17
v_cmp_ne_u32 vcc,0,v17
s_nop 1
s_and_b64 s[30:31],vcc,s[32:33]
s_cbranch_scc0 .L_gdYo
s_add_u32 s34,s34,1
s_cmp_gt_u32 s34,1000000
s_cbranch_scc1 .L_gdYo
s_sleep 4
buffer_load_dwordx4 v[28:31],v44,s[84:87],s68 offen sc1
buffer_load_dwordx4 v[32:35],v44,s[84:87],s69 offen sc1
buffer_load_dwordx4 v[36:39],v44,s[84:87],s70 offen sc1
buffer_load_dwordx4 v[40:43],v44,s[84:87],s71 offen sc1
s_branch .L_gYo
.L_gdYo:
qw 5
qz 48,96,112
v_mul_f32 v16,v70,v70
v_fma_f32 v36,v22,v40,v36
v_fma_f32 v38,v22,v42,v38
qt 110,3,4608
qz 52,96,184
v_fma_f32 v16,v16,v13,v11
v_fma_f32 v36,-v23,v42,v36
qt 96,4,32768
qw 5
qz 56,100,112
v_mul_f32 v16,v16,v70
v_fma_f32 v38,v23,v40,v38
qt 98,4,33280
qz 60,100,184
v_exp_f32 v16,v16
v_fma_f32 v32,v22,v36,v32
qt 100,5,32768
qw 5
qm 48,104,116
v_fma_f32 v16,v16,v15,v15
v_fma_f32 v34,v22,v38,v34
qt 102,5,33280
qm 52,104,188
v_rcp_f32 v16,v16
v_fma_f32 v32,-v23,v38,v32
qt 104,4,36864
qw 5
qm 56,108,116
v_mul_f32 v70,v70,v16
v_fma_f32 v34,v23,v36,v34
qt 106,4,37376
qm 60,108,188
v_mul_f32 v16,v71,v71
v_fma_f32 v28,v22,v32,v28
qt 108,5,36864
qw 5
qm 48,96,120
v_fma_f32 v16,v16,v13,v11
v_fma_f32 v30,v22,v34,v30
qt 110,5,37376
qm 52,96,192
v_mul_f32 v16,v16,v71
v_fma_f32 v28,-v23,v34,v28
v_fma_f32 v30,v23,v32,v30
qt 96,4,40960
qw 5
qm 56,100,120
v_exp_f32 v16,v16
v_mul_f32 v20,v24,v28
qt 98,4,41472
qm 60,100,192
v_fma_f32 v16,v16,v15,v15
v_mul_f32 v21,v24,v30
qt 100,5,40960
qw 5
qm 48,104,124
v_rcp_f32 v16,v16
v_fma_f32 v20,-v25,v30,v20
qt 102,5,41472
qm 52,104,196
v_mul_f32 v71,v71,v16
v_fma_f32 v21,v25,v28,v21
qt 104,4,45056
qw 5
qm 56,108,124
v_mul_f32 v16,v76,v76
v_lshlrev_b32 v26,3,v0
qt 106,4,45568
qm 60,108,196
v_fma_f32 v16,v16,v13,v11
v_lshl_add_u32 v26,s7,9,v26
qt 108,5,45056
qw 5
qm 48,96,128
v_mul_f32 v16,v16,v76
v_add_u32 v26,125440,v26
qt 110,5,45568
qm 52,96,200
v_exp_f32 v16,v16
ds_write_b64 v26,v[20:21]
qt 96,2,8192
qw 6
qm 56,100,128
v_fma_f32 v16,v16,v15,v15
qt 98,2,8704
qm 60,100,200
v_rcp_f32 v16,v16
qt 100,3,8192
qw 6
qm 48,104,132
v_mul_f32 v76,v76,v16
qt 102,3,8704
qm 52,104,204
v_mul_f32 v16,v77,v77
qt 104,2,12288
qw 6
qm 56,108,132
qw 5
s_barrier
v_fma_f32 v16,v16,v13,v11
qt 106,2,12800
qm 60,108,204
v_mul_f32 v16,v16,v77
qt 108,3,12288
qw 5
qm 48,96,136
v_exp_f32 v16,v16
qt 110,3,12800
qm 52,96,208
v_fma_f32 v16,v16,v15,v15
qt 96,2,16384
qw 5
qm 56,100,136
v_rcp_f32 v16,v16
qt 98,2,16896
qm 60,100,208
v_mul_f32 v77,v77,v16
qt 100,3,16384
qw 5
qm 48,104,140
v_mul_f32 v16,v78,v78
qt 102,3,16896
qm 52,104,212
v_fma_f32 v16,v16,v13,v11
qt 104,2,20480
qw 5
qm 56,108,140
v_mul_f32 v16,v16,v78
qt 106,2,20992
qm 60,108,212
v_exp_f32 v16,v16
qt 108,3,20480
qw 5
qm 48,96,144
v_fma_f32 v16,v16,v15,v15
qt 110,3,20992
qm 52,96,216
v_rcp_f32 v16,v16
qt 96,2,24576
qw 5
qm 56,100,144
v_mul_f32 v78,v78,v16
qt 98,2,25088
qm 60,100,216
v_mul_f32 v16,v79,v79
qt 100,3,24576
qw 5
qm 48,104,148
v_fma_f32 v16,v16,v13,v11
qt 102,3,25088
qm 52,104,220
v_mul_f32 v16,v16,v79
qt 104,2,28672
qw 5
qm 56,108,148
v_exp_f32 v16,v16
qt 106,2,29184
qm 60,108,220
v_fma_f32 v16,v16,v15,v15
qt 108,3,28672
qw 5
qm 48,96,152
v_rcp_f32 v16,v16
qt 110,3,29184
qm 52,96,224
v_mul_f32 v79,v79,v16
qt 96,2,32768
qw 5
qm 56,100,152
v_mul_f32 v64,v64,v80
qt 98,2,33280
qm 60,100,224
v_mul_f32 v65,v65,v81
qt 100,3,32768
qw 5
qm 48,104,156
v_cvt_pk_bf16_f32 v64,v64,v65
qt 102,3,33280
qm 52,104,228
v_mul_f32 v66,v66,v82
qt 104,2,36864
qw 5
qm 56,108,156
s_barrier
v_mul_f32 v67,v67,v83
qt 106,2,37376
qm 60,108,228
v_cvt_pk_bf16_f32 v65,v66,v67
qt 108,3,36864
qw 5
qm 48,96,160
v_mul_f32 v72,v72,v88
v_and_b32 v44,15,v0
v_lshrrev_b32 v45,4,v0
qt 110,3,37376
qm 52,96,232
v_mul_f32 v73,v73,v89
v_lshl_add_u32 v17,v44,2,v45
qt 96,2,40960
qw 5
qm 56,100,160
v_cvt_pk_bf16_f32 v66,v72,v73
v_lshl_add_u32 v45,s7,2,v45
qt 98,2,41472
qm 60,100,232
v_mul_f32 v74,v74,v90
v_lshlrev_b32 v28,4,v44
qt 100,3,40960
qw 5
qm 48,104,164
v_mul_f32 v75,v75,v91
v_add_u32 v28,123904,v28
qt 102,3,41472
qm 52,104,236
v_cvt_pk_bf16_f32 v67,v74,v75
v_lshlrev_b32 v44,3,v44
v_lshl_add_u32 v42,v45,9,v44
qt 104,2,45056
qw 5
qm 56,108,164
ds_write_b128 v18,v[64:67] offset:24576
v_mul_f32 v68,v68,v84
v_add_u32 v42,114688,v42
qt 106,2,45568
qm 60,108,236
s_add_u32 s15,s76,24576
buffer_store_dwordx4 v[64:67],v18,s[88:91],s15 offen offset:0 sc1
v_mul_f32 v69,v69,v85
v_add_u32 v44,124928,v44
qt 108,3,45056
qw 6
qm 48,96,168
v_cvt_pk_bf16_f32 v68,v68,v69
qr 28,28,256
qt 110,3,45568
qm 52,96,240
v_mul_f32 v70,v70,v86
qd 32,44,128
qt 96,2,49152
qw 8
qm 56,100,168
v_mul_f32 v71,v71,v87
qd 34,44,640
qd 36,44,1152
qt 98,2,49664
v_min_u32 v8,1664,v8
buffer_load_dword v1,v8,s[48:51],s54 offen
s_add_u32 s54,s54,1792
buffer_load_dword v1,v6,s[60:63],s52 offen
qm 60,100,240
v_cvt_pk_bf16_f32 v69,v70,v71
qd 38,44,1664
qt 100,3,49152
qw 10
qm 48,104,172
v_mul_f32 v76,v76,v92
qd 40,44,2176
qt 102,3,49664
qm 52,104,244
v_mul_f32 v77,v77,v93
qd 42,42,128
qt 104,2,53248
qw 11
qm 56,108,172
v_cvt_pk_bf16_f32 v70,v76,v77
qr 20,18,57344
qt 106,2,53760
qm 60,108,244
v_mul_f32 v78,v78,v94
qr 24,18,58368
v_lshlrev_b32 v17,3,v17
qt 108,3,53248
qw 10
qm 48,96,176
v_mul_f32 v79,v79,v95
v_add_u32 v17,s13,v17
qt 110,3,53760
qm 52,96,248
v_cvt_pk_bf16_f32 v71,v78,v79
v_add_u32 v17,139264,v17
qt 96,2,57344
qw 8
qm 56,100,176
ds_write_b128 v18,v[68:71] offset:25600
v_lshlrev_b32 v45,3,v45
qt 98,2,57856
qm 60,100,248
s_add_u32 s15,s76,24576
buffer_store_dwordx4 v[68:71],v18,s[88:91],s15 offen offset:1024 sc1
v_cvt_f32_u32 v45,v45
qt 100,3,57344
qw 7
qm 48,104,180
qt 102,3,57856
qm 52,104,252
qt 104,2,61440
qw 6
qm 56,108,180
qw 4
s_barrier
qt 106,2,61952
qm 60,108,252
qt 108,3,61440
v_add_f32 v34,v34,v36
v_add_f32 v35,v35,v37
v_add_f32 v38,v38,v40
v_add_f32 v39,v39,v41
v_add_f32 v34,v34,v38
v_add_f32 v35,v35,v39
v_mul_f32 v44,v45,v28
v_mul_f32 v37,v45,v29
v_exp_f32 v44,v44
v_fract_f32 v37,v37
v_cos_f32 v36,v37
v_sin_f32 v37,v37
v_mul_f32 v36,v36,v44
v_mul_f32 v37,v37,v44
v_fma_f32 v38,v36,v34,v42
v_fma_f32 v39,v36,v35,v43
v_fma_f32 v38,-v37,v35,v38
v_fma_f32 v39,v37,v34,v39
v_lshlrev_b32 v34,16,v20
v_and_b32 v35,0xffff0000,v20
v_fma_f32 v40,v32,v38,v34
v_fma_f32 v41,v32,v39,v35
v_fma_f32 v40,-v33,v39,v40
v_fma_f32 v41,v33,v38,v41
v_mul_f32 v36,v30,v40
v_mul_f32 v42,v30,v41
v_fma_f32 v36,-v31,v41,v36
v_fma_f32 v42,v31,v40,v42
v_lshlrev_b32 v34,16,v21
v_and_b32 v35,0xffff0000,v21
v_fma_f32 v38,v32,v40,v34
v_fma_f32 v39,v32,v41,v35
v_fma_f32 v38,-v33,v41,v38
v_fma_f32 v39,v33,v40,v39
v_mul_f32 v37,v30,v38
v_mul_f32 v43,v30,v39
v_fma_f32 v37,-v31,v39,v37
v_fma_f32 v43,v31,v38,v43
v_cvt_pk_bf16_f32 v20,v36,v37
v_cvt_pk_bf16_f32 v28,-v42,-v43
v_lshlrev_b32 v34,16,v22
v_and_b32 v35,0xffff0000,v22
v_fma_f32 v40,v32,v38,v34
v_fma_f32 v41,v32,v39,v35
v_fma_f32 v40,-v33,v39,v40
v_fma_f32 v41,v33,v38,v41
v_mul_f32 v36,v30,v40
v_mul_f32 v42,v30,v41
v_fma_f32 v36,-v31,v41,v36
v_fma_f32 v42,v31,v40,v42
v_lshlrev_b32 v34,16,v23
v_and_b32 v35,0xffff0000,v23
v_fma_f32 v38,v32,v40,v34
v_fma_f32 v39,v32,v41,v35
v_fma_f32 v38,-v33,v41,v38
v_fma_f32 v39,v33,v40,v39
v_mul_f32 v37,v30,v38
v_mul_f32 v43,v30,v39
v_fma_f32 v37,-v31,v39,v37
v_fma_f32 v43,v31,v38,v43
v_cvt_pk_bf16_f32 v21,v36,v37
v_cvt_pk_bf16_f32 v29,-v42,-v43
ds_write_b64 v17,v[20:21] offset:0
ds_write_b64 v17,v[28:29] offset:512
v_lshlrev_b32 v34,16,v24
v_and_b32 v35,0xffff0000,v24
v_fma_f32 v40,v32,v38,v34
v_fma_f32 v41,v32,v39,v35
v_fma_f32 v40,-v33,v39,v40
v_fma_f32 v41,v33,v38,v41
v_mul_f32 v36,v30,v40
v_mul_f32 v42,v30,v41
v_fma_f32 v36,-v31,v41,v36
v_fma_f32 v42,v31,v40,v42
v_lshlrev_b32 v34,16,v25
v_and_b32 v35,0xffff0000,v25
v_fma_f32 v38,v32,v40,v34
v_fma_f32 v39,v32,v41,v35
v_fma_f32 v38,-v33,v41,v38
v_fma_f32 v39,v33,v40,v39
v_mul_f32 v37,v30,v38
v_mul_f32 v43,v30,v39
v_fma_f32 v37,-v31,v39,v37
v_fma_f32 v43,v31,v38,v43
v_cvt_pk_bf16_f32 v24,v36,v37
v_cvt_pk_bf16_f32 v28,-v42,-v43
v_lshlrev_b32 v34,16,v26
v_and_b32 v35,0xffff0000,v26
v_fma_f32 v40,v32,v38,v34
v_fma_f32 v41,v32,v39,v35
v_fma_f32 v40,-v33,v39,v40
v_fma_f32 v41,v33,v38,v41
v_mul_f32 v36,v30,v40
v_mul_f32 v42,v30,v41
v_fma_f32 v36,-v31,v41,v36
v_fma_f32 v42,v31,v40,v42
v_lshlrev_b32 v34,16,v27
v_and_b32 v35,0xffff0000,v27
v_fma_f32 v38,v32,v40,v34
v_fma_f32 v39,v32,v41,v35
v_fma_f32 v38,-v33,v41,v38
v_fma_f32 v39,v33,v40,v39
v_mul_f32 v37,v30,v38
v_mul_f32 v43,v30,v39
v_fma_f32 v37,-v31,v39,v37
v_fma_f32 v43,v31,v38,v43
v_cvt_pk_bf16_f32 v25,v36,v37
v_cvt_pk_bf16_f32 v29,-v42,-v43
ds_write_b64 v17,v[24:25] offset:1024
ds_write_b64 v17,v[28:29] offset:1536
qw 9
qz 80,96,112
qt 110,3,61952
qz 84,96,184
v_mul_f32 v16,v48,v48
qt 96,4,0
qw 9
qz 88,100,112
v_fma_f32 v16,v16,v12,v10
v_lshlrev_b32 v36,3,v0
v_add_u32 v36,s13,v36
qt 98,4,512
qz 92,100,184
v_mul_f32 v16,v16,v48
v_add_u32 v36,131072,v36
qt 100,5,0
qw 9
qm 80,104,116
v_exp_f32 v16,v16
qt 28,36,8192
qt 102,5,512
qm 84,104,188
v_fma_f32 v16,v16,v14,v14
qt 30,36,8704
qt 104,4,4096
qw 7
qm 88,108,116
v_rcp_f32 v16,v16
qt 32,36,9216
qt 106,4,4608
qm 92,108,188
v_mul_f32 v48,v48,v16
qt 34,36,9728
qt 108,5,4096
qw 9
qm 80,96,120
v_mul_f32 v16,v49,v49
qt 110,5,4608
qm 84,96,192
v_fma_f32 v16,v16,v12,v10
qt 96,4,8192
qw 8
qm 88,100,120
v_mul_f32 v16,v16,v49
qt 98,4,8704
qm 92,100,192
v_exp_f32 v16,v16
qt 100,5,8192
qw 6
qm 80,104,124
v_fma_f32 v16,v16,v14,v14
qt 102,5,8704
qm 84,104,196
v_rcp_f32 v16,v16
qt 104,4,12288
qw 5
qm 88,108,124
v_mul_f32 v49,v49,v16
qt 106,4,12800
qm 92,108,196
v_mul_f32 v16,v50,v50
s_add_u32 s15,s76,73728
buffer_store_dwordx4 v[28:31],v18,s[88:91],s15 offen offset:0 sc1
qt 108,5,12288
qw 5
qm 80,96,128
v_fma_f32 v16,v16,v12,v10
s_add_u32 s15,s76,73728
buffer_store_dwordx4 v[32:35],v18,s[88:91],s15 offen offset:1024 sc1
qt 110,5,12800
qm 84,96,200
v_mul_f32 v16,v16,v50
qt 96,2,0
qw 5
qm 88,100,128
v_exp_f32 v16,v16
qt 98,2,512
qm 92,100,200
v_fma_f32 v16,v16,v14,v14
qt 100,3,0
qw 5
qm 80,104,132
v_rcp_f32 v16,v16
qt 102,3,512
qm 84,104,204
v_mul_f32 v50,v50,v16
qt 104,2,4096
qw 5
qm 88,108,132
s_barrier
v_mul_f32 v16,v51,v51
qt 106,2,4608
qm 92,108,204
v_fma_f32 v16,v16,v12,v10
qt 108,3,4096
qw 5
qm 80,96,136
v_mul_f32 v16,v16,v51
qt 110,3,4608
qm 84,96,208
v_exp_f32 v16,v16
qt 96,2,8192
qw 5
qm 88,100,136
v_fma_f32 v16,v16,v14,v14
qt 98,2,8704
qm 92,100,208
v_rcp_f32 v16,v16
qt 100,3,8192
qw 5
qm 80,104,140
v_mul_f32 v51,v51,v16
qt 102,3,8704
qm 84,104,212
v_mul_f32 v16,v56,v56
qt 104,2,12288
qw 5
qm 88,108,140
v_fma_f32 v16,v16,v12,v10
qt 106,2,12800
qm 92,108,212
v_mul_f32 v16,v16,v56
qt 108,3,12288
qw 5
qm 80,96,144
v_exp_f32 v16,v16
qt 110,3,12800
qm 84,96,216
v_fma_f32 v16,v16,v14,v14
qt 96,2,16384
qw 5
qm 88,100,144
v_rcp_f32 v16,v16
qt 98,2,16896
qm 92,100,216
v_mul_f32 v56,v56,v16
qt 100,3,16384
qw 5
qm 80,104,148
v_mul_f32 v16,v57,v57
qt 102,3,16896
qm 84,104,220
v_fma_f32 v16,v16,v12,v10
qt 104,2,20480
qw 5
qm 88,108,148
v_mul_f32 v16,v16,v57
qt 106,2,20992
qm 92,108,220
v_exp_f32 v16,v16
qt 108,3,20480
qw 5
qm 80,96,152
v_fma_f32 v16,v16,v14,v14
qt 110,3,20992
qm 84,96,224
v_rcp_f32 v16,v16
qt 96,2,24576
qw 5
qm 88,100,152
v_mul_f32 v57,v57,v16
qt 98,2,25088
qm 92,100,224
v_mul_f32 v16,v58,v58
qt 100,3,24576
qw 5
qm 80,104,156
v_fma_f32 v16,v16,v12,v10
qt 102,3,25088
qm 84,104,228
v_mul_f32 v16,v16,v58
qt 104,2,28672
qw 5
qm 88,108,156
s_barrier
v_exp_f32 v16,v16
qt 106,2,29184
qm 92,108,228
v_fma_f32 v16,v16,v14,v14
qt 108,3,28672
qw 5
qm 80,96,160
v_rcp_f32 v16,v16
v_and_b32 v44,15,v0
v_lshrrev_b32 v45,4,v0
qt 110,3,29184
qm 84,96,232
v_mul_f32 v58,v58,v16
v_lshl_add_u32 v17,v44,2,v45
qt 96,2,32768
qw 5
qm 88,100,160
v_mul_f32 v16,v59,v59
v_lshl_add_u32 v45,s7,2,v45
qt 98,2,33280
qm 92,100,232
v_fma_f32 v16,v16,v12,v10
v_lshlrev_b32 v28,4,v44
qt 100,3,32768
qw 5
qm 80,104,164
v_mul_f32 v16,v16,v59
v_add_u32 v28,123904,v28
qt 102,3,33280
qm 84,104,236
v_exp_f32 v16,v16
v_lshlrev_b32 v44,3,v44
v_lshl_add_u32 v42,v45,9,v44
qt 104,2,36864
qw 5
qm 88,108,164
v_fma_f32 v16,v16,v14,v14
v_add_u32 v42,114688,v42
qt 106,2,37376
qm 92,108,236
v_rcp_f32 v16,v16
v_add_u32 v44,124928,v44
qt 108,3,36864
qw 5
qm 80,96,168
v_mul_f32 v59,v59,v16
qr 28,28,768
qt 110,3,37376
qm 84,96,240
v_mul_f32 v16,v52,v52
qd 32,44,384
qt 96,2,40960
qw 7
qm 88,100,168
v_fma_f32 v16,v16,v13,v11
qd 34,44,896
qd 36,44,1408
qt 98,2,41472
buffer_load_dword v1,v8,s[48:51],s54 offen
buffer_load_dword v1,v6,s[64:67],s52 offen
qm 92,100,240
v_mul_f32 v16,v16,v52
qd 38,44,1920
qt 100,3,40960
qw 10
qm 80,104,172
v_exp_f32 v16,v16
qd 40,44,2432
qt 102,3,41472
qm 84,104,244
v_fma_f32 v16,v16,v15,v15
qd 42,42,384
qt 104,2,45056
qw 11
qm 88,108,172
v_rcp_f32 v16,v16
qr 20,19,8192
qt 106,2,45568
qm 92,108,244
v_mul_f32 v52,v52,v16
qr 24,19,9216
v_lshlrev_b32 v17,3,v17
qt 108,3,45056
qw 10
qm 80,96,176
v_mul_f32 v16,v53,v53
v_add_u32 v17,s13,v17
qt 110,3,45568
qm 84,96,248
v_fma_f32 v16,v16,v13,v11
v_add_u32 v17,155648,v17
qt 96,2,49152
qw 8
qm 88,100,176
v_mul_f32 v16,v16,v53
v_lshlrev_b32 v45,3,v45
qt 98,2,49664
qm 92,100,248
v_exp_f32 v16,v16
v_cvt_f32_u32 v45,v45
qt 100,3,49152
qw 6
qm 80,104,180
v_fma_f32 v16,v16,v15,v15
qt 102,3,49664
qm 84,104,252
v_rcp_f32 v16,v16
qt 104,2,53248
qw 5
qm 88,108,180
s_barrier
v_mul_f32 v53,v53,v16
qt 106,2,53760
qm 92,108,252
qt 108,3,53248
v_add_f32 v34,v34,v36
v_add_f32 v35,v35,v37
v_add_f32 v38,v38,v40
v_add_f32 v39,v39,v41
v_add_f32 v34,v34,v38
v_add_f32 v35,v35,v39
v_mul_f32 v44,v45,v28
v_mul_f32 v37,v45,v29
v_exp_f32 v44,v44
v_fract_f32 v37,v37
v_cos_f32 v36,v37
v_sin_f32 v37,v37
v_mul_f32 v36,v36,v44
v_mul_f32 v37,v37,v44
v_fma_f32 v38,v36,v34,v42
v_fma_f32 v39,v36,v35,v43
v_fma_f32 v38,-v37,v35,v38
v_fma_f32 v39,v37,v34,v39
v_lshlrev_b32 v34,16,v20
v_and_b32 v35,0xffff0000,v20
v_fma_f32 v40,v32,v38,v34
v_fma_f32 v41,v32,v39,v35
v_fma_f32 v40,-v33,v39,v40
v_fma_f32 v41,v33,v38,v41
v_mul_f32 v36,v30,v40
v_mul_f32 v42,v30,v41
v_fma_f32 v36,-v31,v41,v36
v_fma_f32 v42,v31,v40,v42
v_lshlrev_b32 v34,16,v21
v_and_b32 v35,0xffff0000,v21
v_fma_f32 v38,v32,v40,v34
v_fma_f32 v39,v32,v41,v35
v_fma_f32 v38,-v33,v41,v38
v_fma_f32 v39,v33,v40,v39
v_mul_f32 v37,v30,v38
v_mul_f32 v43,v30,v39
v_fma_f32 v37,-v31,v39,v37
v_fma_f32 v43,v31,v38,v43
v_cvt_pk_bf16_f32 v20,v36,v37
v_cvt_pk_bf16_f32 v28,-v42,-v43
v_lshlrev_b32 v34,16,v22
v_and_b32 v35,0xffff0000,v22
v_fma_f32 v40,v32,v38,v34
v_fma_f32 v41,v32,v39,v35
v_fma_f32 v40,-v33,v39,v40
v_fma_f32 v41,v33,v38,v41
v_mul_f32 v36,v30,v40
v_mul_f32 v42,v30,v41
v_fma_f32 v36,-v31,v41,v36
v_fma_f32 v42,v31,v40,v42
v_lshlrev_b32 v34,16,v23
v_and_b32 v35,0xffff0000,v23
v_fma_f32 v38,v32,v40,v34
v_fma_f32 v39,v32,v41,v35
v_fma_f32 v38,-v33,v41,v38
v_fma_f32 v39,v33,v40,v39
v_mul_f32 v37,v30,v38
v_mul_f32 v43,v30,v39
v_fma_f32 v37,-v31,v39,v37
v_fma_f32 v43,v31,v38,v43
v_cvt_pk_bf16_f32 v21,v36,v37
v_cvt_pk_bf16_f32 v29,-v42,-v43
ds_write_b64 v17,v[20:21] offset:0
ds_write_b64 v17,v[28:29] offset:512
v_lshlrev_b32 v34,16,v24
v_and_b32 v35,0xffff0000,v24
v_fma_f32 v40,v32,v38,v34
v_fma_f32 v41,v32,v39,v35
v_fma_f32 v40,-v33,v39,v40
v_fma_f32 v41,v33,v38,v41
v_mul_f32 v36,v30,v40
v_mul_f32 v42,v30,v41
v_fma_f32 v36,-v31,v41,v36
v_fma_f32 v42,v31,v40,v42
v_lshlrev_b32 v34,16,v25
v_and_b32 v35,0xffff0000,v25
v_fma_f32 v38,v32,v40,v34
v_fma_f32 v39,v32,v41,v35
v_fma_f32 v38,-v33,v41,v38
v_fma_f32 v39,v33,v40,v39
v_mul_f32 v37,v30,v38
v_mul_f32 v43,v30,v39
v_fma_f32 v37,-v31,v39,v37
v_fma_f32 v43,v31,v38,v43
v_cvt_pk_bf16_f32 v24,v36,v37
v_cvt_pk_bf16_f32 v28,-v42,-v43
v_lshlrev_b32 v34,16,v26
v_and_b32 v35,0xffff0000,v26
v_fma_f32 v40,v32,v38,v34
v_fma_f32 v41,v32,v39,v35
v_fma_f32 v40,-v33,v39,v40
v_fma_f32 v41,v33,v38,v41
v_mul_f32 v36,v30,v40
v_mul_f32 v42,v30,v41
v_fma_f32 v36,-v31,v41,v36
v_fma_f32 v42,v31,v40,v42
v_lshlrev_b32 v34,16,v27
v_and_b32 v35,0xffff0000,v27
v_fma_f32 v38,v32,v40,v34
v_fma_f32 v39,v32,v41,v35
v_fma_f32 v38,-v33,v41,v38
v_fma_f32 v39,v33,v40,v39
v_mul_f32 v37,v30,v38
v_mul_f32 v43,v30,v39
v_fma_f32 v37,-v31,v39,v37
v_fma_f32 v43,v31,v38,v43
v_cvt_pk_bf16_f32 v25,v36,v37
v_cvt_pk_bf16_f32 v29,-v42,-v43
ds_write_b64 v17,v[24:25] offset:1024
ds_write_b64 v17,v[28:29] offset:1536
qw 9
qz 64,96,112
v_mul_f32 v16,v54,v54
qt 110,3,53760
qz 68,96,184
v_fma_f32 v16,v16,v13,v11
v_bfe_u32 v44,v0,2,2
qt 96,2,57344
qw 9
qz 72,100,112
v_mul_f32 v16,v16,v54
v_lshlrev_b32 v36,3,v0
v_add_u32 v36,s13,v36
v_mov_b32 v38,s16
qt 98,2,57856
qz 76,100,184
v_exp_f32 v16,v16
v_add_u32 v36,131072,v36
v_and_b32 v37,48,v0
qt 100,3,57344
qw 9
qm 64,104,116
v_fma_f32 v16,v16,v15,v15
qt 28,36,24576
qt 102,3,57856
qm 68,104,188
v_rcp_f32 v16,v16
qt 30,36,25088
v_lshlrev_b32 v37,24,v37
s_and_b32 s33,s8,3
s_lshl_b32 s33,s33,2
s_add_u32 s33,s33,s7
s_lshl_b32 s33,s33,3
s_lshl_b32 s33,s33,2
s_add_u32 s33,s33,122896
v_lshl_add_u32 v45,v44,2,s33
qt 104,2,61440
qw 7
qm 72,108,116
v_mul_f32 v54,v54,v16
qt 32,36,25600
qt 106,2,61952
qm 76,108,188
v_mul_f32 v16,v55,v55
qt 34,36,26112
v_lshl_or_b32 v37,v0,7,v37
qt 108,3,61440
qw 9
qm 64,96,120
v_fma_f32 v16,v16,v13,v11
s_mul_i32 s72,s10,2048
qt 110,3,61952
qm 68,96,192
v_mul_f32 v16,v16,v55
qt 96,4,0
qw 8
qm 72,100,120
v_exp_f32 v16,v16
s_add_u32 s72,s72,8192
ds_read_b32 v40,v45 offset:0
qt 98,4,512
qm 76,100,192
v_fma_f32 v16,v16,v15,v15
qt 100,5,0
qw 7
qm 64,104,124
v_rcp_f32 v16,v16
s_and_b32 s30,s8,3
qt 102,5,512
qm 68,104,196
v_mul_f32 v55,v55,v16
qt 104,4,4096
qw 6
qm 72,108,124
v_mul_f32 v16,v60,v60
s_lshl_b32 s30,s30,4
ds_read_b32 v41,v45 offset:512
qt 106,4,4608
qm 76,108,196
v_fma_f32 v16,v16,v13,v11
s_add_u32 s15,s76,90112
buffer_store_dwordx4 v[28:31],v18,s[88:91],s15 offen offset:0 sc1
s_and_b32 s31,s8,4
qt 108,5,4096
qw 6
qm 64,96,128
v_mul_f32 v16,v16,v60
s_add_u32 s15,s76,90112
buffer_store_dwordx4 v[32:35],v18,s[88:91],s15 offen offset:1024 sc1
qt 110,5,4608
qm 68,96,200
v_exp_f32 v16,v16
s_add_u32 s30,s30,s31
qt 96,4,8192
qw 6
qm 72,100,128
v_fma_f32 v16,v16,v15,v15
qt 98,4,8704
qm 76,100,200
v_rcp_f32 v16,v16
s_add_u32 s68,s30,s72
qt 100,5,8192
qw 5
qm 64,104,132
v_mul_f32 v60,v60,v16
qt 102,5,8704
qm 68,104,204
v_mul_f32 v16,v61,v61
s_lshl_b32 s31,s8,8
qt 104,4,12288
qw 5
qm 72,108,132
s_barrier
v_fma_f32 v16,v16,v13,v11
s_add_u32 s78,s68,s31
qt 106,4,12800
qm 76,108,204
v_mul_f32 v16,v16,v61
qt 108,5,12288
qw 5
qm 64,96,136
v_exp_f32 v16,v16
s_add_u32 s78,s78,128
qt 110,5,12800
qm 68,96,208
v_fma_f32 v16,v16,v15,v15
qt 96,2,0
qw 5
qm 72,100,136
v_rcp_f32 v16,v16
s_add_u32 s68,s68,8
qt 98,2,512
qm 76,100,208
v_mul_f32 v61,v61,v16
qt 100,3,0
qw 5
qm 64,104,140
v_mul_f32 v16,v62,v62
s_cmp_eq_u32 s6,4
s_cselect_b32 s68,s68,0x7ff00000
qt 102,3,512
qm 68,104,212
v_fma_f32 v16,v16,v13,v11
s_add_u32 s72,s72,s31
qt 104,2,4096
qw 5
qm 72,108,140
v_mul_f32 v16,v16,v62
qt 106,2,4608
qm 76,108,212
v_exp_f32 v16,v16
s_add_u32 s72,s72,128
qt 108,3,4096
qw 5
qm 64,96,144
v_fma_f32 v16,v16,v15,v15
qt 110,3,4608
qm 68,96,216
v_rcp_f32 v16,v16
s_add_u32 s74,s16,8
qt 96,2,8192
qw 5
qm 72,100,144
v_mul_f32 v62,v62,v16
qt 98,2,8704
qm 76,100,216
v_mul_f32 v16,v63,v63
v_and_b32 v42,15,v0
qt 100,3,8192
qw 5
qm 64,104,148
v_fma_f32 v16,v16,v13,v11
v_lshrrev_b32 v43,5,v0
qt 102,3,8704
qm 68,104,220
v_mul_f32 v16,v16,v63
qt 104,2,12288
qw 5
qm 72,108,148
v_exp_f32 v16,v16
v_bfe_u32 v25,v0,4,1
qt 106,2,12800
qm 76,108,220
v_fma_f32 v16,v16,v15,v15
qt 108,3,12288
qw 5
qm 64,96,152
v_rcp_f32 v16,v16
v_lshlrev_b32 v25,3,v25
qt 110,3,12800
qm 68,96,224
v_mul_f32 v63,v63,v16
qt 96,2,16384
qw 5
qm 72,100,152
v_mul_f32 v48,v48,v80
v_add_u32 v25,2048,v25
qt 98,2,16896
qm 76,100,224
v_mul_f32 v49,v49,v81
v_lshl_add_u32 v24,v42,7,v25
qt 100,3,16384
qw 5
qm 64,104,156
v_cvt_pk_bf16_f32 v48,v48,v49
qt 102,3,16896
qm 68,104,228
v_mul_f32 v50,v50,v82
v_lshl_add_u32 v22,v42,5,v25
qt 104,2,20480
qw 5
qm 72,108,156
s_waitcnt vmcnt(0)
s_barrier
v_mul_f32 v51,v51,v83
v_add_u32 v38,1,v38
buffer_store_dword v38,v37,s[96:99],s68 offen sc1
qt 106,2,20992
qm 76,108,228
v_cvt_pk_bf16_f32 v49,v50,v51
v_lshrrev_b32 v42,1,v42
qt 108,3,20480
qw 5
qm 64,96,160
v_mul_f32 v56,v56,v88
qt 110,3,20992
qm 68,96,232
v_mul_f32 v57,v57,v89
v_xor_b32 v25,v42,v43
qt 96,2,24576
qw 5
qm 72,100,160
v_cvt_pk_bf16_f32 v50,v56,v57
v_lshl_add_u32 v24,v25,4,v24
qt 98,2,25088
qm 76,100,232
v_mul_f32 v58,v58,v90
qt 100,3,24576
qw 5
qm 64,104,164
v_mul_f32 v59,v59,v91
v_lshrrev_b32 v42,2,v42
qt 102,3,25088
qm 68,104,236
v_cvt_pk_bf16_f32 v51,v58,v59
qt 104,2,28672
qw 5
qm 72,108,164
ds_write_b128 v18,v[48:51] offset:40960
v_mul_f32 v52,v52,v84
v_xor_b32 v42,v42,v43
qt 106,2,29184
qm 76,108,236
s_add_u32 s15,s76,40960
buffer_store_dwordx4 v[48:51],v18,s[88:91],s15 offen offset:0 sc1
v_mul_f32 v53,v53,v85
qt 108,3,28672
qw 6
qm 64,96,168
v_cvt_pk_bf16_f32 v52,v52,v53
v_lshl_add_u32 v22,v42,4,v22
qt 110,3,29184
qm 68,96,240
v_mul_f32 v54,v54,v86
v_add_u32 v23,0x10000,v22
qt 96,2,32768
qw 6
qm 72,100,168
v_mul_f32 v55,v55,v87
qt 98,2,33280
qm 76,100,240
v_cvt_pk_bf16_f32 v53,v54,v55
v_xor_b32 v25,32,v24
qt 100,3,32768
qw 5
qm 64,104,172
v_mul_f32 v60,v60,v92
qt 102,3,33280
qm 68,104,244
v_mul_f32 v61,v61,v93
v_xor_b32 v26,64,v24
qt 104,2,36864
qw 5
qm 72,108,172
v_cvt_pk_bf16_f32 v54,v60,v61
qt 106,2,37376
qm 76,108,244
v_mul_f32 v62,v62,v94
v_xor_b32 v27,0x60,v24
qt 108,3,36864
qw 5
qm 64,96,176
v_mul_f32 v63,v63,v95
v_lshlrev_b32 v36,3,v0
qt 110,3,37376
qm 68,96,248
v_cvt_pk_bf16_f32 v55,v62,v63
qt 96,2,40960
qw 5
qm 72,100,176
ds_write_b128 v18,v[52:55] offset:41984
v_add_u32 v36,s13,v36
qt 98,2,41472
qm 76,100,248
s_add_u32 s15,s76,40960
buffer_store_dwordx4 v[52:55],v18,s[88:91],s15 offen offset:1024 sc1
qt 100,3,40960
qw 6
qm 64,104,180
buffer_load_dword v39,off,s[96:99],s78 sc1
v_add_u32 v36,131072,v36
qt 102,3,41472
qm 68,104,252
qt 104,2,45056
qw 6
qm 72,108,180
qw 4
s_barrier
qt 106,2,45568
qm 76,108,252
qt 108,3,45056
qw 5
qz 80,96,112
qt 110,3,45568
qz 84,96,184
v_mul_f32 v16,v64,v64
qt 96,2,49152
qw 5
qz 88,100,112
v_fma_f32 v16,v16,v12,v10
qt 98,2,49664
qz 92,100,184
v_mul_f32 v16,v16,v64
qt 100,3,49152
qw 5
qm 80,104,116
v_exp_f32 v16,v16
qt 102,3,49664
qm 84,104,188
v_fma_f32 v16,v16,v14,v14
qt 104,2,53248
qw 5
qm 88,108,116
v_rcp_f32 v16,v16
qt 106,2,53760
qm 92,108,188
v_mul_f32 v64,v64,v16
qt 108,3,53248
qw 5
qm 80,96,120
v_mul_f32 v16,v65,v65
qt 110,3,53760
qm 84,96,192
v_fma_f32 v16,v16,v12,v10
qr 112,18,0
qt 96,2,57344
qw 6
qm 88,100,120
v_mul_f32 v16,v16,v65
qt 98,2,57856
qm 92,100,192
v_exp_f32 v16,v16
qr 184,18,1024
qt 100,3,57344
qw 7
qm 80,104,124
v_fma_f32 v16,v16,v14,v14
qt 102,3,57856
qm 84,104,196
v_rcp_f32 v16,v16
qr 116,18,8192
qt 104,2,61440
qw 8
qm 88,108,124
v_mul_f32 v65,v65,v16
qt 106,2,61952
qm 92,108,196
v_mul_f32 v16,v66,v66
qr 188,18,9216
qt 108,3,61440
qw 8
qm 80,96,128
v_fma_f32 v16,v16,v12,v10
qt 110,3,61952
qm 84,96,200
v_mul_f32 v16,v16,v66
qt 96,4,0
qw 7
qm 88,100,128
v_exp_f32 v16,v16
qt 98,4,512
qm 92,100,200
v_fma_f32 v16,v16,v14,v14
qt 100,5,0
qw 6
qm 80,104,132
v_rcp_f32 v16,v16
qt 102,5,512
qm 84,104,204
v_mul_f32 v66,v66,v16
qt 104,4,4096
qw 5
qm 88,108,132
s_waitcnt vmcnt(0)
s_mov_b32 s35,0
.L_wYo1:
v_readfirstlane_b32 s34,v39
s_sub_u32 s34,s74,s34
s_cmp_le_u32 s34,7
s_cbranch_scc1 .L_wYo1d
s_add_u32 s35,s35,1
s_cmp_gt_u32 s35,1000000
s_cbranch_scc1 .L_wYo1d
s_sleep 2
buffer_load_dword v39,off,s[96:99],s78 sc1
s_waitcnt vmcnt(0)
s_branch .L_wYo1
.L_wYo1d:
s_barrier
v_mul_f32 v16,v67,v67
qt 106,4,4608
qm 92,108,204
v_fma_f32 v16,v16,v12,v10
qt 108,5,4096
qw 5
qm 80,96,136
v_mul_f32 v16,v16,v67
v_add_u32 v28,0x10000,v24
qt 110,5,4608
qm 84,96,208
v_exp_f32 v16,v16
v_add_u32 v29,0x10000,v25
qt 128,36,0
qt 96,4,8192
qw 6
qm 88,100,136
v_fma_f32 v16,v16,v14,v14
qt 130,36,512
qt 98,4,8704
qm 92,100,208
v_rcp_f32 v16,v16
v_add_u32 v30,0x10000,v26
qt 200,36,1024
s_add_u32 s15,s77,0
buffer_load_dwordx4 v[48:51],v18,s[88:91],s15 offen offset:0 sc1
qt 100,5,8192
qw 8
qm 80,104,140
v_mul_f32 v67,v67,v16
v_add_u32 v31,0x10000,v27
qt 202,36,1536
qt 102,5,8704
qm 84,104,212
v_mul_f32 v16,v72,v72
qt 132,36,8192
qt 104,4,12288
qw 10
qm 88,108,140
v_fma_f32 v16,v16,v12,v10
qt 134,36,8704
qt 106,4,12800
qm 92,108,212
v_mul_f32 v16,v16,v72
qt 204,36,9216
qt 108,5,12288
qw 10
qm 80,96,144
v_exp_f32 v16,v16
qt 206,36,9728
qt 110,5,12800
qm 84,96,216
v_fma_f32 v16,v16,v14,v14
qt 136,36,16384
qt 96,2,0
qw 10
qm 88,100,144
v_rcp_f32 v16,v16
qt 138,36,16896
qt 98,2,512
qm 92,100,216
v_mul_f32 v72,v72,v16
qt 208,36,17408
qt 100,3,0
qw 10
qm 80,104,148
v_mul_f32 v16,v73,v73
qt 210,36,17920
s_add_u32 s15,s77,0
buffer_load_dwordx4 v[56:59],v18,s[88:91],s15 offen offset:1024 sc1
qt 102,3,512
qm 84,104,220
v_fma_f32 v16,v16,v12,v10
qt 140,36,24576
qt 104,2,4096
qw 10
qm 88,108,148
v_mul_f32 v16,v16,v73
qt 142,36,25088
qt 106,2,4608
qm 92,108,220
v_exp_f32 v16,v16
qt 212,36,25600
qt 108,3,4096
qw 10
qm 80,96,152
v_fma_f32 v16,v16,v14,v14
qt 214,36,26112
qt 110,3,4608
qm 84,96,224
v_rcp_f32 v16,v16
qt 96,2,8192
qw 9
qm 88,100,152
v_mul_f32 v73,v73,v16
s_mul_i32 s30,s10,1310720
qt 98,2,8704
qm 92,100,224
v_mul_f32 v16,v74,v74
s_lshl_b32 s31,s8,14
qt 100,3,8192
qw 7
qm 80,104,156
v_fma_f32 v16,v16,v12,v10
qt 102,3,8704
qm 84,104,228
v_mul_f32 v16,v16,v74
s_add_u32 s73,s30,s31
qt 104,2,12288
qw 5
qm 88,108,156
s_barrier
v_exp_f32 v16,v16
s_add_u32 s15,s77,8192
buffer_load_dwordx4 v[52:55],v18,s[88:91],s15 offen offset:0 sc1
qt 106,2,12800
qm 92,108,228
v_fma_f32 v16,v16,v14,v14
s_add_u32 s73,s73,57344
qt 108,3,12288
qw 5
qm 80,96,160
v_rcp_f32 v16,v16
qt 110,3,12800
qm 84,96,232
v_mul_f32 v74,v74,v16
s_lshl_b32 s31,s8,11
qt 96,2,16384
qw 5
qm 88,100,160
v_mul_f32 v16,v75,v75
s_add_u32 s69,s30,s31
qt 98,2,16896
qm 92,100,232
v_fma_f32 v16,v16,v12,v10
qt 100,3,16384
qw 5
qm 80,104,164
v_mul_f32 v16,v16,v75
s_lshl_b32 s31,s7,8
qt 102,3,16896
qm 84,104,236
v_exp_f32 v16,v16
qt 104,2,20480
qw 5
qm 88,108,164
v_fma_f32 v16,v16,v14,v14
s_add_u32 s69,s69,s31
qt 106,2,20992
qm 92,108,236
v_rcp_f32 v16,v16
s_add_u32 s15,s77,8192
buffer_load_dwordx4 v[60:63],v18,s[88:91],s15 offen offset:1024 sc1
qt 108,3,20480
qw 5
qm 80,96,168
v_mul_f32 v75,v75,v16
s_add_u32 s69,s69,132096
qt 110,3,20992
qm 84,96,240
v_mul_f32 v16,v68,v68
s_and_b32 s75,s8,4
qt 96,2,24576
qw 5
qm 88,100,168
v_fma_f32 v16,v16,v13,v11
qt 98,2,25088
qm 92,100,240
v_mul_f32 v16,v16,v68
s_lshl_b32 s75,s75,4
qt 100,3,24576
qw 5
qm 80,104,172
v_exp_f32 v16,v16
qt 102,3,25088
qm 84,104,244
v_fma_f32 v16,v16,v15,v15
s_and_b32 s33,s8,3
s_lshl_b32 s33,s33,2
s_add_u32 s33,s33,s7
s_lshl_b32 s33,s33,3
s_lshl_b32 s30,s10,7
s_add_u32 s30,s30,s33
qt 104,2,28672
qw 5
qm 88,108,172
v_rcp_f32 v16,v16
qt 106,2,29184
qm 92,108,244
v_mul_f32 v68,v68,v16
s_add_u32 s30,s30,4
qt 108,3,28672
qw 5
qm 80,96,176
v_mul_f32 v16,v69,v69
s_mul_i32 s30,s30,1152
qt 110,3,29184
qm 84,96,248
v_fma_f32 v16,v16,v13,v11
s_add_u32 s15,s77,16384
buffer_load_dwordx4 v[120:123],v18,s[88:91],s15 offen offset:0 sc1
qd 96,24,32768
qw 5
qm 88,100,176
v_mul_f32 v16,v16,v69
s_add_u32 s70,s30,s75
qd 98,24,36864
qm 92,100,248
v_exp_f32 v16,v16
qd 100,25,32768
qw 5
qm 80,104,180
v_fma_f32 v16,v16,v15,v15
s_lshl_b32 s71,s70,1
qd 102,25,36864
qm 84,104,252
v_rcp_f32 v16,v16
qd 104,26,32768
qw 5
qm 88,108,180
s_barrier
v_mul_f32 v69,v69,v16
qd 106,26,36864
qm 92,108,252
qd 108,27,32768
qw 5
qz 152,96,128
v_mul_f32 v16,v70,v70
v_mul_f32 v17,v71,v71
v_fma_f32 v16,v16,v13,v11
qd 110,27,36864
qz 224,96,200
v_fma_f32 v17,v17,v13,v11
v_mul_f32 v16,v16,v70
qd 96,24,40960
qw 5
qm 152,100,132
v_mul_f32 v17,v17,v71
v_exp_f32 v16,v16
qd 98,24,45056
qm 224,100,204
v_exp_f32 v17,v17
v_fma_f32 v16,v16,v15,v15
v_fma_f32 v17,v17,v15,v15
s_add_u32 s15,s77,16384
buffer_load_dwordx4 v[192:195],v18,s[88:91],s15 offen offset:1024 sc1
qd 100,25,40960
qw 5
qm 152,104,136
v_rcp_f32 v16,v16
v_rcp_f32 v17,v17
qd 102,25,45056
qm 224,104,208
v_mul_f32 v70,v70,v16
v_mul_f32 v71,v71,v17
qd 104,26,40960
qw 5
qm 152,108,140
v_mul_f32 v16,v76,v76
v_mul_f32 v17,v77,v77
v_fma_f32 v16,v16,v13,v11
qd 106,26,45056
qm 224,108,212
v_fma_f32 v17,v17,v13,v11
v_mul_f32 v16,v16,v76
qd 108,27,40960
qw 5
qz 156,96,128
v_mul_f32 v17,v17,v77
v_exp_f32 v16,v16
qd 110,27,45056
qz 228,96,200
v_exp_f32 v17,v17
v_fma_f32 v16,v16,v15,v15
v_fma_f32 v17,v17,v15,v15
qd 96,24,49152
qw 5
qm 156,100,132
v_rcp_f32 v16,v16
v_rcp_f32 v17,v17
qd 98,24,53248
qm 228,100,204
v_mul_f32 v76,v76,v16
v_mul_f32 v77,v77,v17
qd 100,25,49152
qw 5
qm 156,104,136
v_mul_f32 v16,v78,v78
v_mul_f32 v17,v79,v79
v_fma_f32 v16,v16,v13,v11
s_add_u32 s15,s77,24576
buffer_load_dwordx4 v[124:127],v18,s[88:91],s15 offen offset:0 sc1
qd 102,25,53248
qm 228,104,208
v_fma_f32 v17,v17,v13,v11
v_mul_f32 v16,v16,v78
qd 104,26,49152
qw 5
qm 156,108,140
v_mul_f32 v17,v17,v79
v_exp_f32 v16,v16
qd 106,26,53248
qm 228,108,212
v_exp_f32 v17,v17
v_fma_f32 v16,v16,v15,v15
v_fma_f32 v17,v17,v15,v15
qd 108,27,49152
qw 5
qz 160,96,128
v_rcp_f32 v16,v16
v_rcp_f32 v17,v17
qd 110,27,53248
qz 232,96,200
v_mul_f32 v78,v78,v16
v_mul_f32 v79,v79,v17
qr 176,18,16384
qd 96,24,57344
qw 6
qm 160,100,132
v_mul_f32 v64,v64,v80
v_mul_f32 v65,v65,v81
v_cvt_pk_bf16_f32 v64,v64,v65
qd 98,24,61440
qm 232,100,204
v_mul_f32 v66,v66,v82
v_mul_f32 v67,v67,v83
qr 248,18,17408
qd 100,25,57344
qw 7
qm 160,104,136
v_cvt_pk_bf16_f32 v65,v66,v67
v_mul_f32 v72,v72,v88
qd 102,25,61440
qm 232,104,208
v_mul_f32 v73,v73,v89
v_cvt_pk_bf16_f32 v66,v72,v73
v_mul_f32 v74,v74,v90
qr 180,18,24576
qd 104,26,57344
qw 8
qm 160,108,140
qw 1
s_barrier
v_mul_f32 v75,v75,v91
v_cvt_pk_bf16_f32 v67,v74,v75
s_add_u32 s15,s77,24576
buffer_load_dwordx4 v[196:199],v18,s[88:91],s15 offen offset:1024 sc1
qd 106,26,61440
qm 232,108,212
s_add_u32 s15,s76,57344
buffer_store_dwordx4 v[64:67],v18,s[88:91],s15 offen offset:0 sc1
ds_write_b128 v18,v[64:67] offset:8192
qr 252,18,25600
qd 108,27,57344
qz 164,96,128
v_mul_f32 v68,v68,v84
v_mul_f32 v69,v69,v85
v_cvt_pk_bf16_f32 v68,v68,v69
qd 110,27,61440
qz 236,96,200
v_mul_f32 v70,v70,v86
v_mul_f32 v71,v71,v87
qt 96,4,0
qm 164,100,132
v_cvt_pk_bf16_f32 v69,v70,v71
v_mul_f32 v76,v76,v92
qt 98,4,512
qm 236,100,204
v_mul_f32 v77,v77,v93
v_cvt_pk_bf16_f32 v70,v76,v77
v_mul_f32 v78,v78,v94
qt 100,5,0
qw 7
qm 164,104,136
v_mul_f32 v79,v79,v95
v_cvt_pk_bf16_f32 v71,v78,v79
qt 102,5,512
qm 236,104,208
s_add_u32 s15,s76,57344
buffer_store_dwordx4 v[68:71],v18,s[88:91],s15 offen offset:1024 sc1
ds_write_b128 v18,v[68:71] offset:9216
qt 104,4,4096
qw 6
qm 164,108,140
qt 106,4,4608
qm 236,108,212
qt 108,5,4096
qw 6
qm 152,96,112
s_add_u32 s15,s77,32768
buffer_load_dwordx4 v[80:83],v18,s[88:91],s15 offen offset:0 sc1
qt 110,5,4608
qm 224,96,184
qr 168,18,32768
qt 96,4,8192
qw 7
qm 156,100,112
qt 98,4,8704
qm 228,100,184
qr 240,18,33792
qt 100,5,8192
qw 7
qm 152,104,116
qt 102,5,8704
qm 224,104,188
qr 172,18,40960
qt 104,4,12288
qw 8
qm 156,108,116
qt 106,4,12800
qm 228,108,188
qr 244,18,41984
qt 108,5,12288
qw 8
qm 160,96,112
qt 110,5,12800
qm 232,96,184
qt 96,2,0
qw 7
qm 164,100,112
s_add_u32 s15,s77,32768
buffer_load_dwordx4 v[88:91],v18,s[88:91],s15 offen offset:1024 sc1
qt 98,2,512
qm 236,100,184
qt 100,3,0
qw 6
qm 160,104,116
qt 102,3,512
qm 232,104,188
qt 104,2,4096
qw 5
qm 164,108,116
s_barrier
qt 106,2,4608
qm 236,108,188
qt 108,3,4096
qw 5
qm 152,96,176
qt 110,3,4608
qm 224,96,248
qt 96,2,8192
qw 5
qm 156,100,176
qt 98,2,8704
qm 228,100,248
qt 100,3,8192
qw 5
qm 152,104,180
qt 102,3,8704
qm 224,104,252
qt 104,2,12288
qw 5
qm 156,108,180
qt 106,2,12800
qm 228,108,252
qt 108,3,12288
qw 5
qm 160,96,176
qt 110,3,12800
qm 232,96,248
qt 96,2,16384
qw 5
qm 164,100,176
qt 98,2,16896
qm 236,100,248
qt 100,3,16384
qw 5
qm 160,104,180
qt 102,3,16896
qm 232,104,252
qt 104,2,20480
qw 5
qm 164,108,180
qt 106,2,20992
qm 236,108,252
qt 108,3,20480
qw 5
qm 152,96,168
qt 110,3,20992
qm 224,96,240
qt 96,2,24576
qw 5
qm 156,100,168
qt 98,2,25088
qm 228,100,240
qt 100,3,24576
qw 5
qm 152,104,172
buffer_load_dword v39,off,s[96:99],s78 sc1
qt 102,3,25088
qm 224,104,244
qt 104,2,28672
qw 5
qm 156,108,172
s_waitcnt vmcnt(0)
s_barrier
v_add_u32 v38,1,v38
buffer_store_dword v38,v37,s[96:99],s68 offen sc1
qt 106,2,29184
qm 228,108,244
qt 108,3,28672
qw 5
qm 160,96,168
v_and_b32 v1,0xfffffc3f,v18
qt 110,3,29184
qm 232,96,240
qr 64,18,0
qt 96,2,32768
qw 6
qm 164,100,168
qt 98,2,33280
qm 236,100,240
qr 72,18,1024
s_add_u32 s15,s77,40960
buffer_load_dwordx4 v[84:87],v18,s[88:91],s15 offen offset:0 sc1
qt 100,3,32768
qw 7
qm 160,104,172
v_lshrrev_b32 v16,4,v0
qt 102,3,33280
qm 232,104,244
qr 68,18,8192
qt 104,2,36864
qw 8
qm 164,108,172
qt 106,2,37376
qm 236,108,244
qr 76,18,9216
qt 108,3,36864
qw 8
qz 144,96,168
qt 110,3,37376
qz 216,96,240
v_lshl_add_u32 v1,v16,6,v1
qt 96,2,40960
qw 7
qz 148,100,168
qt 98,2,41472
qz 220,100,240
qt 100,3,40960
qw 6
qm 144,104,172
qt 102,3,41472
qm 216,104,244
s_add_u32 s15,s77,40960
buffer_load_dwordx4 v[92:95],v18,s[88:91],s15 offen offset:1024 sc1
qt 104,2,45056
qw 5
qm 148,108,172
v_and_b32 v16,12,v0
qt 106,2,45568
qm 220,108,244
qt 108,3,45056
qw 5
qm 144,96,64
)ASM"
R"ASM(qt 110,3,45568
qm 216,96,72
qt 96,2,49152
qw 5
qm 148,100,64
qt 98,2,49664
qm 220,100,72
v_lshl_add_u32 v1,v16,6,v1
qt 100,3,49152
qw 5
qm 144,104,68
qt 102,3,49664
qm 216,104,76
qt 104,2,53248
qw 5
qm 148,108,68
s_mov_b32 s35,0
.L_wYo2:
v_readfirstlane_b32 s34,v39
s_sub_u32 s34,s74,s34
s_cmp_le_u32 s34,6
s_cbranch_scc1 .L_wYo2d
s_add_u32 s35,s35,1
s_cmp_gt_u32 s35,1000000
s_cbranch_scc1 .L_wYo2d
s_sleep 2
buffer_load_dword v39,off,s[96:99],s78 sc1
s_waitcnt vmcnt(0)
s_branch .L_wYo2
.L_wYo2d:
s_barrier
qt 106,2,53760
qm 220,108,76
qt 108,3,53248
qw 5
qm 152,96,64
s_add_u32 s15,s77,49152
buffer_load_dwordx4 v[128:131],v18,s[88:91],s15 offen offset:0 sc1
qt 110,3,53760
qm 224,96,72
qt 96,2,57344
qw 5
qm 156,100,64
qt 98,2,57856
qm 228,100,72
v_cvt_pk_bf16_f32 v144,v144,v145
qt 100,3,57344
qw 5
qm 152,104,68
v_cvt_pk_bf16_f32 v145,v146,v147
qt 102,3,57856
qm 224,104,76
v_cvt_pk_bf16_f32 v146,v148,v149
qt 104,2,61440
qw 5
qm 156,108,68
s_add_u32 s15,s77,49152
buffer_load_dwordx4 v[200:203],v18,s[88:91],s15 offen offset:1024 sc1
v_cvt_pk_bf16_f32 v147,v150,v151
qt 106,2,61952
qm 228,108,76
qt 108,3,61440
qw 5
qm 160,96,64
v_cvt_pk_bf16_f32 v216,v216,v217
qt 110,3,61952
qm 232,96,72
v_cvt_pk_bf16_f32 v217,v218,v219
qt 96,4,0
qw 5
qm 164,100,64
v_cvt_pk_bf16_f32 v218,v220,v221
qt 98,4,512
qm 236,100,72
v_cvt_pk_bf16_f32 v219,v222,v223
qt 100,5,0
qw 5
qm 160,104,68
s_add_u32 s15,s77,57344
buffer_load_dwordx4 v[132:135],v18,s[88:91],s15 offen offset:0 sc1
qt 102,5,512
qm 232,104,76
qt 104,4,4096
qw 5
qm 164,108,68
qt 106,4,4608
qm 236,108,76
qt 108,5,4096
qw 5
qm 152,96,48
qt 110,5,4608
qm 224,96,56
qt 96,4,8192
qw 5
qm 156,100,48
s_add_u32 s15,s77,57344
buffer_load_dwordx4 v[204:207],v18,s[88:91],s15 offen offset:1024 sc1
qt 98,4,8704
qm 228,100,56
qt 100,5,8192
qw 5
qm 152,104,52
qt 102,5,8704
qm 224,104,60
qt 104,4,12288
qw 5
qm 156,108,52
s_barrier
qt 106,4,12800
qm 228,108,60
qt 108,5,12288
qw 5
qm 160,96,48
s_add_u32 s15,s77,65536
buffer_load_dwordx4 v[136:139],v18,s[88:91],s15 offen offset:0 sc1
qt 110,5,12800
qm 232,96,56
qt 96,2,0
qw 5
qm 164,100,48
qt 98,2,512
qm 236,100,56
qt 100,3,0
qw 5
qm 160,104,52
qt 102,3,512
qm 232,104,60
qt 104,2,4096
qw 5
qm 164,108,52
s_add_u32 s15,s77,65536
buffer_load_dwordx4 v[208:211],v18,s[88:91],s15 offen offset:1024 sc1
qt 106,2,4608
qm 236,108,60
qt 108,3,4096
qw 5
qm 152,96,120
qt 110,3,4608
qm 224,96,192
qt 96,2,8192
qw 5
qm 156,100,120
qt 98,2,8704
qm 228,100,192
qt 100,3,8192
qw 5
qm 152,104,124
s_add_u32 s15,s77,73728
buffer_load_dwordx4 v[140:143],v18,s[88:91],s15 offen offset:0 sc1
qt 102,3,8704
qm 224,104,196
qt 104,2,12288
qw 5
qm 156,108,124
qt 106,2,12800
qm 228,108,196
qt 108,3,12288
qw 5
qm 160,96,120
qt 110,3,12800
qm 232,96,192
qt 96,2,16384
qw 5
qm 164,100,120
s_add_u32 s15,s77,73728
buffer_load_dwordx4 v[212:215],v18,s[88:91],s15 offen offset:1024 sc1
qt 98,2,16896
qm 236,100,192
qt 100,3,16384
qw 5
qm 160,104,124
qt 102,3,16896
qm 232,104,196
qt 104,2,20480
qw 5
qm 164,108,124
s_barrier
qt 106,2,20992
qm 236,108,196
qt 108,3,20480
qw 5
qm 152,96,80
v_and_b32 v15,48,v0
qt 110,3,20992
qm 224,96,88
s_add_u32 s15,s73,1179648
buffer_store_dwordx4 v[144:147],v1,s[88:91],s15 offen offset:0 sc1
qt 96,2,24576
qw 5
qm 156,100,80
v_lshlrev_b32 v10,10,v15
qt 98,2,25088
qm 228,100,88
qt 100,3,24576
s_waitcnt vmcnt(10) lgkmcnt(5)
qm 152,104,84
qt 102,3,25088
s_waitcnt vmcnt(9)
qm 224,104,92
v_and_b32 v16,16,v0
qt 104,2,28672
qw 5
qm 156,108,84
qt 106,2,29184
qm 228,108,92
qt 108,3,28672
qw 5
qm 160,96,80
v_lshrrev_b32 v17,5,v0
qt 110,3,29184
qm 232,96,88
s_add_u32 s15,s73,1179648
buffer_store_dwordx4 v[216:219],v1,s[88:91],s15 offen offset:1024 sc1
qt 96,2,32768
qw 5
qm 164,100,80
v_lshl_add_u32 v12,v17,6,v16
qt 98,2,33280
qm 236,100,88
qt 100,3,32768
qw 5
qm 160,104,84
qt 102,3,33280
qm 232,104,92
v_and_b32 v16,3,v0
qt 104,2,36864
qw 5
qm 164,108,84
qt 106,2,37376
qm 236,108,92
qt 108,3,36864
s_waitcnt vmcnt(9) lgkmcnt(5)
qm 152,96,128
v_lshl_add_u32 v12,v16,2,v12
qt 110,3,37376
s_waitcnt vmcnt(8)
qm 224,96,200
qt 96,2,40960
qw 5
qm 156,100,128
v_lshl_add_u32 v10,v16,6,v10
qt 98,2,41472
qm 228,100,200
qt 100,3,40960
s_waitcnt vmcnt(7) lgkmcnt(5)
qm 152,104,132
qt 102,3,41472
s_waitcnt vmcnt(6)
qm 224,104,204
v_bfe_u32 v11,v0,2,2
qt 104,2,45056
qw 5
qm 156,108,132
s_barrier
qt 106,2,45568
qm 228,108,204
qt 108,3,45056
s_waitcnt vmcnt(5) lgkmcnt(5)
qm 152,96,136
v_lshl_add_u32 v10,v11,4,v10
qt 110,3,45568
s_waitcnt vmcnt(4)
qm 224,96,208
qt 96,2,49152
qw 5
qm 156,100,136
v_lshlrev_b32 v12,1,v12
qt 98,2,49664
qm 228,100,208
qt 100,3,49152
s_waitcnt vmcnt(3) lgkmcnt(5)
qm 152,104,140
qt 102,3,49664
s_waitcnt vmcnt(2)
qm 224,104,212
v_mul_u32_u24 v11,1152,v11
qt 104,2,53248
qw 5
qm 156,108,140
qt 106,2,53760
qm 228,108,212
qt 108,3,53248
qw 5
qm 160,96,128
v_add_u32 v11,v11,v12
qt 110,3,53760
qm 232,96,200
qt 96,2,57344
qw 5
qm 164,100,128
v_lshlrev_b32 v13,1,v11
qt 98,2,57856
qm 236,100,200
v_cvt_pk_bf16_f32 v152,v152,v153
qt 100,3,57344
qw 5
qm 160,104,132
v_cvt_pk_bf16_f32 v153,v154,v155
qt 102,3,57856
qm 232,104,204
v_cvt_pk_bf16_f32 v154,v156,v157
v_lshl_or_b32 v14,v17,30,v13
qt 104,2,61440
qw 5
qm 164,108,132
v_cvt_pk_bf16_f32 v155,v158,v159
qt 106,2,61952
qm 236,108,204
qt 108,3,61440
qw 5
qm 160,96,136
v_cvt_pk_bf16_f32 v224,v224,v225
s_add_u32 s15,s73,655360
buffer_store_dwordx4 v[152:155],v1,s[88:91],s15 offen offset:0 sc1
s_and_b32 s30,s8,3
s_lshl_b32 s30,s30,4
s_mov_b64 s[32:33],0xffff
s_lshl_b64 s[32:33],s[32:33],s30
s_bitcmp1_b32 s8,2
s_cselect_b64 s[40:41],0,s[32:33]
s_cselect_b64 s[42:43],s[32:33],0
qt 110,3,61952
qm 232,96,208
v_cvt_pk_bf16_f32 v225,v226,v227
qt 96,4,0
qw 5
qm 164,100,136
v_cvt_pk_bf16_f32 v226,v228,v229
s_mov_b64 s[52:53],0
qt 98,4,512
qm 236,100,208
v_cvt_pk_bf16_f32 v227,v230,v231
qt 100,5,0
qw 5
qm 160,104,140
qt 102,5,512
qm 232,104,212
s_mov_b64 s[54:55],0
qt 104,4,4096
qw 5
qm 164,108,140
s_barrier
qt 106,4,4608
qm 236,108,212
qt 108,5,4096
qw 5
qz 168,96,112
s_add_u32 s15,s73,655360
buffer_store_dwordx4 v[224:227],v1,s[88:91],s15 offen offset:1024 sc1
s_mov_b64 s[56:57],0
qt 110,5,4608
qz 240,96,184
qt 96,4,8192
qw 5
qz 172,100,112
s_mov_b64 s[58:59],0
qt 98,4,8704
qz 244,100,184
v_cvt_pk_bf16_f32 v160,v160,v161
qt 100,5,8192
qw 5
qm 168,104,116
v_cvt_pk_bf16_f32 v161,v162,v163
qt 102,5,8704
qm 240,104,188
v_cvt_pk_bf16_f32 v162,v164,v165
s_mov_b32 s76,0
qt 104,4,12288
qw 5
qm 172,108,116
v_cvt_pk_bf16_f32 v163,v166,v167
qt 106,4,12800
qm 244,108,188
qt 108,5,12288
qw 5
qz 176,96,112
v_cvt_pk_bf16_f32 v232,v232,v233
s_add_u32 s15,s73,786432
buffer_store_dwordx4 v[160:163],v1,s[88:91],s15 offen offset:0 sc1
qt 110,5,12800
qz 248,96,184
v_cvt_pk_bf16_f32 v233,v234,v235
qt 96,2,0
qw 5
qz 180,100,112
v_cvt_pk_bf16_f32 v234,v236,v237
qt 98,2,512
qz 252,100,184
v_cvt_pk_bf16_f32 v235,v238,v239
qt 100,3,0
qw 5
qm 176,104,116
qt 102,3,512
qm 248,104,188
v_and_b32 v16,1,v0
qt 104,2,4096
qw 5
qm 180,108,116
qt 106,2,4608
qm 252,108,188
v_lshlrev_b32 v16,4,v16
qt 108,3,4096
qw 5
qm 168,96,48
s_add_u32 s15,s73,786432
buffer_store_dwordx4 v[232:235],v1,s[88:91],s15 offen offset:1024 sc1
qt 110,3,4608
qm 240,96,56
v_mov_b32 v17,0x3f80
qt 96,2,8192
qw 5
qm 172,100,48
qt 98,2,8704
qm 244,100,56
v_lshlrev_b32 v16,v16,v17
qt 100,3,8192
qw 5
qm 168,104,52
qt 102,3,8704
qm 240,104,60
v_bfe_u32 v17,v0,1,3
qt 104,2,12288
qw 5
qm 172,108,52
s_barrier
qt 106,2,12800
qm 244,108,60
v_cmp_eq_u32 vcc,0,v17
s_nop 1
v_cndmask_b32 v152,0,v16,vcc
qt 108,3,12288
qw 5
qm 176,96,48
qt 110,3,12800
qm 248,96,56
v_cmp_eq_u32 vcc,1,v17
s_nop 1
v_cndmask_b32 v153,0,v16,vcc
qt 96,2,16384
qw 5
qm 180,100,48
qt 98,2,16896
qm 252,100,56
v_cmp_eq_u32 vcc,2,v17
s_nop 1
v_cndmask_b32 v154,0,v16,vcc
qt 100,3,16384
qw 5
qm 176,104,52
qt 102,3,16896
qm 248,104,60
v_cmp_eq_u32 vcc,3,v17
s_nop 1
v_cndmask_b32 v155,0,v16,vcc
qt 104,2,20480
qw 5
qm 180,108,52
qt 106,2,20992
qm 252,108,60
v_cmp_eq_u32 vcc,4,v17
s_nop 1
v_cndmask_b32 v156,0,v16,vcc
qt 108,3,20480
qw 5
qm 168,96,120
qt 110,3,20992
qm 240,96,192
v_cmp_eq_u32 vcc,5,v17
s_nop 1
v_cndmask_b32 v157,0,v16,vcc
qt 96,2,24576
qw 5
qm 172,100,120
qt 98,2,25088
qm 244,100,192
v_cmp_eq_u32 vcc,6,v17
s_nop 1
v_cndmask_b32 v158,0,v16,vcc
qt 100,3,24576
qw 5
qm 168,104,124
qt 102,3,25088
qm 240,104,196
v_cmp_eq_u32 vcc,7,v17
s_nop 1
v_cndmask_b32 v159,0,v16,vcc
qt 104,2,28672
qw 5
qm 172,108,124
qt 106,2,29184
qm 244,108,196
qt 108,3,28672
qw 5
qm 176,96,120
qt 110,3,29184
qm 248,96,192
qt 96,2,32768
qw 5
qm 180,100,120
qt 98,2,33280
qm 252,100,192
qt 100,3,32768
qw 5
qm 176,104,124
qt 102,3,33280
qm 248,104,196
qt 104,2,36864
qw 5
qm 180,108,124
s_waitcnt vmcnt(0)
s_barrier
v_add_u32 v38,1,v38
buffer_store_dword v38,v37,s[96:99],s68 offen sc1
qt 106,2,37376
qm 252,108,196
qt 108,3,36864
qw 5
qm 168,96,80
buffer_load_dwordx2 v[224:225],v11,s[36:39],s70 offen offset:512
qt 110,3,37376
qm 240,96,88
qr 120,18,16384
qt 96,2,40960
qw 6
qm 172,100,80
qt 98,2,41472
qm 244,100,88
qr 192,18,17408
qt 100,3,40960
qw 7
qm 168,104,84
qt 102,3,41472
qm 240,104,92
qr 124,18,24576
qt 104,2,45056
qw 8
qm 172,108,84
buffer_load_dwordx2 v[226:227],v12,s[80:83],s75 offen offset:512
qt 106,2,45568
qm 244,108,92
qr 196,18,25600
qt 108,3,45056
qw 8
qm 176,96,80
qt 110,3,45568
qm 248,96,88
qt 96,2,49152
qw 7
qm 180,100,80
qt 98,2,49664
qm 252,100,88
qt 100,3,49152
qw 6
qm 176,104,84
buffer_load_dwordx2 v[228:229],v11,s[36:39],s70 offen offset:0
qt 102,3,49664
qm 248,104,92
qt 104,2,53248
qw 5
qm 180,108,84
qt 106,2,53760
qm 252,108,92
qt 108,3,53248
qw 5
qm 168,96,128
buffer_load_dwordx4 v[220:223],v15,s[96:99],s72 offen sc1
qt 110,3,53760
qm 240,96,200
qt 80,36,0
qt 96,2,57344
qw 6
qm 172,100,128
qt 82,36,512
buffer_load_dwordx2 v[230:231],v12,s[80:83],s75 offen offset:0
qt 98,2,57856
qm 244,100,200
qt 88,36,1024
qt 100,3,57344
qw 8
qm 168,104,132
qt 90,36,1536
qt 102,3,57856
qm 240,104,204
qt 84,36,8192
qt 104,2,61440
qw 10
qm 172,108,132
qw 1
s_barrier
qt 86,36,8704
qt 106,2,61952
qm 244,108,204
qt 92,36,9216
qt 108,3,61440
qm 176,96,128
qt 94,36,9728
buffer_load_dwordx2 v[42:43],v11,s[36:39],s70 offen offset:640
qt 110,3,61952
qm 248,96,200
qt 96,4,0
qm 180,100,128
qt 98,4,512
qm 252,100,200
qt 100,5,0
qw 7
qm 176,104,132
qt 102,5,512
qm 248,104,204
qt 104,4,4096
qw 5
qm 180,108,132
buffer_load_dwordx2 v[44:45],v12,s[80:83],s75 offen offset:640
qt 106,4,4608
qm 252,108,204
qt 108,5,4096
qw 5
qm 168,96,136
qt 110,5,4608
qm 240,96,208
qt 128,36,16384
qt 96,4,8192
qw 6
qm 172,100,136
qt 130,36,16896
qt 98,4,8704
qm 244,100,208
qt 200,36,17408
qt 100,5,8192
qw 8
qm 168,104,140
qt 202,36,17920
qt 102,5,8704
qm 240,104,212
qt 132,36,24576
qt 104,4,12288
qw 10
qm 172,108,140
qt 134,36,25088
qt 106,4,12800
qm 244,108,212
qt 204,36,25600
qt 108,5,12288
v_mov_b32 v16,0x7ff00000
s_waitcnt vmcnt(3)
v_sub_u32 v220,s74,v220
v_sub_u32 v221,s74,v221
v_sub_u32 v222,s74,v222
v_sub_u32 v223,s74,v223
v_cndmask_b32 v222,v222,0,s[40:41]
v_cndmask_b32 v223,v223,0,s[42:43]
qw 10
qm 176,96,136
qt 206,36,26112
qt 110,5,12800
v_cmp_ge_u32 s[52:53],5,v220
v_cmp_ge_u32 s[54:55],5,v221
v_cmp_ge_u32 s[56:57],5,v222
v_cmp_ge_u32 s[58:59],5,v223
qm 248,96,208
qt 96,2,0
v_cndmask_b32 v32,v16,v10,s[52:53]
v_cndmask_b32 v33,v16,v10,s[54:55]
v_cndmask_b32 v34,v16,v10,s[56:57]
v_cndmask_b32 v35,v16,v10,s[58:59]
qw 9
qm 180,100,136
s_add_u32 s31,s69,524288
buffer_load_dwordx4 v[160:163],v32,s[88:91],s31 offen sc1
qt 98,2,512
qm 252,100,208
qt 100,3,0
qw 7
qm 176,104,140
qt 102,3,512
qm 248,104,212
qt 104,2,4096
qw 5
qm 180,108,140
s_barrier
s_add_u32 s31,s69,0
buffer_load_dwordx4 v[112:115],v32,s[88:91],s31 offen sc1
qt 106,2,4608
qm 252,108,212
qt 108,3,4096
qw 5
qm 168,96,64
qt 110,3,4608
qm 240,96,72
qr 136,18,32768
qt 96,2,8192
qw 6
qm 172,100,64
s_add_u32 s31,s69,131072
buffer_load_dwordx4 v[184:187],v32,s[88:91],s31 offen sc1
qt 98,2,8704
qm 244,100,72
qr 208,18,33792
qt 100,3,8192
qw 7
qm 168,104,68
qt 102,3,8704
qm 240,104,76
qr 140,18,40960
qt 104,2,12288
qw 8
qm 172,108,68
s_add_u32 s31,s69,589824
buffer_load_dwordx4 v[164:167],v33,s[88:91],s31 offen sc1
qt 106,2,12800
qm 244,108,76
qr 212,18,41984
qt 108,3,12288
qw 8
qm 168,96,120
qt 110,3,12800
qm 240,96,192
qd 96,24,16384
qw 7
qm 172,100,120
s_add_u32 s31,s69,65536
buffer_load_dwordx4 v[116:119],v33,s[88:91],s31 offen sc1
qd 98,24,20480
qm 244,100,192
qd 100,25,16384
qw 6
qm 168,104,124
qd 102,25,20480
qm 240,104,196
qd 104,26,16384
qw 5
qm 172,108,124
s_add_u32 s31,s69,196608
buffer_load_dwordx4 v[188:191],v33,s[88:91],s31 offen sc1
qd 106,26,20480
qm 244,108,196
qd 108,27,16384
qw 5
qm 168,96,80
qd 110,27,20480
qm 240,96,88
qd 96,24,24576
qw 5
qm 168,100,84
s_add_u32 s31,s69,1179648
buffer_load_dwordx4 v[232:235],v34,s[88:91],s31 offen sc1
qd 98,24,28672
qm 240,100,92
qd 100,25,24576
qw 5
qm 168,104,128
qd 102,25,28672
qm 240,104,200
qd 104,26,24576
qw 5
qm 168,108,132
s_barrier
s_add_u32 s31,s69,655360
buffer_load_dwordx4 v[48:51],v34,s[88:91],s31 offen sc1
qd 106,26,28672
qm 240,108,204
qd 108,27,24576
qw 5
qm 172,96,80
qd 110,27,28672
qm 244,96,88
qt 96,2,32768
qw 5
qm 172,100,84
s_add_u32 s31,s69,786432
buffer_load_dwordx4 v[56:59],v34,s[88:91],s31 offen sc1
qt 98,2,33280
qm 244,100,92
qt 100,3,32768
qw 5
qm 172,104,128
qt 102,3,33280
qm 244,104,200
qt 104,2,36864
qw 5
qm 172,108,132
s_add_u32 s31,s69,1245184
buffer_load_dwordx4 v[236:239],v35,s[88:91],s31 offen sc1
qt 106,2,37376
qm 244,108,204
qt 108,3,36864
qw 5
qm 168,96,136
qt 110,3,37376
qm 240,96,208
qt 96,2,40960
qw 5
qm 172,100,136
s_add_u32 s31,s69,720896
buffer_load_dwordx4 v[52:55],v35,s[88:91],s31 offen sc1
qt 98,2,41472
qm 244,100,208
qt 100,3,40960
qw 5
qm 168,104,140
qt 102,3,41472
qm 240,104,212
qt 104,2,45056
qw 5
qm 172,108,140
s_add_u32 s31,s69,851968
buffer_load_dwordx4 v[60:63],v35,s[88:91],s31 offen sc1
qt 106,2,45568
qm 244,108,212
qt 108,3,45056
qw 5
qm 176,96,64
qt 110,3,45568
qm 248,96,72
qt 96,2,49152
qw 5
qm 180,100,64
qt 98,2,49664
qm 252,100,72
v_cvt_pk_bf16_f32 v168,v168,v169
qt 100,3,49152
qw 5
qm 176,104,68
v_cvt_pk_bf16_f32 v169,v170,v171
qt 102,3,49664
qm 248,104,76
v_cvt_pk_bf16_f32 v170,v172,v173
qt 104,2,53248
qw 5
qm 180,108,68
s_barrier
v_cvt_pk_bf16_f32 v171,v174,v175
qt 106,2,53760
qm 252,108,76
qt 108,3,53248
qw 5
qm 176,96,120
v_cvt_pk_bf16_f32 v240,v240,v241
s_add_u32 s15,s73,917504
buffer_store_dwordx4 v[168:171],v1,s[88:91],s15 offen offset:0 sc1
qt 110,3,53760
qm 248,96,192
v_cvt_pk_bf16_f32 v241,v242,v243
qd 96,24,57344
qw 5
qm 180,100,120
v_cvt_pk_bf16_f32 v242,v244,v245
qd 98,24,61440
qm 252,100,192
v_cvt_pk_bf16_f32 v243,v246,v247
qd 100,25,57344
qw 5
qm 176,104,124
qd 102,25,61440
qm 248,104,196
qd 104,26,57344
qw 5
qm 180,108,124
qd 106,26,61440
qm 252,108,196
qd 108,27,57344
qw 5
qm 176,96,80
s_add_u32 s15,s73,917504
buffer_store_dwordx4 v[240:243],v1,s[88:91],s15 offen offset:1024 sc1
qd 110,27,61440
qm 248,96,88
qd 96,28,0
qw 5
qm 176,100,84
qd 98,28,4096
qm 248,100,92
qd 100,29,0
qw 5
qm 176,104,128
qd 102,29,4096
qm 248,104,200
qd 104,30,0
qw 5
qm 176,108,132
qd 106,30,4096
qm 248,108,204
qd 108,31,0
qw 5
qm 180,96,80
qd 110,31,4096
qm 252,96,88
qt 96,4,8192
qw 5
qm 180,100,84
qt 98,4,8704
qm 252,100,92
qt 100,5,8192
qw 5
qm 180,104,128
qt 102,5,8704
qm 252,104,200
qt 104,4,12288
qw 5
qm 180,108,132
s_barrier
qt 106,4,12800
qm 252,108,204
qt 108,5,12288
qw 5
qm 176,96,136
qt 110,5,12800
qm 248,96,208
qw 4
qm 180,100,136
qm 252,100,208
qw 2
qm 176,104,140
qm 248,104,212
qw 0
qm 180,108,140
qm 252,108,212
s_nop 3
v_cvt_pk_bf16_f32 v176,v176,v177
v_cvt_pk_bf16_f32 v177,v178,v179
s_nop 0
v_cvt_pk_bf16_f32 v178,v180,v181
v_cvt_pk_bf16_f32 v179,v182,v183
s_add_u32 s15,s73,1048576
buffer_store_dwordx4 v[176:179],v1,s[88:91],s15 offen offset:0 sc1
v_cvt_pk_bf16_f32 v248,v248,v249
v_cvt_pk_bf16_f32 v249,v250,v251
v_cvt_pk_bf16_f32 v250,v252,v253
v_cvt_pk_bf16_f32 v251,v254,v255
s_add_u32 s15,s73,1048576
buffer_store_dwordx4 v[248:251],v1,s[88:91],s15 offen offset:1024 sc1
v_lshlrev_b32 v16,16,v226
v_lshlrev_b32 v144,16,v224
v_mul_f32 v16,v16,v40
v_add_f32 v17,v144,v41
v_fma_f32 v144,v17,v16,v144
v_and_b32 v16,0xffff0000,v226
v_and_b32 v145,0xffff0000,v224
v_mul_f32 v16,v16,v40
v_add_f32 v17,v145,v41
v_fma_f32 v145,v17,v16,v145
v_lshlrev_b32 v16,16,v227
v_lshlrev_b32 v146,16,v225
v_mul_f32 v16,v16,v40
v_add_f32 v17,v146,v41
v_fma_f32 v146,v17,v16,v146
v_and_b32 v16,0xffff0000,v227
v_and_b32 v147,0xffff0000,v225
v_mul_f32 v16,v16,v40
v_add_f32 v17,v147,v41
v_fma_f32 v147,v17,v16,v147
s_waitcnt vmcnt(18)
v_lshlrev_b32 v16,16,v230
v_lshlrev_b32 v148,16,v228
v_mul_f32 v16,v16,v40
v_add_f32 v17,v148,v41
v_fma_f32 v148,v17,v16,v148
v_and_b32 v16,0xffff0000,v230
v_and_b32 v149,0xffff0000,v228
v_mul_f32 v16,v16,v40
v_add_f32 v17,v149,v41
v_fma_f32 v149,v17,v16,v149
v_lshlrev_b32 v16,16,v231
v_lshlrev_b32 v150,16,v229
v_mul_f32 v16,v16,v40
v_add_f32 v17,v150,v41
v_fma_f32 v150,v17,v16,v150
v_and_b32 v16,0xffff0000,v231
v_and_b32 v151,0xffff0000,v229
v_mul_f32 v16,v16,v40
v_add_f32 v17,v151,v41
v_fma_f32 v151,v17,v16,v151
s_waitcnt vmcnt(16)
v_lshlrev_b32 v16,16,v44
v_lshlrev_b32 v216,16,v42
v_mul_f32 v16,v16,v40
v_add_f32 v17,v216,v41
v_fma_f32 v216,v17,v16,v216
v_and_b32 v16,0xffff0000,v44
v_and_b32 v217,0xffff0000,v42
v_mul_f32 v16,v16,v40
v_add_f32 v17,v217,v41
v_fma_f32 v217,v17,v16,v217
v_lshlrev_b32 v16,16,v45
v_lshlrev_b32 v218,16,v43
v_mul_f32 v16,v16,v40
v_add_f32 v17,v218,v41
v_fma_f32 v218,v17,v16,v218
v_and_b32 v16,0xffff0000,v45
v_and_b32 v219,0xffff0000,v43
v_mul_f32 v16,v16,v40
v_add_f32 v17,v219,v41
v_fma_f32 v219,v17,v16,v219
buffer_load_dwordx4 v[220:223],v15,s[96:99],s72 offen sc1
v_add_u32 v38,5,v38
s_setprio 3
s_waitcnt vmcnt(0)
s_barrier
buffer_store_dword v38,v37,s[96:99],s68 offen sc1
s_setprio 0
.L_tYo1:
v_sub_u32 v220,s74,v220
v_sub_u32 v221,s74,v221
v_sub_u32 v222,s74,v222
v_sub_u32 v223,s74,v223
v_cndmask_b32 v222,v222,0,s[40:41]
v_cndmask_b32 v223,v223,0,s[42:43]
v_cmp_ge_u32 vcc,5,v220
s_nop 1
s_andn2_b64 exec,vcc,s[52:53]
s_cbranch_scc0 .L_tYo1r10
s_add_u32 s31,s69,524288
buffer_load_dwordx4 v[160:163],v10,s[88:91],s31 offen sc1
s_add_u32 s31,s69,0
buffer_load_dwordx4 v[112:115],v10,s[88:91],s31 offen sc1
s_add_u32 s31,s69,131072
buffer_load_dwordx4 v[184:187],v10,s[88:91],s31 offen sc1
.L_tYo1r10:
s_mov_b64 exec,-1
s_or_b64 s[52:53],s[52:53],vcc
v_cmp_ge_u32 vcc,5,v221
s_nop 1
s_andn2_b64 exec,vcc,s[54:55]
s_cbranch_scc0 .L_tYo1r11
s_add_u32 s31,s69,589824
buffer_load_dwordx4 v[164:167],v10,s[88:91],s31 offen sc1
s_add_u32 s31,s69,65536
buffer_load_dwordx4 v[116:119],v10,s[88:91],s31 offen sc1
s_add_u32 s31,s69,196608
buffer_load_dwordx4 v[188:191],v10,s[88:91],s31 offen sc1
.L_tYo1r11:
s_mov_b64 exec,-1
s_or_b64 s[54:55],s[54:55],vcc
v_cmp_ge_u32 vcc,5,v222
s_nop 1
s_andn2_b64 exec,vcc,s[56:57]
s_cbranch_scc0 .L_tYo1r12
s_add_u32 s31,s69,1179648
buffer_load_dwordx4 v[232:235],v10,s[88:91],s31 offen sc1
s_add_u32 s31,s69,655360
buffer_load_dwordx4 v[48:51],v10,s[88:91],s31 offen sc1
s_add_u32 s31,s69,786432
buffer_load_dwordx4 v[56:59],v10,s[88:91],s31 offen sc1
.L_tYo1r12:
s_mov_b64 exec,-1
s_or_b64 s[56:57],s[56:57],vcc
v_cmp_ge_u32 vcc,5,v223
s_nop 1
s_andn2_b64 exec,vcc,s[58:59]
s_cbranch_scc0 .L_tYo1r13
s_add_u32 s31,s69,1245184
buffer_load_dwordx4 v[236:239],v10,s[88:91],s31 offen sc1
s_add_u32 s31,s69,720896
buffer_load_dwordx4 v[52:55],v10,s[88:91],s31 offen sc1
s_add_u32 s31,s69,851968
buffer_load_dwordx4 v[60:63],v10,s[88:91],s31 offen sc1
.L_tYo1r13:
s_mov_b64 exec,-1
s_or_b64 s[58:59],s[58:59],vcc
.L_tYo1c:
s_and_b64 s[32:33],s[52:53],s[54:55]
s_and_b64 s[32:33],s[32:33],s[56:57]
s_and_b64 s[32:33],s[32:33],s[58:59]
s_cmp_eq_u64 s[32:33],-1
s_cbranch_scc1 .L_tYo1x
s_add_u32 s76,s76,1
s_cmp_gt_u32 s76,1000000
s_cbranch_scc1 .L_tYo1x
s_sleep 4
buffer_load_dwordx4 v[220:223],v15,s[96:99],s72 offen sc1
s_waitcnt vmcnt(0)
s_branch .L_tYo1
.L_tYo1x:
s_waitcnt vmcnt(0)
qm 144,152,160
qm 148,152,112
qm 216,152,48
qm 144,152,164
qm 148,152,116
qm 216,152,52
qm 144,152,232
qm 148,156,184
qm 216,156,56
qm 144,152,236
qm 148,156,188
qm 216,156,60
s_nop 5
buffer_store_dwordx4 v[144:147],v14,s[92:95],s71 offen offset:1024 sc1
buffer_store_dwordx4 v[148:151],v13,s[92:95],s71 offen offset:0 sc1
buffer_store_dwordx4 v[216:219],v13,s[92:95],s71 offen offset:1280 sc1
s_endpgm
.p2align 7
.L_end:

)ASM"
      :
      : "{s[0:1]}"(__builtin_amdgcn_kernarg_segment_ptr()), "{s2}"(blockIdx.x), "{v0}"(threadIdx.x), "{s3}"((u32)(size_t)(__attribute__((address_space(3))) char*)smem)
      : "s4", "s5", "s6", "s7", "s8", "s9", "s10", "s11", "s12", "s13", "s14", "s15", "s16", "s17", "s18", "s19", "s20", "s21", "s22", "s23", "s24", "s25",
        "s26", "s27", "s28", "s29", "s30", "s31", "s33", "s34", "s35", "s36", "s37", "s38", "s39", "s40", "s41", "s42", "s43", "s44", "s45", "s46", "s47",
        "s48", "s49", "s50", "s51", "s52", "s53", "s54", "s55", "s56", "s57", "s58", "s59", "s60", "s61", "s62", "s63", "s64", "s65", "s66", "s67", "s68",
        "s69", "s70", "s71", "s72", "s73", "s74", "s75", "s76", "s77", "s78", "s79", "s80", "s81", "s82", "s83", "s84", "s85", "s86", "s87", "s88", "s89",
        "s90", "s91", "s92", "s93", "s94", "s95", "s96", "s97", "s98", "s99", "v1", "v2", "v3", "v4", "v5", "v6", "v7", "v8", "v9", "v10", "v11", "v12", "v13",
        "v14", "v15", "v16", "v17", "v18", "v19", "v20", "v21", "v22", "v23", "v24", "v25", "v26", "v27", "v28", "v29", "v30", "v31", "v32", "v33", "v34",
        "v35", "v36", "v37", "v38", "v39", "v40", "v41", "v42", "v43", "v44", "v45", "v46", "v47", "v48", "v49", "v50", "v51", "v52", "v53", "v54", "v55",
        "v56", "v57", "v58", "v59", "v60", "v61", "v62", "v63", "v64", "v65", "v66", "v67", "v68", "v69", "v70", "v71", "v72", "v73", "v74", "v75", "v76",
        "v77", "v78", "v79", "v80", "v81", "v82", "v83", "v84", "v85", "v86", "v87", "v88", "v89", "v90", "v91", "v92", "v93", "v94", "v95", "v96", "v97",
        "v98", "v99", "v100", "v101", "v102", "v103", "v104", "v105", "v106", "v107", "v108", "v109", "v110", "v111", "v112", "v113", "v114", "v115", "v116",
        "v117", "v118", "v119", "v120", "v121", "v122", "v123", "v124", "v125", "v126", "v127", "v128", "v129", "v130", "v131", "v132", "v133", "v134", "v135",
        "v136", "v137", "v138", "v139", "v140", "v141", "v142", "v143", "v144", "v145", "v146", "v147", "v148", "v149", "v150", "v151", "v152", "v153", "v154",
        "v155", "v156", "v157", "v158", "v159", "v160", "v161", "v162", "v163", "v164", "v165", "v166", "v167", "v168", "v169", "v170", "v171", "v172", "v173",
        "v174", "v175", "v176", "v177", "v178", "v179", "v180", "v181", "v182", "v183", "v184", "v185", "v186", "v187", "v188", "v189", "v190", "v191", "v192",
        "v193", "v194", "v195", "v196", "v197", "v198", "v199", "v200", "v201", "v202", "v203", "v204", "v205", "v206", "v207", "v208", "v209", "v210", "v211",
        "v212", "v213", "v214", "v215", "v216", "v217", "v218", "v219", "v220", "v221", "v222", "v223", "v224", "v225", "v226", "v227", "v228", "v229", "v230",
        "v231", "v232", "v233", "v234", "v235", "v236", "v237", "v238", "v239", "v240", "v241", "v242", "v243", "v244", "v245", "v246", "v247", "v248", "v249",
        "v250", "v251", "v252", "v253", "v254", "v255", "vcc", "scc", "memory");
}

extern "C" void kernel_launch(void* const* d_in, const int* in_sizes, int n_in, void* d_out, int out_size, void* d_ws, size_t ws_size,
                              hipStream_t stream) {
  char* ws = (char*)d_ws;
  Args a;
  const int idx[12] = {0, 10, 11, 12, 3, 4, 5, 6, 1, 2, 7, 8};
  for (int i = 0; i < 12; ++i) a.d[i] = Srd{d_in[idx[i]], (u32)in_sizes[idx[i]] * 2u, 0x00020000u};
  a.d[12] = Srd{ws + WS_SX, 16u * 576u * 16u, 0x00020000u};
  a.d[13] = Srd{ws + WS_PART, (u32)PART_N, 0x00020000u};
  a.d[14] = Srd{d_out, 2048u * 576u * 4u, 0x00020000u};
  a.d[15] = Srd{ws + WS_MISC, 393216u, 0x00020000u};
  k1_main<<<256, 512, 0, stream>>>(a);
}
```
